```python
import torch

def _softmax(x, axis=-1):
    return torch.softmax(x, dim=axis)
def _gelu(x):
    return torch.nn.functional.gelu(x, approximate="tanh")
def _silu(x):
    return torch.nn.functional.silu(x)
def _sigmoid(x):
    return torch.sigmoid(x)
def _layernorm(x, axis=-1, eps=1e-5):
    m = x.mean(dim=axis, keepdim=True)
    v = x.var(dim=axis, keepdim=True, unbiased=False)
    return (x - m) / torch.sqrt(v + eps)
def _scatter_add(tgt, idx, src, axis=0):
    return tgt.clone().index_add_(axis, idx, src)
def _segment_sum(src, seg, n):
    out = torch.zeros((n,) + src.shape[1:], dtype=src.dtype)
    return out.index_add_(0, seg, src)
def _pad(x, pw):
    flat = tuple(v for pair in reversed(pw) for v in pair)
    return torch.nn.functional.pad(x, flat)

kernel_name = 'recurrent_scan__mha_block_cached__glu_gated_reglu__prompt'

def setup_inputs():

    _w = (lambda a: (lambda t: t.to(torch.bfloat16) if t.is_floating_point() else t)(torch.as_tensor(a)))
    def _A_setup():
        B, Sq, D = 1, 2048, 1024
        H, Sk, Dh = 32, 2048, 128
        return {
            "x": _w((lambda *s: torch.randn(*s, dtype=torch.bfloat16) * 0.05)(B, Sq, D)),
            "k_cache": _w((lambda *s: torch.randn(*s, dtype=torch.bfloat16) * 1.6)(B, H, Sk, Dh)),
            "v_cache": _w((lambda *s: torch.randn(*s, dtype=torch.bfloat16) * 1.6)(B, H, Sk, Dh)),
            "wq": _w((lambda *s: torch.randn(*s, dtype=torch.bfloat16) * 1.6)(D, H * Dh) * (D ** -0.5)),
            "wo": _w((lambda *s: torch.randn(*s, dtype=torch.bfloat16) * 1.6)(H * Dh, D) * ((H * Dh) ** -0.5)),
        }
    _A_in = _A_setup()

    def _B_setup():
        B, S, D, F = 1, 2048, 1024, 3584
        return {
            "x": _w((lambda *s: torch.randn(*s, dtype=torch.bfloat16) * 0.05)(B, S, D)),
            "W_gate": _w((lambda *s: torch.randn(*s, dtype=torch.bfloat16) * 0.2)(D, F) * (2.0 / D) ** 0.5),
            "W_up": _w((lambda *s: torch.randn(*s, dtype=torch.bfloat16) * 0.2)(D, F) * (2.0 / D) ** 0.5),
            "W_down": _w((lambda *s: torch.randn(*s, dtype=torch.bfloat16) * 0.2)(F, D) * (1.0 / F) ** 0.5),
        }
    _B_in = _B_setup()

    return {
        "A_x": _A_in["x"],
        "A_k_cache": _A_in["k_cache"],
        "A_v_cache": _A_in["v_cache"],
        "A_wq": _A_in["wq"],
        "A_wo": _A_in["wo"],
        "B_x": _B_in["x"],
        "B_W_gate": _B_in["W_gate"],
        "B_W_up": _B_in["W_up"],
        "B_W_down": _B_in["W_down"],
    }

def reference(A_x, A_k_cache, A_v_cache, A_wq, A_wo, B_x, B_W_gate, B_W_up, B_W_down):
    def _A_ref(x, k_cache, v_cache, wq, wo):
        B, Sq, D = x.shape
        H, Dh = 32, 128
        q = (lambda x, *d: x.permute(*d))((x @ wq).reshape(B, Sq, H, Dh), 0, 2, 1, 3)
        scores = torch.einsum("bhqd,bhkd->bhqk", q, k_cache) * (Dh ** -0.5)
        p = _softmax(scores, axis=-1)
        o = torch.einsum("bhqk,bhkd->bhqd", p, v_cache)
        return (lambda x, *d: x.permute(*d))(o, 0, 2, 1, 3).reshape(B, Sq, H * Dh) @ wo

    def _B_ref(x, W_gate, W_up, W_down):
        gate = torch.einsum("bsd,df->bsf", x, W_gate)
        up = torch.einsum("bsd,df->bsf", x, W_up)
        gate = gate * (lambda x, dt: x.to(dt))(gate > 0, gate.dtype)
        h = gate * up
        return torch.einsum("bsf,fd->bsd", h, W_down)

    x = A_x
    xn = _layernorm(x, axis=-1)
    s = _A_ref(xn, A_k_cache, A_v_cache, A_wq, A_wo)
    g = _silu(_B_ref(xn, B_W_gate, B_W_up, B_W_down))
    return x + s * g
```

```cpp
#include <hip/hip_runtime.h>
#include <hip/hip_bf16.h>
typedef unsigned short u16;
typedef short bf16x8 __attribute__((ext_vector_type(8)));
typedef float f32x4 __attribute__((ext_vector_type(4)));
__device__ __forceinline__ float bf2f(u16 v){ return __uint_as_float(((unsigned)v)<<16); }
__device__ __forceinline__ u16 f2bf(float f){ __hip_bfloat16 b = __float2bfloat16(f); return *reinterpret_cast<u16*>(&b); }
__device__ __forceinline__ float rbf(float f){ return bf2f(f2bf(f)); }

constexpr int S=2048, D=1024, H=32, DH=128, F=3584;

__global__ void ln_kernel(const u16* __restrict__ x, u16* __restrict__ xn){
  int row = blockIdx.x*4 + (threadIdx.x>>6); int lane = threadIdx.x&63;
  const u16* xr = x + (size_t)row*D;
  float v[16]; float s=0;
  for(int i=0;i<2;i++){ bf16x8 t = *(const bf16x8*)(xr + i*512 + lane*8); for(int j=0;j<8;j++){ v[i*8+j]=bf2f((u16)t[j]); s+=v[i*8+j]; } }
  for(int o=32;o>=1;o>>=1) s += __shfl_xor(s,o);
  float mean = s*(1.0f/D);
  float q=0; for(int i=0;i<16;i++){ float d=v[i]-mean; q+=d*d; }
  for(int o=32;o>=1;o>>=1) q += __shfl_xor(q,o);
  float var = rbf(q*(1.0f/D)); float mb = rbf(mean);
  float den = rbf(sqrtf(rbf(var+1e-5f)));
  for(int i=0;i<2;i++){ bf16x8 t; for(int j=0;j<8;j++){ t[j]=(short)f2bf(rbf(v[i*8+j]-mb)/den); } *(bf16x8*)(xn + (size_t)row*D + i*512 + lane*8)=t; }
}

__global__ __launch_bounds__(256) void gemm_simple(const u16* __restrict__ A, const u16* __restrict__ B, float* __restrict__ C, int M, int N, int K){
  __shared__ u16 As[64][40]; __shared__ u16 Bs[64][40];
  int bm = blockIdx.y*64, bn = blockIdx.x*64; int tid=threadIdx.x, wid=tid>>6, lane=tid&63, wr=wid>>1, wc=wid&1;
  f32x4 acc[2][2]={};
  for(int k0=0;k0<K;k0+=32){
    { int r=tid>>2, c=(tid&3)*8; *(bf16x8*)&As[r][c] = *(const bf16x8*)(A+(size_t)(bm+r)*K+k0+c); }
    { int kr=tid>>3, c=(tid&7)*8; bf16x8 t=*(const bf16x8*)(B+(size_t)(k0+kr)*N+bn+c); for(int j=0;j<8;j++) Bs[c+j][kr]=(u16)t[j]; }
    __syncthreads();
    for(int m=0;m<2;m++)for(int n=0;n<2;n++){
      bf16x8 a=*(const bf16x8*)&As[wr*32+m*16+(lane&15)][(lane>>4)*8];
      bf16x8 b=*(const bf16x8*)&Bs[wc*32+n*16+(lane&15)][(lane>>4)*8];
      acc[m][n]=__builtin_amdgcn_mfma_f32_16x16x32_bf16(a,b,acc[m][n],0,0,0);
    }
    __syncthreads();
  }
  for(int m=0;m<2;m++)for(int n=0;n<2;n++)for(int j=0;j<4;j++)
    C[(size_t)(bm+wr*32+m*16+(lane>>4)*4+j)*N + bn+wc*32+n*16+(lane&15)] = acc[m][n][j];
}
__global__ void q_cvt(const float* q, u16* qb, int n){ int i=blockIdx.x*256+threadIdx.x; if(i<n) qb[i]=f2bf(q[i]*0.12751743082f); }
__global__ void h_cvt(const float* g, const float* u, u16* h, int n){ int i=blockIdx.x*256+threadIdx.x; if(i<n){ float gg=g[i]; h[i]=f2bf((gg>0?gg:0.f)*u[i]); } }
__global__ void fin(const u16* x, const float* s, const float* f, u16* out, int n){ int i=blockIdx.x*256+threadIdx.x; if(i<n){ float ff=f[i]; float g=ff/(1.f+__expf(-ff)); out[i]=f2bf(bf2f(x[i])+s[i]*g); } }

__global__ __launch_bounds__(256) void attn_naive(const u16* __restrict__ qb, const u16* __restrict__ K, const u16* __restrict__ V, u16* __restrict__ O){
  __shared__ float ps[4][2048];
  int w=threadIdx.x>>6, lane=threadIdx.x&63; int row=blockIdx.x*4+w, h=blockIdx.y;
  __shared__ float qs[4][128];
  qs[w][lane]=bf2f(qb[(size_t)row*4096+h*128+lane]); qs[w][lane+64]=bf2f(qb[(size_t)row*4096+h*128+lane+64]);
  __syncthreads();
  const u16* Kh=K+(size_t)h*S*DH; const u16* Vh=V+(size_t)h*S*DH;
  float mx=-1e30f;
  for(int k=lane;k<S;k+=64){ float s=0; for(int d=0;d<128;d+=8){ bf16x8 t=*(const bf16x8*)(Kh+(size_t)k*DH+d); for(int j=0;j<8;j++) s+=qs[w][d+j]*bf2f((u16)t[j]); } ps[w][k]=s; mx=fmaxf(mx,s);}
  for(int o=32;o>=1;o>>=1) mx=fmaxf(mx,__shfl_xor(mx,o));
  float l=0; for(int k=lane;k<S;k+=64){ float p=exp2f(ps[w][k]-mx); l+=p; ps[w][k]=rbf(p);}
  for(int o=32;o>=1;o>>=1) l+=__shfl_xor(l,o);
  __syncthreads();
  float o0=0,o1=0; for(int k=0;k<S;k++){ float p=ps[w][k]; unsigned vv=*(const unsigned*)(Vh+(size_t)k*DH+lane*2); o0+=p*bf2f((u16)(vv&0xffff)); o1+=p*bf2f((u16)(vv>>16)); }
  O[(size_t)row*4096+h*128+lane*2]=f2bf(o0/l); O[(size_t)row*4096+h*128+lane*2+1]=f2bf(o1/l);
}

extern "C" void kernel_launch(void* const* d_in, const int* in_sizes, int n_in, void* d_out, int out_size, void* d_ws, size_t ws_size, hipStream_t stream){
  const u16* x=(const u16*)d_in[0]; const u16* kc=(const u16*)d_in[1]; const u16* vc=(const u16*)d_in[2]; const u16* wq=(const u16*)d_in[3]; const u16* wo=(const u16*)d_in[4];
  const u16* wg=(const u16*)d_in[6]; const u16* wu=(const u16*)d_in[7]; const u16* wd=(const u16*)d_in[8];
  char* ws=(char*)d_ws;
  u16* xn=(u16*)ws; float* qf=(float*)(ws+(4<<20)); u16* qb=(u16*)(ws+(40<<20)); u16* O=(u16*)(ws+(60<<20));
  float* g=(float*)(ws+(80<<20)); float* u=(float*)(ws+(112<<20)); u16* hb=(u16*)(ws+(144<<20)); float* sf=(float*)(ws+(160<<20)); float* ff=(float*)(ws+(170<<20));
  ln_kernel<<<S/4,256,0,stream>>>(x,xn);
  gemm_simple<<<dim3(4096/64,S/64),256,0,stream>>>(xn,wq,qf,S,4096,D);
  q_cvt<<<S*4096/256,256,0,stream>>>(qf,qb,S*4096);
  attn_naive<<<dim3(S/4,H),256,0,stream>>>(qb,kc,vc,O);
  gemm_simple<<<dim3(F/64,S/64),256,0,stream>>>(xn,wg,g,S,F,D);
  gemm_simple<<<dim3(F/64,S/64),256,0,stream>>>(xn,wu,u,S,F,D);
  h_cvt<<<S*F/256,256,0,stream>>>(g,u,hb,S*F);
  gemm_simple<<<dim3(D/64,S/64),256,0,stream>>>(O,wo,sf,S,D,4096);
  gemm_simple<<<dim3(D/64,S/64),256,0,stream>>>(hb,wd,ff,S,D,F);
  fin<<<S*D/256,256,0,stream>>>(x,sf,ff,(u16*)d_out,S*D);
}
```

```cpp
#include <hip/hip_runtime.h>
#include <hip/hip_bf16.h>
typedef unsigned short u16;

__global__ __launch_bounds__(256, 1) void kernBC(const u16* __restrict__ xnf, const u16* __restrict__ wq, const u16* __restrict__ wg, const u16* __restrict__ wu,
                                                const u16* __restrict__ kc, const u16* __restrict__ vc, u16* __restrict__ Hf, u16* __restrict__ Of,
                                                const u16* __restrict__ wo, const u16* __restrict__ wd, unsigned* __restrict__ flags,
                                                const u16* __restrict__ xin_, u16* __restrict__ out_, char* __restrict__ pbuf_) {
  __shared__ __attribute__((aligned(16))) char lds[163840];
  const unsigned long long ka = (unsigned long long)__builtin_amdgcn_kernarg_segment_ptr();
  unsigned tid = threadIdx.x + (unsigned)(size_t)lds;
  asm volatile(R"ASM(
LK_start:
  s_getpc_b64 s[4:5]
  s_load_dwordx16 s[52:67], %[s_ka], 0x0
  s_load_dwordx8 s[20:27], %[s_ka], 0x40
  s_load_dwordx4 s[28:31], %[s_ka], 0x60
  s_memrealtime s[96:97]
  v_mov_b32 v0, %[v_tid]
  s_mov_b32 s8, %[s_bid]
  v_lshrrev_b32 v1, 6, v0
  s_nop 0
  v_readfirstlane_b32 s9, v1
  s_and_b32 s17, s8, 7
  s_lshr_b32 s18, s8, 3
  s_and_b32 s19, s18, 3
  s_lshl_b32 s10, s17, 2
  s_add_u32 s10, s10, s19
  s_lshr_b32 s11, s18, 2
  s_lshl_b32 s19, s19, 1
  s_add_u32 s12, s11, s19
  s_and_b32 s12, s12, 7
  s_lshr_b32 s13, s17, 1
  s_and_b32 s14, s18, 7
  s_and_b32 s15, s8, 1
  s_lshl_b32 s15, s15, 2
  s_lshr_b32 s18, s18, 3
  s_add_u32 s15, s15, s18
  s_add_u32 s16, s15, s14
  s_and_b32 s16, s16, 3
  s_lshl_b32 s17, s10, 19
  s_and_b32 s5, s5, 0xffff
  s_mov_b32 s6, LK_end-LK_start
  s_mov_b32 s7, 0x20000
  s_lshr_b32 s3, s8, 3
  s_lshl_b32 s3, s3, 12
  v_lshlrev_b32 v21, 4, v0
  s_or_b32 s18, s8, s9
  s_cmp_eq_u32 s18, 0
  s_cbranch_scc1 LH_nc
  buffer_load_dwordx4 v[24:27], v21, s[4:7], s3 offen
LH_nc:
  v_and_b32 v12, 63, v0
  v_lshrrev_b32 v13, 5, v12
  v_and_b32 v14, 31, v12
  s_lshl_b32 s72, s11, 8
  s_lshl_b32 s73, s10, 3
  s_add_u32 s72, s72, s73
  s_lshl_b32 s73, s9, 1
  s_add_u32 s72, s72, s73
  v_add_u32 v13, s72, v13
  v_lshlrev_b32 v15, 11, v13
  v_lshl_add_u32 v15, v14, 6, v15
  s_lshl_b32 s19, s11, 5
  s_add_u32 s19, s19, s10
  s_or_b32 s18, s19, s9
  v_mov_b32 v116, 0
  v_mov_b32 v117, 0
  v_mov_b32 v118, 0
  v_mov_b32 v119, 0
  s_lshl_b32 s73, s19, 3
  s_add_u32 s73, s73, 69632
  v_mov_b32 v18, s73
  s_lshl_b32 s73, s19, 7
  v_mov_b32 v19, s73
  v_lshlrev_b32 v17, 4, v12
  v_add_u32 v17, 65536, v17
  s_waitcnt lgkmcnt(0)
  s_mov_b32 s92, s24
  s_and_b32 s93, s25, 0xffff
  s_mov_b32 s94, 77824
  s_mov_b32 s95, 0x20000
  s_mov_b32 s88, s26
  s_and_b32 s89, s27, 0xffff
  s_mov_b32 s90, 4194304
  s_mov_b32 s91, 0x20000
  v_mov_b32 v112, s96
  v_mov_b32 v113, s97
  s_cmp_lg_u32 s9, 0
  s_cbranch_scc1 LH_a
  s_mov_b64 exec, 1
  buffer_store_dwordx2 v[112:113], v18, s[92:95], 0 offen sc1
  buffer_store_dwordx2 v[116:117], v19, s[92:95], 0 offen sc1
  s_mov_b64 exec, -1
  s_cmp_lg_u32 s19, 0
  s_cbranch_scc1 LH_a
  buffer_store_dwordx4 v[116:119], v17, s[92:95], 0 offen sc1
  buffer_store_dwordx4 v[116:119], v17, s[92:95], 0 offen offset:1024 sc1
  buffer_store_dwordx4 v[116:119], v17, s[92:95], 0 offen offset:2048 sc1
  v_add_u32 v19, 4096, v17
  s_waitcnt vmcnt(0)
  buffer_load_dwordx4 v[40:43], v19, s[92:95], 0 offen sc1
  buffer_load_dwordx4 v[44:47], v19, s[92:95], 0 offen offset:1024 sc1
LH_a:
  v_add_u32 v17, 4096, v17
  buffer_load_dwordx4 v[48:51], v15, s[88:91], 0 offen offset:0
  buffer_load_dwordx4 v[52:55], v15, s[88:91], 0 offen offset:16
  buffer_load_dwordx4 v[56:59], v15, s[88:91], 0 offen offset:32
  buffer_load_dwordx4 v[60:63], v15, s[88:91], 0 offen offset:48
  s_mov_b32 s48, s52
  s_and_b32 s49, s53, 0xffff
  s_mov_b32 s50, 4194304
  s_mov_b32 s51, 0x20000
  s_mov_b32 s44, s54
  s_and_b32 s45, s55, 0xffff
  s_mov_b32 s46, 8388608
  s_mov_b32 s47, 0x20000
  s_mov_b32 s36, s56
  s_and_b32 s37, s57, 0xffff
  s_mov_b32 s38, 7340032
  s_mov_b32 s39, 0x20000
  s_mov_b32 s40, s58
  s_and_b32 s41, s59, 0xffff
  s_mov_b32 s42, 7340032
  s_mov_b32 s43, 0x20000
  s_add_u32 s52, s60, s17
  s_addc_u32 s53, s61, 0
  s_sub_u32 s52, s52, 1024
  s_subb_u32 s53, s53, 0
  s_and_b32 s53, s53, 0xffff
  s_mov_b32 s54, 525312
  s_mov_b32 s55, 0x20000
  s_add_u32 s56, s62, s17
  s_addc_u32 s57, s63, 0
  s_sub_u32 s56, s56, 1024
  s_subb_u32 s57, s57, 0
  s_and_b32 s57, s57, 0xffff
  s_mov_b32 s58, 525312
  s_mov_b32 s59, 0x20000
  s_mov_b32 s60, s64
  s_and_b32 s61, s65, 0xffff
  s_mov_b32 s62, 14680064
  s_mov_b32 s63, 0x20000
  s_mov_b32 s64, s66
  s_and_b32 s65, s67, 0xffff
  s_mov_b32 s66, 16777216
  s_mov_b32 s67, 0x20000
  v_mul_u32_u24 v15, 528, v14
  v_and_b32 v19, 7, v13
  v_lshl_add_u32 v15, v19, 4, v15
  v_lshrrev_b32 v19, 5, v12
  s_lshl_b32 s73, s9, 3
  v_add_u32 v19, s73, v19
  v_lshlrev_b32 v16, 12, v19
  v_mul_u32_u24 v19, 528, v19
  v_lshl_add_u32 v19, v14, 4, v19
  v_lshrrev_b32 v13, 3, v14
  v_lshl_add_u32 v16, v13, 8, v16
  v_and_b32 v13, 7, v14
  v_lshl_add_u32 v16, v13, 4, v16
  v_mov_b32 v14, v19
  s_and_b32 s72, s72, 0xfffffff8
  s_lshr_b32 s73, s72, 6
  s_lshl_b32 s73, s73, 17
  v_add_u32 v16, s73, v16
  s_bfe_u32 s73, s72, 0x20004
  s_lshl_b32 s73, s73, 10
  v_add_u32 v16, s73, v16
  s_and_b32 s73, s72, 15
  s_lshl_b32 s73, s73, 4
  v_add_u32 v16, s73, v16
  s_mov_b32 s79, 8192
  s_mov_b32 s80, 16384
  s_mov_b32 s82, 24576
  v_and_b32 v32, 63, v0
  v_lshrrev_b32 v33, 4, v32
  v_bfe_u32 v34, v32, 2, 2
  v_and_b32 v35, 3, v32
  v_lshlrev_b32 v36, 4, v32
  v_mul_u32_u24 v37, 1032, v33
  v_lshl_add_u32 v37, v34, 6, v37
  v_lshl_add_u32 v37, v35, 4, v37
  v_add_u32 v1, 42624, v37
  v_add_u32 v2, 87808, v37
  v_add_u32 v28, 132992, v37
  v_lshl_add_u32 v37, v33, 2, v34
  v_mul_u32_u24 v37, 224, v37
  v_lshl_add_u32 v37, v35, 3, v37
  v_add_u32 v3, 28288, v37
  v_add_u32 v4, 73472, v37
  v_add_u32 v29, 118656, v37
  s_lshl_b32 s72, s9, 16
  s_lshl_b32 s73, s10, 8
  s_add_u32 s72, s72, s73
  v_lshlrev_b32 v37, 13, v34
  v_lshl_add_u32 v37, v33, 6, v37
  v_lshl_add_u32 v37, v35, 4, v37
  v_add_u32 v5, s72, v37
  v_add_u32 v6, 31736, v5
  s_mul_i32 s72, s9, 57344
  s_mul_i32 s73, s10, 224
  s_add_u32 s72, s72, s73
  s_and_b32 s73, s10, 1
  s_lshl_b32 s73, s73, 1
  v_mov_b32 v39, v32
  v_mul_u32_u24 v37, 4682, v39
  v_lshrrev_b32 v37, 16, v37
  v_mul_u32_u24 v38, 14, v37
  v_sub_u32 v38, v39, v38
  v_add_u32 v38, s73, v38
  v_subrev_u32 v39, 14, v38
  v_min_u32 v38, v38, v39
  v_mul_u32_u24 v37, 7168, v37
  v_lshl_add_u32 v37, v38, 4, v37
  v_add_u32 v7, s72, v37
  v_add_u32 v39, 64, v32
  v_min_u32 v39, 111, v39
  v_mul_u32_u24 v37, 4682, v39
  v_lshrrev_b32 v37, 16, v37
  v_mul_u32_u24 v38, 14, v37
  v_sub_u32 v38, v39, v38
  v_add_u32 v38, s73, v38
  v_subrev_u32 v39, 14, v38
  v_min_u32 v38, v38, v39
  v_mul_u32_u24 v37, 7168, v37
  v_lshl_add_u32 v37, v38, 4, v37
  v_add_u32 v8, s72, v37
  v_subrev_u32 v8, 1024, v8
  s_lshl_b32 s72, s11, 2
  s_add_u32 s72, s72, s9
  s_lshl_b32 s73, s72, 17
  v_add_u32 v9, s73, v36
  s_mov_b32 s68, 0
  s_mul_i32 s74, s9, 1792
  s_add_u32 s74, s74, 28288
  s_mul_i32 s75, s9, 2064
  s_add_u32 s75, s75, 42624
  s_mov_b32 s86, -1
  s_mov_b32 s87, 0xffff
  s_cmp_eq_u32 s18, 0
  s_cbranch_scc1 LH_i1
  s_waitcnt vmcnt(0)
  s_add_u32 s72, s68, s12
  s_and_b32 s72, s72, 7
  s_and_b32 s73, s68, 0xfffffff8
  s_or_b32 s72, s72, s73
  s_mul_i32 s69, s72, 229376
  s_lshl_b32 s70, s72, 18
  s_lshl_b32 s71, s72, 12
  s_add_u32 s68, s68, 1
  s_add_u32 m0, s75, 90368
  s_nop 0
  buffer_load_dwordx4 v5, s[44:47], s70 offen offset:0 lds
  s_nop 0
  buffer_load_dwordx4 v6, s[44:47], s70 offen offset:1032 lds
  s_add_u32 m0, s74, 90368
  s_nop 0
  buffer_load_dwordx4 v7, s[36:39], s69 offen offset:0 lds
  s_nop 0
  s_mov_b64 exec, s[86:87]
  buffer_load_dwordx4 v8, s[36:39], s69 offen offset:1024 lds
  s_mov_b64 exec, -1
  s_nop 0
  s_add_u32 m0, s74, 97536
  s_nop 0
  buffer_load_dwordx4 v7, s[40:43], s69 offen offset:0 lds
  s_nop 0
  s_mov_b64 exec, s[86:87]
  buffer_load_dwordx4 v8, s[40:43], s69 offen offset:1024 lds
  s_mov_b64 exec, -1
  s_nop 0
  s_mov_b32 s17, s71
  s_branch LH_i2
LH_i1:
  s_waitcnt vmcnt(4)
  buffer_store_dwordx4 v[40:43], v17, s[92:95], 0 offen offset:2048 sc1
  buffer_store_dwordx4 v[44:47], v17, s[92:95], 0 offen offset:3072 sc1
  s_nop 1
  buffer_load_dwordx4 v[40:43], v17, s[92:95], 0 offen sc1
  buffer_load_dwordx4 v[44:47], v17, s[92:95], 0 offen offset:1024 sc1
  s_waitcnt vmcnt(0)
  buffer_store_dwordx4 v[40:43], v17, s[92:95], 0 offen offset:2048 sc1
  buffer_store_dwordx4 v[44:47], v17, s[92:95], 0 offen offset:3072 sc1
  s_nop 1
  buffer_load_dwordx4 v[40:43], v17, s[92:95], 0 offen sc1
  buffer_load_dwordx4 v[44:47], v17, s[92:95], 0 offen offset:1024 sc1
LH_i2:
  v_lshlrev_b32 v64, 16, v48
  v_and_b32 v65, 0xffff0000, v48
  v_lshlrev_b32 v66, 16, v49
  v_and_b32 v67, 0xffff0000, v49
  v_lshlrev_b32 v68, 16, v50
  v_and_b32 v69, 0xffff0000, v50
  v_lshlrev_b32 v70, 16, v51
  v_and_b32 v71, 0xffff0000, v51
  v_lshlrev_b32 v72, 16, v52
  v_and_b32 v73, 0xffff0000, v52
  v_lshlrev_b32 v74, 16, v53
  v_and_b32 v75, 0xffff0000, v53
  v_lshlrev_b32 v76, 16, v54
  v_and_b32 v77, 0xffff0000, v54
  v_lshlrev_b32 v78, 16, v55
  v_and_b32 v79, 0xffff0000, v55
  v_lshlrev_b32 v80, 16, v56
  v_and_b32 v81, 0xffff0000, v56
  v_lshlrev_b32 v82, 16, v57
  v_and_b32 v83, 0xffff0000, v57
  v_lshlrev_b32 v84, 16, v58
  v_and_b32 v85, 0xffff0000, v58
  v_lshlrev_b32 v86, 16, v59
  v_and_b32 v87, 0xffff0000, v59
  v_lshlrev_b32 v88, 16, v60
  v_and_b32 v89, 0xffff0000, v60
  v_lshlrev_b32 v90, 16, v61
  v_and_b32 v91, 0xffff0000, v61
  v_lshlrev_b32 v92, 16, v62
  v_and_b32 v93, 0xffff0000, v62
  v_lshlrev_b32 v94, 16, v63
  v_and_b32 v95, 0xffff0000, v63
  v_add_f32 v120, v64, v68
  v_add_f32 v121, v65, v69
  v_add_f32 v122, v66, v70
  v_add_f32 v123, v67, v71
  v_add_f32 v120, v120, v72
  v_add_f32 v121, v121, v73
  v_add_f32 v122, v122, v74
  v_add_f32 v123, v123, v75
  v_add_f32 v120, v120, v76
  v_add_f32 v121, v121, v77
  v_add_f32 v122, v122, v78
  v_add_f32 v123, v123, v79
  v_add_f32 v120, v120, v80
  v_add_f32 v121, v121, v81
  v_add_f32 v122, v122, v82
  v_add_f32 v123, v123, v83
  v_add_f32 v120, v120, v84
  v_add_f32 v121, v121, v85
  v_add_f32 v122, v122, v86
  v_add_f32 v123, v123, v87
  v_add_f32 v120, v120, v88
  v_add_f32 v121, v121, v89
  v_add_f32 v122, v122, v90
  v_add_f32 v123, v123, v91
  v_add_f32 v120, v120, v92
  v_add_f32 v121, v121, v93
  v_add_f32 v122, v122, v94
  v_add_f32 v123, v123, v95
  v_add_f32 v120, v120, v121
  v_add_f32 v122, v122, v123
  v_add_f32 v120, v120, v122
  s_nop 1
  v_add_f32_dpp v120, v120, v120 quad_perm:[1,0,3,2] row_mask:0xf bank_mask:0xf
  s_nop 1
  v_add_f32_dpp v120, v120, v120 quad_perm:[2,3,0,1] row_mask:0xf bank_mask:0xf
  s_nop 1
  v_add_f32_dpp v120, v120, v120 row_half_mirror row_mask:0xf bank_mask:0xf
  s_nop 1
  v_add_f32_dpp v120, v120, v120 row_mirror row_mask:0xf bank_mask:0xf
  v_mov_b32 v128, v120
  s_nop 1
  v_permlane16_swap_b32 v120, v128
  v_add_f32 v120, v120, v128
  v_mul_f32 v124, 0x3a800000, v120
  v_sub_f32 v96, v64, v124
  v_mul_f32 v120, v96, v96
  v_sub_f32 v97, v65, v124
  v_mul_f32 v121, v97, v97
  v_sub_f32 v98, v66, v124
  v_mul_f32 v122, v98, v98
  v_sub_f32 v99, v67, v124
  v_mul_f32 v123, v99, v99
  v_sub_f32 v100, v68, v124
  v_fmac_f32 v120, v100, v100
  v_sub_f32 v101, v69, v124
  v_fmac_f32 v121, v101, v101
  v_sub_f32 v102, v70, v124
  v_fmac_f32 v122, v102, v102
  v_sub_f32 v103, v71, v124
  v_fmac_f32 v123, v103, v103
  v_sub_f32 v96, v72, v124
  v_fmac_f32 v120, v96, v96
  v_sub_f32 v97, v73, v124
  v_fmac_f32 v121, v97, v97
  v_sub_f32 v98, v74, v124
  v_fmac_f32 v122, v98, v98
  v_sub_f32 v99, v75, v124
  v_fmac_f32 v123, v99, v99
  v_sub_f32 v100, v76, v124
  v_fmac_f32 v120, v100, v100
  v_sub_f32 v101, v77, v124
  v_fmac_f32 v121, v101, v101
  v_sub_f32 v102, v78, v124
  v_fmac_f32 v122, v102, v102
  v_sub_f32 v103, v79, v124
  v_fmac_f32 v123, v103, v103
  v_sub_f32 v96, v80, v124
  v_fmac_f32 v120, v96, v96
  v_sub_f32 v97, v81, v124
  v_fmac_f32 v121, v97, v97
  v_sub_f32 v98, v82, v124
  v_fmac_f32 v122, v98, v98
  v_sub_f32 v99, v83, v124
  v_fmac_f32 v123, v99, v99
  v_sub_f32 v100, v84, v124
  v_fmac_f32 v120, v100, v100
  v_sub_f32 v101, v85, v124
  v_fmac_f32 v121, v101, v101
  v_sub_f32 v102, v86, v124
  v_fmac_f32 v122, v102, v102
  v_sub_f32 v103, v87, v124
  v_fmac_f32 v123, v103, v103
  v_sub_f32 v96, v88, v124
  v_fmac_f32 v120, v96, v96
  v_sub_f32 v97, v89, v124
  v_fmac_f32 v121, v97, v97
  v_sub_f32 v98, v90, v124
  v_fmac_f32 v122, v98, v98
  v_sub_f32 v99, v91, v124
  v_fmac_f32 v123, v99, v99
  v_sub_f32 v100, v92, v124
  v_fmac_f32 v120, v100, v100
  v_sub_f32 v101, v93, v124
  v_fmac_f32 v121, v101, v101
  v_sub_f32 v102, v94, v124
  v_fmac_f32 v122, v102, v102
  v_sub_f32 v103, v95, v124
  v_fmac_f32 v123, v103, v103
  v_add_f32 v120, v120, v121
  v_add_f32 v122, v122, v123
  v_add_f32 v120, v120, v122
  s_nop 1
  v_add_f32_dpp v120, v120, v120 quad_perm:[1,0,3,2] row_mask:0xf bank_mask:0xf
  s_nop 1
  v_add_f32_dpp v120, v120, v120 quad_perm:[2,3,0,1] row_mask:0xf bank_mask:0xf
  s_nop 1
  v_add_f32_dpp v120, v120, v120 row_half_mirror row_mask:0xf bank_mask:0xf
  s_nop 1
  v_add_f32_dpp v120, v120, v120 row_mirror row_mask:0xf bank_mask:0xf
  v_mov_b32 v128, v120
  s_nop 1
  v_permlane16_swap_b32 v120, v128
  v_add_f32 v120, v120, v128
  v_mul_f32 v126, 0x3a800000, v120
  v_cvt_pk_bf16_f32 v126, v126, v126
  v_and_b32 v126, 0xffff0000, v126
  v_add_f32 v126, 0x3727c5ac, v126
  v_cvt_pk_bf16_f32 v126, v126, v126
  v_and_b32 v126, 0xffff0000, v126
  v_sqrt_f32 v126, v126
  s_nop 0
  v_cvt_pk_bf16_f32 v126, v126, v126
  v_and_b32 v126, 0xffff0000, v126
  v_mov_b32 v125, v124
  v_cvt_pk_bf16_f32 v125, v125, v125
  v_and_b32 v125, 0xffff0000, v125
  v_rcp_f32 v127, v126
  s_nop 0
  v_fma_f32 v128, -v126, v127, 1.0
  v_fmac_f32 v127, v128, v127
  s_cmp_lg_u32 s18, 0
  s_cbranch_scc1 LH_e2
  s_waitcnt vmcnt(0)
  buffer_store_dwordx4 v[40:43], v17, s[92:95], 0 offen offset:2048 sc1
  buffer_store_dwordx4 v[44:47], v17, s[92:95], 0 offen offset:3072 sc1
  s_nop 1
LH_e2:
  v_sub_f32 v64, v64, v125
  v_sub_f32 v65, v65, v125
  v_cvt_pk_bf16_f32 v128, v64, v65
  v_lshlrev_b32 v64, 16, v128
  v_and_b32 v65, 0xffff0000, v128
  v_mul_f32 v120, v64, v127
  v_mul_f32 v121, v65, v127
  v_fma_f32 v64, -v120, v126, v64
  v_fma_f32 v65, -v121, v126, v65
  v_fmac_f32 v120, v64, v127
  v_fmac_f32 v121, v65, v127
  v_cvt_pk_bf16_f32 v96, v120, v121
  v_sub_f32 v66, v66, v125
  v_sub_f32 v67, v67, v125
  v_cvt_pk_bf16_f32 v128, v66, v67
  v_lshlrev_b32 v66, 16, v128
  v_and_b32 v67, 0xffff0000, v128
  v_mul_f32 v120, v66, v127
  v_mul_f32 v121, v67, v127
  v_fma_f32 v66, -v120, v126, v66
  v_fma_f32 v67, -v121, v126, v67
  v_fmac_f32 v120, v66, v127
  v_fmac_f32 v121, v67, v127
  v_cvt_pk_bf16_f32 v97, v120, v121
  v_sub_f32 v68, v68, v125
  v_sub_f32 v69, v69, v125
  v_cvt_pk_bf16_f32 v128, v68, v69
  v_lshlrev_b32 v68, 16, v128
  v_and_b32 v69, 0xffff0000, v128
  v_mul_f32 v120, v68, v127
  v_mul_f32 v121, v69, v127
  v_fma_f32 v68, -v120, v126, v68
  v_fma_f32 v69, -v121, v126, v69
  v_fmac_f32 v120, v68, v127
  v_fmac_f32 v121, v69, v127
  v_cvt_pk_bf16_f32 v100, v120, v121
  v_sub_f32 v70, v70, v125
  v_sub_f32 v71, v71, v125
  v_cvt_pk_bf16_f32 v128, v70, v71
  v_lshlrev_b32 v70, 16, v128
  v_and_b32 v71, 0xffff0000, v128
  v_mul_f32 v120, v70, v127
  v_mul_f32 v121, v71, v127
  v_fma_f32 v70, -v120, v126, v70
  v_fma_f32 v71, -v121, v126, v71
  v_fmac_f32 v120, v70, v127
  v_fmac_f32 v121, v71, v127
  v_cvt_pk_bf16_f32 v101, v120, v121
  v_sub_f32 v72, v72, v125
  v_sub_f32 v73, v73, v125
  v_cvt_pk_bf16_f32 v128, v72, v73
  v_lshlrev_b32 v72, 16, v128
  v_and_b32 v73, 0xffff0000, v128
  v_mul_f32 v120, v72, v127
  v_mul_f32 v121, v73, v127
  v_fma_f32 v72, -v120, v126, v72
  v_fma_f32 v73, -v121, v126, v73
  v_fmac_f32 v120, v72, v127
  v_fmac_f32 v121, v73, v127
  v_cvt_pk_bf16_f32 v104, v120, v121
  v_sub_f32 v74, v74, v125
  v_sub_f32 v75, v75, v125
  v_cvt_pk_bf16_f32 v128, v74, v75
  v_lshlrev_b32 v74, 16, v128
  v_and_b32 v75, 0xffff0000, v128
  v_mul_f32 v120, v74, v127
  v_mul_f32 v121, v75, v127
  v_fma_f32 v74, -v120, v126, v74
  v_fma_f32 v75, -v121, v126, v75
  v_fmac_f32 v120, v74, v127
  v_fmac_f32 v121, v75, v127
  v_cvt_pk_bf16_f32 v105, v120, v121
  v_sub_f32 v76, v76, v125
  v_sub_f32 v77, v77, v125
  v_cvt_pk_bf16_f32 v128, v76, v77
  v_lshlrev_b32 v76, 16, v128
  v_and_b32 v77, 0xffff0000, v128
  v_mul_f32 v120, v76, v127
  v_mul_f32 v121, v77, v127
  v_fma_f32 v76, -v120, v126, v76
  v_fma_f32 v77, -v121, v126, v77
  v_fmac_f32 v120, v76, v127
  v_fmac_f32 v121, v77, v127
  v_cvt_pk_bf16_f32 v108, v120, v121
  v_sub_f32 v78, v78, v125
  v_sub_f32 v79, v79, v125
  v_cvt_pk_bf16_f32 v128, v78, v79
  v_lshlrev_b32 v78, 16, v128
  v_and_b32 v79, 0xffff0000, v128
  v_mul_f32 v120, v78, v127
  v_mul_f32 v121, v79, v127
  v_fma_f32 v78, -v120, v126, v78
  v_fma_f32 v79, -v121, v126, v79
  v_fmac_f32 v120, v78, v127
  v_fmac_f32 v121, v79, v127
  v_cvt_pk_bf16_f32 v109, v120, v121
  v_sub_f32 v80, v80, v125
  v_sub_f32 v81, v81, v125
  v_cvt_pk_bf16_f32 v128, v80, v81
  v_lshlrev_b32 v80, 16, v128
  v_and_b32 v81, 0xffff0000, v128
  v_mul_f32 v120, v80, v127
  v_mul_f32 v121, v81, v127
  v_fma_f32 v80, -v120, v126, v80
  v_fma_f32 v81, -v121, v126, v81
  v_fmac_f32 v120, v80, v127
  v_fmac_f32 v121, v81, v127
  v_cvt_pk_bf16_f32 v98, v120, v121
  v_sub_f32 v82, v82, v125
  v_sub_f32 v83, v83, v125
  v_cvt_pk_bf16_f32 v128, v82, v83
  v_lshlrev_b32 v82, 16, v128
  v_and_b32 v83, 0xffff0000, v128
  v_mul_f32 v120, v82, v127
  v_mul_f32 v121, v83, v127
  v_fma_f32 v82, -v120, v126, v82
  v_fma_f32 v83, -v121, v126, v83
  v_fmac_f32 v120, v82, v127
  v_fmac_f32 v121, v83, v127
  v_cvt_pk_bf16_f32 v99, v120, v121
  v_sub_f32 v84, v84, v125
  v_sub_f32 v85, v85, v125
  v_cvt_pk_bf16_f32 v128, v84, v85
  v_lshlrev_b32 v84, 16, v128
  v_and_b32 v85, 0xffff0000, v128
  v_mul_f32 v120, v84, v127
  v_mul_f32 v121, v85, v127
  v_fma_f32 v84, -v120, v126, v84
  v_fma_f32 v85, -v121, v126, v85
  v_fmac_f32 v120, v84, v127
  v_fmac_f32 v121, v85, v127
  v_cvt_pk_bf16_f32 v102, v120, v121
  v_sub_f32 v86, v86, v125
  v_sub_f32 v87, v87, v125
  v_cvt_pk_bf16_f32 v128, v86, v87
  v_lshlrev_b32 v86, 16, v128
  v_and_b32 v87, 0xffff0000, v128
  v_mul_f32 v120, v86, v127
  v_mul_f32 v121, v87, v127
  v_fma_f32 v86, -v120, v126, v86
  v_fma_f32 v87, -v121, v126, v87
  v_fmac_f32 v120, v86, v127
  v_fmac_f32 v121, v87, v127
  v_cvt_pk_bf16_f32 v103, v120, v121
  v_sub_f32 v88, v88, v125
  v_sub_f32 v89, v89, v125
  v_cvt_pk_bf16_f32 v128, v88, v89
  v_lshlrev_b32 v88, 16, v128
  v_and_b32 v89, 0xffff0000, v128
  v_mul_f32 v120, v88, v127
  v_mul_f32 v121, v89, v127
  v_fma_f32 v88, -v120, v126, v88
  v_fma_f32 v89, -v121, v126, v89
  v_fmac_f32 v120, v88, v127
  v_fmac_f32 v121, v89, v127
  v_cvt_pk_bf16_f32 v106, v120, v121
  v_sub_f32 v90, v90, v125
  v_sub_f32 v91, v91, v125
  v_cvt_pk_bf16_f32 v128, v90, v91
  v_lshlrev_b32 v90, 16, v128
  v_and_b32 v91, 0xffff0000, v128
  v_mul_f32 v120, v90, v127
  v_mul_f32 v121, v91, v127
  v_fma_f32 v90, -v120, v126, v90
  v_fma_f32 v91, -v121, v126, v91
  v_fmac_f32 v120, v90, v127
  v_fmac_f32 v121, v91, v127
  v_cvt_pk_bf16_f32 v107, v120, v121
  v_sub_f32 v92, v92, v125
  v_sub_f32 v93, v93, v125
  v_cvt_pk_bf16_f32 v128, v92, v93
  v_lshlrev_b32 v92, 16, v128
  v_and_b32 v93, 0xffff0000, v128
  v_mul_f32 v120, v92, v127
  v_mul_f32 v121, v93, v127
  v_fma_f32 v92, -v120, v126, v92
  v_fma_f32 v93, -v121, v126, v93
  v_fmac_f32 v120, v92, v127
  v_fmac_f32 v121, v93, v127
  v_cvt_pk_bf16_f32 v110, v120, v121
  v_sub_f32 v94, v94, v125
  v_sub_f32 v95, v95, v125
  v_cvt_pk_bf16_f32 v128, v94, v95
  v_lshlrev_b32 v94, 16, v128
  v_and_b32 v95, 0xffff0000, v128
  v_mul_f32 v120, v94, v127
  v_mul_f32 v121, v95, v127
  v_fma_f32 v94, -v120, v126, v94
  v_fma_f32 v95, -v121, v126, v95
  v_fmac_f32 v120, v94, v127
  v_fmac_f32 v121, v95, v127
  v_cvt_pk_bf16_f32 v111, v120, v121
  ds_write_b128 v15, v[96:99] offset:0
  ds_write_b128 v15, v[100:103] offset:128
  ds_write_b128 v15, v[104:107] offset:256
  ds_write_b128 v15, v[108:111] offset:384
  s_waitcnt lgkmcnt(0)
  s_barrier
  ds_read_b128 v[96:99], v14 offset:0
  ds_read_b128 v[100:103], v14 offset:1056
  ds_read_b128 v[104:107], v14 offset:2112
  ds_read_b128 v[108:111], v14 offset:3168
  s_waitcnt lgkmcnt(3)
  buffer_store_dwordx4 v[96:99], v16, s[48:51], 0 offen sc1
  s_waitcnt lgkmcnt(2)
  buffer_store_dwordx4 v[100:103], v16, s[48:51], s79 offen sc1
  s_waitcnt lgkmcnt(1)
  buffer_store_dwordx4 v[104:107], v16, s[48:51], s80 offen sc1
  s_waitcnt lgkmcnt(0)
  buffer_store_dwordx4 v[108:111], v16, s[48:51], s82 offen sc1
  s_cmp_eq_u32 s9, 0
  s_cbranch_scc1 LH_w0
  s_waitcnt vmcnt(0)
  s_barrier
  s_add_u32 s72, s68, s12
  s_and_b32 s72, s72, 7
  s_and_b32 s73, s68, 0xfffffff8
  s_or_b32 s72, s72, s73
  s_mul_i32 s69, s72, 229376
  s_lshl_b32 s70, s72, 18
  s_lshl_b32 s71, s72, 12
  s_add_u32 s68, s68, 1
  s_add_u32 m0, s75, 112960
  s_nop 0
  buffer_load_dwordx4 v5, s[44:47], s70 offen offset:0 lds
  s_nop 0
  buffer_load_dwordx4 v6, s[44:47], s70 offen offset:1032 lds
  s_add_u32 m0, s74, 112960
  s_nop 0
  buffer_load_dwordx4 v7, s[36:39], s69 offen offset:0 lds
  s_nop 0
  s_mov_b64 exec, s[86:87]
  buffer_load_dwordx4 v8, s[36:39], s69 offen offset:1024 lds
  s_mov_b64 exec, -1
  s_nop 0
  s_add_u32 m0, s74, 120128
  s_nop 0
  buffer_load_dwordx4 v7, s[40:43], s69 offen offset:0 lds
  s_nop 0
  s_mov_b64 exec, s[86:87]
  buffer_load_dwordx4 v8, s[40:43], s69 offen offset:1024 lds
  s_mov_b64 exec, -1
  s_nop 0
  s_branch LH_join
LH_w0:
  s_mov_b32 s76, 0
LH_ack:
  buffer_load_dwordx2 v[118:119], v18, s[92:95], 0 offen offset:2048 sc1
  s_waitcnt vmcnt(0)
  v_cmp_ne_u64 vcc, v[112:113], v[118:119]
  s_add_u32 s76, s76, 1
  s_nop 0
  s_cbranch_vccz LH_ack_ok
  s_cmp_lg_u32 s18, 0
  s_cbranch_scc1 LH_e3
  buffer_load_dwordx4 v[40:43], v17, s[92:95], 0 offen sc1
  buffer_load_dwordx4 v[44:47], v17, s[92:95], 0 offen offset:1024 sc1
  s_waitcnt vmcnt(0)
  buffer_store_dwordx4 v[40:43], v17, s[92:95], 0 offen offset:2048 sc1
  buffer_store_dwordx4 v[44:47], v17, s[92:95], 0 offen offset:3072 sc1
  s_nop 1
LH_e3:
  s_cmp_lt_u32 s76, 0x40000
  s_cbranch_scc1 LH_ack
LH_ack_ok:
  s_barrier
  v_mov_b32 v114, 1
  s_lshl_b32 s76, s19, 2
  s_add_u32 s76, s76, 67584
  v_mov_b32 v19, s76
  s_mov_b64 exec, 1
  buffer_store_dword v114, v19, s[92:95], 0 offen sc1
  s_mov_b64 exec, -1
  s_lshl_b32 s76, s11, 7
  s_add_u32 s76, s76, 67584
  v_and_b32 v19, 7, v12
  v_lshlrev_b32 v19, 4, v19
  v_add_u32 v19, s76, v19
  s_cmp_lg_u32 s18, 0
  s_cbranch_scc1 LH_e5
  v_lshlrev_b32 v19, 4, v12
  v_add_u32 v19, 67584, v19
  s_add_u32 s72, s68, s12
  s_and_b32 s72, s72, 7
  s_and_b32 s73, s68, 0xfffffff8
  s_or_b32 s72, s72, s73
  s_mul_i32 s69, s72, 229376
  s_lshl_b32 s70, s72, 18
  s_lshl_b32 s71, s72, 12
  s_add_u32 s68, s68, 1
  s_add_u32 m0, s75, 90368
  s_nop 0
  buffer_load_dwordx4 v5, s[44:47], s70 offen offset:0 lds
  s_nop 0
  buffer_load_dwordx4 v6, s[44:47], s70 offen offset:1032 lds
  s_add_u32 m0, s74, 90368
  s_nop 0
  buffer_load_dwordx4 v7, s[36:39], s69 offen offset:0 lds
  s_nop 0
  s_mov_b64 exec, s[86:87]
  buffer_load_dwordx4 v8, s[36:39], s69 offen offset:1024 lds
  s_mov_b64 exec, -1
  s_nop 0
  s_add_u32 m0, s74, 97536
  s_nop 0
  buffer_load_dwordx4 v7, s[40:43], s69 offen offset:0 lds
  s_nop 0
  s_mov_b64 exec, s[86:87]
  buffer_load_dwordx4 v8, s[40:43], s69 offen offset:1024 lds
  s_mov_b64 exec, -1
  s_nop 0
  s_mov_b32 s17, s71
LH_e5:
  s_add_u32 s72, s68, s12
  s_and_b32 s72, s72, 7
  s_and_b32 s73, s68, 0xfffffff8
  s_or_b32 s72, s72, s73
  s_mul_i32 s69, s72, 229376
  s_lshl_b32 s70, s72, 18
  s_lshl_b32 s71, s72, 12
  s_add_u32 s68, s68, 1
  s_add_u32 m0, s75, 112960
  s_nop 0
  buffer_load_dwordx4 v5, s[44:47], s70 offen offset:0 lds
  s_nop 0
  buffer_load_dwordx4 v6, s[44:47], s70 offen offset:1032 lds
  s_add_u32 m0, s74, 112960
  s_nop 0
  buffer_load_dwordx4 v7, s[36:39], s69 offen offset:0 lds
  s_nop 0
  s_mov_b64 exec, s[86:87]
  buffer_load_dwordx4 v8, s[36:39], s69 offen offset:1024 lds
  s_mov_b64 exec, -1
  s_nop 0
  s_add_u32 m0, s74, 120128
  s_nop 0
  buffer_load_dwordx4 v7, s[40:43], s69 offen offset:0 lds
  s_nop 0
  s_mov_b64 exec, s[86:87]
  buffer_load_dwordx4 v8, s[40:43], s69 offen offset:1024 lds
  s_mov_b64 exec, -1
  s_nop 0
  s_mov_b32 s76, 0
LH_cnt:
  s_cmp_lg_u32 s18, 0
  s_cbranch_scc1 LH_e4
  buffer_load_dwordx4 v[40:43], v17, s[92:95], 0 offen sc1
  buffer_load_dwordx4 v[44:47], v17, s[92:95], 0 offen offset:1024 sc1
  s_waitcnt vmcnt(0)
  buffer_store_dwordx4 v[40:43], v17, s[92:95], 0 offen offset:2048 sc1
  buffer_store_dwordx4 v[44:47], v17, s[92:95], 0 offen offset:3072 sc1
  s_nop 1
LH_e4:
  buffer_load_dwordx4 v[120:123], v19, s[92:95], 0 offen sc1
  s_waitcnt vmcnt(0)
  v_and_b32 v120, v120, v121
  v_and_b32 v122, v122, v123
  v_and_b32 v120, v120, v122
  v_cmp_ne_u32 vcc, 1, v120
  s_add_u32 s76, s76, 1
  s_nop 0
  s_cbranch_vccz LH_join
  s_cmp_lt_u32 s76, 0x40000
  s_cbranch_scc1 LH_cnt
LH_join:
  s_barrier
  buffer_load_dwordx4 a[192:195], v9, s[48:51], s17 offen offset:0 sc1
  buffer_load_dwordx4 a[196:199], v9, s[48:51], s17 offen offset:1024 sc1
  buffer_load_dwordx4 a[200:203], v9, s[48:51], s17 offen offset:2048 sc1
  buffer_load_dwordx4 a[204:207], v9, s[48:51], s17 offen offset:3072 sc1
  buffer_load_dwordx4 a[208:211], v9, s[48:51], s71 offen offset:0 sc1
  buffer_load_dwordx4 a[212:215], v9, s[48:51], s71 offen offset:1024 sc1
  buffer_load_dwordx4 a[216:219], v9, s[48:51], s71 offen offset:2048 sc1
  buffer_load_dwordx4 a[220:223], v9, s[48:51], s71 offen offset:3072 sc1
  v_and_b32 v32, 63, v0
  v_lshrrev_b32 v33, 4, v32
  v_bfe_u32 v34, v32, 2, 2
  v_and_b32 v35, 3, v32
  v_lshlrev_b32 v36, 4, v32
  s_lshl_b32 s72, s11, 2
  s_add_u32 s72, s72, s9
  s_mul_i32 s79, s72, 458752
  s_lshl_b32 s80, s72, 19
  s_mul_i32 s72, s10, 7
  s_add_u32 s73, s72, 1
  s_lshr_b32 s73, s73, 1
  s_lshl_b32 s73, s73, 12
  s_add_u32 s73, s73, s79
  v_add_u32 v10, s73, v36
  s_and_b32 s73, s10, 1
  s_cmp_eq_u32 s73, 0
  s_cselect_b32 s73, 6, 0
  s_add_u32 s72, s72, s73
  s_lshr_b32 s73, s72, 1
  s_lshl_b32 s73, s73, 12
  s_and_b32 s72, s72, 1
  s_lshl_b32 s72, s72, 3
  s_add_u32 s72, s72, s73
  s_add_u32 s72, s72, s79
  v_add_u32 v11, s72, v36
  s_lshl_b32 s72, s10, 14
  s_add_u32 s72, s72, s80
  v_add_u32 v22, s72, v36
  s_lshl_b32 s72, s9, 10
  v_add_u32 v21, s72, v36
  s_mov_b32 s79, 0
  s_mov_b32 s80, 0
  v_sub_u32 v37, 4, v34
  v_and_b32 v37, 3, v37
  v_xor_b32 v37, v33, v37
  v_lshlrev_b32 v37, 4, v37
  v_and_b32 v38, 7, v32
  v_lshl_add_u32 v37, v38, 6, v37
  v_bfe_u32 v38, v32, 3, 1
  v_lshl_add_u32 v12, v38, 11, v37
  v_lshrrev_b32 v37, 1, v33
  v_lshlrev_b32 v37, 11, v37
  v_and_b32 v38, 1, v33
  v_lshl_add_u32 v37, v38, 8, v37
  v_lshl_add_u32 v37, v34, 6, v37
  v_lshl_add_u32 v37, v35, 3, v37
  v_add_u32 v37, 65536, v37
  v_lshl_add_u32 v13, v38, 5, v37
  v_xor_b32 v14, 32, v13
  s_lshl_b32 s72, s9, 12
  s_add_u32 s72, s72, 1024
  v_bfe_u32 v37, v32, 2, 3
  v_lshlrev_b32 v37, 8, v37
  v_lshrrev_b32 v39, 5, v32
  v_lshl_add_u32 v37, v39, 6, v37
  v_add_u32 v37, s72, v37
  v_sub_u32 v39, 4, v38
  v_and_b32 v39, 3, v39
  v_xor_b32 v39, v35, v39
  v_lshl_add_u32 v15, v39, 4, v37
  v_subrev_u32 v16, 896, v15
  v_sub_u32 v39, 2, v38
  v_xor_b32 v39, v35, v39
  v_lshl_add_u32 v17, v39, 4, v37
  v_subrev_u32 v18, 896, v17
  v_lshlrev_b32 v39, 1, v38
  v_xor_b32 v39, v35, v39
  v_lshl_add_u32 v19, v39, 4, v37
  v_subrev_u32 v20, 896, v19
  s_lshl_b32 s77, s9, 12
  s_add_u32 s78, s77, 65536
  s_mov_b32 s82, 1233125376
  s_mov_b32 s83, 4096
  s_mov_b32 s84, 8192
  s_mov_b32 s85, 12288
  s_mov_b32 s76, 2
  s_waitcnt vmcnt(0)
  s_barrier
  ds_read_b64_tr_b16 a[224:225], v28 offset:0
  ds_read_b64_tr_b16 a[226:227], v28 offset:4128
  ds_read_b64_tr_b16 a[228:229], v28 offset:22592
  ds_read_b64_tr_b16 a[230:231], v28 offset:26720
  ds_read_b64_tr_b16 a[232:233], v28 offset:8
  ds_read_b64_tr_b16 a[234:235], v28 offset:4136
  ds_read_b64_tr_b16 a[236:237], v28 offset:22600
  ds_read_b64_tr_b16 a[238:239], v28 offset:26728
  s_waitcnt lgkmcnt(4)
  v_mfma_f32_16x16x32_bf16 a[0:3], a[224:227], a[192:195], 0
  s_add_u32 s72, s68, s12
  s_and_b32 s72, s72, 7
  s_and_b32 s73, s68, 0xfffffff8
  s_or_b32 s72, s72, s73
  s_mul_i32 s69, s72, 229376
  s_lshl_b32 s70, s72, 18
  s_lshl_b32 s71, s72, 12
  s_add_u32 s68, s68, 1
  s_add_u32 m0, s75, 0
  ds_read_b64_tr_b16 a[240:241], v28 offset:256
  v_mfma_f32_16x16x32_bf16 a[0:3], a[228:231], a[208:211], a[0:3]
  ds_read_b64_tr_b16 a[242:243], v28 offset:4384
  v_mfma_f32_16x16x32_bf16 a[4:7], a[228:231], a[212:215], 0
  buffer_load_dwordx4 v5, s[44:47], s70 offen offset:0 lds
  v_mfma_f32_16x16x32_bf16 a[4:7], a[224:227], a[196:199], a[4:7]
  ds_read_b64_tr_b16 a[244:245], v28 offset:22848
  v_mfma_f32_16x16x32_bf16 a[8:11], a[224:227], a[200:203], 0
  v_mfma_f32_16x16x32_bf16 a[8:11], a[228:231], a[216:219], a[8:11]
  ds_read_b64_tr_b16 a[246:247], v28 offset:26976
  v_mfma_f32_16x16x32_bf16 a[12:15], a[228:231], a[220:223], 0
  buffer_load_dwordx4 v6, s[44:47], s70 offen offset:1032 lds
  s_add_u32 m0, s74, 0
  v_mfma_f32_16x16x32_bf16 a[12:15], a[224:227], a[204:207], a[12:15]
  s_waitcnt lgkmcnt(4)
  ds_read_b64_tr_b16 a[248:249], v28 offset:264
  v_mfma_f32_16x16x32_bf16 a[16:19], a[232:235], a[192:195], 0
  v_mfma_f32_16x16x32_bf16 a[16:19], a[236:239], a[208:211], a[16:19]
  ds_read_b64_tr_b16 a[250:251], v28 offset:4392
  v_mfma_f32_16x16x32_bf16 a[20:23], a[236:239], a[212:215], 0
  buffer_load_dwordx4 v7, s[36:39], s69 offen offset:0 lds
  v_mfma_f32_16x16x32_bf16 a[20:23], a[232:235], a[196:199], a[20:23]
  ds_read_b64_tr_b16 a[252:253], v28 offset:22856
  v_mfma_f32_16x16x32_bf16 a[24:27], a[232:235], a[200:203], 0
  s_mov_b64 exec, s[86:87]
  buffer_load_dwordx4 v8, s[36:39], s69 offen offset:1024 lds
  s_mov_b64 exec, -1
  v_mfma_f32_16x16x32_bf16 a[24:27], a[236:239], a[216:219], a[24:27]
  s_add_u32 m0, s74, 7168
  ds_read_b64_tr_b16 a[254:255], v28 offset:26984
  v_mfma_f32_16x16x32_bf16 a[28:31], a[236:239], a[220:223], 0
  v_mfma_f32_16x16x32_bf16 a[28:31], a[232:235], a[204:207], a[28:31]
  s_waitcnt lgkmcnt(4)
  ds_read_b64_tr_b16 a[224:225], v28 offset:512
  v_mfma_f32_16x16x32_bf16 a[32:35], a[240:243], a[192:195], 0
  buffer_load_dwordx4 v7, s[40:43], s69 offen offset:0 lds
  v_mfma_f32_16x16x32_bf16 a[32:35], a[244:247], a[208:211], a[32:35]
  ds_read_b64_tr_b16 a[226:227], v28 offset:4640
  v_mfma_f32_16x16x32_bf16 a[36:39], a[244:247], a[212:215], 0
  v_mfma_f32_16x16x32_bf16 a[36:39], a[240:243], a[196:199], a[36:39]
  ds_read_b64_tr_b16 a[228:229], v28 offset:23104
  v_mfma_f32_16x16x32_bf16 a[40:43], a[240:243], a[200:203], 0
  s_mov_b64 exec, s[86:87]
  buffer_load_dwordx4 v8, s[40:43], s69 offen offset:1024 lds
  s_mov_b64 exec, -1
  v_mfma_f32_16x16x32_bf16 a[40:43], a[244:247], a[216:219], a[40:43]
  ds_read_b64_tr_b16 a[230:231], v28 offset:27232
  v_mfma_f32_16x16x32_bf16 a[44:47], a[244:247], a[220:223], 0
  ds_read_b64_tr_b16 a[232:233], v28 offset:520
  v_mfma_f32_16x16x32_bf16 a[44:47], a[240:243], a[204:207], a[44:47]
  s_waitcnt lgkmcnt(5)
  v_mfma_f32_16x16x32_bf16 a[48:51], a[248:251], a[192:195], 0
  ds_read_b64_tr_b16 a[234:235], v28 offset:4648
  v_mfma_f32_16x16x32_bf16 a[48:51], a[252:255], a[208:211], a[48:51]
  buffer_load_dwordx4 a[128:131], v9, s[48:51], s71 offen offset:0 sc1
  v_mfma_f32_16x16x32_bf16 a[52:55], a[252:255], a[212:215], 0
  ds_read_b64_tr_b16 a[236:237], v28 offset:23112
  v_mfma_f32_16x16x32_bf16 a[52:55], a[248:251], a[196:199], a[52:55]
  buffer_load_dwordx4 a[132:135], v9, s[48:51], s71 offen offset:1024 sc1
  v_mfma_f32_16x16x32_bf16 a[56:59], a[248:251], a[200:203], 0
  ds_read_b64_tr_b16 a[238:239], v28 offset:27240
  v_mfma_f32_16x16x32_bf16 a[56:59], a[252:255], a[216:219], a[56:59]
  v_mfma_f32_16x16x32_bf16 a[60:63], a[252:255], a[220:223], 0
  ds_read_b64_tr_b16 a[240:241], v28 offset:768
  v_mfma_f32_16x16x32_bf16 a[60:63], a[248:251], a[204:207], a[60:63]
  s_waitcnt lgkmcnt(5)
  v_mfma_f32_16x16x32_bf16 a[64:67], a[224:227], a[192:195], 0
  ds_read_b64_tr_b16 a[242:243], v28 offset:4896
  v_mfma_f32_16x16x32_bf16 a[64:67], a[228:231], a[208:211], a[64:67]
  buffer_load_dwordx4 a[136:139], v9, s[48:51], s71 offen offset:2048 sc1
  v_mfma_f32_16x16x32_bf16 a[68:71], a[228:231], a[212:215], 0
  ds_read_b64_tr_b16 a[244:245], v28 offset:23360
  v_mfma_f32_16x16x32_bf16 a[68:71], a[224:227], a[196:199], a[68:71]
  buffer_load_dwordx4 a[140:143], v9, s[48:51], s71 offen offset:3072 sc1
  v_mfma_f32_16x16x32_bf16 a[72:75], a[224:227], a[200:203], 0
  s_add_u32 s72, s68, s12
  s_and_b32 s72, s72, 7
  s_and_b32 s73, s68, 0xfffffff8
  s_or_b32 s72, s72, s73
  s_mul_i32 s69, s72, 229376
  s_lshl_b32 s70, s72, 18
  s_lshl_b32 s71, s72, 12
  s_add_u32 s68, s68, 1
  s_add_u32 m0, s75, 22592
  ds_read_b64_tr_b16 a[246:247], v28 offset:27488
  v_mfma_f32_16x16x32_bf16 a[72:75], a[228:231], a[216:219], a[72:75]
  v_mfma_f32_16x16x32_bf16 a[76:79], a[228:231], a[220:223], 0
  ds_read_b64_tr_b16 a[248:249], v28 offset:776
  v_mfma_f32_16x16x32_bf16 a[76:79], a[224:227], a[204:207], a[76:79]
  s_waitcnt lgkmcnt(5)
  v_mfma_f32_16x16x32_bf16 a[80:83], a[232:235], a[192:195], 0
  ds_read_b64_tr_b16 a[250:251], v28 offset:4904
  v_mfma_f32_16x16x32_bf16 a[80:83], a[236:239], a[208:211], a[80:83]
  buffer_load_dwordx4 v5, s[44:47], s70 offen offset:0 lds
  v_mfma_f32_16x16x32_bf16 a[84:87], a[236:239], a[212:215], 0
  ds_read_b64_tr_b16 a[252:253], v28 offset:23368
  v_mfma_f32_16x16x32_bf16 a[84:87], a[232:235], a[196:199], a[84:87]
  ds_read_b64_tr_b16 a[254:255], v28 offset:27496
  v_mfma_f32_16x16x32_bf16 a[88:91], a[232:235], a[200:203], 0
  buffer_load_dwordx4 v6, s[44:47], s70 offen offset:1032 lds
  s_add_u32 m0, s74, 22592
  v_mfma_f32_16x16x32_bf16 a[88:91], a[236:239], a[216:219], a[88:91]
  ds_read_b64_tr_b16 a[224:225], v29 offset:0
  v_mfma_f32_16x16x32_bf16 a[92:95], a[236:239], a[220:223], 0
  buffer_load_dwordx4 v7, s[36:39], s69 offen offset:0 lds
  v_mfma_f32_16x16x32_bf16 a[92:95], a[232:235], a[204:207], a[92:95]
  s_waitcnt lgkmcnt(5)
  ds_read_b64_tr_b16 a[226:227], v29 offset:3584
  v_mfma_f32_16x16x32_bf16 a[96:99], a[240:243], a[192:195], 0
  v_mfma_f32_16x16x32_bf16 a[96:99], a[244:247], a[208:211], a[96:99]
  ds_read_b64_tr_b16 a[228:229], v29 offset:22592
  v_mfma_f32_16x16x32_bf16 a[100:103], a[244:247], a[212:215], 0
  s_mov_b64 exec, s[86:87]
  buffer_load_dwordx4 v8, s[36:39], s69 offen offset:1024 lds
  s_mov_b64 exec, -1
  v_mfma_f32_16x16x32_bf16 a[100:103], a[240:243], a[196:199], a[100:103]
  s_add_u32 m0, s74, 29760
  ds_read_b64_tr_b16 a[230:231], v29 offset:26176
  v_mfma_f32_16x16x32_bf16 a[104:107], a[240:243], a[200:203], 0
  buffer_load_dwordx4 v7, s[40:43], s69 offen offset:0 lds
  v_mfma_f32_16x16x32_bf16 a[104:107], a[244:247], a[216:219], a[104:107]
  ds_read_b64_tr_b16 a[232:233], v29 offset:7168
  v_mfma_f32_16x16x32_bf16 a[108:111], a[244:247], a[220:223], 0
  v_mfma_f32_16x16x32_bf16 a[108:111], a[240:243], a[204:207], a[108:111]
  s_waitcnt lgkmcnt(5)
  ds_read_b64_tr_b16 a[234:235], v29 offset:10752
  v_mfma_f32_16x16x32_bf16 a[112:115], a[248:251], a[192:195], 0
  s_mov_b64 exec, s[86:87]
  buffer_load_dwordx4 v8, s[40:43], s69 offen offset:1024 lds
  s_mov_b64 exec, -1
  v_mfma_f32_16x16x32_bf16 a[112:115], a[252:255], a[208:211], a[112:115]
  ds_read_b64_tr_b16 a[236:237], v29 offset:29760
  v_mfma_f32_16x16x32_bf16 a[116:119], a[252:255], a[212:215], 0
  v_mfma_f32_16x16x32_bf16 a[116:119], a[248:251], a[196:199], a[116:119]
  ds_read_b64_tr_b16 a[238:239], v29 offset:33344
  v_mfma_f32_16x16x32_bf16 a[120:123], a[248:251], a[200:203], 0
  buffer_load_dwordx4 a[144:147], v9, s[48:51], s71 offen offset:0 sc1
  v_mfma_f32_16x16x32_bf16 a[120:123], a[252:255], a[216:219], a[120:123]
  ds_read_b64_tr_b16 a[240:241], v29 offset:32
  v_mfma_f32_16x16x32_bf16 a[124:127], a[252:255], a[220:223], 0
  v_mfma_f32_16x16x32_bf16 a[124:127], a[248:251], a[204:207], a[124:127]
  s_waitcnt lgkmcnt(5)
  ds_read_b64_tr_b16 a[242:243], v29 offset:3616
  v_mfma_f32_16x16x32_bf16 v[32:35], a[224:227], a[192:195], 0
  ds_read_b64_tr_b16 a[244:245], v29 offset:22624
  v_mfma_f32_16x16x32_bf16 v[32:35], a[228:231], a[208:211], v[32:35]
  buffer_load_dwordx4 a[148:151], v9, s[48:51], s71 offen offset:1024 sc1
  v_mfma_f32_16x16x32_bf16 v[36:39], a[228:231], a[212:215], 0
  ds_read_b64_tr_b16 a[246:247], v29 offset:26208
  v_mfma_f32_16x16x32_bf16 v[36:39], a[224:227], a[196:199], v[36:39]
  buffer_load_dwordx4 a[152:155], v9, s[48:51], s71 offen offset:2048 sc1
  v_mfma_f32_16x16x32_bf16 v[40:43], a[224:227], a[200:203], 0
  ds_read_b64_tr_b16 a[248:249], v29 offset:7200
  v_mfma_f32_16x16x32_bf16 v[40:43], a[228:231], a[216:219], v[40:43]
  v_mfma_f32_16x16x32_bf16 v[44:47], a[228:231], a[220:223], 0
  ds_read_b64_tr_b16 a[250:251], v29 offset:10784
  v_mfma_f32_16x16x32_bf16 v[44:47], a[224:227], a[204:207], v[44:47]
  s_waitcnt lgkmcnt(6)
  v_mfma_f32_16x16x32_bf16 v[48:51], a[232:235], a[192:195], 0
  ds_read_b64_tr_b16 a[252:253], v29 offset:29792
  v_mfma_f32_16x16x32_bf16 v[48:51], a[236:239], a[208:211], v[48:51]
  buffer_load_dwordx4 a[156:159], v9, s[48:51], s71 offen offset:3072 sc1
  v_mfma_f32_16x16x32_bf16 v[52:55], a[236:239], a[212:215], 0
  s_add_u32 s72, s68, s12
  s_and_b32 s72, s72, 7
  s_and_b32 s73, s68, 0xfffffff8
  s_or_b32 s72, s72, s73
  s_mul_i32 s69, s72, 229376
  s_lshl_b32 s70, s72, 18
  s_lshl_b32 s71, s72, 12
  s_add_u32 s68, s68, 1
  s_add_u32 m0, s75, 45184
  ds_read_b64_tr_b16 a[254:255], v29 offset:33376
  v_mfma_f32_16x16x32_bf16 v[52:55], a[232:235], a[196:199], v[52:55]
  buffer_load_dwordx4 v5, s[44:47], s70 offen offset:0 lds
  v_mfma_f32_16x16x32_bf16 v[56:59], a[232:235], a[200:203], 0
  ds_read_b64_tr_b16 a[224:225], v29 offset:64
  v_mfma_f32_16x16x32_bf16 v[56:59], a[236:239], a[216:219], v[56:59]
  v_mfma_f32_16x16x32_bf16 v[60:63], a[236:239], a[220:223], 0
  ds_read_b64_tr_b16 a[226:227], v29 offset:3648
  v_mfma_f32_16x16x32_bf16 v[60:63], a[232:235], a[204:207], v[60:63]
  s_waitcnt lgkmcnt(6)
  v_mfma_f32_16x16x32_bf16 v[64:67], a[240:243], a[192:195], 0
  ds_read_b64_tr_b16 a[228:229], v29 offset:22656
  v_mfma_f32_16x16x32_bf16 v[64:67], a[244:247], a[208:211], v[64:67]
  buffer_load_dwordx4 v6, s[44:47], s70 offen offset:1032 lds
  s_add_u32 m0, s74, 45184
  v_mfma_f32_16x16x32_bf16 v[68:71], a[244:247], a[212:215], 0
  ds_read_b64_tr_b16 a[230:231], v29 offset:26240
  v_mfma_f32_16x16x32_bf16 v[68:71], a[240:243], a[196:199], v[68:71]
  buffer_load_dwordx4 v7, s[36:39], s69 offen offset:0 lds
  v_mfma_f32_16x16x32_bf16 v[72:75], a[240:243], a[200:203], 0
  ds_read_b64_tr_b16 a[232:233], v29 offset:7232
  v_mfma_f32_16x16x32_bf16 v[72:75], a[244:247], a[216:219], v[72:75]
  s_mov_b64 exec, s[86:87]
  buffer_load_dwordx4 v8, s[36:39], s69 offen offset:1024 lds
  s_mov_b64 exec, -1
  v_mfma_f32_16x16x32_bf16 v[76:79], a[244:247], a[220:223], 0
  s_add_u32 m0, s74, 52352
  ds_read_b64_tr_b16 a[234:235], v29 offset:10816
  v_mfma_f32_16x16x32_bf16 v[76:79], a[240:243], a[204:207], v[76:79]
  s_waitcnt lgkmcnt(6)
  ds_read_b64_tr_b16 a[236:237], v29 offset:29824
  v_mfma_f32_16x16x32_bf16 v[80:83], a[248:251], a[192:195], 0
  v_mfma_f32_16x16x32_bf16 v[80:83], a[252:255], a[208:211], v[80:83]
  ds_read_b64_tr_b16 a[238:239], v29 offset:33408
  v_mfma_f32_16x16x32_bf16 v[84:87], a[252:255], a[212:215], 0
  buffer_load_dwordx4 v7, s[40:43], s69 offen offset:0 lds
  v_mfma_f32_16x16x32_bf16 v[84:87], a[248:251], a[196:199], v[84:87]
  ds_read_b64_tr_b16 a[240:241], v29 offset:96
  v_mfma_f32_16x16x32_bf16 v[88:91], a[248:251], a[200:203], 0
  s_mov_b64 exec, s[86:87]
  buffer_load_dwordx4 v8, s[40:43], s69 offen offset:1024 lds
  s_mov_b64 exec, -1
  v_mfma_f32_16x16x32_bf16 v[88:91], a[252:255], a[216:219], v[88:91]
  ds_read_b64_tr_b16 a[242:243], v29 offset:3680
  v_mfma_f32_16x16x32_bf16 v[92:95], a[252:255], a[220:223], 0
  v_mfma_f32_16x16x32_bf16 v[92:95], a[248:251], a[204:207], v[92:95]
  s_waitcnt lgkmcnt(6)
  ds_read_b64_tr_b16 a[244:245], v29 offset:22688
  v_mfma_f32_16x16x32_bf16 v[96:99], a[224:227], a[192:195], 0
  buffer_load_dwordx4 a[160:163], v9, s[48:51], s71 offen offset:0 sc1
  v_mfma_f32_16x16x32_bf16 v[96:99], a[228:231], a[208:211], v[96:99]
  ds_read_b64_tr_b16 a[246:247], v29 offset:26272
  v_mfma_f32_16x16x32_bf16 v[100:103], a[228:231], a[212:215], 0
  v_mfma_f32_16x16x32_bf16 v[100:103], a[224:227], a[196:199], v[100:103]
  ds_read_b64_tr_b16 a[248:249], v29 offset:7264
  v_mfma_f32_16x16x32_bf16 v[104:107], a[224:227], a[200:203], 0
  buffer_load_dwordx4 a[164:167], v9, s[48:51], s71 offen offset:1024 sc1
  v_mfma_f32_16x16x32_bf16 v[104:107], a[228:231], a[216:219], v[104:107]
  ds_read_b64_tr_b16 a[250:251], v29 offset:10848
  v_mfma_f32_16x16x32_bf16 v[108:111], a[228:231], a[220:223], 0
  v_mfma_f32_16x16x32_bf16 v[108:111], a[224:227], a[204:207], v[108:111]
  s_waitcnt lgkmcnt(6)
  ds_read_b64_tr_b16 a[252:253], v29 offset:29856
  v_mfma_f32_16x16x32_bf16 v[112:115], a[232:235], a[192:195], 0
  buffer_load_dwordx4 a[168:171], v9, s[48:51], s71 offen offset:2048 sc1
  v_mfma_f32_16x16x32_bf16 v[112:115], a[236:239], a[208:211], v[112:115]
  ds_read_b64_tr_b16 a[254:255], v29 offset:33440
  v_mfma_f32_16x16x32_bf16 v[116:119], a[236:239], a[212:215], 0
  v_mfma_f32_16x16x32_bf16 v[116:119], a[232:235], a[196:199], v[116:119]
  ds_read_b64_tr_b16 a[224:225], v29 offset:128
  v_mfma_f32_16x16x32_bf16 v[120:123], a[232:235], a[200:203], 0
  ds_read_b64_tr_b16 a[226:227], v29 offset:3712
  v_mfma_f32_16x16x32_bf16 v[120:123], a[236:239], a[216:219], v[120:123]
  buffer_load_dwordx4 a[172:175], v9, s[48:51], s71 offen offset:3072 sc1
  v_mfma_f32_16x16x32_bf16 v[124:127], a[236:239], a[220:223], 0
  s_add_u32 s72, s68, s12
  s_and_b32 s72, s72, 7
  s_and_b32 s73, s68, 0xfffffff8
  s_or_b32 s72, s72, s73
  s_mul_i32 s69, s72, 229376
  s_lshl_b32 s70, s72, 18
  s_lshl_b32 s71, s72, 12
  s_add_u32 s68, s68, 1
  s_add_u32 m0, s75, 67776
  ds_read_b64_tr_b16 a[228:229], v29 offset:22720
  v_mfma_f32_16x16x32_bf16 v[124:127], a[232:235], a[204:207], v[124:127]
  s_waitcnt lgkmcnt(7)
  v_mfma_f32_16x16x32_bf16 v[128:131], a[240:243], a[192:195], 0
  ds_read_b64_tr_b16 a[230:231], v29 offset:26304
  v_mfma_f32_16x16x32_bf16 v[128:131], a[244:247], a[208:211], v[128:131]
  buffer_load_dwordx4 v5, s[44:47], s70 offen offset:0 lds
  v_mfma_f32_16x16x32_bf16 v[132:135], a[244:247], a[212:215], 0
  ds_read_b64_tr_b16 a[232:233], v29 offset:7296
  v_mfma_f32_16x16x32_bf16 v[132:135], a[240:243], a[196:199], v[132:135]
  buffer_load_dwordx4 v6, s[44:47], s70 offen offset:1032 lds
  s_add_u32 m0, s74, 67776
  v_mfma_f32_16x16x32_bf16 v[136:139], a[240:243], a[200:203], 0
  ds_read_b64_tr_b16 a[234:235], v29 offset:10880
  v_mfma_f32_16x16x32_bf16 v[136:139], a[244:247], a[216:219], v[136:139]
  v_mfma_f32_16x16x32_bf16 v[140:143], a[244:247], a[220:223], 0
  ds_read_b64_tr_b16 a[236:237], v29 offset:29888
  v_mfma_f32_16x16x32_bf16 v[140:143], a[240:243], a[204:207], v[140:143]
  s_waitcnt lgkmcnt(7)
  v_mfma_f32_16x16x32_bf16 v[144:147], a[248:251], a[192:195], 0
  ds_read_b64_tr_b16 a[238:239], v29 offset:33472
  v_mfma_f32_16x16x32_bf16 v[144:147], a[252:255], a[208:211], v[144:147]
  buffer_load_dwordx4 v7, s[36:39], s69 offen offset:0 lds
  v_mfma_f32_16x16x32_bf16 v[148:151], a[252:255], a[212:215], 0
  ds_read_b64_tr_b16 a[240:241], v29 offset:160
  v_mfma_f32_16x16x32_bf16 v[148:151], a[248:251], a[196:199], v[148:151]
  s_mov_b64 exec, s[86:87]
  buffer_load_dwordx4 v8, s[36:39], s69 offen offset:1024 lds
  s_mov_b64 exec, -1
  v_mfma_f32_16x16x32_bf16 v[152:155], a[248:251], a[200:203], 0
  s_add_u32 m0, s74, 74944
  ds_read_b64_tr_b16 a[242:243], v29 offset:3744
  v_mfma_f32_16x16x32_bf16 v[152:155], a[252:255], a[216:219], v[152:155]
  buffer_load_dwordx4 v7, s[40:43], s69 offen offset:0 lds
  v_mfma_f32_16x16x32_bf16 v[156:159], a[252:255], a[220:223], 0
  ds_read_b64_tr_b16 a[244:245], v29 offset:22752
  v_mfma_f32_16x16x32_bf16 v[156:159], a[248:251], a[204:207], v[156:159]
  s_waitcnt lgkmcnt(7)
  v_mfma_f32_16x16x32_bf16 v[160:163], a[224:227], a[192:195], 0
  ds_read_b64_tr_b16 a[246:247], v29 offset:26336
  v_mfma_f32_16x16x32_bf16 v[160:163], a[228:231], a[208:211], v[160:163]
  ds_read_b64_tr_b16 a[248:249], v29 offset:7328
  v_mfma_f32_16x16x32_bf16 v[164:167], a[228:231], a[212:215], 0
  s_mov_b64 exec, s[86:87]
  buffer_load_dwordx4 v8, s[40:43], s69 offen offset:1024 lds
  s_mov_b64 exec, -1
  v_mfma_f32_16x16x32_bf16 v[164:167], a[224:227], a[196:199], v[164:167]
  ds_read_b64_tr_b16 a[250:251], v29 offset:10912
  v_mfma_f32_16x16x32_bf16 v[168:171], a[224:227], a[200:203], 0
  buffer_load_dwordx4 a[176:179], v9, s[48:51], s71 offen offset:0 sc1
  v_mfma_f32_16x16x32_bf16 v[168:171], a[228:231], a[216:219], v[168:171]
  ds_read_b64_tr_b16 a[252:253], v29 offset:29920
  v_mfma_f32_16x16x32_bf16 v[172:175], a[228:231], a[220:223], 0
  v_mfma_f32_16x16x32_bf16 v[172:175], a[224:227], a[204:207], v[172:175]
  s_waitcnt lgkmcnt(7)
  ds_read_b64_tr_b16 a[254:255], v29 offset:33504
  v_mfma_f32_16x16x32_bf16 v[176:179], a[232:235], a[192:195], 0
  buffer_load_dwordx4 a[180:183], v9, s[48:51], s71 offen offset:1024 sc1
  v_mfma_f32_16x16x32_bf16 v[176:179], a[236:239], a[208:211], v[176:179]
  ds_read_b64_tr_b16 a[224:225], v29 offset:192
  v_mfma_f32_16x16x32_bf16 v[180:183], a[236:239], a[212:215], 0
  v_mfma_f32_16x16x32_bf16 v[180:183], a[232:235], a[196:199], v[180:183]
  ds_read_b64_tr_b16 a[226:227], v29 offset:3776
  v_mfma_f32_16x16x32_bf16 v[184:187], a[232:235], a[200:203], 0
  buffer_load_dwordx4 a[184:187], v9, s[48:51], s71 offen offset:2048 sc1
  v_mfma_f32_16x16x32_bf16 v[184:187], a[236:239], a[216:219], v[184:187]
  ds_read_b64_tr_b16 a[228:229], v29 offset:22784
  v_mfma_f32_16x16x32_bf16 v[188:191], a[236:239], a[220:223], 0
  v_mfma_f32_16x16x32_bf16 v[188:191], a[232:235], a[204:207], v[188:191]
  s_waitcnt lgkmcnt(7)
  ds_read_b64_tr_b16 a[230:231], v29 offset:26368
  v_mfma_f32_16x16x32_bf16 v[192:195], a[240:243], a[192:195], 0
  buffer_load_dwordx4 a[188:191], v9, s[48:51], s71 offen offset:3072 sc1
  v_mfma_f32_16x16x32_bf16 v[192:195], a[244:247], a[208:211], v[192:195]
  ds_read_b64_tr_b16 a[232:233], v29 offset:7360
  v_mfma_f32_16x16x32_bf16 v[196:199], a[244:247], a[212:215], 0
  v_mfma_f32_16x16x32_bf16 v[196:199], a[240:243], a[196:199], v[196:199]
  ds_read_b64_tr_b16 a[234:235], v29 offset:10944
  v_mfma_f32_16x16x32_bf16 v[200:203], a[240:243], a[200:203], 0
  v_mfma_f32_16x16x32_bf16 v[200:203], a[244:247], a[216:219], v[200:203]
  ds_read_b64_tr_b16 a[236:237], v29 offset:29952
  v_mfma_f32_16x16x32_bf16 v[204:207], a[244:247], a[220:223], 0
  v_mfma_f32_16x16x32_bf16 v[204:207], a[240:243], a[204:207], v[204:207]
  s_waitcnt lgkmcnt(7)
  ds_read_b64_tr_b16 a[238:239], v29 offset:33536
  v_mfma_f32_16x16x32_bf16 v[208:211], a[248:251], a[192:195], 0
  v_mfma_f32_16x16x32_bf16 v[208:211], a[252:255], a[208:211], v[208:211]
  v_mfma_f32_16x16x32_bf16 v[212:215], a[252:255], a[212:215], 0
  v_mfma_f32_16x16x32_bf16 v[212:215], a[248:251], a[196:199], v[212:215]
  v_mfma_f32_16x16x32_bf16 v[216:219], a[248:251], a[200:203], 0
  v_mfma_f32_16x16x32_bf16 v[216:219], a[252:255], a[216:219], v[216:219]
  v_mfma_f32_16x16x32_bf16 v[220:223], a[252:255], a[220:223], 0
  v_mfma_f32_16x16x32_bf16 v[220:223], a[248:251], a[204:207], v[220:223]
  s_waitcnt lgkmcnt(4)
  s_waitcnt lgkmcnt(0)
  s_waitcnt vmcnt(20)
  s_barrier
  v_mfma_f32_16x16x32_bf16 v[224:227], a[224:227], a[192:195], 0
  ds_read_b64_tr_b16 a[240:241], v1 offset:0
  v_mfma_f32_16x16x32_bf16 v[224:227], a[228:231], a[208:211], v[224:227]
  v_mfma_f32_16x16x32_bf16 v[228:231], a[228:231], a[212:215], 0
  ds_read_b64_tr_b16 a[242:243], v1 offset:4128
  v_mfma_f32_16x16x32_bf16 v[228:231], a[224:227], a[196:199], v[228:231]
  v_mfma_f32_16x16x32_bf16 v[232:235], a[224:227], a[200:203], 0
  ds_read_b64_tr_b16 a[244:245], v1 offset:22592
  v_mfma_f32_16x16x32_bf16 v[232:235], a[228:231], a[216:219], v[232:235]
  v_mfma_f32_16x16x32_bf16 v[236:239], a[228:231], a[220:223], 0
  ds_read_b64_tr_b16 a[246:247], v1 offset:26720
  v_mfma_f32_16x16x32_bf16 v[236:239], a[224:227], a[204:207], v[236:239]
  s_waitcnt lgkmcnt(4)
  v_mfma_f32_16x16x32_bf16 v[240:243], a[232:235], a[192:195], 0
  ds_read_b64_tr_b16 a[248:249], v1 offset:8
  v_mfma_f32_16x16x32_bf16 v[240:243], a[236:239], a[208:211], v[240:243]
  v_mfma_f32_16x16x32_bf16 v[244:247], a[236:239], a[212:215], 0
  ds_read_b64_tr_b16 a[250:251], v1 offset:4136
  v_mfma_f32_16x16x32_bf16 v[244:247], a[232:235], a[196:199], v[244:247]
  v_mfma_f32_16x16x32_bf16 v[248:251], a[232:235], a[200:203], 0
  ds_read_b64_tr_b16 a[252:253], v1 offset:22600
  v_mfma_f32_16x16x32_bf16 v[248:251], a[236:239], a[216:219], v[248:251]
  v_mfma_f32_16x16x32_bf16 v[252:255], a[236:239], a[220:223], 0
  ds_read_b64_tr_b16 a[254:255], v1 offset:26728
  v_mfma_f32_16x16x32_bf16 v[252:255], a[232:235], a[204:207], v[252:255]
  s_waitcnt lgkmcnt(4)
  v_mfma_f32_16x16x32_bf16 a[0:3], a[240:243], a[128:131], a[0:3]
  s_add_u32 s72, s68, s12
  ds_read_b64_tr_b16 a[224:225], v1 offset:256
  v_mfma_f32_16x16x32_bf16 a[0:3], a[244:247], a[144:147], a[0:3]
  s_and_b32 s72, s72, 7
  ds_read_b64_tr_b16 a[226:227], v1 offset:4384
  v_mfma_f32_16x16x32_bf16 a[4:7], a[244:247], a[148:151], a[4:7]
  s_and_b32 s73, s68, 0xfffffff8
  v_mfma_f32_16x16x32_bf16 a[4:7], a[240:243], a[132:135], a[4:7]
  s_or_b32 s72, s72, s73
  ds_read_b64_tr_b16 a[228:229], v1 offset:22848
  v_mfma_f32_16x16x32_bf16 a[8:11], a[240:243], a[136:139], a[8:11]
  s_mul_i32 s69, s72, 229376
  v_mfma_f32_16x16x32_bf16 a[8:11], a[244:247], a[152:155], a[8:11]
  s_lshl_b32 s70, s72, 18
  ds_read_b64_tr_b16 a[230:231], v1 offset:26976
  buffer_load_dwordx4 v[24:27], v21, s[4:7], s3 offen
  v_mfma_f32_16x16x32_bf16 a[12:15], a[244:247], a[156:159], a[12:15]
  s_lshl_b32 s71, s72, 12
  v_mfma_f32_16x16x32_bf16 a[12:15], a[240:243], a[140:143], a[12:15]
  s_waitcnt lgkmcnt(4)
  s_add_u32 s68, s68, 1
  ds_read_b64_tr_b16 a[232:233], v1 offset:264
  v_mfma_f32_16x16x32_bf16 a[16:19], a[248:251], a[128:131], a[16:19]
  s_add_u32 m0, s75, 90368
  v_mfma_f32_16x16x32_bf16 a[16:19], a[252:255], a[144:147], a[16:19]
  ds_read_b64_tr_b16 a[234:235], v1 offset:4392
  v_mfma_f32_16x16x32_bf16 a[20:23], a[252:255], a[148:151], a[20:23]
  buffer_load_dwordx4 v5, s[44:47], s70 offen offset:0 lds
  v_mfma_f32_16x16x32_bf16 a[20:23], a[248:251], a[132:135], a[20:23]
  ds_read_b64_tr_b16 a[236:237], v1 offset:22856
  v_mfma_f32_16x16x32_bf16 a[24:27], a[248:251], a[136:139], a[24:27]
  v_mfma_f32_16x16x32_bf16 a[24:27], a[252:255], a[152:155], a[24:27]
  ds_read_b64_tr_b16 a[238:239], v1 offset:26984
  v_mfma_f32_16x16x32_bf16 a[28:31], a[252:255], a[156:159], a[28:31]
  v_mfma_f32_16x16x32_bf16 a[28:31], a[248:251], a[140:143], a[28:31]
  s_waitcnt lgkmcnt(4)
  ds_read_b64_tr_b16 a[240:241], v1 offset:512
  v_mfma_f32_16x16x32_bf16 a[32:35], a[224:227], a[128:131], a[32:35]
  v_mfma_f32_16x16x32_bf16 a[32:35], a[228:231], a[144:147], a[32:35]
  ds_read_b64_tr_b16 a[242:243], v1 offset:4640
  v_mfma_f32_16x16x32_bf16 a[36:39], a[228:231], a[148:151], a[36:39]
  buffer_load_dwordx4 v6, s[44:47], s70 offen offset:1032 lds
  s_add_u32 m0, s74, 90368
  v_mfma_f32_16x16x32_bf16 a[36:39], a[224:227], a[132:135], a[36:39]
  ds_read_b64_tr_b16 a[244:245], v1 offset:23104
  v_mfma_f32_16x16x32_bf16 a[40:43], a[224:227], a[136:139], a[40:43]
  v_mfma_f32_16x16x32_bf16 a[40:43], a[228:231], a[152:155], a[40:43]
  ds_read_b64_tr_b16 a[246:247], v1 offset:27232
  v_mfma_f32_16x16x32_bf16 a[44:47], a[228:231], a[156:159], a[44:47]
  ds_read_b64_tr_b16 a[248:249], v1 offset:520
  v_mfma_f32_16x16x32_bf16 a[44:47], a[224:227], a[140:143], a[44:47]
  s_waitcnt lgkmcnt(5)
  v_mfma_f32_16x16x32_bf16 a[48:51], a[232:235], a[128:131], a[48:51]
  ds_read_b64_tr_b16 a[250:251], v1 offset:4648
  v_mfma_f32_16x16x32_bf16 a[48:51], a[236:239], a[144:147], a[48:51]
  buffer_load_dwordx4 v7, s[36:39], s69 offen offset:0 lds
  v_mfma_f32_16x16x32_bf16 a[52:55], a[236:239], a[148:151], a[52:55]
  ds_read_b64_tr_b16 a[252:253], v1 offset:23112
  v_mfma_f32_16x16x32_bf16 a[52:55], a[232:235], a[132:135], a[52:55]
  v_mfma_f32_16x16x32_bf16 a[56:59], a[232:235], a[136:139], a[56:59]
  ds_read_b64_tr_b16 a[254:255], v1 offset:27240
  v_mfma_f32_16x16x32_bf16 a[56:59], a[236:239], a[152:155], a[56:59]
  v_mfma_f32_16x16x32_bf16 a[60:63], a[236:239], a[156:159], a[60:63]
  ds_read_b64_tr_b16 a[224:225], v1 offset:768
  v_mfma_f32_16x16x32_bf16 a[60:63], a[232:235], a[140:143], a[60:63]
  s_waitcnt lgkmcnt(5)
  v_mfma_f32_16x16x32_bf16 a[64:67], a[240:243], a[128:131], a[64:67]
  ds_read_b64_tr_b16 a[226:227], v1 offset:4896
  v_mfma_f32_16x16x32_bf16 a[64:67], a[244:247], a[144:147], a[64:67]
  s_mov_b64 exec, s[86:87]
  buffer_load_dwordx4 v8, s[36:39], s69 offen offset:1024 lds
  s_mov_b64 exec, -1
  v_mfma_f32_16x16x32_bf16 a[68:71], a[244:247], a[148:151], a[68:71]
  s_add_u32 m0, s74, 97536
  ds_read_b64_tr_b16 a[228:229], v1 offset:23360
  v_mfma_f32_16x16x32_bf16 a[68:71], a[240:243], a[132:135], a[68:71]
  v_mfma_f32_16x16x32_bf16 a[72:75], a[240:243], a[136:139], a[72:75]
  ds_read_b64_tr_b16 a[230:231], v1 offset:27488
  v_mfma_f32_16x16x32_bf16 a[72:75], a[244:247], a[152:155], a[72:75]
  v_mfma_f32_16x16x32_bf16 a[76:79], a[244:247], a[156:159], a[76:79]
  ds_read_b64_tr_b16 a[232:233], v1 offset:776
  v_mfma_f32_16x16x32_bf16 a[76:79], a[240:243], a[140:143], a[76:79]
  s_waitcnt lgkmcnt(5)
  v_mfma_f32_16x16x32_bf16 a[80:83], a[248:251], a[128:131], a[80:83]
  ds_read_b64_tr_b16 a[234:235], v1 offset:4904
  v_mfma_f32_16x16x32_bf16 a[80:83], a[252:255], a[144:147], a[80:83]
  buffer_load_dwordx4 v7, s[40:43], s69 offen offset:0 lds
  v_mfma_f32_16x16x32_bf16 a[84:87], a[252:255], a[148:151], a[84:87]
  ds_read_b64_tr_b16 a[236:237], v1 offset:23368
  v_mfma_f32_16x16x32_bf16 a[84:87], a[248:251], a[132:135], a[84:87]
  ds_read_b64_tr_b16 a[238:239], v1 offset:27496
  v_mfma_f32_16x16x32_bf16 a[88:91], a[248:251], a[136:139], a[88:91]
  v_mfma_f32_16x16x32_bf16 a[88:91], a[252:255], a[152:155], a[88:91]
  ds_read_b64_tr_b16 a[240:241], v3 offset:0
  v_mfma_f32_16x16x32_bf16 a[92:95], a[252:255], a[156:159], a[92:95]
  s_mov_b64 exec, s[86:87]
  buffer_load_dwordx4 v8, s[40:43], s69 offen offset:1024 lds
  s_mov_b64 exec, -1
  v_mfma_f32_16x16x32_bf16 a[92:95], a[248:251], a[140:143], a[92:95]
  s_waitcnt lgkmcnt(5)
  ds_read_b64_tr_b16 a[242:243], v3 offset:3584
  v_mfma_f32_16x16x32_bf16 a[96:99], a[224:227], a[128:131], a[96:99]
  v_mfma_f32_16x16x32_bf16 a[96:99], a[228:231], a[144:147], a[96:99]
  ds_read_b64_tr_b16 a[244:245], v3 offset:22592
  v_mfma_f32_16x16x32_bf16 a[100:103], a[228:231], a[148:151], a[100:103]
  v_mfma_f32_16x16x32_bf16 a[100:103], a[224:227], a[132:135], a[100:103]
  ds_read_b64_tr_b16 a[246:247], v3 offset:26176
  v_mfma_f32_16x16x32_bf16 a[104:107], a[224:227], a[136:139], a[104:107]
  v_mfma_f32_16x16x32_bf16 a[104:107], a[228:231], a[152:155], a[104:107]
  ds_read_b64_tr_b16 a[248:249], v3 offset:7168
  v_mfma_f32_16x16x32_bf16 a[108:111], a[228:231], a[156:159], a[108:111]
  buffer_load_dwordx4 a[192:195], v9, s[48:51], s71 offen offset:0 sc1
  v_mfma_f32_16x16x32_bf16 a[108:111], a[224:227], a[140:143], a[108:111]
  s_waitcnt lgkmcnt(5)
  ds_read_b64_tr_b16 a[250:251], v3 offset:10752
  v_mfma_f32_16x16x32_bf16 a[112:115], a[232:235], a[128:131], a[112:115]
  v_mfma_f32_16x16x32_bf16 a[112:115], a[236:239], a[144:147], a[112:115]
  ds_read_b64_tr_b16 a[252:253], v3 offset:29760
  v_mfma_f32_16x16x32_bf16 a[116:119], a[236:239], a[148:151], a[116:119]
  v_mfma_f32_16x16x32_bf16 a[116:119], a[232:235], a[132:135], a[116:119]
  ds_read_b64_tr_b16 a[254:255], v3 offset:33344
  v_mfma_f32_16x16x32_bf16 a[120:123], a[232:235], a[136:139], a[120:123]
  buffer_load_dwordx4 a[196:199], v9, s[48:51], s71 offen offset:1024 sc1
  v_mfma_f32_16x16x32_bf16 a[120:123], a[236:239], a[152:155], a[120:123]
  ds_read_b64_tr_b16 a[224:225], v3 offset:32
  v_mfma_f32_16x16x32_bf16 a[124:127], a[236:239], a[156:159], a[124:127]
  v_mfma_f32_16x16x32_bf16 a[124:127], a[232:235], a[140:143], a[124:127]
  s_waitcnt lgkmcnt(5)
  ds_read_b64_tr_b16 a[226:227], v3 offset:3616
  v_mfma_f32_16x16x32_bf16 v[32:35], a[240:243], a[128:131], v[32:35]
  ds_read_b64_tr_b16 a[228:229], v3 offset:22624
  v_mfma_f32_16x16x32_bf16 v[32:35], a[244:247], a[144:147], v[32:35]
  v_mfma_f32_16x16x32_bf16 v[36:39], a[244:247], a[148:151], v[36:39]
  ds_read_b64_tr_b16 a[230:231], v3 offset:26208
  v_mfma_f32_16x16x32_bf16 v[36:39], a[240:243], a[132:135], v[36:39]
  v_mfma_f32_16x16x32_bf16 v[40:43], a[240:243], a[136:139], v[40:43]
  ds_read_b64_tr_b16 a[232:233], v3 offset:7200
  v_mfma_f32_16x16x32_bf16 v[40:43], a[244:247], a[152:155], v[40:43]
  buffer_load_dwordx4 a[200:203], v9, s[48:51], s71 offen offset:2048 sc1
  v_mfma_f32_16x16x32_bf16 v[44:47], a[244:247], a[156:159], v[44:47]
  ds_read_b64_tr_b16 a[234:235], v3 offset:10784
  v_mfma_f32_16x16x32_bf16 v[44:47], a[240:243], a[140:143], v[44:47]
  s_waitcnt lgkmcnt(6)
  v_mfma_f32_16x16x32_bf16 v[48:51], a[248:251], a[128:131], v[48:51]
  ds_read_b64_tr_b16 a[236:237], v3 offset:29792
  v_mfma_f32_16x16x32_bf16 v[48:51], a[252:255], a[144:147], v[48:51]
  v_mfma_f32_16x16x32_bf16 v[52:55], a[252:255], a[148:151], v[52:55]
  ds_read_b64_tr_b16 a[238:239], v3 offset:33376
  v_mfma_f32_16x16x32_bf16 v[52:55], a[248:251], a[132:135], v[52:55]
  buffer_load_dwordx4 a[204:207], v9, s[48:51], s71 offen offset:3072 sc1
  v_mfma_f32_16x16x32_bf16 v[56:59], a[248:251], a[136:139], v[56:59]
  s_add_u32 s72, s68, s12
  ds_read_b64_tr_b16 a[240:241], v3 offset:64
  v_mfma_f32_16x16x32_bf16 v[56:59], a[252:255], a[152:155], v[56:59]
  s_and_b32 s72, s72, 7
  v_mfma_f32_16x16x32_bf16 v[60:63], a[252:255], a[156:159], v[60:63]
  s_and_b32 s73, s68, 0xfffffff8
  ds_read_b64_tr_b16 a[242:243], v3 offset:3648
  v_mfma_f32_16x16x32_bf16 v[60:63], a[248:251], a[140:143], v[60:63]
  s_waitcnt lgkmcnt(6)
  s_or_b32 s72, s72, s73
  v_mfma_f32_16x16x32_bf16 v[64:67], a[224:227], a[128:131], v[64:67]
  s_mul_i32 s69, s72, 229376
  ds_read_b64_tr_b16 a[244:245], v3 offset:22656
  v_mfma_f32_16x16x32_bf16 v[64:67], a[228:231], a[144:147], v[64:67]
  s_lshl_b32 s70, s72, 18
  v_mfma_f32_16x16x32_bf16 v[68:71], a[228:231], a[148:151], v[68:71]
  s_lshl_b32 s71, s72, 12
  ds_read_b64_tr_b16 a[246:247], v3 offset:26240
  v_mfma_f32_16x16x32_bf16 v[68:71], a[224:227], a[132:135], v[68:71]
  s_add_u32 s68, s68, 1
  v_mfma_f32_16x16x32_bf16 v[72:75], a[224:227], a[136:139], v[72:75]
  s_add_u32 m0, s75, 112960
  ds_read_b64_tr_b16 a[248:249], v3 offset:7232
  v_mfma_f32_16x16x32_bf16 v[72:75], a[228:231], a[152:155], v[72:75]
  buffer_load_dwordx4 v5, s[44:47], s70 offen offset:0 lds
  v_mfma_f32_16x16x32_bf16 v[76:79], a[228:231], a[156:159], v[76:79]
  ds_read_b64_tr_b16 a[250:251], v3 offset:10816
  v_mfma_f32_16x16x32_bf16 v[76:79], a[224:227], a[140:143], v[76:79]
  s_waitcnt lgkmcnt(6)
  ds_read_b64_tr_b16 a[252:253], v3 offset:29824
  v_mfma_f32_16x16x32_bf16 v[80:83], a[232:235], a[128:131], v[80:83]
  v_mfma_f32_16x16x32_bf16 v[80:83], a[236:239], a[144:147], v[80:83]
  ds_read_b64_tr_b16 a[254:255], v3 offset:33408
  v_mfma_f32_16x16x32_bf16 v[84:87], a[236:239], a[148:151], v[84:87]
  buffer_load_dwordx4 v6, s[44:47], s70 offen offset:1032 lds
  s_add_u32 m0, s74, 112960
  v_mfma_f32_16x16x32_bf16 v[84:87], a[232:235], a[132:135], v[84:87]
  ds_read_b64_tr_b16 a[224:225], v3 offset:96
  v_mfma_f32_16x16x32_bf16 v[88:91], a[232:235], a[136:139], v[88:91]
  v_mfma_f32_16x16x32_bf16 v[88:91], a[236:239], a[152:155], v[88:91]
  ds_read_b64_tr_b16 a[226:227], v3 offset:3680
  v_mfma_f32_16x16x32_bf16 v[92:95], a[236:239], a[156:159], v[92:95]
  v_mfma_f32_16x16x32_bf16 v[92:95], a[232:235], a[140:143], v[92:95]
  s_waitcnt lgkmcnt(6)
  ds_read_b64_tr_b16 a[228:229], v3 offset:22688
  v_mfma_f32_16x16x32_bf16 v[96:99], a[240:243], a[128:131], v[96:99]
  v_mfma_f32_16x16x32_bf16 v[96:99], a[244:247], a[144:147], v[96:99]
  ds_read_b64_tr_b16 a[230:231], v3 offset:26272
  v_mfma_f32_16x16x32_bf16 v[100:103], a[244:247], a[148:151], v[100:103]
  buffer_load_dwordx4 v7, s[36:39], s69 offen offset:0 lds
  v_mfma_f32_16x16x32_bf16 v[100:103], a[240:243], a[132:135], v[100:103]
  ds_read_b64_tr_b16 a[232:233], v3 offset:7264
  v_mfma_f32_16x16x32_bf16 v[104:107], a[240:243], a[136:139], v[104:107]
  v_mfma_f32_16x16x32_bf16 v[104:107], a[244:247], a[152:155], v[104:107]
  ds_read_b64_tr_b16 a[234:235], v3 offset:10848
  v_mfma_f32_16x16x32_bf16 v[108:111], a[244:247], a[156:159], v[108:111]
  v_mfma_f32_16x16x32_bf16 v[108:111], a[240:243], a[140:143], v[108:111]
  s_waitcnt lgkmcnt(6)
  ds_read_b64_tr_b16 a[236:237], v3 offset:29856
  v_mfma_f32_16x16x32_bf16 v[112:115], a[248:251], a[128:131], v[112:115]
  s_mov_b64 exec, s[86:87]
  buffer_load_dwordx4 v8, s[36:39], s69 offen offset:1024 lds
  s_mov_b64 exec, -1
  v_mfma_f32_16x16x32_bf16 v[112:115], a[252:255], a[144:147], v[112:115]
  s_add_u32 m0, s74, 120128
  ds_read_b64_tr_b16 a[238:239], v3 offset:33440
  v_mfma_f32_16x16x32_bf16 v[116:119], a[252:255], a[148:151], v[116:119]
  v_mfma_f32_16x16x32_bf16 v[116:119], a[248:251], a[132:135], v[116:119]
  ds_read_b64_tr_b16 a[240:241], v3 offset:128
  v_mfma_f32_16x16x32_bf16 v[120:123], a[248:251], a[136:139], v[120:123]
  ds_read_b64_tr_b16 a[242:243], v3 offset:3712
  v_mfma_f32_16x16x32_bf16 v[120:123], a[252:255], a[152:155], v[120:123]
  v_mfma_f32_16x16x32_bf16 v[124:127], a[252:255], a[156:159], v[124:127]
  ds_read_b64_tr_b16 a[244:245], v3 offset:22720
  v_mfma_f32_16x16x32_bf16 v[124:127], a[248:251], a[140:143], v[124:127]
  s_waitcnt lgkmcnt(7)
  v_mfma_f32_16x16x32_bf16 v[128:131], a[224:227], a[128:131], v[128:131]
  ds_read_b64_tr_b16 a[246:247], v3 offset:26304
  v_mfma_f32_16x16x32_bf16 v[128:131], a[228:231], a[144:147], v[128:131]
  buffer_load_dwordx4 v7, s[40:43], s69 offen offset:0 lds
  v_mfma_f32_16x16x32_bf16 v[132:135], a[228:231], a[148:151], v[132:135]
  ds_read_b64_tr_b16 a[248:249], v3 offset:7296
  v_mfma_f32_16x16x32_bf16 v[132:135], a[224:227], a[132:135], v[132:135]
  v_mfma_f32_16x16x32_bf16 v[136:139], a[224:227], a[136:139], v[136:139]
  ds_read_b64_tr_b16 a[250:251], v3 offset:10880
  v_mfma_f32_16x16x32_bf16 v[136:139], a[228:231], a[152:155], v[136:139]
  v_mfma_f32_16x16x32_bf16 v[140:143], a[228:231], a[156:159], v[140:143]
  ds_read_b64_tr_b16 a[252:253], v3 offset:29888
  v_mfma_f32_16x16x32_bf16 v[140:143], a[224:227], a[140:143], v[140:143]
  s_waitcnt lgkmcnt(7)
  v_mfma_f32_16x16x32_bf16 v[144:147], a[232:235], a[128:131], v[144:147]
  ds_read_b64_tr_b16 a[254:255], v3 offset:33472
  v_mfma_f32_16x16x32_bf16 v[144:147], a[236:239], a[144:147], v[144:147]
  s_mov_b64 exec, s[86:87]
  buffer_load_dwordx4 v8, s[40:43], s69 offen offset:1024 lds
  s_mov_b64 exec, -1
  v_mfma_f32_16x16x32_bf16 v[148:151], a[236:239], a[148:151], v[148:151]
  ds_read_b64_tr_b16 a[224:225], v3 offset:160
  v_mfma_f32_16x16x32_bf16 v[148:151], a[232:235], a[132:135], v[148:151]
  v_mfma_f32_16x16x32_bf16 v[152:155], a[232:235], a[136:139], v[152:155]
  ds_read_b64_tr_b16 a[226:227], v3 offset:3744
  v_mfma_f32_16x16x32_bf16 v[152:155], a[236:239], a[152:155], v[152:155]
  v_mfma_f32_16x16x32_bf16 v[156:159], a[236:239], a[156:159], v[156:159]
  ds_read_b64_tr_b16 a[228:229], v3 offset:22752
  buffer_load_dwordx4 a[208:211], v9, s[48:51], s71 offen offset:0 sc1
  v_mfma_f32_16x16x32_bf16 v[156:159], a[232:235], a[140:143], v[156:159]
  s_waitcnt lgkmcnt(7)
  v_mfma_f32_16x16x32_bf16 v[160:163], a[240:243], a[128:131], v[160:163]
  ds_read_b64_tr_b16 a[230:231], v3 offset:26336
  v_mfma_f32_16x16x32_bf16 v[160:163], a[244:247], a[144:147], v[160:163]
  ds_read_b64_tr_b16 a[232:233], v3 offset:7328
  v_mfma_f32_16x16x32_bf16 v[164:167], a[244:247], a[148:151], v[164:167]
  v_mfma_f32_16x16x32_bf16 v[164:167], a[240:243], a[132:135], v[164:167]
  ds_read_b64_tr_b16 a[234:235], v3 offset:10912
  v_mfma_f32_16x16x32_bf16 v[168:171], a[240:243], a[136:139], v[168:171]
  v_mfma_f32_16x16x32_bf16 v[168:171], a[244:247], a[152:155], v[168:171]
  ds_read_b64_tr_b16 a[236:237], v3 offset:29920
  v_mfma_f32_16x16x32_bf16 v[172:175], a[244:247], a[156:159], v[172:175]
  buffer_load_dwordx4 a[212:215], v9, s[48:51], s71 offen offset:1024 sc1
  v_mfma_f32_16x16x32_bf16 v[172:175], a[240:243], a[140:143], v[172:175]
  s_waitcnt lgkmcnt(7)
  ds_read_b64_tr_b16 a[238:239], v3 offset:33504
  v_mfma_f32_16x16x32_bf16 v[176:179], a[248:251], a[128:131], v[176:179]
  v_mfma_f32_16x16x32_bf16 v[176:179], a[252:255], a[144:147], v[176:179]
  ds_read_b64_tr_b16 a[240:241], v3 offset:192
  v_mfma_f32_16x16x32_bf16 v[180:183], a[252:255], a[148:151], v[180:183]
  v_mfma_f32_16x16x32_bf16 v[180:183], a[248:251], a[132:135], v[180:183]
  ds_read_b64_tr_b16 a[242:243], v3 offset:3776
  v_mfma_f32_16x16x32_bf16 v[184:187], a[248:251], a[136:139], v[184:187]
  buffer_load_dwordx4 a[216:219], v9, s[48:51], s71 offen offset:2048 sc1
  v_mfma_f32_16x16x32_bf16 v[184:187], a[252:255], a[152:155], v[184:187]
  ds_read_b64_tr_b16 a[244:245], v3 offset:22784
  v_mfma_f32_16x16x32_bf16 v[188:191], a[252:255], a[156:159], v[188:191]
  v_mfma_f32_16x16x32_bf16 v[188:191], a[248:251], a[140:143], v[188:191]
  s_waitcnt lgkmcnt(7)
  ds_read_b64_tr_b16 a[246:247], v3 offset:26368
  v_mfma_f32_16x16x32_bf16 v[192:195], a[224:227], a[128:131], v[192:195]
  v_mfma_f32_16x16x32_bf16 v[192:195], a[228:231], a[144:147], v[192:195]
  ds_read_b64_tr_b16 a[248:249], v3 offset:7360
  v_mfma_f32_16x16x32_bf16 v[196:199], a[228:231], a[148:151], v[196:199]
  v_mfma_f32_16x16x32_bf16 v[196:199], a[224:227], a[132:135], v[196:199]
  ds_read_b64_tr_b16 a[250:251], v3 offset:10944
  v_mfma_f32_16x16x32_bf16 v[200:203], a[224:227], a[136:139], v[200:203]
  buffer_load_dwordx4 a[220:223], v9, s[48:51], s71 offen offset:3072 sc1
  v_mfma_f32_16x16x32_bf16 v[200:203], a[228:231], a[152:155], v[200:203]
  ds_read_b64_tr_b16 a[252:253], v3 offset:29952
  v_mfma_f32_16x16x32_bf16 v[204:207], a[228:231], a[156:159], v[204:207]
  v_mfma_f32_16x16x32_bf16 v[204:207], a[224:227], a[140:143], v[204:207]
  s_waitcnt lgkmcnt(7)
  ds_read_b64_tr_b16 a[254:255], v3 offset:33536
  v_mfma_f32_16x16x32_bf16 v[208:211], a[232:235], a[128:131], v[208:211]
  v_mfma_f32_16x16x32_bf16 v[208:211], a[236:239], a[144:147], v[208:211]
  v_mfma_f32_16x16x32_bf16 v[212:215], a[236:239], a[148:151], v[212:215]
  v_mfma_f32_16x16x32_bf16 v[212:215], a[232:235], a[132:135], v[212:215]
  v_mfma_f32_16x16x32_bf16 v[216:219], a[232:235], a[136:139], v[216:219]
  v_mfma_f32_16x16x32_bf16 v[216:219], a[236:239], a[152:155], v[216:219]
  v_mfma_f32_16x16x32_bf16 v[220:223], a[236:239], a[156:159], v[220:223]
  v_mfma_f32_16x16x32_bf16 v[220:223], a[232:235], a[140:143], v[220:223]
  s_waitcnt lgkmcnt(4)
  s_waitcnt lgkmcnt(0)
  s_waitcnt vmcnt(21)
  s_barrier
  v_mfma_f32_16x16x32_bf16 v[224:227], a[240:243], a[128:131], v[224:227]
  ds_read_b64_tr_b16 a[224:225], v2 offset:0
  v_mfma_f32_16x16x32_bf16 v[224:227], a[244:247], a[144:147], v[224:227]
  v_mfma_f32_16x16x32_bf16 v[228:231], a[244:247], a[148:151], v[228:231]
  ds_read_b64_tr_b16 a[226:227], v2 offset:4128
  v_mfma_f32_16x16x32_bf16 v[228:231], a[240:243], a[132:135], v[228:231]
  v_mfma_f32_16x16x32_bf16 v[232:235], a[240:243], a[136:139], v[232:235]
  ds_read_b64_tr_b16 a[228:229], v2 offset:22592
  v_mfma_f32_16x16x32_bf16 v[232:235], a[244:247], a[152:155], v[232:235]
  v_mfma_f32_16x16x32_bf16 v[236:239], a[244:247], a[156:159], v[236:239]
  ds_read_b64_tr_b16 a[230:231], v2 offset:26720
  v_mfma_f32_16x16x32_bf16 v[236:239], a[240:243], a[140:143], v[236:239]
  s_waitcnt lgkmcnt(4)
  v_mfma_f32_16x16x32_bf16 v[240:243], a[248:251], a[128:131], v[240:243]
  ds_read_b64_tr_b16 a[232:233], v2 offset:8
  v_mfma_f32_16x16x32_bf16 v[240:243], a[252:255], a[144:147], v[240:243]
  v_mfma_f32_16x16x32_bf16 v[244:247], a[252:255], a[148:151], v[244:247]
  ds_read_b64_tr_b16 a[234:235], v2 offset:4136
  v_mfma_f32_16x16x32_bf16 v[244:247], a[248:251], a[132:135], v[244:247]
  v_mfma_f32_16x16x32_bf16 v[248:251], a[248:251], a[136:139], v[248:251]
  ds_read_b64_tr_b16 a[236:237], v2 offset:22600
  v_mfma_f32_16x16x32_bf16 v[248:251], a[252:255], a[152:155], v[248:251]
  v_mfma_f32_16x16x32_bf16 v[252:255], a[252:255], a[156:159], v[252:255]
  ds_read_b64_tr_b16 a[238:239], v2 offset:26728
  v_mfma_f32_16x16x32_bf16 v[252:255], a[248:251], a[140:143], v[252:255]
  s_waitcnt lgkmcnt(4)
LB_gloop:
  v_mfma_f32_16x16x32_bf16 a[0:3], a[224:227], a[160:163], a[0:3]
  s_add_u32 s72, s68, s12
  ds_read_b64_tr_b16 a[240:241], v2 offset:256
  v_mfma_f32_16x16x32_bf16 a[0:3], a[228:231], a[176:179], a[0:3]
  s_and_b32 s72, s72, 7
  ds_read_b64_tr_b16 a[242:243], v2 offset:4384
  v_mfma_f32_16x16x32_bf16 a[4:7], a[228:231], a[180:183], a[4:7]
  s_and_b32 s73, s68, 0xfffffff8
  v_mfma_f32_16x16x32_bf16 a[4:7], a[224:227], a[164:167], a[4:7]
  s_or_b32 s72, s72, s73
  ds_read_b64_tr_b16 a[244:245], v2 offset:22848
  v_mfma_f32_16x16x32_bf16 a[8:11], a[224:227], a[168:171], a[8:11]
  s_mul_i32 s69, s72, 229376
  v_mfma_f32_16x16x32_bf16 a[8:11], a[228:231], a[184:187], a[8:11]
  s_lshl_b32 s70, s72, 18
  ds_read_b64_tr_b16 a[246:247], v2 offset:26976
  v_mfma_f32_16x16x32_bf16 a[12:15], a[228:231], a[188:191], a[12:15]
  s_lshl_b32 s71, s72, 12
  v_mfma_f32_16x16x32_bf16 a[12:15], a[224:227], a[172:175], a[12:15]
  s_waitcnt lgkmcnt(4)
  s_add_u32 s68, s68, 1
  ds_read_b64_tr_b16 a[248:249], v2 offset:264
  v_mfma_f32_16x16x32_bf16 a[16:19], a[232:235], a[160:163], a[16:19]
  s_add_u32 m0, s75, 0
  v_mfma_f32_16x16x32_bf16 a[16:19], a[236:239], a[176:179], a[16:19]
  ds_read_b64_tr_b16 a[250:251], v2 offset:4392
  v_mfma_f32_16x16x32_bf16 a[20:23], a[236:239], a[180:183], a[20:23]
  buffer_load_dwordx4 v5, s[44:47], s70 offen offset:0 lds
  v_mfma_f32_16x16x32_bf16 a[20:23], a[232:235], a[164:167], a[20:23]
  ds_read_b64_tr_b16 a[252:253], v2 offset:22856
  v_mfma_f32_16x16x32_bf16 a[24:27], a[232:235], a[168:171], a[24:27]
  v_mfma_f32_16x16x32_bf16 a[24:27], a[236:239], a[184:187], a[24:27]
  ds_read_b64_tr_b16 a[254:255], v2 offset:26984
  v_mfma_f32_16x16x32_bf16 a[28:31], a[236:239], a[188:191], a[28:31]
  v_mfma_f32_16x16x32_bf16 a[28:31], a[232:235], a[172:175], a[28:31]
  s_waitcnt lgkmcnt(4)
  ds_read_b64_tr_b16 a[224:225], v2 offset:512
  v_mfma_f32_16x16x32_bf16 a[32:35], a[240:243], a[160:163], a[32:35]
  v_mfma_f32_16x16x32_bf16 a[32:35], a[244:247], a[176:179], a[32:35]
  ds_read_b64_tr_b16 a[226:227], v2 offset:4640
  v_mfma_f32_16x16x32_bf16 a[36:39], a[244:247], a[180:183], a[36:39]
  buffer_load_dwordx4 v6, s[44:47], s70 offen offset:1032 lds
  s_add_u32 m0, s74, 0
  v_mfma_f32_16x16x32_bf16 a[36:39], a[240:243], a[164:167], a[36:39]
  ds_read_b64_tr_b16 a[228:229], v2 offset:23104
  v_mfma_f32_16x16x32_bf16 a[40:43], a[240:243], a[168:171], a[40:43]
  v_mfma_f32_16x16x32_bf16 a[40:43], a[244:247], a[184:187], a[40:43]
  ds_read_b64_tr_b16 a[230:231], v2 offset:27232
  v_mfma_f32_16x16x32_bf16 a[44:47], a[244:247], a[188:191], a[44:47]
  ds_read_b64_tr_b16 a[232:233], v2 offset:520
  v_mfma_f32_16x16x32_bf16 a[44:47], a[240:243], a[172:175], a[44:47]
  s_waitcnt lgkmcnt(5)
  v_mfma_f32_16x16x32_bf16 a[48:51], a[248:251], a[160:163], a[48:51]
  ds_read_b64_tr_b16 a[234:235], v2 offset:4648
  v_mfma_f32_16x16x32_bf16 a[48:51], a[252:255], a[176:179], a[48:51]
  buffer_load_dwordx4 v7, s[36:39], s69 offen offset:0 lds
  v_mfma_f32_16x16x32_bf16 a[52:55], a[252:255], a[180:183], a[52:55]
  ds_read_b64_tr_b16 a[236:237], v2 offset:23112
  v_mfma_f32_16x16x32_bf16 a[52:55], a[248:251], a[164:167], a[52:55]
  v_mfma_f32_16x16x32_bf16 a[56:59], a[248:251], a[168:171], a[56:59]
  ds_read_b64_tr_b16 a[238:239], v2 offset:27240
  v_mfma_f32_16x16x32_bf16 a[56:59], a[252:255], a[184:187], a[56:59]
  v_mfma_f32_16x16x32_bf16 a[60:63], a[252:255], a[188:191], a[60:63]
  ds_read_b64_tr_b16 a[240:241], v2 offset:768
  v_mfma_f32_16x16x32_bf16 a[60:63], a[248:251], a[172:175], a[60:63]
  s_waitcnt lgkmcnt(5)
  v_mfma_f32_16x16x32_bf16 a[64:67], a[224:227], a[160:163], a[64:67]
  ds_read_b64_tr_b16 a[242:243], v2 offset:4896
  v_mfma_f32_16x16x32_bf16 a[64:67], a[228:231], a[176:179], a[64:67]
  s_mov_b64 exec, s[86:87]
  buffer_load_dwordx4 v8, s[36:39], s69 offen offset:1024 lds
  s_mov_b64 exec, -1
  v_mfma_f32_16x16x32_bf16 a[68:71], a[228:231], a[180:183], a[68:71]
  s_add_u32 m0, s74, 7168
  ds_read_b64_tr_b16 a[244:245], v2 offset:23360
  v_mfma_f32_16x16x32_bf16 a[68:71], a[224:227], a[164:167], a[68:71]
  v_mfma_f32_16x16x32_bf16 a[72:75], a[224:227], a[168:171], a[72:75]
  ds_read_b64_tr_b16 a[246:247], v2 offset:27488
  v_mfma_f32_16x16x32_bf16 a[72:75], a[228:231], a[184:187], a[72:75]
  v_mfma_f32_16x16x32_bf16 a[76:79], a[228:231], a[188:191], a[76:79]
  ds_read_b64_tr_b16 a[248:249], v2 offset:776
  v_mfma_f32_16x16x32_bf16 a[76:79], a[224:227], a[172:175], a[76:79]
  s_waitcnt lgkmcnt(5)
  v_mfma_f32_16x16x32_bf16 a[80:83], a[232:235], a[160:163], a[80:83]
  ds_read_b64_tr_b16 a[250:251], v2 offset:4904
  v_mfma_f32_16x16x32_bf16 a[80:83], a[236:239], a[176:179], a[80:83]
  buffer_load_dwordx4 v7, s[40:43], s69 offen offset:0 lds
  v_mfma_f32_16x16x32_bf16 a[84:87], a[236:239], a[180:183], a[84:87]
  ds_read_b64_tr_b16 a[252:253], v2 offset:23368
  v_mfma_f32_16x16x32_bf16 a[84:87], a[232:235], a[164:167], a[84:87]
  ds_read_b64_tr_b16 a[254:255], v2 offset:27496
  v_mfma_f32_16x16x32_bf16 a[88:91], a[232:235], a[168:171], a[88:91]
  v_mfma_f32_16x16x32_bf16 a[88:91], a[236:239], a[184:187], a[88:91]
  ds_read_b64_tr_b16 a[224:225], v4 offset:0
  v_mfma_f32_16x16x32_bf16 a[92:95], a[236:239], a[188:191], a[92:95]
  s_mov_b64 exec, s[86:87]
  buffer_load_dwordx4 v8, s[40:43], s69 offen offset:1024 lds
  s_mov_b64 exec, -1
  v_mfma_f32_16x16x32_bf16 a[92:95], a[232:235], a[172:175], a[92:95]
  s_waitcnt lgkmcnt(5)
  ds_read_b64_tr_b16 a[226:227], v4 offset:3584
  v_mfma_f32_16x16x32_bf16 a[96:99], a[240:243], a[160:163], a[96:99]
  v_mfma_f32_16x16x32_bf16 a[96:99], a[244:247], a[176:179], a[96:99]
  ds_read_b64_tr_b16 a[228:229], v4 offset:22592
  v_mfma_f32_16x16x32_bf16 a[100:103], a[244:247], a[180:183], a[100:103]
  v_mfma_f32_16x16x32_bf16 a[100:103], a[240:243], a[164:167], a[100:103]
  ds_read_b64_tr_b16 a[230:231], v4 offset:26176
  v_mfma_f32_16x16x32_bf16 a[104:107], a[240:243], a[168:171], a[104:107]
  v_mfma_f32_16x16x32_bf16 a[104:107], a[244:247], a[184:187], a[104:107]
  ds_read_b64_tr_b16 a[232:233], v4 offset:7168
  v_mfma_f32_16x16x32_bf16 a[108:111], a[244:247], a[188:191], a[108:111]
  buffer_load_dwordx4 a[128:131], v9, s[48:51], s71 offen offset:0 sc1
  v_mfma_f32_16x16x32_bf16 a[108:111], a[240:243], a[172:175], a[108:111]
  s_waitcnt lgkmcnt(5)
  ds_read_b64_tr_b16 a[234:235], v4 offset:10752
  v_mfma_f32_16x16x32_bf16 a[112:115], a[248:251], a[160:163], a[112:115]
  v_mfma_f32_16x16x32_bf16 a[112:115], a[252:255], a[176:179], a[112:115]
  ds_read_b64_tr_b16 a[236:237], v4 offset:29760
  v_mfma_f32_16x16x32_bf16 a[116:119], a[252:255], a[180:183], a[116:119]
  v_mfma_f32_16x16x32_bf16 a[116:119], a[248:251], a[164:167], a[116:119]
  ds_read_b64_tr_b16 a[238:239], v4 offset:33344
  v_mfma_f32_16x16x32_bf16 a[120:123], a[248:251], a[168:171], a[120:123]
  buffer_load_dwordx4 a[132:135], v9, s[48:51], s71 offen offset:1024 sc1
  v_mfma_f32_16x16x32_bf16 a[120:123], a[252:255], a[184:187], a[120:123]
  ds_read_b64_tr_b16 a[240:241], v4 offset:32
  v_mfma_f32_16x16x32_bf16 a[124:127], a[252:255], a[188:191], a[124:127]
  v_mfma_f32_16x16x32_bf16 a[124:127], a[248:251], a[172:175], a[124:127]
  s_waitcnt lgkmcnt(5)
  ds_read_b64_tr_b16 a[242:243], v4 offset:3616
  v_mfma_f32_16x16x32_bf16 v[32:35], a[224:227], a[160:163], v[32:35]
  ds_read_b64_tr_b16 a[244:245], v4 offset:22624
  v_mfma_f32_16x16x32_bf16 v[32:35], a[228:231], a[176:179], v[32:35]
  v_mfma_f32_16x16x32_bf16 v[36:39], a[228:231], a[180:183], v[36:39]
  ds_read_b64_tr_b16 a[246:247], v4 offset:26208
  v_mfma_f32_16x16x32_bf16 v[36:39], a[224:227], a[164:167], v[36:39]
  v_mfma_f32_16x16x32_bf16 v[40:43], a[224:227], a[168:171], v[40:43]
  ds_read_b64_tr_b16 a[248:249], v4 offset:7200
  v_mfma_f32_16x16x32_bf16 v[40:43], a[228:231], a[184:187], v[40:43]
  buffer_load_dwordx4 a[136:139], v9, s[48:51], s71 offen offset:2048 sc1
  v_mfma_f32_16x16x32_bf16 v[44:47], a[228:231], a[188:191], v[44:47]
  ds_read_b64_tr_b16 a[250:251], v4 offset:10784
  v_mfma_f32_16x16x32_bf16 v[44:47], a[224:227], a[172:175], v[44:47]
  s_waitcnt lgkmcnt(6)
  v_mfma_f32_16x16x32_bf16 v[48:51], a[232:235], a[160:163], v[48:51]
  ds_read_b64_tr_b16 a[252:253], v4 offset:29792
  v_mfma_f32_16x16x32_bf16 v[48:51], a[236:239], a[176:179], v[48:51]
  v_mfma_f32_16x16x32_bf16 v[52:55], a[236:239], a[180:183], v[52:55]
  ds_read_b64_tr_b16 a[254:255], v4 offset:33376
  v_mfma_f32_16x16x32_bf16 v[52:55], a[232:235], a[164:167], v[52:55]
  buffer_load_dwordx4 a[140:143], v9, s[48:51], s71 offen offset:3072 sc1
  v_mfma_f32_16x16x32_bf16 v[56:59], a[232:235], a[168:171], v[56:59]
  s_add_u32 s72, s68, s12
  ds_read_b64_tr_b16 a[224:225], v4 offset:64
  v_mfma_f32_16x16x32_bf16 v[56:59], a[236:239], a[184:187], v[56:59]
  s_and_b32 s72, s72, 7
  v_mfma_f32_16x16x32_bf16 v[60:63], a[236:239], a[188:191], v[60:63]
  s_and_b32 s73, s68, 0xfffffff8
  ds_read_b64_tr_b16 a[226:227], v4 offset:3648
  v_mfma_f32_16x16x32_bf16 v[60:63], a[232:235], a[172:175], v[60:63]
  s_waitcnt lgkmcnt(6)
  s_or_b32 s72, s72, s73
  v_mfma_f32_16x16x32_bf16 v[64:67], a[240:243], a[160:163], v[64:67]
  s_mul_i32 s69, s72, 229376
  ds_read_b64_tr_b16 a[228:229], v4 offset:22656
  v_mfma_f32_16x16x32_bf16 v[64:67], a[244:247], a[176:179], v[64:67]
  s_lshl_b32 s70, s72, 18
  v_mfma_f32_16x16x32_bf16 v[68:71], a[244:247], a[180:183], v[68:71]
  s_lshl_b32 s71, s72, 12
  ds_read_b64_tr_b16 a[230:231], v4 offset:26240
  v_mfma_f32_16x16x32_bf16 v[68:71], a[240:243], a[164:167], v[68:71]
  s_add_u32 s68, s68, 1
  v_mfma_f32_16x16x32_bf16 v[72:75], a[240:243], a[168:171], v[72:75]
  s_add_u32 m0, s75, 22592
  ds_read_b64_tr_b16 a[232:233], v4 offset:7232
  v_mfma_f32_16x16x32_bf16 v[72:75], a[244:247], a[184:187], v[72:75]
  buffer_load_dwordx4 v5, s[44:47], s70 offen offset:0 lds
  v_mfma_f32_16x16x32_bf16 v[76:79], a[244:247], a[188:191], v[76:79]
  ds_read_b64_tr_b16 a[234:235], v4 offset:10816
  v_mfma_f32_16x16x32_bf16 v[76:79], a[240:243], a[172:175], v[76:79]
  s_waitcnt lgkmcnt(6)
  ds_read_b64_tr_b16 a[236:237], v4 offset:29824
  v_mfma_f32_16x16x32_bf16 v[80:83], a[248:251], a[160:163], v[80:83]
  v_mfma_f32_16x16x32_bf16 v[80:83], a[252:255], a[176:179], v[80:83]
  ds_read_b64_tr_b16 a[238:239], v4 offset:33408
  v_mfma_f32_16x16x32_bf16 v[84:87], a[252:255], a[180:183], v[84:87]
  buffer_load_dwordx4 v6, s[44:47], s70 offen offset:1032 lds
  s_add_u32 m0, s74, 22592
  v_mfma_f32_16x16x32_bf16 v[84:87], a[248:251], a[164:167], v[84:87]
  ds_read_b64_tr_b16 a[240:241], v4 offset:96
  v_mfma_f32_16x16x32_bf16 v[88:91], a[248:251], a[168:171], v[88:91]
  v_mfma_f32_16x16x32_bf16 v[88:91], a[252:255], a[184:187], v[88:91]
  ds_read_b64_tr_b16 a[242:243], v4 offset:3680
  v_mfma_f32_16x16x32_bf16 v[92:95], a[252:255], a[188:191], v[92:95]
  v_mfma_f32_16x16x32_bf16 v[92:95], a[248:251], a[172:175], v[92:95]
  s_waitcnt lgkmcnt(6)
  ds_read_b64_tr_b16 a[244:245], v4 offset:22688
  v_mfma_f32_16x16x32_bf16 v[96:99], a[224:227], a[160:163], v[96:99]
  v_mfma_f32_16x16x32_bf16 v[96:99], a[228:231], a[176:179], v[96:99]
  ds_read_b64_tr_b16 a[246:247], v4 offset:26272
  v_mfma_f32_16x16x32_bf16 v[100:103], a[228:231], a[180:183], v[100:103]
  buffer_load_dwordx4 v7, s[36:39], s69 offen offset:0 lds
  v_mfma_f32_16x16x32_bf16 v[100:103], a[224:227], a[164:167], v[100:103]
  ds_read_b64_tr_b16 a[248:249], v4 offset:7264
  v_mfma_f32_16x16x32_bf16 v[104:107], a[224:227], a[168:171], v[104:107]
  v_mfma_f32_16x16x32_bf16 v[104:107], a[228:231], a[184:187], v[104:107]
  ds_read_b64_tr_b16 a[250:251], v4 offset:10848
  v_mfma_f32_16x16x32_bf16 v[108:111], a[228:231], a[188:191], v[108:111]
  v_mfma_f32_16x16x32_bf16 v[108:111], a[224:227], a[172:175], v[108:111]
  s_waitcnt lgkmcnt(6)
  ds_read_b64_tr_b16 a[252:253], v4 offset:29856
  v_mfma_f32_16x16x32_bf16 v[112:115], a[232:235], a[160:163], v[112:115]
  s_mov_b64 exec, s[86:87]
  buffer_load_dwordx4 v8, s[36:39], s69 offen offset:1024 lds
  s_mov_b64 exec, -1
  v_mfma_f32_16x16x32_bf16 v[112:115], a[236:239], a[176:179], v[112:115]
  s_add_u32 m0, s74, 29760
  ds_read_b64_tr_b16 a[254:255], v4 offset:33440
  v_mfma_f32_16x16x32_bf16 v[116:119], a[236:239], a[180:183], v[116:119]
  v_mfma_f32_16x16x32_bf16 v[116:119], a[232:235], a[164:167], v[116:119]
  ds_read_b64_tr_b16 a[224:225], v4 offset:128
  v_mfma_f32_16x16x32_bf16 v[120:123], a[232:235], a[168:171], v[120:123]
  ds_read_b64_tr_b16 a[226:227], v4 offset:3712
  v_mfma_f32_16x16x32_bf16 v[120:123], a[236:239], a[184:187], v[120:123]
  v_mfma_f32_16x16x32_bf16 v[124:127], a[236:239], a[188:191], v[124:127]
  ds_read_b64_tr_b16 a[228:229], v4 offset:22720
  v_mfma_f32_16x16x32_bf16 v[124:127], a[232:235], a[172:175], v[124:127]
  s_waitcnt lgkmcnt(7)
  v_mfma_f32_16x16x32_bf16 v[128:131], a[240:243], a[160:163], v[128:131]
  ds_read_b64_tr_b16 a[230:231], v4 offset:26304
  v_mfma_f32_16x16x32_bf16 v[128:131], a[244:247], a[176:179], v[128:131]
  buffer_load_dwordx4 v7, s[40:43], s69 offen offset:0 lds
  v_mfma_f32_16x16x32_bf16 v[132:135], a[244:247], a[180:183], v[132:135]
  ds_read_b64_tr_b16 a[232:233], v4 offset:7296
  v_mfma_f32_16x16x32_bf16 v[132:135], a[240:243], a[164:167], v[132:135]
  v_mfma_f32_16x16x32_bf16 v[136:139], a[240:243], a[168:171], v[136:139]
  ds_read_b64_tr_b16 a[234:235], v4 offset:10880
  v_mfma_f32_16x16x32_bf16 v[136:139], a[244:247], a[184:187], v[136:139]
  v_mfma_f32_16x16x32_bf16 v[140:143], a[244:247], a[188:191], v[140:143]
  ds_read_b64_tr_b16 a[236:237], v4 offset:29888
  v_mfma_f32_16x16x32_bf16 v[140:143], a[240:243], a[172:175], v[140:143]
  s_waitcnt lgkmcnt(7)
  v_mfma_f32_16x16x32_bf16 v[144:147], a[248:251], a[160:163], v[144:147]
  ds_read_b64_tr_b16 a[238:239], v4 offset:33472
  v_mfma_f32_16x16x32_bf16 v[144:147], a[252:255], a[176:179], v[144:147]
  s_mov_b64 exec, s[86:87]
  buffer_load_dwordx4 v8, s[40:43], s69 offen offset:1024 lds
  s_mov_b64 exec, -1
  v_mfma_f32_16x16x32_bf16 v[148:151], a[252:255], a[180:183], v[148:151]
  ds_read_b64_tr_b16 a[240:241], v4 offset:160
  v_mfma_f32_16x16x32_bf16 v[148:151], a[248:251], a[164:167], v[148:151]
  v_mfma_f32_16x16x32_bf16 v[152:155], a[248:251], a[168:171], v[152:155]
  ds_read_b64_tr_b16 a[242:243], v4 offset:3744
  v_mfma_f32_16x16x32_bf16 v[152:155], a[252:255], a[184:187], v[152:155]
  v_mfma_f32_16x16x32_bf16 v[156:159], a[252:255], a[188:191], v[156:159]
  ds_read_b64_tr_b16 a[244:245], v4 offset:22752
  buffer_load_dwordx4 a[144:147], v9, s[48:51], s71 offen offset:0 sc1
  v_mfma_f32_16x16x32_bf16 v[156:159], a[248:251], a[172:175], v[156:159]
  s_waitcnt lgkmcnt(7)
  v_mfma_f32_16x16x32_bf16 v[160:163], a[224:227], a[160:163], v[160:163]
  ds_read_b64_tr_b16 a[246:247], v4 offset:26336
  v_mfma_f32_16x16x32_bf16 v[160:163], a[228:231], a[176:179], v[160:163]
  ds_read_b64_tr_b16 a[248:249], v4 offset:7328
  v_mfma_f32_16x16x32_bf16 v[164:167], a[228:231], a[180:183], v[164:167]
  v_mfma_f32_16x16x32_bf16 v[164:167], a[224:227], a[164:167], v[164:167]
  ds_read_b64_tr_b16 a[250:251], v4 offset:10912
  v_mfma_f32_16x16x32_bf16 v[168:171], a[224:227], a[168:171], v[168:171]
  v_mfma_f32_16x16x32_bf16 v[168:171], a[228:231], a[184:187], v[168:171]
  ds_read_b64_tr_b16 a[252:253], v4 offset:29920
  v_mfma_f32_16x16x32_bf16 v[172:175], a[228:231], a[188:191], v[172:175]
  buffer_load_dwordx4 a[148:151], v9, s[48:51], s71 offen offset:1024 sc1
  v_mfma_f32_16x16x32_bf16 v[172:175], a[224:227], a[172:175], v[172:175]
  s_waitcnt lgkmcnt(7)
  ds_read_b64_tr_b16 a[254:255], v4 offset:33504
  v_mfma_f32_16x16x32_bf16 v[176:179], a[232:235], a[160:163], v[176:179]
  v_mfma_f32_16x16x32_bf16 v[176:179], a[236:239], a[176:179], v[176:179]
  ds_read_b64_tr_b16 a[224:225], v4 offset:192
  v_mfma_f32_16x16x32_bf16 v[180:183], a[236:239], a[180:183], v[180:183]
  v_mfma_f32_16x16x32_bf16 v[180:183], a[232:235], a[164:167], v[180:183]
  ds_read_b64_tr_b16 a[226:227], v4 offset:3776
  v_mfma_f32_16x16x32_bf16 v[184:187], a[232:235], a[168:171], v[184:187]
  buffer_load_dwordx4 a[152:155], v9, s[48:51], s71 offen offset:2048 sc1
  v_mfma_f32_16x16x32_bf16 v[184:187], a[236:239], a[184:187], v[184:187]
  ds_read_b64_tr_b16 a[228:229], v4 offset:22784
  v_mfma_f32_16x16x32_bf16 v[188:191], a[236:239], a[188:191], v[188:191]
  v_mfma_f32_16x16x32_bf16 v[188:191], a[232:235], a[172:175], v[188:191]
  s_waitcnt lgkmcnt(7)
  ds_read_b64_tr_b16 a[230:231], v4 offset:26368
  v_mfma_f32_16x16x32_bf16 v[192:195], a[240:243], a[160:163], v[192:195]
  v_mfma_f32_16x16x32_bf16 v[192:195], a[244:247], a[176:179], v[192:195]
  ds_read_b64_tr_b16 a[232:233], v4 offset:7360
  v_mfma_f32_16x16x32_bf16 v[196:199], a[244:247], a[180:183], v[196:199]
  v_mfma_f32_16x16x32_bf16 v[196:199], a[240:243], a[164:167], v[196:199]
  ds_read_b64_tr_b16 a[234:235], v4 offset:10944
  v_mfma_f32_16x16x32_bf16 v[200:203], a[240:243], a[168:171], v[200:203]
  buffer_load_dwordx4 a[156:159], v9, s[48:51], s71 offen offset:3072 sc1
  v_mfma_f32_16x16x32_bf16 v[200:203], a[244:247], a[184:187], v[200:203]
  ds_read_b64_tr_b16 a[236:237], v4 offset:29952
  v_mfma_f32_16x16x32_bf16 v[204:207], a[244:247], a[188:191], v[204:207]
  v_mfma_f32_16x16x32_bf16 v[204:207], a[240:243], a[172:175], v[204:207]
  s_waitcnt lgkmcnt(7)
  ds_read_b64_tr_b16 a[238:239], v4 offset:33536
  v_mfma_f32_16x16x32_bf16 v[208:211], a[248:251], a[160:163], v[208:211]
  v_mfma_f32_16x16x32_bf16 v[208:211], a[252:255], a[176:179], v[208:211]
  v_mfma_f32_16x16x32_bf16 v[212:215], a[252:255], a[180:183], v[212:215]
  v_mfma_f32_16x16x32_bf16 v[212:215], a[248:251], a[164:167], v[212:215]
  v_mfma_f32_16x16x32_bf16 v[216:219], a[248:251], a[168:171], v[216:219]
  v_mfma_f32_16x16x32_bf16 v[216:219], a[252:255], a[184:187], v[216:219]
  v_mfma_f32_16x16x32_bf16 v[220:223], a[252:255], a[188:191], v[220:223]
  v_mfma_f32_16x16x32_bf16 v[220:223], a[248:251], a[172:175], v[220:223]
  s_waitcnt lgkmcnt(4)
  s_waitcnt lgkmcnt(0)
  s_waitcnt vmcnt(20)
  s_barrier
  v_mfma_f32_16x16x32_bf16 v[224:227], a[224:227], a[160:163], v[224:227]
  ds_read_b64_tr_b16 a[240:241], v28 offset:0
  v_mfma_f32_16x16x32_bf16 v[224:227], a[228:231], a[176:179], v[224:227]
  v_mfma_f32_16x16x32_bf16 v[228:231], a[228:231], a[180:183], v[228:231]
  ds_read_b64_tr_b16 a[242:243], v28 offset:4128
  v_mfma_f32_16x16x32_bf16 v[228:231], a[224:227], a[164:167], v[228:231]
  v_mfma_f32_16x16x32_bf16 v[232:235], a[224:227], a[168:171], v[232:235]
  ds_read_b64_tr_b16 a[244:245], v28 offset:22592
  v_mfma_f32_16x16x32_bf16 v[232:235], a[228:231], a[184:187], v[232:235]
  v_mfma_f32_16x16x32_bf16 v[236:239], a[228:231], a[188:191], v[236:239]
  ds_read_b64_tr_b16 a[246:247], v28 offset:26720
  v_mfma_f32_16x16x32_bf16 v[236:239], a[224:227], a[172:175], v[236:239]
  s_waitcnt lgkmcnt(4)
  v_mfma_f32_16x16x32_bf16 v[240:243], a[232:235], a[160:163], v[240:243]
  ds_read_b64_tr_b16 a[248:249], v28 offset:8
  v_mfma_f32_16x16x32_bf16 v[240:243], a[236:239], a[176:179], v[240:243]
  v_mfma_f32_16x16x32_bf16 v[244:247], a[236:239], a[180:183], v[244:247]
  ds_read_b64_tr_b16 a[250:251], v28 offset:4136
  v_mfma_f32_16x16x32_bf16 v[244:247], a[232:235], a[164:167], v[244:247]
  v_mfma_f32_16x16x32_bf16 v[248:251], a[232:235], a[168:171], v[248:251]
  ds_read_b64_tr_b16 a[252:253], v28 offset:22600
  v_mfma_f32_16x16x32_bf16 v[248:251], a[236:239], a[184:187], v[248:251]
  v_mfma_f32_16x16x32_bf16 v[252:255], a[236:239], a[188:191], v[252:255]
  ds_read_b64_tr_b16 a[254:255], v28 offset:26728
  v_mfma_f32_16x16x32_bf16 v[252:255], a[232:235], a[172:175], v[252:255]
  s_waitcnt lgkmcnt(4)
  v_mfma_f32_16x16x32_bf16 a[0:3], a[240:243], a[192:195], a[0:3]
  s_add_u32 s72, s68, s12
  ds_read_b64_tr_b16 a[224:225], v28 offset:256
  v_mfma_f32_16x16x32_bf16 a[0:3], a[244:247], a[208:211], a[0:3]
  s_and_b32 s72, s72, 7
  ds_read_b64_tr_b16 a[226:227], v28 offset:4384
  v_mfma_f32_16x16x32_bf16 a[4:7], a[244:247], a[212:215], a[4:7]
  s_and_b32 s73, s68, 0xfffffff8
  v_mfma_f32_16x16x32_bf16 a[4:7], a[240:243], a[196:199], a[4:7]
  s_or_b32 s72, s72, s73
  ds_read_b64_tr_b16 a[228:229], v28 offset:22848
  v_mfma_f32_16x16x32_bf16 a[8:11], a[240:243], a[200:203], a[8:11]
  s_mul_i32 s69, s72, 229376
  v_mfma_f32_16x16x32_bf16 a[8:11], a[244:247], a[216:219], a[8:11]
  s_lshl_b32 s70, s72, 18
  ds_read_b64_tr_b16 a[230:231], v28 offset:26976
  buffer_load_dwordx4 v[24:27], v21, s[4:7], s3 offen
  v_mfma_f32_16x16x32_bf16 a[12:15], a[244:247], a[220:223], a[12:15]
  s_lshl_b32 s71, s72, 12
  v_mfma_f32_16x16x32_bf16 a[12:15], a[240:243], a[204:207], a[12:15]
  s_waitcnt lgkmcnt(4)
  s_add_u32 s68, s68, 1
  ds_read_b64_tr_b16 a[232:233], v28 offset:264
  v_mfma_f32_16x16x32_bf16 a[16:19], a[248:251], a[192:195], a[16:19]
  s_add_u32 m0, s75, 45184
  v_mfma_f32_16x16x32_bf16 a[16:19], a[252:255], a[208:211], a[16:19]
  ds_read_b64_tr_b16 a[234:235], v28 offset:4392
  v_mfma_f32_16x16x32_bf16 a[20:23], a[252:255], a[212:215], a[20:23]
  buffer_load_dwordx4 v5, s[44:47], s70 offen offset:0 lds
  v_mfma_f32_16x16x32_bf16 a[20:23], a[248:251], a[196:199], a[20:23]
  ds_read_b64_tr_b16 a[236:237], v28 offset:22856
  v_mfma_f32_16x16x32_bf16 a[24:27], a[248:251], a[200:203], a[24:27]
  v_mfma_f32_16x16x32_bf16 a[24:27], a[252:255], a[216:219], a[24:27]
  ds_read_b64_tr_b16 a[238:239], v28 offset:26984
  v_mfma_f32_16x16x32_bf16 a[28:31], a[252:255], a[220:223], a[28:31]
  v_mfma_f32_16x16x32_bf16 a[28:31], a[248:251], a[204:207], a[28:31]
  s_waitcnt lgkmcnt(4)
  ds_read_b64_tr_b16 a[240:241], v28 offset:512
  v_mfma_f32_16x16x32_bf16 a[32:35], a[224:227], a[192:195], a[32:35]
  v_mfma_f32_16x16x32_bf16 a[32:35], a[228:231], a[208:211], a[32:35]
  ds_read_b64_tr_b16 a[242:243], v28 offset:4640
  v_mfma_f32_16x16x32_bf16 a[36:39], a[228:231], a[212:215], a[36:39]
  buffer_load_dwordx4 v6, s[44:47], s70 offen offset:1032 lds
  s_add_u32 m0, s74, 45184
  v_mfma_f32_16x16x32_bf16 a[36:39], a[224:227], a[196:199], a[36:39]
  ds_read_b64_tr_b16 a[244:245], v28 offset:23104
  v_mfma_f32_16x16x32_bf16 a[40:43], a[224:227], a[200:203], a[40:43]
  v_mfma_f32_16x16x32_bf16 a[40:43], a[228:231], a[216:219], a[40:43]
  ds_read_b64_tr_b16 a[246:247], v28 offset:27232
  v_mfma_f32_16x16x32_bf16 a[44:47], a[228:231], a[220:223], a[44:47]
  ds_read_b64_tr_b16 a[248:249], v28 offset:520
  v_mfma_f32_16x16x32_bf16 a[44:47], a[224:227], a[204:207], a[44:47]
  s_waitcnt lgkmcnt(5)
  v_mfma_f32_16x16x32_bf16 a[48:51], a[232:235], a[192:195], a[48:51]
  ds_read_b64_tr_b16 a[250:251], v28 offset:4648
  v_mfma_f32_16x16x32_bf16 a[48:51], a[236:239], a[208:211], a[48:51]
  buffer_load_dwordx4 v7, s[36:39], s69 offen offset:0 lds
  v_mfma_f32_16x16x32_bf16 a[52:55], a[236:239], a[212:215], a[52:55]
  ds_read_b64_tr_b16 a[252:253], v28 offset:23112
  v_mfma_f32_16x16x32_bf16 a[52:55], a[232:235], a[196:199], a[52:55]
  v_mfma_f32_16x16x32_bf16 a[56:59], a[232:235], a[200:203], a[56:59]
  ds_read_b64_tr_b16 a[254:255], v28 offset:27240
  v_mfma_f32_16x16x32_bf16 a[56:59], a[236:239], a[216:219], a[56:59]
  v_mfma_f32_16x16x32_bf16 a[60:63], a[236:239], a[220:223], a[60:63]
  ds_read_b64_tr_b16 a[224:225], v28 offset:768
  v_mfma_f32_16x16x32_bf16 a[60:63], a[232:235], a[204:207], a[60:63]
  s_waitcnt lgkmcnt(5)
  v_mfma_f32_16x16x32_bf16 a[64:67], a[240:243], a[192:195], a[64:67]
  ds_read_b64_tr_b16 a[226:227], v28 offset:4896
  v_mfma_f32_16x16x32_bf16 a[64:67], a[244:247], a[208:211], a[64:67]
  s_mov_b64 exec, s[86:87]
  buffer_load_dwordx4 v8, s[36:39], s69 offen offset:1024 lds
  s_mov_b64 exec, -1
  v_mfma_f32_16x16x32_bf16 a[68:71], a[244:247], a[212:215], a[68:71]
  s_add_u32 m0, s74, 52352
  ds_read_b64_tr_b16 a[228:229], v28 offset:23360
  v_mfma_f32_16x16x32_bf16 a[68:71], a[240:243], a[196:199], a[68:71]
  v_mfma_f32_16x16x32_bf16 a[72:75], a[240:243], a[200:203], a[72:75]
  ds_read_b64_tr_b16 a[230:231], v28 offset:27488
  v_mfma_f32_16x16x32_bf16 a[72:75], a[244:247], a[216:219], a[72:75]
  v_mfma_f32_16x16x32_bf16 a[76:79], a[244:247], a[220:223], a[76:79]
  ds_read_b64_tr_b16 a[232:233], v28 offset:776
  v_mfma_f32_16x16x32_bf16 a[76:79], a[240:243], a[204:207], a[76:79]
  s_waitcnt lgkmcnt(5)
  v_mfma_f32_16x16x32_bf16 a[80:83], a[248:251], a[192:195], a[80:83]
  ds_read_b64_tr_b16 a[234:235], v28 offset:4904
  v_mfma_f32_16x16x32_bf16 a[80:83], a[252:255], a[208:211], a[80:83]
  buffer_load_dwordx4 v7, s[40:43], s69 offen offset:0 lds
  v_mfma_f32_16x16x32_bf16 a[84:87], a[252:255], a[212:215], a[84:87]
  ds_read_b64_tr_b16 a[236:237], v28 offset:23368
  v_mfma_f32_16x16x32_bf16 a[84:87], a[248:251], a[196:199], a[84:87]
  ds_read_b64_tr_b16 a[238:239], v28 offset:27496
  v_mfma_f32_16x16x32_bf16 a[88:91], a[248:251], a[200:203], a[88:91]
  v_mfma_f32_16x16x32_bf16 a[88:91], a[252:255], a[216:219], a[88:91]
  ds_read_b64_tr_b16 a[240:241], v29 offset:0
  v_mfma_f32_16x16x32_bf16 a[92:95], a[252:255], a[220:223], a[92:95]
  s_mov_b64 exec, s[86:87]
  buffer_load_dwordx4 v8, s[40:43], s69 offen offset:1024 lds
  s_mov_b64 exec, -1
  v_mfma_f32_16x16x32_bf16 a[92:95], a[248:251], a[204:207], a[92:95]
  s_waitcnt lgkmcnt(5)
  ds_read_b64_tr_b16 a[242:243], v29 offset:3584
  v_mfma_f32_16x16x32_bf16 a[96:99], a[224:227], a[192:195], a[96:99]
  v_mfma_f32_16x16x32_bf16 a[96:99], a[228:231], a[208:211], a[96:99]
  ds_read_b64_tr_b16 a[244:245], v29 offset:22592
  v_mfma_f32_16x16x32_bf16 a[100:103], a[228:231], a[212:215], a[100:103]
  v_mfma_f32_16x16x32_bf16 a[100:103], a[224:227], a[196:199], a[100:103]
  ds_read_b64_tr_b16 a[246:247], v29 offset:26176
  v_mfma_f32_16x16x32_bf16 a[104:107], a[224:227], a[200:203], a[104:107]
  v_mfma_f32_16x16x32_bf16 a[104:107], a[228:231], a[216:219], a[104:107]
  ds_read_b64_tr_b16 a[248:249], v29 offset:7168
  v_mfma_f32_16x16x32_bf16 a[108:111], a[228:231], a[220:223], a[108:111]
  buffer_load_dwordx4 a[160:163], v9, s[48:51], s71 offen offset:0 sc1
  v_mfma_f32_16x16x32_bf16 a[108:111], a[224:227], a[204:207], a[108:111]
  s_waitcnt lgkmcnt(5)
  ds_read_b64_tr_b16 a[250:251], v29 offset:10752
  v_mfma_f32_16x16x32_bf16 a[112:115], a[232:235], a[192:195], a[112:115]
  v_mfma_f32_16x16x32_bf16 a[112:115], a[236:239], a[208:211], a[112:115]
  ds_read_b64_tr_b16 a[252:253], v29 offset:29760
  v_mfma_f32_16x16x32_bf16 a[116:119], a[236:239], a[212:215], a[116:119]
  v_mfma_f32_16x16x32_bf16 a[116:119], a[232:235], a[196:199], a[116:119]
  ds_read_b64_tr_b16 a[254:255], v29 offset:33344
  v_mfma_f32_16x16x32_bf16 a[120:123], a[232:235], a[200:203], a[120:123]
  buffer_load_dwordx4 a[164:167], v9, s[48:51], s71 offen offset:1024 sc1
  v_mfma_f32_16x16x32_bf16 a[120:123], a[236:239], a[216:219], a[120:123]
  ds_read_b64_tr_b16 a[224:225], v29 offset:32
  v_mfma_f32_16x16x32_bf16 a[124:127], a[236:239], a[220:223], a[124:127]
  v_mfma_f32_16x16x32_bf16 a[124:127], a[232:235], a[204:207], a[124:127]
  s_waitcnt lgkmcnt(5)
  ds_read_b64_tr_b16 a[226:227], v29 offset:3616
  v_mfma_f32_16x16x32_bf16 v[32:35], a[240:243], a[192:195], v[32:35]
  ds_read_b64_tr_b16 a[228:229], v29 offset:22624
  v_mfma_f32_16x16x32_bf16 v[32:35], a[244:247], a[208:211], v[32:35]
  v_mfma_f32_16x16x32_bf16 v[36:39], a[244:247], a[212:215], v[36:39]
  ds_read_b64_tr_b16 a[230:231], v29 offset:26208
  v_mfma_f32_16x16x32_bf16 v[36:39], a[240:243], a[196:199], v[36:39]
  v_mfma_f32_16x16x32_bf16 v[40:43], a[240:243], a[200:203], v[40:43]
  ds_read_b64_tr_b16 a[232:233], v29 offset:7200
  v_mfma_f32_16x16x32_bf16 v[40:43], a[244:247], a[216:219], v[40:43]
  buffer_load_dwordx4 a[168:171], v9, s[48:51], s71 offen offset:2048 sc1
  v_mfma_f32_16x16x32_bf16 v[44:47], a[244:247], a[220:223], v[44:47]
  ds_read_b64_tr_b16 a[234:235], v29 offset:10784
  v_mfma_f32_16x16x32_bf16 v[44:47], a[240:243], a[204:207], v[44:47]
  s_waitcnt lgkmcnt(6)
  v_mfma_f32_16x16x32_bf16 v[48:51], a[248:251], a[192:195], v[48:51]
  ds_read_b64_tr_b16 a[236:237], v29 offset:29792
  v_mfma_f32_16x16x32_bf16 v[48:51], a[252:255], a[208:211], v[48:51]
  v_mfma_f32_16x16x32_bf16 v[52:55], a[252:255], a[212:215], v[52:55]
  ds_read_b64_tr_b16 a[238:239], v29 offset:33376
  v_mfma_f32_16x16x32_bf16 v[52:55], a[248:251], a[196:199], v[52:55]
  buffer_load_dwordx4 a[172:175], v9, s[48:51], s71 offen offset:3072 sc1
  v_mfma_f32_16x16x32_bf16 v[56:59], a[248:251], a[200:203], v[56:59]
  s_add_u32 s72, s68, s12
  ds_read_b64_tr_b16 a[240:241], v29 offset:64
  v_mfma_f32_16x16x32_bf16 v[56:59], a[252:255], a[216:219], v[56:59]
  s_and_b32 s72, s72, 7
  v_mfma_f32_16x16x32_bf16 v[60:63], a[252:255], a[220:223], v[60:63]
  s_and_b32 s73, s68, 0xfffffff8
  ds_read_b64_tr_b16 a[242:243], v29 offset:3648
  v_mfma_f32_16x16x32_bf16 v[60:63], a[248:251], a[204:207], v[60:63]
  s_waitcnt lgkmcnt(6)
  s_or_b32 s72, s72, s73
  v_mfma_f32_16x16x32_bf16 v[64:67], a[224:227], a[192:195], v[64:67]
  s_mul_i32 s69, s72, 229376
  ds_read_b64_tr_b16 a[244:245], v29 offset:22656
  v_mfma_f32_16x16x32_bf16 v[64:67], a[228:231], a[208:211], v[64:67]
  s_lshl_b32 s70, s72, 18
  v_mfma_f32_16x16x32_bf16 v[68:71], a[228:231], a[212:215], v[68:71]
  s_lshl_b32 s71, s72, 12
  ds_read_b64_tr_b16 a[246:247], v29 offset:26240
  v_mfma_f32_16x16x32_bf16 v[68:71], a[224:227], a[196:199], v[68:71]
  s_add_u32 s68, s68, 1
  v_mfma_f32_16x16x32_bf16 v[72:75], a[224:227], a[200:203], v[72:75]
  s_add_u32 m0, s75, 67776
  ds_read_b64_tr_b16 a[248:249], v29 offset:7232
  v_mfma_f32_16x16x32_bf16 v[72:75], a[228:231], a[216:219], v[72:75]
  buffer_load_dwordx4 v5, s[44:47], s70 offen offset:0 lds
  v_mfma_f32_16x16x32_bf16 v[76:79], a[228:231], a[220:223], v[76:79]
  ds_read_b64_tr_b16 a[250:251], v29 offset:10816
  v_mfma_f32_16x16x32_bf16 v[76:79], a[224:227], a[204:207], v[76:79]
  s_waitcnt lgkmcnt(6)
  ds_read_b64_tr_b16 a[252:253], v29 offset:29824
  v_mfma_f32_16x16x32_bf16 v[80:83], a[232:235], a[192:195], v[80:83]
  v_mfma_f32_16x16x32_bf16 v[80:83], a[236:239], a[208:211], v[80:83]
  ds_read_b64_tr_b16 a[254:255], v29 offset:33408
  v_mfma_f32_16x16x32_bf16 v[84:87], a[236:239], a[212:215], v[84:87]
  buffer_load_dwordx4 v6, s[44:47], s70 offen offset:1032 lds
  s_add_u32 m0, s74, 67776
  v_mfma_f32_16x16x32_bf16 v[84:87], a[232:235], a[196:199], v[84:87]
  ds_read_b64_tr_b16 a[224:225], v29 offset:96
  v_mfma_f32_16x16x32_bf16 v[88:91], a[232:235], a[200:203], v[88:91]
  v_mfma_f32_16x16x32_bf16 v[88:91], a[236:239], a[216:219], v[88:91]
  ds_read_b64_tr_b16 a[226:227], v29 offset:3680
  v_mfma_f32_16x16x32_bf16 v[92:95], a[236:239], a[220:223], v[92:95]
  v_mfma_f32_16x16x32_bf16 v[92:95], a[232:235], a[204:207], v[92:95]
  s_waitcnt lgkmcnt(6)
  ds_read_b64_tr_b16 a[228:229], v29 offset:22688
  v_mfma_f32_16x16x32_bf16 v[96:99], a[240:243], a[192:195], v[96:99]
  v_mfma_f32_16x16x32_bf16 v[96:99], a[244:247], a[208:211], v[96:99]
  ds_read_b64_tr_b16 a[230:231], v29 offset:26272
  v_mfma_f32_16x16x32_bf16 v[100:103], a[244:247], a[212:215], v[100:103]
  buffer_load_dwordx4 v7, s[36:39], s69 offen offset:0 lds
  v_mfma_f32_16x16x32_bf16 v[100:103], a[240:243], a[196:199], v[100:103]
  ds_read_b64_tr_b16 a[232:233], v29 offset:7264
  v_mfma_f32_16x16x32_bf16 v[104:107], a[240:243], a[200:203], v[104:107]
  v_mfma_f32_16x16x32_bf16 v[104:107], a[244:247], a[216:219], v[104:107]
  ds_read_b64_tr_b16 a[234:235], v29 offset:10848
  v_mfma_f32_16x16x32_bf16 v[108:111], a[244:247], a[220:223], v[108:111]
  v_mfma_f32_16x16x32_bf16 v[108:111], a[240:243], a[204:207], v[108:111]
  s_waitcnt lgkmcnt(6)
  ds_read_b64_tr_b16 a[236:237], v29 offset:29856
  v_mfma_f32_16x16x32_bf16 v[112:115], a[248:251], a[192:195], v[112:115]
  s_mov_b64 exec, s[86:87]
  buffer_load_dwordx4 v8, s[36:39], s69 offen offset:1024 lds
  s_mov_b64 exec, -1
  v_mfma_f32_16x16x32_bf16 v[112:115], a[252:255], a[208:211], v[112:115]
  s_add_u32 m0, s74, 74944
  ds_read_b64_tr_b16 a[238:239], v29 offset:33440
  v_mfma_f32_16x16x32_bf16 v[116:119], a[252:255], a[212:215], v[116:119]
  v_mfma_f32_16x16x32_bf16 v[116:119], a[248:251], a[196:199], v[116:119]
  ds_read_b64_tr_b16 a[240:241], v29 offset:128
  v_mfma_f32_16x16x32_bf16 v[120:123], a[248:251], a[200:203], v[120:123]
  ds_read_b64_tr_b16 a[242:243], v29 offset:3712
  v_mfma_f32_16x16x32_bf16 v[120:123], a[252:255], a[216:219], v[120:123]
  v_mfma_f32_16x16x32_bf16 v[124:127], a[252:255], a[220:223], v[124:127]
  ds_read_b64_tr_b16 a[244:245], v29 offset:22720
  v_mfma_f32_16x16x32_bf16 v[124:127], a[248:251], a[204:207], v[124:127]
  s_waitcnt lgkmcnt(7)
  v_mfma_f32_16x16x32_bf16 v[128:131], a[224:227], a[192:195], v[128:131]
  ds_read_b64_tr_b16 a[246:247], v29 offset:26304
  v_mfma_f32_16x16x32_bf16 v[128:131], a[228:231], a[208:211], v[128:131]
  buffer_load_dwordx4 v7, s[40:43], s69 offen offset:0 lds
  v_mfma_f32_16x16x32_bf16 v[132:135], a[228:231], a[212:215], v[132:135]
  ds_read_b64_tr_b16 a[248:249], v29 offset:7296
  v_mfma_f32_16x16x32_bf16 v[132:135], a[224:227], a[196:199], v[132:135]
  v_mfma_f32_16x16x32_bf16 v[136:139], a[224:227], a[200:203], v[136:139]
  ds_read_b64_tr_b16 a[250:251], v29 offset:10880
  v_mfma_f32_16x16x32_bf16 v[136:139], a[228:231], a[216:219], v[136:139]
  v_mfma_f32_16x16x32_bf16 v[140:143], a[228:231], a[220:223], v[140:143]
  ds_read_b64_tr_b16 a[252:253], v29 offset:29888
  v_mfma_f32_16x16x32_bf16 v[140:143], a[224:227], a[204:207], v[140:143]
  s_waitcnt lgkmcnt(7)
  v_mfma_f32_16x16x32_bf16 v[144:147], a[232:235], a[192:195], v[144:147]
  ds_read_b64_tr_b16 a[254:255], v29 offset:33472
  v_mfma_f32_16x16x32_bf16 v[144:147], a[236:239], a[208:211], v[144:147]
  s_mov_b64 exec, s[86:87]
  buffer_load_dwordx4 v8, s[40:43], s69 offen offset:1024 lds
  s_mov_b64 exec, -1
  v_mfma_f32_16x16x32_bf16 v[148:151], a[236:239], a[212:215], v[148:151]
  ds_read_b64_tr_b16 a[224:225], v29 offset:160
  v_mfma_f32_16x16x32_bf16 v[148:151], a[232:235], a[196:199], v[148:151]
  v_mfma_f32_16x16x32_bf16 v[152:155], a[232:235], a[200:203], v[152:155]
  ds_read_b64_tr_b16 a[226:227], v29 offset:3744
  v_mfma_f32_16x16x32_bf16 v[152:155], a[236:239], a[216:219], v[152:155]
  v_mfma_f32_16x16x32_bf16 v[156:159], a[236:239], a[220:223], v[156:159]
  ds_read_b64_tr_b16 a[228:229], v29 offset:22752
  buffer_load_dwordx4 a[176:179], v9, s[48:51], s71 offen offset:0 sc1
  v_mfma_f32_16x16x32_bf16 v[156:159], a[232:235], a[204:207], v[156:159]
  s_waitcnt lgkmcnt(7)
  v_mfma_f32_16x16x32_bf16 v[160:163], a[240:243], a[192:195], v[160:163]
  ds_read_b64_tr_b16 a[230:231], v29 offset:26336
  v_mfma_f32_16x16x32_bf16 v[160:163], a[244:247], a[208:211], v[160:163]
  ds_read_b64_tr_b16 a[232:233], v29 offset:7328
  v_mfma_f32_16x16x32_bf16 v[164:167], a[244:247], a[212:215], v[164:167]
  v_mfma_f32_16x16x32_bf16 v[164:167], a[240:243], a[196:199], v[164:167]
  ds_read_b64_tr_b16 a[234:235], v29 offset:10912
  v_mfma_f32_16x16x32_bf16 v[168:171], a[240:243], a[200:203], v[168:171]
  v_mfma_f32_16x16x32_bf16 v[168:171], a[244:247], a[216:219], v[168:171]
  ds_read_b64_tr_b16 a[236:237], v29 offset:29920
  v_mfma_f32_16x16x32_bf16 v[172:175], a[244:247], a[220:223], v[172:175]
  buffer_load_dwordx4 a[180:183], v9, s[48:51], s71 offen offset:1024 sc1
  v_mfma_f32_16x16x32_bf16 v[172:175], a[240:243], a[204:207], v[172:175]
  s_waitcnt lgkmcnt(7)
  ds_read_b64_tr_b16 a[238:239], v29 offset:33504
  v_mfma_f32_16x16x32_bf16 v[176:179], a[248:251], a[192:195], v[176:179]
  v_mfma_f32_16x16x32_bf16 v[176:179], a[252:255], a[208:211], v[176:179]
  ds_read_b64_tr_b16 a[240:241], v29 offset:192
  v_mfma_f32_16x16x32_bf16 v[180:183], a[252:255], a[212:215], v[180:183]
  v_mfma_f32_16x16x32_bf16 v[180:183], a[248:251], a[196:199], v[180:183]
  ds_read_b64_tr_b16 a[242:243], v29 offset:3776
  v_mfma_f32_16x16x32_bf16 v[184:187], a[248:251], a[200:203], v[184:187]
  buffer_load_dwordx4 a[184:187], v9, s[48:51], s71 offen offset:2048 sc1
  v_mfma_f32_16x16x32_bf16 v[184:187], a[252:255], a[216:219], v[184:187]
  ds_read_b64_tr_b16 a[244:245], v29 offset:22784
  v_mfma_f32_16x16x32_bf16 v[188:191], a[252:255], a[220:223], v[188:191]
  v_mfma_f32_16x16x32_bf16 v[188:191], a[248:251], a[204:207], v[188:191]
  s_waitcnt lgkmcnt(7)
  ds_read_b64_tr_b16 a[246:247], v29 offset:26368
  v_mfma_f32_16x16x32_bf16 v[192:195], a[224:227], a[192:195], v[192:195]
  v_mfma_f32_16x16x32_bf16 v[192:195], a[228:231], a[208:211], v[192:195]
  ds_read_b64_tr_b16 a[248:249], v29 offset:7360
  v_mfma_f32_16x16x32_bf16 v[196:199], a[228:231], a[212:215], v[196:199]
  v_mfma_f32_16x16x32_bf16 v[196:199], a[224:227], a[196:199], v[196:199]
  ds_read_b64_tr_b16 a[250:251], v29 offset:10944
  v_mfma_f32_16x16x32_bf16 v[200:203], a[224:227], a[200:203], v[200:203]
  buffer_load_dwordx4 a[188:191], v9, s[48:51], s71 offen offset:3072 sc1
  v_mfma_f32_16x16x32_bf16 v[200:203], a[228:231], a[216:219], v[200:203]
  ds_read_b64_tr_b16 a[252:253], v29 offset:29952
  v_mfma_f32_16x16x32_bf16 v[204:207], a[228:231], a[220:223], v[204:207]
  v_mfma_f32_16x16x32_bf16 v[204:207], a[224:227], a[204:207], v[204:207]
  s_waitcnt lgkmcnt(7)
  ds_read_b64_tr_b16 a[254:255], v29 offset:33536
  v_mfma_f32_16x16x32_bf16 v[208:211], a[232:235], a[192:195], v[208:211]
  v_mfma_f32_16x16x32_bf16 v[208:211], a[236:239], a[208:211], v[208:211]
  v_mfma_f32_16x16x32_bf16 v[212:215], a[236:239], a[212:215], v[212:215]
  v_mfma_f32_16x16x32_bf16 v[212:215], a[232:235], a[196:199], v[212:215]
  v_mfma_f32_16x16x32_bf16 v[216:219], a[232:235], a[200:203], v[216:219]
  v_mfma_f32_16x16x32_bf16 v[216:219], a[236:239], a[216:219], v[216:219]
  v_mfma_f32_16x16x32_bf16 v[220:223], a[236:239], a[220:223], v[220:223]
  v_mfma_f32_16x16x32_bf16 v[220:223], a[232:235], a[204:207], v[220:223]
  s_waitcnt lgkmcnt(4)
  s_waitcnt lgkmcnt(0)
  s_waitcnt vmcnt(21)
  s_barrier
  v_mfma_f32_16x16x32_bf16 v[224:227], a[240:243], a[192:195], v[224:227]
  ds_read_b64_tr_b16 a[224:225], v1 offset:0
  v_mfma_f32_16x16x32_bf16 v[224:227], a[244:247], a[208:211], v[224:227]
  v_mfma_f32_16x16x32_bf16 v[228:231], a[244:247], a[212:215], v[228:231]
  ds_read_b64_tr_b16 a[226:227], v1 offset:4128
  v_mfma_f32_16x16x32_bf16 v[228:231], a[240:243], a[196:199], v[228:231]
  v_mfma_f32_16x16x32_bf16 v[232:235], a[240:243], a[200:203], v[232:235]
  ds_read_b64_tr_b16 a[228:229], v1 offset:22592
  v_mfma_f32_16x16x32_bf16 v[232:235], a[244:247], a[216:219], v[232:235]
  v_mfma_f32_16x16x32_bf16 v[236:239], a[244:247], a[220:223], v[236:239]
  ds_read_b64_tr_b16 a[230:231], v1 offset:26720
  v_mfma_f32_16x16x32_bf16 v[236:239], a[240:243], a[204:207], v[236:239]
  s_waitcnt lgkmcnt(4)
  v_mfma_f32_16x16x32_bf16 v[240:243], a[248:251], a[192:195], v[240:243]
  ds_read_b64_tr_b16 a[232:233], v1 offset:8
  v_mfma_f32_16x16x32_bf16 v[240:243], a[252:255], a[208:211], v[240:243]
  v_mfma_f32_16x16x32_bf16 v[244:247], a[252:255], a[212:215], v[244:247]
  ds_read_b64_tr_b16 a[234:235], v1 offset:4136
  v_mfma_f32_16x16x32_bf16 v[244:247], a[248:251], a[196:199], v[244:247]
  v_mfma_f32_16x16x32_bf16 v[248:251], a[248:251], a[200:203], v[248:251]
  ds_read_b64_tr_b16 a[236:237], v1 offset:22600
  v_mfma_f32_16x16x32_bf16 v[248:251], a[252:255], a[216:219], v[248:251]
  v_mfma_f32_16x16x32_bf16 v[252:255], a[252:255], a[220:223], v[252:255]
  ds_read_b64_tr_b16 a[238:239], v1 offset:26728
  v_mfma_f32_16x16x32_bf16 v[252:255], a[248:251], a[204:207], v[252:255]
  s_waitcnt lgkmcnt(4)
  v_mfma_f32_16x16x32_bf16 a[0:3], a[224:227], a[128:131], a[0:3]
  s_add_u32 s72, s68, s12
  ds_read_b64_tr_b16 a[240:241], v1 offset:256
  v_mfma_f32_16x16x32_bf16 a[0:3], a[228:231], a[144:147], a[0:3]
  s_and_b32 s72, s72, 7
  ds_read_b64_tr_b16 a[242:243], v1 offset:4384
  v_mfma_f32_16x16x32_bf16 a[4:7], a[228:231], a[148:151], a[4:7]
  s_and_b32 s73, s68, 0xfffffff8
  v_mfma_f32_16x16x32_bf16 a[4:7], a[224:227], a[132:135], a[4:7]
  s_or_b32 s72, s72, s73
  ds_read_b64_tr_b16 a[244:245], v1 offset:22848
  v_mfma_f32_16x16x32_bf16 a[8:11], a[224:227], a[136:139], a[8:11]
  s_mul_i32 s69, s72, 229376
  v_mfma_f32_16x16x32_bf16 a[8:11], a[228:231], a[152:155], a[8:11]
  s_lshl_b32 s70, s72, 18
  ds_read_b64_tr_b16 a[246:247], v1 offset:26976
  v_mfma_f32_16x16x32_bf16 a[12:15], a[228:231], a[156:159], a[12:15]
  s_lshl_b32 s71, s72, 12
  v_mfma_f32_16x16x32_bf16 a[12:15], a[224:227], a[140:143], a[12:15]
  s_waitcnt lgkmcnt(4)
  s_add_u32 s68, s68, 1
  ds_read_b64_tr_b16 a[248:249], v1 offset:264
  v_mfma_f32_16x16x32_bf16 a[16:19], a[232:235], a[128:131], a[16:19]
  s_add_u32 m0, s75, 90368
  v_mfma_f32_16x16x32_bf16 a[16:19], a[236:239], a[144:147], a[16:19]
  ds_read_b64_tr_b16 a[250:251], v1 offset:4392
  v_mfma_f32_16x16x32_bf16 a[20:23], a[236:239], a[148:151], a[20:23]
  buffer_load_dwordx4 v5, s[44:47], s70 offen offset:0 lds
  v_mfma_f32_16x16x32_bf16 a[20:23], a[232:235], a[132:135], a[20:23]
  ds_read_b64_tr_b16 a[252:253], v1 offset:22856
  v_mfma_f32_16x16x32_bf16 a[24:27], a[232:235], a[136:139], a[24:27]
  v_mfma_f32_16x16x32_bf16 a[24:27], a[236:239], a[152:155], a[24:27]
  ds_read_b64_tr_b16 a[254:255], v1 offset:26984
  v_mfma_f32_16x16x32_bf16 a[28:31], a[236:239], a[156:159], a[28:31]
  v_mfma_f32_16x16x32_bf16 a[28:31], a[232:235], a[140:143], a[28:31]
  s_waitcnt lgkmcnt(4)
  ds_read_b64_tr_b16 a[224:225], v1 offset:512
  v_mfma_f32_16x16x32_bf16 a[32:35], a[240:243], a[128:131], a[32:35]
  v_mfma_f32_16x16x32_bf16 a[32:35], a[244:247], a[144:147], a[32:35]
  ds_read_b64_tr_b16 a[226:227], v1 offset:4640
  v_mfma_f32_16x16x32_bf16 a[36:39], a[244:247], a[148:151], a[36:39]
  buffer_load_dwordx4 v6, s[44:47], s70 offen offset:1032 lds
  s_add_u32 m0, s74, 90368
  v_mfma_f32_16x16x32_bf16 a[36:39], a[240:243], a[132:135], a[36:39]
  ds_read_b64_tr_b16 a[228:229], v1 offset:23104
  v_mfma_f32_16x16x32_bf16 a[40:43], a[240:243], a[136:139], a[40:43]
  v_mfma_f32_16x16x32_bf16 a[40:43], a[244:247], a[152:155], a[40:43]
  ds_read_b64_tr_b16 a[230:231], v1 offset:27232
  v_mfma_f32_16x16x32_bf16 a[44:47], a[244:247], a[156:159], a[44:47]
  ds_read_b64_tr_b16 a[232:233], v1 offset:520
  v_mfma_f32_16x16x32_bf16 a[44:47], a[240:243], a[140:143], a[44:47]
  s_waitcnt lgkmcnt(5)
  v_mfma_f32_16x16x32_bf16 a[48:51], a[248:251], a[128:131], a[48:51]
  ds_read_b64_tr_b16 a[234:235], v1 offset:4648
  v_mfma_f32_16x16x32_bf16 a[48:51], a[252:255], a[144:147], a[48:51]
  buffer_load_dwordx4 v7, s[36:39], s69 offen offset:0 lds
  v_mfma_f32_16x16x32_bf16 a[52:55], a[252:255], a[148:151], a[52:55]
  ds_read_b64_tr_b16 a[236:237], v1 offset:23112
  v_mfma_f32_16x16x32_bf16 a[52:55], a[248:251], a[132:135], a[52:55]
  v_mfma_f32_16x16x32_bf16 a[56:59], a[248:251], a[136:139], a[56:59]
  ds_read_b64_tr_b16 a[238:239], v1 offset:27240
  v_mfma_f32_16x16x32_bf16 a[56:59], a[252:255], a[152:155], a[56:59]
  v_mfma_f32_16x16x32_bf16 a[60:63], a[252:255], a[156:159], a[60:63]
  ds_read_b64_tr_b16 a[240:241], v1 offset:768
  v_mfma_f32_16x16x32_bf16 a[60:63], a[248:251], a[140:143], a[60:63]
  s_waitcnt lgkmcnt(5)
  v_mfma_f32_16x16x32_bf16 a[64:67], a[224:227], a[128:131], a[64:67]
  ds_read_b64_tr_b16 a[242:243], v1 offset:4896
  v_mfma_f32_16x16x32_bf16 a[64:67], a[228:231], a[144:147], a[64:67]
  s_mov_b64 exec, s[86:87]
  buffer_load_dwordx4 v8, s[36:39], s69 offen offset:1024 lds
  s_mov_b64 exec, -1
  v_mfma_f32_16x16x32_bf16 a[68:71], a[228:231], a[148:151], a[68:71]
  s_add_u32 m0, s74, 97536
  ds_read_b64_tr_b16 a[244:245], v1 offset:23360
  v_mfma_f32_16x16x32_bf16 a[68:71], a[224:227], a[132:135], a[68:71]
  v_mfma_f32_16x16x32_bf16 a[72:75], a[224:227], a[136:139], a[72:75]
  ds_read_b64_tr_b16 a[246:247], v1 offset:27488
  v_mfma_f32_16x16x32_bf16 a[72:75], a[228:231], a[152:155], a[72:75]
  v_mfma_f32_16x16x32_bf16 a[76:79], a[228:231], a[156:159], a[76:79]
  ds_read_b64_tr_b16 a[248:249], v1 offset:776
  v_mfma_f32_16x16x32_bf16 a[76:79], a[224:227], a[140:143], a[76:79]
  s_waitcnt lgkmcnt(5)
  v_mfma_f32_16x16x32_bf16 a[80:83], a[232:235], a[128:131], a[80:83]
  ds_read_b64_tr_b16 a[250:251], v1 offset:4904
  v_mfma_f32_16x16x32_bf16 a[80:83], a[236:239], a[144:147], a[80:83]
  buffer_load_dwordx4 v7, s[40:43], s69 offen offset:0 lds
  v_mfma_f32_16x16x32_bf16 a[84:87], a[236:239], a[148:151], a[84:87]
  ds_read_b64_tr_b16 a[252:253], v1 offset:23368
  v_mfma_f32_16x16x32_bf16 a[84:87], a[232:235], a[132:135], a[84:87]
  ds_read_b64_tr_b16 a[254:255], v1 offset:27496
  v_mfma_f32_16x16x32_bf16 a[88:91], a[232:235], a[136:139], a[88:91]
  v_mfma_f32_16x16x32_bf16 a[88:91], a[236:239], a[152:155], a[88:91]
  ds_read_b64_tr_b16 a[224:225], v3 offset:0
  v_mfma_f32_16x16x32_bf16 a[92:95], a[236:239], a[156:159], a[92:95]
  s_mov_b64 exec, s[86:87]
  buffer_load_dwordx4 v8, s[40:43], s69 offen offset:1024 lds
  s_mov_b64 exec, -1
  v_mfma_f32_16x16x32_bf16 a[92:95], a[232:235], a[140:143], a[92:95]
  s_waitcnt lgkmcnt(5)
  ds_read_b64_tr_b16 a[226:227], v3 offset:3584
  v_mfma_f32_16x16x32_bf16 a[96:99], a[240:243], a[128:131], a[96:99]
  v_mfma_f32_16x16x32_bf16 a[96:99], a[244:247], a[144:147], a[96:99]
  ds_read_b64_tr_b16 a[228:229], v3 offset:22592
  v_mfma_f32_16x16x32_bf16 a[100:103], a[244:247], a[148:151], a[100:103]
  v_mfma_f32_16x16x32_bf16 a[100:103], a[240:243], a[132:135], a[100:103]
  ds_read_b64_tr_b16 a[230:231], v3 offset:26176
  v_mfma_f32_16x16x32_bf16 a[104:107], a[240:243], a[136:139], a[104:107]
  v_mfma_f32_16x16x32_bf16 a[104:107], a[244:247], a[152:155], a[104:107]
  ds_read_b64_tr_b16 a[232:233], v3 offset:7168
  v_mfma_f32_16x16x32_bf16 a[108:111], a[244:247], a[156:159], a[108:111]
  buffer_load_dwordx4 a[192:195], v9, s[48:51], s71 offen offset:0 sc1
  v_mfma_f32_16x16x32_bf16 a[108:111], a[240:243], a[140:143], a[108:111]
  s_waitcnt lgkmcnt(5)
  ds_read_b64_tr_b16 a[234:235], v3 offset:10752
  v_mfma_f32_16x16x32_bf16 a[112:115], a[248:251], a[128:131], a[112:115]
  v_mfma_f32_16x16x32_bf16 a[112:115], a[252:255], a[144:147], a[112:115]
  ds_read_b64_tr_b16 a[236:237], v3 offset:29760
  v_mfma_f32_16x16x32_bf16 a[116:119], a[252:255], a[148:151], a[116:119]
  v_mfma_f32_16x16x32_bf16 a[116:119], a[248:251], a[132:135], a[116:119]
  ds_read_b64_tr_b16 a[238:239], v3 offset:33344
  v_mfma_f32_16x16x32_bf16 a[120:123], a[248:251], a[136:139], a[120:123]
  buffer_load_dwordx4 a[196:199], v9, s[48:51], s71 offen offset:1024 sc1
  v_mfma_f32_16x16x32_bf16 a[120:123], a[252:255], a[152:155], a[120:123]
  ds_read_b64_tr_b16 a[240:241], v3 offset:32
  v_mfma_f32_16x16x32_bf16 a[124:127], a[252:255], a[156:159], a[124:127]
  v_mfma_f32_16x16x32_bf16 a[124:127], a[248:251], a[140:143], a[124:127]
  s_waitcnt lgkmcnt(5)
  ds_read_b64_tr_b16 a[242:243], v3 offset:3616
  v_mfma_f32_16x16x32_bf16 v[32:35], a[224:227], a[128:131], v[32:35]
  ds_read_b64_tr_b16 a[244:245], v3 offset:22624
  v_mfma_f32_16x16x32_bf16 v[32:35], a[228:231], a[144:147], v[32:35]
  v_mfma_f32_16x16x32_bf16 v[36:39], a[228:231], a[148:151], v[36:39]
  ds_read_b64_tr_b16 a[246:247], v3 offset:26208
  v_mfma_f32_16x16x32_bf16 v[36:39], a[224:227], a[132:135], v[36:39]
  v_mfma_f32_16x16x32_bf16 v[40:43], a[224:227], a[136:139], v[40:43]
  ds_read_b64_tr_b16 a[248:249], v3 offset:7200
  v_mfma_f32_16x16x32_bf16 v[40:43], a[228:231], a[152:155], v[40:43]
  buffer_load_dwordx4 a[200:203], v9, s[48:51], s71 offen offset:2048 sc1
  v_mfma_f32_16x16x32_bf16 v[44:47], a[228:231], a[156:159], v[44:47]
  ds_read_b64_tr_b16 a[250:251], v3 offset:10784
  v_mfma_f32_16x16x32_bf16 v[44:47], a[224:227], a[140:143], v[44:47]
  s_waitcnt lgkmcnt(6)
  v_mfma_f32_16x16x32_bf16 v[48:51], a[232:235], a[128:131], v[48:51]
  ds_read_b64_tr_b16 a[252:253], v3 offset:29792
  v_mfma_f32_16x16x32_bf16 v[48:51], a[236:239], a[144:147], v[48:51]
  v_mfma_f32_16x16x32_bf16 v[52:55], a[236:239], a[148:151], v[52:55]
  ds_read_b64_tr_b16 a[254:255], v3 offset:33376
  v_mfma_f32_16x16x32_bf16 v[52:55], a[232:235], a[132:135], v[52:55]
  buffer_load_dwordx4 a[204:207], v9, s[48:51], s71 offen offset:3072 sc1
  v_mfma_f32_16x16x32_bf16 v[56:59], a[232:235], a[136:139], v[56:59]
  s_add_u32 s72, s68, s12
  ds_read_b64_tr_b16 a[224:225], v3 offset:64
  v_mfma_f32_16x16x32_bf16 v[56:59], a[236:239], a[152:155], v[56:59]
  s_and_b32 s72, s72, 7
  v_mfma_f32_16x16x32_bf16 v[60:63], a[236:239], a[156:159], v[60:63]
  s_and_b32 s73, s68, 0xfffffff8
  ds_read_b64_tr_b16 a[226:227], v3 offset:3648
  v_mfma_f32_16x16x32_bf16 v[60:63], a[232:235], a[140:143], v[60:63]
  s_waitcnt lgkmcnt(6)
  s_or_b32 s72, s72, s73
  v_mfma_f32_16x16x32_bf16 v[64:67], a[240:243], a[128:131], v[64:67]
  s_mul_i32 s69, s72, 229376
  ds_read_b64_tr_b16 a[228:229], v3 offset:22656
  v_mfma_f32_16x16x32_bf16 v[64:67], a[244:247], a[144:147], v[64:67]
  s_lshl_b32 s70, s72, 18
  v_mfma_f32_16x16x32_bf16 v[68:71], a[244:247], a[148:151], v[68:71]
  s_lshl_b32 s71, s72, 12
  ds_read_b64_tr_b16 a[230:231], v3 offset:26240
  v_mfma_f32_16x16x32_bf16 v[68:71], a[240:243], a[132:135], v[68:71]
  s_add_u32 s68, s68, 1
  v_mfma_f32_16x16x32_bf16 v[72:75], a[240:243], a[136:139], v[72:75]
  s_add_u32 m0, s75, 112960
  ds_read_b64_tr_b16 a[232:233], v3 offset:7232
  v_mfma_f32_16x16x32_bf16 v[72:75], a[244:247], a[152:155], v[72:75]
  buffer_load_dwordx4 v5, s[44:47], s70 offen offset:0 lds
  v_mfma_f32_16x16x32_bf16 v[76:79], a[244:247], a[156:159], v[76:79]
  ds_read_b64_tr_b16 a[234:235], v3 offset:10816
  v_mfma_f32_16x16x32_bf16 v[76:79], a[240:243], a[140:143], v[76:79]
  s_waitcnt lgkmcnt(6)
  ds_read_b64_tr_b16 a[236:237], v3 offset:29824
  v_mfma_f32_16x16x32_bf16 v[80:83], a[248:251], a[128:131], v[80:83]
  v_mfma_f32_16x16x32_bf16 v[80:83], a[252:255], a[144:147], v[80:83]
  ds_read_b64_tr_b16 a[238:239], v3 offset:33408
  v_mfma_f32_16x16x32_bf16 v[84:87], a[252:255], a[148:151], v[84:87]
  buffer_load_dwordx4 v6, s[44:47], s70 offen offset:1032 lds
  s_add_u32 m0, s74, 112960
  v_mfma_f32_16x16x32_bf16 v[84:87], a[248:251], a[132:135], v[84:87]
  ds_read_b64_tr_b16 a[240:241], v3 offset:96
  v_mfma_f32_16x16x32_bf16 v[88:91], a[248:251], a[136:139], v[88:91]
  v_mfma_f32_16x16x32_bf16 v[88:91], a[252:255], a[152:155], v[88:91]
  ds_read_b64_tr_b16 a[242:243], v3 offset:3680
  v_mfma_f32_16x16x32_bf16 v[92:95], a[252:255], a[156:159], v[92:95]
  v_mfma_f32_16x16x32_bf16 v[92:95], a[248:251], a[140:143], v[92:95]
  s_waitcnt lgkmcnt(6)
  ds_read_b64_tr_b16 a[244:245], v3 offset:22688
  v_mfma_f32_16x16x32_bf16 v[96:99], a[224:227], a[128:131], v[96:99]
  v_mfma_f32_16x16x32_bf16 v[96:99], a[228:231], a[144:147], v[96:99]
  ds_read_b64_tr_b16 a[246:247], v3 offset:26272
  v_mfma_f32_16x16x32_bf16 v[100:103], a[228:231], a[148:151], v[100:103]
  buffer_load_dwordx4 v7, s[36:39], s69 offen offset:0 lds
  v_mfma_f32_16x16x32_bf16 v[100:103], a[224:227], a[132:135], v[100:103]
  ds_read_b64_tr_b16 a[248:249], v3 offset:7264
  v_mfma_f32_16x16x32_bf16 v[104:107], a[224:227], a[136:139], v[104:107]
  v_mfma_f32_16x16x32_bf16 v[104:107], a[228:231], a[152:155], v[104:107]
  ds_read_b64_tr_b16 a[250:251], v3 offset:10848
  v_mfma_f32_16x16x32_bf16 v[108:111], a[228:231], a[156:159], v[108:111]
  v_mfma_f32_16x16x32_bf16 v[108:111], a[224:227], a[140:143], v[108:111]
  s_waitcnt lgkmcnt(6)
  ds_read_b64_tr_b16 a[252:253], v3 offset:29856
  v_mfma_f32_16x16x32_bf16 v[112:115], a[232:235], a[128:131], v[112:115]
  s_mov_b64 exec, s[86:87]
  buffer_load_dwordx4 v8, s[36:39], s69 offen offset:1024 lds
  s_mov_b64 exec, -1
  v_mfma_f32_16x16x32_bf16 v[112:115], a[236:239], a[144:147], v[112:115]
  s_add_u32 m0, s74, 120128
  ds_read_b64_tr_b16 a[254:255], v3 offset:33440
  v_mfma_f32_16x16x32_bf16 v[116:119], a[236:239], a[148:151], v[116:119]
  v_mfma_f32_16x16x32_bf16 v[116:119], a[232:235], a[132:135], v[116:119]
  ds_read_b64_tr_b16 a[224:225], v3 offset:128
  v_mfma_f32_16x16x32_bf16 v[120:123], a[232:235], a[136:139], v[120:123]
  ds_read_b64_tr_b16 a[226:227], v3 offset:3712
  v_mfma_f32_16x16x32_bf16 v[120:123], a[236:239], a[152:155], v[120:123]
  v_mfma_f32_16x16x32_bf16 v[124:127], a[236:239], a[156:159], v[124:127]
  ds_read_b64_tr_b16 a[228:229], v3 offset:22720
  v_mfma_f32_16x16x32_bf16 v[124:127], a[232:235], a[140:143], v[124:127]
  s_waitcnt lgkmcnt(7)
  v_mfma_f32_16x16x32_bf16 v[128:131], a[240:243], a[128:131], v[128:131]
  ds_read_b64_tr_b16 a[230:231], v3 offset:26304
  v_mfma_f32_16x16x32_bf16 v[128:131], a[244:247], a[144:147], v[128:131]
  buffer_load_dwordx4 v7, s[40:43], s69 offen offset:0 lds
  v_mfma_f32_16x16x32_bf16 v[132:135], a[244:247], a[148:151], v[132:135]
  ds_read_b64_tr_b16 a[232:233], v3 offset:7296
  v_mfma_f32_16x16x32_bf16 v[132:135], a[240:243], a[132:135], v[132:135]
  v_mfma_f32_16x16x32_bf16 v[136:139], a[240:243], a[136:139], v[136:139]
  ds_read_b64_tr_b16 a[234:235], v3 offset:10880
  v_mfma_f32_16x16x32_bf16 v[136:139], a[244:247], a[152:155], v[136:139]
  v_mfma_f32_16x16x32_bf16 v[140:143], a[244:247], a[156:159], v[140:143]
  ds_read_b64_tr_b16 a[236:237], v3 offset:29888
  v_mfma_f32_16x16x32_bf16 v[140:143], a[240:243], a[140:143], v[140:143]
  s_waitcnt lgkmcnt(7)
  v_mfma_f32_16x16x32_bf16 v[144:147], a[248:251], a[128:131], v[144:147]
  ds_read_b64_tr_b16 a[238:239], v3 offset:33472
  v_mfma_f32_16x16x32_bf16 v[144:147], a[252:255], a[144:147], v[144:147]
  s_mov_b64 exec, s[86:87]
  buffer_load_dwordx4 v8, s[40:43], s69 offen offset:1024 lds
  s_mov_b64 exec, -1
  v_mfma_f32_16x16x32_bf16 v[148:151], a[252:255], a[148:151], v[148:151]
  ds_read_b64_tr_b16 a[240:241], v3 offset:160
  v_mfma_f32_16x16x32_bf16 v[148:151], a[248:251], a[132:135], v[148:151]
  v_mfma_f32_16x16x32_bf16 v[152:155], a[248:251], a[136:139], v[152:155]
  ds_read_b64_tr_b16 a[242:243], v3 offset:3744
  v_mfma_f32_16x16x32_bf16 v[152:155], a[252:255], a[152:155], v[152:155]
  v_mfma_f32_16x16x32_bf16 v[156:159], a[252:255], a[156:159], v[156:159]
  ds_read_b64_tr_b16 a[244:245], v3 offset:22752
  buffer_load_dwordx4 a[208:211], v9, s[48:51], s71 offen offset:0 sc1
  v_mfma_f32_16x16x32_bf16 v[156:159], a[248:251], a[140:143], v[156:159]
  s_waitcnt lgkmcnt(7)
  v_mfma_f32_16x16x32_bf16 v[160:163], a[224:227], a[128:131], v[160:163]
  ds_read_b64_tr_b16 a[246:247], v3 offset:26336
  v_mfma_f32_16x16x32_bf16 v[160:163], a[228:231], a[144:147], v[160:163]
  ds_read_b64_tr_b16 a[248:249], v3 offset:7328
  v_mfma_f32_16x16x32_bf16 v[164:167], a[228:231], a[148:151], v[164:167]
  v_mfma_f32_16x16x32_bf16 v[164:167], a[224:227], a[132:135], v[164:167]
  ds_read_b64_tr_b16 a[250:251], v3 offset:10912
  v_mfma_f32_16x16x32_bf16 v[168:171], a[224:227], a[136:139], v[168:171]
  v_mfma_f32_16x16x32_bf16 v[168:171], a[228:231], a[152:155], v[168:171]
  ds_read_b64_tr_b16 a[252:253], v3 offset:29920
  v_mfma_f32_16x16x32_bf16 v[172:175], a[228:231], a[156:159], v[172:175]
  buffer_load_dwordx4 a[212:215], v9, s[48:51], s71 offen offset:1024 sc1
  v_mfma_f32_16x16x32_bf16 v[172:175], a[224:227], a[140:143], v[172:175]
  s_waitcnt lgkmcnt(7)
  ds_read_b64_tr_b16 a[254:255], v3 offset:33504
  v_mfma_f32_16x16x32_bf16 v[176:179], a[232:235], a[128:131], v[176:179]
  v_mfma_f32_16x16x32_bf16 v[176:179], a[236:239], a[144:147], v[176:179]
  ds_read_b64_tr_b16 a[224:225], v3 offset:192
  v_mfma_f32_16x16x32_bf16 v[180:183], a[236:239], a[148:151], v[180:183]
  v_mfma_f32_16x16x32_bf16 v[180:183], a[232:235], a[132:135], v[180:183]
  ds_read_b64_tr_b16 a[226:227], v3 offset:3776
  v_mfma_f32_16x16x32_bf16 v[184:187], a[232:235], a[136:139], v[184:187]
  buffer_load_dwordx4 a[216:219], v9, s[48:51], s71 offen offset:2048 sc1
  v_mfma_f32_16x16x32_bf16 v[184:187], a[236:239], a[152:155], v[184:187]
  ds_read_b64_tr_b16 a[228:229], v3 offset:22784
  v_mfma_f32_16x16x32_bf16 v[188:191], a[236:239], a[156:159], v[188:191]
  v_mfma_f32_16x16x32_bf16 v[188:191], a[232:235], a[140:143], v[188:191]
  s_waitcnt lgkmcnt(7)
  ds_read_b64_tr_b16 a[230:231], v3 offset:26368
  v_mfma_f32_16x16x32_bf16 v[192:195], a[240:243], a[128:131], v[192:195]
  v_mfma_f32_16x16x32_bf16 v[192:195], a[244:247], a[144:147], v[192:195]
  ds_read_b64_tr_b16 a[232:233], v3 offset:7360
  v_mfma_f32_16x16x32_bf16 v[196:199], a[244:247], a[148:151], v[196:199]
  v_mfma_f32_16x16x32_bf16 v[196:199], a[240:243], a[132:135], v[196:199]
  ds_read_b64_tr_b16 a[234:235], v3 offset:10944
  v_mfma_f32_16x16x32_bf16 v[200:203], a[240:243], a[136:139], v[200:203]
  buffer_load_dwordx4 a[220:223], v9, s[48:51], s71 offen offset:3072 sc1
  v_mfma_f32_16x16x32_bf16 v[200:203], a[244:247], a[152:155], v[200:203]
  ds_read_b64_tr_b16 a[236:237], v3 offset:29952
  v_mfma_f32_16x16x32_bf16 v[204:207], a[244:247], a[156:159], v[204:207]
  v_mfma_f32_16x16x32_bf16 v[204:207], a[240:243], a[140:143], v[204:207]
  s_waitcnt lgkmcnt(7)
  ds_read_b64_tr_b16 a[238:239], v3 offset:33536
  v_mfma_f32_16x16x32_bf16 v[208:211], a[248:251], a[128:131], v[208:211]
  v_mfma_f32_16x16x32_bf16 v[208:211], a[252:255], a[144:147], v[208:211]
  v_mfma_f32_16x16x32_bf16 v[212:215], a[252:255], a[148:151], v[212:215]
  v_mfma_f32_16x16x32_bf16 v[212:215], a[248:251], a[132:135], v[212:215]
  v_mfma_f32_16x16x32_bf16 v[216:219], a[248:251], a[136:139], v[216:219]
  v_mfma_f32_16x16x32_bf16 v[216:219], a[252:255], a[152:155], v[216:219]
  v_mfma_f32_16x16x32_bf16 v[220:223], a[252:255], a[156:159], v[220:223]
  v_mfma_f32_16x16x32_bf16 v[220:223], a[248:251], a[140:143], v[220:223]
  s_waitcnt lgkmcnt(4)
  s_waitcnt lgkmcnt(0)
  s_waitcnt vmcnt(20)
  s_barrier
  v_mfma_f32_16x16x32_bf16 v[224:227], a[224:227], a[128:131], v[224:227]
  ds_read_b64_tr_b16 a[240:241], v2 offset:0
  v_mfma_f32_16x16x32_bf16 v[224:227], a[228:231], a[144:147], v[224:227]
  v_mfma_f32_16x16x32_bf16 v[228:231], a[228:231], a[148:151], v[228:231]
  ds_read_b64_tr_b16 a[242:243], v2 offset:4128
  v_mfma_f32_16x16x32_bf16 v[228:231], a[224:227], a[132:135], v[228:231]
  v_mfma_f32_16x16x32_bf16 v[232:235], a[224:227], a[136:139], v[232:235]
  ds_read_b64_tr_b16 a[244:245], v2 offset:22592
  v_mfma_f32_16x16x32_bf16 v[232:235], a[228:231], a[152:155], v[232:235]
  v_mfma_f32_16x16x32_bf16 v[236:239], a[228:231], a[156:159], v[236:239]
  ds_read_b64_tr_b16 a[246:247], v2 offset:26720
  v_mfma_f32_16x16x32_bf16 v[236:239], a[224:227], a[140:143], v[236:239]
  s_waitcnt lgkmcnt(4)
  v_mfma_f32_16x16x32_bf16 v[240:243], a[232:235], a[128:131], v[240:243]
  ds_read_b64_tr_b16 a[248:249], v2 offset:8
  v_mfma_f32_16x16x32_bf16 v[240:243], a[236:239], a[144:147], v[240:243]
  v_mfma_f32_16x16x32_bf16 v[244:247], a[236:239], a[148:151], v[244:247]
  ds_read_b64_tr_b16 a[250:251], v2 offset:4136
  v_mfma_f32_16x16x32_bf16 v[244:247], a[232:235], a[132:135], v[244:247]
  v_mfma_f32_16x16x32_bf16 v[248:251], a[232:235], a[136:139], v[248:251]
  ds_read_b64_tr_b16 a[252:253], v2 offset:22600
  v_mfma_f32_16x16x32_bf16 v[248:251], a[236:239], a[152:155], v[248:251]
  v_mfma_f32_16x16x32_bf16 v[252:255], a[236:239], a[156:159], v[252:255]
  ds_read_b64_tr_b16 a[254:255], v2 offset:26728
  v_mfma_f32_16x16x32_bf16 v[252:255], a[232:235], a[140:143], v[252:255]
  s_waitcnt lgkmcnt(4)
  v_mfma_f32_16x16x32_bf16 a[0:3], a[240:243], a[160:163], a[0:3]
  s_add_u32 s72, s68, s12
  ds_read_b64_tr_b16 a[224:225], v2 offset:256
  v_mfma_f32_16x16x32_bf16 a[0:3], a[244:247], a[176:179], a[0:3]
  s_and_b32 s72, s72, 7
  ds_read_b64_tr_b16 a[226:227], v2 offset:4384
  v_mfma_f32_16x16x32_bf16 a[4:7], a[244:247], a[180:183], a[4:7]
  s_and_b32 s73, s68, 0xfffffff8
  v_mfma_f32_16x16x32_bf16 a[4:7], a[240:243], a[164:167], a[4:7]
  s_or_b32 s72, s72, s73
  ds_read_b64_tr_b16 a[228:229], v2 offset:22848
  v_mfma_f32_16x16x32_bf16 a[8:11], a[240:243], a[168:171], a[8:11]
  s_mul_i32 s69, s72, 229376
  v_mfma_f32_16x16x32_bf16 a[8:11], a[244:247], a[184:187], a[8:11]
  s_lshl_b32 s70, s72, 18
  ds_read_b64_tr_b16 a[230:231], v2 offset:26976
  buffer_load_dwordx4 v[24:27], v21, s[4:7], s3 offen
  v_mfma_f32_16x16x32_bf16 a[12:15], a[244:247], a[188:191], a[12:15]
  s_lshl_b32 s71, s72, 12
  v_mfma_f32_16x16x32_bf16 a[12:15], a[240:243], a[172:175], a[12:15]
  s_waitcnt lgkmcnt(4)
  s_add_u32 s68, s68, 1
  ds_read_b64_tr_b16 a[232:233], v2 offset:264
  v_mfma_f32_16x16x32_bf16 a[16:19], a[248:251], a[160:163], a[16:19]
  s_add_u32 m0, s75, 0
  v_mfma_f32_16x16x32_bf16 a[16:19], a[252:255], a[176:179], a[16:19]
  ds_read_b64_tr_b16 a[234:235], v2 offset:4392
  v_mfma_f32_16x16x32_bf16 a[20:23], a[252:255], a[180:183], a[20:23]
  buffer_load_dwordx4 v5, s[44:47], s70 offen offset:0 lds
  v_mfma_f32_16x16x32_bf16 a[20:23], a[248:251], a[164:167], a[20:23]
  ds_read_b64_tr_b16 a[236:237], v2 offset:22856
  v_mfma_f32_16x16x32_bf16 a[24:27], a[248:251], a[168:171], a[24:27]
  v_mfma_f32_16x16x32_bf16 a[24:27], a[252:255], a[184:187], a[24:27]
  ds_read_b64_tr_b16 a[238:239], v2 offset:26984
  v_mfma_f32_16x16x32_bf16 a[28:31], a[252:255], a[188:191], a[28:31]
  v_mfma_f32_16x16x32_bf16 a[28:31], a[248:251], a[172:175], a[28:31]
  s_waitcnt lgkmcnt(4)
  ds_read_b64_tr_b16 a[240:241], v2 offset:512
  v_mfma_f32_16x16x32_bf16 a[32:35], a[224:227], a[160:163], a[32:35]
  v_mfma_f32_16x16x32_bf16 a[32:35], a[228:231], a[176:179], a[32:35]
  ds_read_b64_tr_b16 a[242:243], v2 offset:4640
  v_mfma_f32_16x16x32_bf16 a[36:39], a[228:231], a[180:183], a[36:39]
  buffer_load_dwordx4 v6, s[44:47], s70 offen offset:1032 lds
  s_add_u32 m0, s74, 0
  v_mfma_f32_16x16x32_bf16 a[36:39], a[224:227], a[164:167], a[36:39]
  ds_read_b64_tr_b16 a[244:245], v2 offset:23104
  v_mfma_f32_16x16x32_bf16 a[40:43], a[224:227], a[168:171], a[40:43]
  v_mfma_f32_16x16x32_bf16 a[40:43], a[228:231], a[184:187], a[40:43]
  ds_read_b64_tr_b16 a[246:247], v2 offset:27232
  v_mfma_f32_16x16x32_bf16 a[44:47], a[228:231], a[188:191], a[44:47]
  ds_read_b64_tr_b16 a[248:249], v2 offset:520
  v_mfma_f32_16x16x32_bf16 a[44:47], a[224:227], a[172:175], a[44:47]
  s_waitcnt lgkmcnt(5)
  v_mfma_f32_16x16x32_bf16 a[48:51], a[232:235], a[160:163], a[48:51]
  ds_read_b64_tr_b16 a[250:251], v2 offset:4648
  v_mfma_f32_16x16x32_bf16 a[48:51], a[236:239], a[176:179], a[48:51]
  buffer_load_dwordx4 v7, s[36:39], s69 offen offset:0 lds
  v_mfma_f32_16x16x32_bf16 a[52:55], a[236:239], a[180:183], a[52:55]
  ds_read_b64_tr_b16 a[252:253], v2 offset:23112
  v_mfma_f32_16x16x32_bf16 a[52:55], a[232:235], a[164:167], a[52:55]
  v_mfma_f32_16x16x32_bf16 a[56:59], a[232:235], a[168:171], a[56:59]
  ds_read_b64_tr_b16 a[254:255], v2 offset:27240
  v_mfma_f32_16x16x32_bf16 a[56:59], a[236:239], a[184:187], a[56:59]
  v_mfma_f32_16x16x32_bf16 a[60:63], a[236:239], a[188:191], a[60:63]
  ds_read_b64_tr_b16 a[224:225], v2 offset:768
  v_mfma_f32_16x16x32_bf16 a[60:63], a[232:235], a[172:175], a[60:63]
  s_waitcnt lgkmcnt(5)
  v_mfma_f32_16x16x32_bf16 a[64:67], a[240:243], a[160:163], a[64:67]
  ds_read_b64_tr_b16 a[226:227], v2 offset:4896
  v_mfma_f32_16x16x32_bf16 a[64:67], a[244:247], a[176:179], a[64:67]
  s_mov_b64 exec, s[86:87]
  buffer_load_dwordx4 v8, s[36:39], s69 offen offset:1024 lds
  s_mov_b64 exec, -1
  v_mfma_f32_16x16x32_bf16 a[68:71], a[244:247], a[180:183], a[68:71]
  s_add_u32 m0, s74, 7168
  ds_read_b64_tr_b16 a[228:229], v2 offset:23360
  v_mfma_f32_16x16x32_bf16 a[68:71], a[240:243], a[164:167], a[68:71]
  v_mfma_f32_16x16x32_bf16 a[72:75], a[240:243], a[168:171], a[72:75]
  ds_read_b64_tr_b16 a[230:231], v2 offset:27488
  v_mfma_f32_16x16x32_bf16 a[72:75], a[244:247], a[184:187], a[72:75]
  v_mfma_f32_16x16x32_bf16 a[76:79], a[244:247], a[188:191], a[76:79]
  ds_read_b64_tr_b16 a[232:233], v2 offset:776
  v_mfma_f32_16x16x32_bf16 a[76:79], a[240:243], a[172:175], a[76:79]
  s_waitcnt lgkmcnt(5)
  v_mfma_f32_16x16x32_bf16 a[80:83], a[248:251], a[160:163], a[80:83]
  ds_read_b64_tr_b16 a[234:235], v2 offset:4904
  v_mfma_f32_16x16x32_bf16 a[80:83], a[252:255], a[176:179], a[80:83]
  buffer_load_dwordx4 v7, s[40:43], s69 offen offset:0 lds
  v_mfma_f32_16x16x32_bf16 a[84:87], a[252:255], a[180:183], a[84:87]
  ds_read_b64_tr_b16 a[236:237], v2 offset:23368
  v_mfma_f32_16x16x32_bf16 a[84:87], a[248:251], a[164:167], a[84:87]
  ds_read_b64_tr_b16 a[238:239], v2 offset:27496
  v_mfma_f32_16x16x32_bf16 a[88:91], a[248:251], a[168:171], a[88:91]
  v_mfma_f32_16x16x32_bf16 a[88:91], a[252:255], a[184:187], a[88:91]
  ds_read_b64_tr_b16 a[240:241], v4 offset:0
  v_mfma_f32_16x16x32_bf16 a[92:95], a[252:255], a[188:191], a[92:95]
  s_mov_b64 exec, s[86:87]
  buffer_load_dwordx4 v8, s[40:43], s69 offen offset:1024 lds
  s_mov_b64 exec, -1
  v_mfma_f32_16x16x32_bf16 a[92:95], a[248:251], a[172:175], a[92:95]
  s_waitcnt lgkmcnt(5)
  ds_read_b64_tr_b16 a[242:243], v4 offset:3584
  v_mfma_f32_16x16x32_bf16 a[96:99], a[224:227], a[160:163], a[96:99]
  v_mfma_f32_16x16x32_bf16 a[96:99], a[228:231], a[176:179], a[96:99]
  ds_read_b64_tr_b16 a[244:245], v4 offset:22592
  v_mfma_f32_16x16x32_bf16 a[100:103], a[228:231], a[180:183], a[100:103]
  v_mfma_f32_16x16x32_bf16 a[100:103], a[224:227], a[164:167], a[100:103]
  ds_read_b64_tr_b16 a[246:247], v4 offset:26176
  v_mfma_f32_16x16x32_bf16 a[104:107], a[224:227], a[168:171], a[104:107]
  v_mfma_f32_16x16x32_bf16 a[104:107], a[228:231], a[184:187], a[104:107]
  ds_read_b64_tr_b16 a[248:249], v4 offset:7168
  v_mfma_f32_16x16x32_bf16 a[108:111], a[228:231], a[188:191], a[108:111]
  buffer_load_dwordx4 a[128:131], v9, s[48:51], s71 offen offset:0 sc1
  v_mfma_f32_16x16x32_bf16 a[108:111], a[224:227], a[172:175], a[108:111]
  s_waitcnt lgkmcnt(5)
  ds_read_b64_tr_b16 a[250:251], v4 offset:10752
  v_mfma_f32_16x16x32_bf16 a[112:115], a[232:235], a[160:163], a[112:115]
  v_mfma_f32_16x16x32_bf16 a[112:115], a[236:239], a[176:179], a[112:115]
  ds_read_b64_tr_b16 a[252:253], v4 offset:29760
  v_mfma_f32_16x16x32_bf16 a[116:119], a[236:239], a[180:183], a[116:119]
  v_mfma_f32_16x16x32_bf16 a[116:119], a[232:235], a[164:167], a[116:119]
  ds_read_b64_tr_b16 a[254:255], v4 offset:33344
  v_mfma_f32_16x16x32_bf16 a[120:123], a[232:235], a[168:171], a[120:123]
  buffer_load_dwordx4 a[132:135], v9, s[48:51], s71 offen offset:1024 sc1
  v_mfma_f32_16x16x32_bf16 a[120:123], a[236:239], a[184:187], a[120:123]
  ds_read_b64_tr_b16 a[224:225], v4 offset:32
  v_mfma_f32_16x16x32_bf16 a[124:127], a[236:239], a[188:191], a[124:127]
  v_mfma_f32_16x16x32_bf16 a[124:127], a[232:235], a[172:175], a[124:127]
  s_waitcnt lgkmcnt(5)
  ds_read_b64_tr_b16 a[226:227], v4 offset:3616
  v_mfma_f32_16x16x32_bf16 v[32:35], a[240:243], a[160:163], v[32:35]
  ds_read_b64_tr_b16 a[228:229], v4 offset:22624
  v_mfma_f32_16x16x32_bf16 v[32:35], a[244:247], a[176:179], v[32:35]
  v_mfma_f32_16x16x32_bf16 v[36:39], a[244:247], a[180:183], v[36:39]
  ds_read_b64_tr_b16 a[230:231], v4 offset:26208
  v_mfma_f32_16x16x32_bf16 v[36:39], a[240:243], a[164:167], v[36:39]
  v_mfma_f32_16x16x32_bf16 v[40:43], a[240:243], a[168:171], v[40:43]
  ds_read_b64_tr_b16 a[232:233], v4 offset:7200
  v_mfma_f32_16x16x32_bf16 v[40:43], a[244:247], a[184:187], v[40:43]
  buffer_load_dwordx4 a[136:139], v9, s[48:51], s71 offen offset:2048 sc1
  v_mfma_f32_16x16x32_bf16 v[44:47], a[244:247], a[188:191], v[44:47]
  ds_read_b64_tr_b16 a[234:235], v4 offset:10784
  v_mfma_f32_16x16x32_bf16 v[44:47], a[240:243], a[172:175], v[44:47]
  s_waitcnt lgkmcnt(6)
  v_mfma_f32_16x16x32_bf16 v[48:51], a[248:251], a[160:163], v[48:51]
  ds_read_b64_tr_b16 a[236:237], v4 offset:29792
  v_mfma_f32_16x16x32_bf16 v[48:51], a[252:255], a[176:179], v[48:51]
  v_mfma_f32_16x16x32_bf16 v[52:55], a[252:255], a[180:183], v[52:55]
  ds_read_b64_tr_b16 a[238:239], v4 offset:33376
  v_mfma_f32_16x16x32_bf16 v[52:55], a[248:251], a[164:167], v[52:55]
  buffer_load_dwordx4 a[140:143], v9, s[48:51], s71 offen offset:3072 sc1
  v_mfma_f32_16x16x32_bf16 v[56:59], a[248:251], a[168:171], v[56:59]
  s_add_u32 s72, s68, s12
  ds_read_b64_tr_b16 a[240:241], v4 offset:64
  v_mfma_f32_16x16x32_bf16 v[56:59], a[252:255], a[184:187], v[56:59]
  s_and_b32 s72, s72, 7
  v_mfma_f32_16x16x32_bf16 v[60:63], a[252:255], a[188:191], v[60:63]
  s_and_b32 s73, s68, 0xfffffff8
  ds_read_b64_tr_b16 a[242:243], v4 offset:3648
  v_mfma_f32_16x16x32_bf16 v[60:63], a[248:251], a[172:175], v[60:63]
  s_waitcnt lgkmcnt(6)
  s_or_b32 s72, s72, s73
  v_mfma_f32_16x16x32_bf16 v[64:67], a[224:227], a[160:163], v[64:67]
  s_mul_i32 s69, s72, 229376
  ds_read_b64_tr_b16 a[244:245], v4 offset:22656
  v_mfma_f32_16x16x32_bf16 v[64:67], a[228:231], a[176:179], v[64:67]
  s_lshl_b32 s70, s72, 18
  v_mfma_f32_16x16x32_bf16 v[68:71], a[228:231], a[180:183], v[68:71]
  s_lshl_b32 s71, s72, 12
  ds_read_b64_tr_b16 a[246:247], v4 offset:26240
  v_mfma_f32_16x16x32_bf16 v[68:71], a[224:227], a[164:167], v[68:71]
  s_add_u32 s68, s68, 1
  v_mfma_f32_16x16x32_bf16 v[72:75], a[224:227], a[168:171], v[72:75]
  s_add_u32 m0, s75, 22592
  ds_read_b64_tr_b16 a[248:249], v4 offset:7232
  v_mfma_f32_16x16x32_bf16 v[72:75], a[228:231], a[184:187], v[72:75]
  buffer_load_dwordx4 v5, s[44:47], s70 offen offset:0 lds
  v_mfma_f32_16x16x32_bf16 v[76:79], a[228:231], a[188:191], v[76:79]
  ds_read_b64_tr_b16 a[250:251], v4 offset:10816
  v_mfma_f32_16x16x32_bf16 v[76:79], a[224:227], a[172:175], v[76:79]
  s_waitcnt lgkmcnt(6)
  ds_read_b64_tr_b16 a[252:253], v4 offset:29824
  v_mfma_f32_16x16x32_bf16 v[80:83], a[232:235], a[160:163], v[80:83]
  v_mfma_f32_16x16x32_bf16 v[80:83], a[236:239], a[176:179], v[80:83]
  ds_read_b64_tr_b16 a[254:255], v4 offset:33408
  v_mfma_f32_16x16x32_bf16 v[84:87], a[236:239], a[180:183], v[84:87]
  buffer_load_dwordx4 v6, s[44:47], s70 offen offset:1032 lds
  s_add_u32 m0, s74, 22592
  v_mfma_f32_16x16x32_bf16 v[84:87], a[232:235], a[164:167], v[84:87]
  ds_read_b64_tr_b16 a[224:225], v4 offset:96
  v_mfma_f32_16x16x32_bf16 v[88:91], a[232:235], a[168:171], v[88:91]
  v_mfma_f32_16x16x32_bf16 v[88:91], a[236:239], a[184:187], v[88:91]
  ds_read_b64_tr_b16 a[226:227], v4 offset:3680
  v_mfma_f32_16x16x32_bf16 v[92:95], a[236:239], a[188:191], v[92:95]
  v_mfma_f32_16x16x32_bf16 v[92:95], a[232:235], a[172:175], v[92:95]
  s_waitcnt lgkmcnt(6)
  ds_read_b64_tr_b16 a[228:229], v4 offset:22688
  v_mfma_f32_16x16x32_bf16 v[96:99], a[240:243], a[160:163], v[96:99]
  v_mfma_f32_16x16x32_bf16 v[96:99], a[244:247], a[176:179], v[96:99]
  ds_read_b64_tr_b16 a[230:231], v4 offset:26272
  v_mfma_f32_16x16x32_bf16 v[100:103], a[244:247], a[180:183], v[100:103]
  buffer_load_dwordx4 v7, s[36:39], s69 offen offset:0 lds
  v_mfma_f32_16x16x32_bf16 v[100:103], a[240:243], a[164:167], v[100:103]
  ds_read_b64_tr_b16 a[232:233], v4 offset:7264
  v_mfma_f32_16x16x32_bf16 v[104:107], a[240:243], a[168:171], v[104:107]
  v_mfma_f32_16x16x32_bf16 v[104:107], a[244:247], a[184:187], v[104:107]
  ds_read_b64_tr_b16 a[234:235], v4 offset:10848
  v_mfma_f32_16x16x32_bf16 v[108:111], a[244:247], a[188:191], v[108:111]
  v_mfma_f32_16x16x32_bf16 v[108:111], a[240:243], a[172:175], v[108:111]
  s_waitcnt lgkmcnt(6)
  ds_read_b64_tr_b16 a[236:237], v4 offset:29856
  v_mfma_f32_16x16x32_bf16 v[112:115], a[248:251], a[160:163], v[112:115]
  s_mov_b64 exec, s[86:87]
  buffer_load_dwordx4 v8, s[36:39], s69 offen offset:1024 lds
  s_mov_b64 exec, -1
  v_mfma_f32_16x16x32_bf16 v[112:115], a[252:255], a[176:179], v[112:115]
  s_add_u32 m0, s74, 29760
  ds_read_b64_tr_b16 a[238:239], v4 offset:33440
  v_mfma_f32_16x16x32_bf16 v[116:119], a[252:255], a[180:183], v[116:119]
  v_mfma_f32_16x16x32_bf16 v[116:119], a[248:251], a[164:167], v[116:119]
  ds_read_b64_tr_b16 a[240:241], v4 offset:128
  v_mfma_f32_16x16x32_bf16 v[120:123], a[248:251], a[168:171], v[120:123]
  ds_read_b64_tr_b16 a[242:243], v4 offset:3712
  v_mfma_f32_16x16x32_bf16 v[120:123], a[252:255], a[184:187], v[120:123]
  v_mfma_f32_16x16x32_bf16 v[124:127], a[252:255], a[188:191], v[124:127]
  ds_read_b64_tr_b16 a[244:245], v4 offset:22720
  v_mfma_f32_16x16x32_bf16 v[124:127], a[248:251], a[172:175], v[124:127]
  s_waitcnt lgkmcnt(7)
  v_mfma_f32_16x16x32_bf16 v[128:131], a[224:227], a[160:163], v[128:131]
  ds_read_b64_tr_b16 a[246:247], v4 offset:26304
  v_mfma_f32_16x16x32_bf16 v[128:131], a[228:231], a[176:179], v[128:131]
  buffer_load_dwordx4 v7, s[40:43], s69 offen offset:0 lds
  v_mfma_f32_16x16x32_bf16 v[132:135], a[228:231], a[180:183], v[132:135]
  ds_read_b64_tr_b16 a[248:249], v4 offset:7296
  v_mfma_f32_16x16x32_bf16 v[132:135], a[224:227], a[164:167], v[132:135]
  v_mfma_f32_16x16x32_bf16 v[136:139], a[224:227], a[168:171], v[136:139]
  ds_read_b64_tr_b16 a[250:251], v4 offset:10880
  v_mfma_f32_16x16x32_bf16 v[136:139], a[228:231], a[184:187], v[136:139]
  v_mfma_f32_16x16x32_bf16 v[140:143], a[228:231], a[188:191], v[140:143]
  ds_read_b64_tr_b16 a[252:253], v4 offset:29888
  v_mfma_f32_16x16x32_bf16 v[140:143], a[224:227], a[172:175], v[140:143]
  s_waitcnt lgkmcnt(7)
  v_mfma_f32_16x16x32_bf16 v[144:147], a[232:235], a[160:163], v[144:147]
  ds_read_b64_tr_b16 a[254:255], v4 offset:33472
  v_mfma_f32_16x16x32_bf16 v[144:147], a[236:239], a[176:179], v[144:147]
  s_mov_b64 exec, s[86:87]
  buffer_load_dwordx4 v8, s[40:43], s69 offen offset:1024 lds
  s_mov_b64 exec, -1
  v_mfma_f32_16x16x32_bf16 v[148:151], a[236:239], a[180:183], v[148:151]
  ds_read_b64_tr_b16 a[224:225], v4 offset:160
  v_mfma_f32_16x16x32_bf16 v[148:151], a[232:235], a[164:167], v[148:151]
  v_mfma_f32_16x16x32_bf16 v[152:155], a[232:235], a[168:171], v[152:155]
  ds_read_b64_tr_b16 a[226:227], v4 offset:3744
  v_mfma_f32_16x16x32_bf16 v[152:155], a[236:239], a[184:187], v[152:155]
  v_mfma_f32_16x16x32_bf16 v[156:159], a[236:239], a[188:191], v[156:159]
  ds_read_b64_tr_b16 a[228:229], v4 offset:22752
  buffer_load_dwordx4 a[144:147], v9, s[48:51], s71 offen offset:0 sc1
  v_mfma_f32_16x16x32_bf16 v[156:159], a[232:235], a[172:175], v[156:159]
  s_waitcnt lgkmcnt(7)
  v_mfma_f32_16x16x32_bf16 v[160:163], a[240:243], a[160:163], v[160:163]
  ds_read_b64_tr_b16 a[230:231], v4 offset:26336
  v_mfma_f32_16x16x32_bf16 v[160:163], a[244:247], a[176:179], v[160:163]
  ds_read_b64_tr_b16 a[232:233], v4 offset:7328
  v_mfma_f32_16x16x32_bf16 v[164:167], a[244:247], a[180:183], v[164:167]
  v_mfma_f32_16x16x32_bf16 v[164:167], a[240:243], a[164:167], v[164:167]
  ds_read_b64_tr_b16 a[234:235], v4 offset:10912
  v_mfma_f32_16x16x32_bf16 v[168:171], a[240:243], a[168:171], v[168:171]
  v_mfma_f32_16x16x32_bf16 v[168:171], a[244:247], a[184:187], v[168:171]
  ds_read_b64_tr_b16 a[236:237], v4 offset:29920
  v_mfma_f32_16x16x32_bf16 v[172:175], a[244:247], a[188:191], v[172:175]
  buffer_load_dwordx4 a[148:151], v9, s[48:51], s71 offen offset:1024 sc1
  v_mfma_f32_16x16x32_bf16 v[172:175], a[240:243], a[172:175], v[172:175]
  s_waitcnt lgkmcnt(7)
  ds_read_b64_tr_b16 a[238:239], v4 offset:33504
  v_mfma_f32_16x16x32_bf16 v[176:179], a[248:251], a[160:163], v[176:179]
  v_mfma_f32_16x16x32_bf16 v[176:179], a[252:255], a[176:179], v[176:179]
  ds_read_b64_tr_b16 a[240:241], v4 offset:192
  v_mfma_f32_16x16x32_bf16 v[180:183], a[252:255], a[180:183], v[180:183]
  v_mfma_f32_16x16x32_bf16 v[180:183], a[248:251], a[164:167], v[180:183]
  ds_read_b64_tr_b16 a[242:243], v4 offset:3776
  v_mfma_f32_16x16x32_bf16 v[184:187], a[248:251], a[168:171], v[184:187]
  buffer_load_dwordx4 a[152:155], v9, s[48:51], s71 offen offset:2048 sc1
  v_mfma_f32_16x16x32_bf16 v[184:187], a[252:255], a[184:187], v[184:187]
  ds_read_b64_tr_b16 a[244:245], v4 offset:22784
  v_mfma_f32_16x16x32_bf16 v[188:191], a[252:255], a[188:191], v[188:191]
  v_mfma_f32_16x16x32_bf16 v[188:191], a[248:251], a[172:175], v[188:191]
  s_waitcnt lgkmcnt(7)
  ds_read_b64_tr_b16 a[246:247], v4 offset:26368
  v_mfma_f32_16x16x32_bf16 v[192:195], a[224:227], a[160:163], v[192:195]
  v_mfma_f32_16x16x32_bf16 v[192:195], a[228:231], a[176:179], v[192:195]
  ds_read_b64_tr_b16 a[248:249], v4 offset:7360
  v_mfma_f32_16x16x32_bf16 v[196:199], a[228:231], a[180:183], v[196:199]
  v_mfma_f32_16x16x32_bf16 v[196:199], a[224:227], a[164:167], v[196:199]
  ds_read_b64_tr_b16 a[250:251], v4 offset:10944
  v_mfma_f32_16x16x32_bf16 v[200:203], a[224:227], a[168:171], v[200:203]
  buffer_load_dwordx4 a[156:159], v9, s[48:51], s71 offen offset:3072 sc1
  v_mfma_f32_16x16x32_bf16 v[200:203], a[228:231], a[184:187], v[200:203]
  ds_read_b64_tr_b16 a[252:253], v4 offset:29952
  v_mfma_f32_16x16x32_bf16 v[204:207], a[228:231], a[188:191], v[204:207]
  v_mfma_f32_16x16x32_bf16 v[204:207], a[224:227], a[172:175], v[204:207]
  s_waitcnt lgkmcnt(7)
  ds_read_b64_tr_b16 a[254:255], v4 offset:33536
  v_mfma_f32_16x16x32_bf16 v[208:211], a[232:235], a[160:163], v[208:211]
  v_mfma_f32_16x16x32_bf16 v[208:211], a[236:239], a[176:179], v[208:211]
  v_mfma_f32_16x16x32_bf16 v[212:215], a[236:239], a[180:183], v[212:215]
  v_mfma_f32_16x16x32_bf16 v[212:215], a[232:235], a[164:167], v[212:215]
  v_mfma_f32_16x16x32_bf16 v[216:219], a[232:235], a[168:171], v[216:219]
  v_mfma_f32_16x16x32_bf16 v[216:219], a[236:239], a[184:187], v[216:219]
  v_mfma_f32_16x16x32_bf16 v[220:223], a[236:239], a[188:191], v[220:223]
  v_mfma_f32_16x16x32_bf16 v[220:223], a[232:235], a[172:175], v[220:223]
  s_waitcnt lgkmcnt(4)
  s_waitcnt lgkmcnt(0)
  s_waitcnt vmcnt(21)
  s_barrier
  v_mfma_f32_16x16x32_bf16 v[224:227], a[240:243], a[160:163], v[224:227]
  ds_read_b64_tr_b16 a[224:225], v28 offset:0
  v_mfma_f32_16x16x32_bf16 v[224:227], a[244:247], a[176:179], v[224:227]
  v_mfma_f32_16x16x32_bf16 v[228:231], a[244:247], a[180:183], v[228:231]
  ds_read_b64_tr_b16 a[226:227], v28 offset:4128
  v_mfma_f32_16x16x32_bf16 v[228:231], a[240:243], a[164:167], v[228:231]
  v_mfma_f32_16x16x32_bf16 v[232:235], a[240:243], a[168:171], v[232:235]
  ds_read_b64_tr_b16 a[228:229], v28 offset:22592
  v_mfma_f32_16x16x32_bf16 v[232:235], a[244:247], a[184:187], v[232:235]
  v_mfma_f32_16x16x32_bf16 v[236:239], a[244:247], a[188:191], v[236:239]
  ds_read_b64_tr_b16 a[230:231], v28 offset:26720
  v_mfma_f32_16x16x32_bf16 v[236:239], a[240:243], a[172:175], v[236:239]
  s_waitcnt lgkmcnt(4)
  v_mfma_f32_16x16x32_bf16 v[240:243], a[248:251], a[160:163], v[240:243]
  ds_read_b64_tr_b16 a[232:233], v28 offset:8
  v_mfma_f32_16x16x32_bf16 v[240:243], a[252:255], a[176:179], v[240:243]
  v_mfma_f32_16x16x32_bf16 v[244:247], a[252:255], a[180:183], v[244:247]
  ds_read_b64_tr_b16 a[234:235], v28 offset:4136
  v_mfma_f32_16x16x32_bf16 v[244:247], a[248:251], a[164:167], v[244:247]
  v_mfma_f32_16x16x32_bf16 v[248:251], a[248:251], a[168:171], v[248:251]
  ds_read_b64_tr_b16 a[236:237], v28 offset:22600
  v_mfma_f32_16x16x32_bf16 v[248:251], a[252:255], a[184:187], v[248:251]
  v_mfma_f32_16x16x32_bf16 v[252:255], a[252:255], a[188:191], v[252:255]
  ds_read_b64_tr_b16 a[238:239], v28 offset:26728
  v_mfma_f32_16x16x32_bf16 v[252:255], a[248:251], a[172:175], v[252:255]
  s_waitcnt lgkmcnt(4)
  v_mfma_f32_16x16x32_bf16 a[0:3], a[224:227], a[192:195], a[0:3]
  s_add_u32 s72, s68, s12
  ds_read_b64_tr_b16 a[240:241], v28 offset:256
  v_mfma_f32_16x16x32_bf16 a[0:3], a[228:231], a[208:211], a[0:3]
  s_and_b32 s72, s72, 7
  ds_read_b64_tr_b16 a[242:243], v28 offset:4384
  v_mfma_f32_16x16x32_bf16 a[4:7], a[228:231], a[212:215], a[4:7]
  s_and_b32 s73, s68, 0xfffffff8
  v_mfma_f32_16x16x32_bf16 a[4:7], a[224:227], a[196:199], a[4:7]
  s_or_b32 s72, s72, s73
  ds_read_b64_tr_b16 a[244:245], v28 offset:22848
  v_mfma_f32_16x16x32_bf16 a[8:11], a[224:227], a[200:203], a[8:11]
  s_mul_i32 s69, s72, 229376
  v_mfma_f32_16x16x32_bf16 a[8:11], a[228:231], a[216:219], a[8:11]
  s_lshl_b32 s70, s72, 18
  ds_read_b64_tr_b16 a[246:247], v28 offset:26976
  v_mfma_f32_16x16x32_bf16 a[12:15], a[228:231], a[220:223], a[12:15]
  s_lshl_b32 s71, s72, 12
  v_mfma_f32_16x16x32_bf16 a[12:15], a[224:227], a[204:207], a[12:15]
  s_waitcnt lgkmcnt(4)
  s_add_u32 s68, s68, 1
  ds_read_b64_tr_b16 a[248:249], v28 offset:264
  v_mfma_f32_16x16x32_bf16 a[16:19], a[232:235], a[192:195], a[16:19]
  s_add_u32 m0, s75, 45184
  v_mfma_f32_16x16x32_bf16 a[16:19], a[236:239], a[208:211], a[16:19]
  ds_read_b64_tr_b16 a[250:251], v28 offset:4392
  v_mfma_f32_16x16x32_bf16 a[20:23], a[236:239], a[212:215], a[20:23]
  buffer_load_dwordx4 v5, s[44:47], s70 offen offset:0 lds
  v_mfma_f32_16x16x32_bf16 a[20:23], a[232:235], a[196:199], a[20:23]
  ds_read_b64_tr_b16 a[252:253], v28 offset:22856
  v_mfma_f32_16x16x32_bf16 a[24:27], a[232:235], a[200:203], a[24:27]
  v_mfma_f32_16x16x32_bf16 a[24:27], a[236:239], a[216:219], a[24:27]
  ds_read_b64_tr_b16 a[254:255], v28 offset:26984
  v_mfma_f32_16x16x32_bf16 a[28:31], a[236:239], a[220:223], a[28:31]
  v_mfma_f32_16x16x32_bf16 a[28:31], a[232:235], a[204:207], a[28:31]
  s_waitcnt lgkmcnt(4)
  ds_read_b64_tr_b16 a[224:225], v28 offset:512
  v_mfma_f32_16x16x32_bf16 a[32:35], a[240:243], a[192:195], a[32:35]
  v_mfma_f32_16x16x32_bf16 a[32:35], a[244:247], a[208:211], a[32:35]
  ds_read_b64_tr_b16 a[226:227], v28 offset:4640
  v_mfma_f32_16x16x32_bf16 a[36:39], a[244:247], a[212:215], a[36:39]
  buffer_load_dwordx4 v6, s[44:47], s70 offen offset:1032 lds
  s_add_u32 m0, s74, 45184
  v_mfma_f32_16x16x32_bf16 a[36:39], a[240:243], a[196:199], a[36:39]
  ds_read_b64_tr_b16 a[228:229], v28 offset:23104
  v_mfma_f32_16x16x32_bf16 a[40:43], a[240:243], a[200:203], a[40:43]
  v_mfma_f32_16x16x32_bf16 a[40:43], a[244:247], a[216:219], a[40:43]
  ds_read_b64_tr_b16 a[230:231], v28 offset:27232
  v_mfma_f32_16x16x32_bf16 a[44:47], a[244:247], a[220:223], a[44:47]
  ds_read_b64_tr_b16 a[232:233], v28 offset:520
  v_mfma_f32_16x16x32_bf16 a[44:47], a[240:243], a[204:207], a[44:47]
  s_waitcnt lgkmcnt(5)
  v_mfma_f32_16x16x32_bf16 a[48:51], a[248:251], a[192:195], a[48:51]
  ds_read_b64_tr_b16 a[234:235], v28 offset:4648
  v_mfma_f32_16x16x32_bf16 a[48:51], a[252:255], a[208:211], a[48:51]
  buffer_load_dwordx4 v7, s[36:39], s69 offen offset:0 lds
  v_mfma_f32_16x16x32_bf16 a[52:55], a[252:255], a[212:215], a[52:55]
  ds_read_b64_tr_b16 a[236:237], v28 offset:23112
  v_mfma_f32_16x16x32_bf16 a[52:55], a[248:251], a[196:199], a[52:55]
  v_mfma_f32_16x16x32_bf16 a[56:59], a[248:251], a[200:203], a[56:59]
  ds_read_b64_tr_b16 a[238:239], v28 offset:27240
  v_mfma_f32_16x16x32_bf16 a[56:59], a[252:255], a[216:219], a[56:59]
  v_mfma_f32_16x16x32_bf16 a[60:63], a[252:255], a[220:223], a[60:63]
  ds_read_b64_tr_b16 a[240:241], v28 offset:768
  v_mfma_f32_16x16x32_bf16 a[60:63], a[248:251], a[204:207], a[60:63]
  s_waitcnt lgkmcnt(5)
  v_mfma_f32_16x16x32_bf16 a[64:67], a[224:227], a[192:195], a[64:67]
  ds_read_b64_tr_b16 a[242:243], v28 offset:4896
  v_mfma_f32_16x16x32_bf16 a[64:67], a[228:231], a[208:211], a[64:67]
  s_mov_b64 exec, s[86:87]
  buffer_load_dwordx4 v8, s[36:39], s69 offen offset:1024 lds
  s_mov_b64 exec, -1
  v_mfma_f32_16x16x32_bf16 a[68:71], a[228:231], a[212:215], a[68:71]
  s_add_u32 m0, s74, 52352
  ds_read_b64_tr_b16 a[244:245], v28 offset:23360
  v_mfma_f32_16x16x32_bf16 a[68:71], a[224:227], a[196:199], a[68:71]
  v_mfma_f32_16x16x32_bf16 a[72:75], a[224:227], a[200:203], a[72:75]
  ds_read_b64_tr_b16 a[246:247], v28 offset:27488
  v_mfma_f32_16x16x32_bf16 a[72:75], a[228:231], a[216:219], a[72:75]
  v_mfma_f32_16x16x32_bf16 a[76:79], a[228:231], a[220:223], a[76:79]
  ds_read_b64_tr_b16 a[248:249], v28 offset:776
  v_mfma_f32_16x16x32_bf16 a[76:79], a[224:227], a[204:207], a[76:79]
  s_waitcnt lgkmcnt(5)
  v_mfma_f32_16x16x32_bf16 a[80:83], a[232:235], a[192:195], a[80:83]
  ds_read_b64_tr_b16 a[250:251], v28 offset:4904
  v_mfma_f32_16x16x32_bf16 a[80:83], a[236:239], a[208:211], a[80:83]
  buffer_load_dwordx4 v7, s[40:43], s69 offen offset:0 lds
  v_mfma_f32_16x16x32_bf16 a[84:87], a[236:239], a[212:215], a[84:87]
  ds_read_b64_tr_b16 a[252:253], v28 offset:23368
  v_mfma_f32_16x16x32_bf16 a[84:87], a[232:235], a[196:199], a[84:87]
  ds_read_b64_tr_b16 a[254:255], v28 offset:27496
  v_mfma_f32_16x16x32_bf16 a[88:91], a[232:235], a[200:203], a[88:91]
  v_mfma_f32_16x16x32_bf16 a[88:91], a[236:239], a[216:219], a[88:91]
  ds_read_b64_tr_b16 a[224:225], v29 offset:0
  v_mfma_f32_16x16x32_bf16 a[92:95], a[236:239], a[220:223], a[92:95]
  s_mov_b64 exec, s[86:87]
  buffer_load_dwordx4 v8, s[40:43], s69 offen offset:1024 lds
  s_mov_b64 exec, -1
  v_mfma_f32_16x16x32_bf16 a[92:95], a[232:235], a[204:207], a[92:95]
  s_waitcnt lgkmcnt(5)
  ds_read_b64_tr_b16 a[226:227], v29 offset:3584
  v_mfma_f32_16x16x32_bf16 a[96:99], a[240:243], a[192:195], a[96:99]
  v_mfma_f32_16x16x32_bf16 a[96:99], a[244:247], a[208:211], a[96:99]
  ds_read_b64_tr_b16 a[228:229], v29 offset:22592
  v_mfma_f32_16x16x32_bf16 a[100:103], a[244:247], a[212:215], a[100:103]
  v_mfma_f32_16x16x32_bf16 a[100:103], a[240:243], a[196:199], a[100:103]
  ds_read_b64_tr_b16 a[230:231], v29 offset:26176
  v_mfma_f32_16x16x32_bf16 a[104:107], a[240:243], a[200:203], a[104:107]
  v_mfma_f32_16x16x32_bf16 a[104:107], a[244:247], a[216:219], a[104:107]
  ds_read_b64_tr_b16 a[232:233], v29 offset:7168
  v_mfma_f32_16x16x32_bf16 a[108:111], a[244:247], a[220:223], a[108:111]
  buffer_load_dwordx4 a[160:163], v9, s[48:51], s71 offen offset:0 sc1
  v_mfma_f32_16x16x32_bf16 a[108:111], a[240:243], a[204:207], a[108:111]
  s_waitcnt lgkmcnt(5)
  ds_read_b64_tr_b16 a[234:235], v29 offset:10752
  v_mfma_f32_16x16x32_bf16 a[112:115], a[248:251], a[192:195], a[112:115]
  v_mfma_f32_16x16x32_bf16 a[112:115], a[252:255], a[208:211], a[112:115]
  ds_read_b64_tr_b16 a[236:237], v29 offset:29760
  v_mfma_f32_16x16x32_bf16 a[116:119], a[252:255], a[212:215], a[116:119]
  v_mfma_f32_16x16x32_bf16 a[116:119], a[248:251], a[196:199], a[116:119]
  ds_read_b64_tr_b16 a[238:239], v29 offset:33344
  v_mfma_f32_16x16x32_bf16 a[120:123], a[248:251], a[200:203], a[120:123]
  buffer_load_dwordx4 a[164:167], v9, s[48:51], s71 offen offset:1024 sc1
  v_mfma_f32_16x16x32_bf16 a[120:123], a[252:255], a[216:219], a[120:123]
  ds_read_b64_tr_b16 a[240:241], v29 offset:32
  v_mfma_f32_16x16x32_bf16 a[124:127], a[252:255], a[220:223], a[124:127]
  v_mfma_f32_16x16x32_bf16 a[124:127], a[248:251], a[204:207], a[124:127]
  s_waitcnt lgkmcnt(5)
  ds_read_b64_tr_b16 a[242:243], v29 offset:3616
  v_mfma_f32_16x16x32_bf16 v[32:35], a[224:227], a[192:195], v[32:35]
  ds_read_b64_tr_b16 a[244:245], v29 offset:22624
  v_mfma_f32_16x16x32_bf16 v[32:35], a[228:231], a[208:211], v[32:35]
  v_mfma_f32_16x16x32_bf16 v[36:39], a[228:231], a[212:215], v[36:39]
  ds_read_b64_tr_b16 a[246:247], v29 offset:26208
  v_mfma_f32_16x16x32_bf16 v[36:39], a[224:227], a[196:199], v[36:39]
  v_mfma_f32_16x16x32_bf16 v[40:43], a[224:227], a[200:203], v[40:43]
  ds_read_b64_tr_b16 a[248:249], v29 offset:7200
  v_mfma_f32_16x16x32_bf16 v[40:43], a[228:231], a[216:219], v[40:43]
  buffer_load_dwordx4 a[168:171], v9, s[48:51], s71 offen offset:2048 sc1
  v_mfma_f32_16x16x32_bf16 v[44:47], a[228:231], a[220:223], v[44:47]
  ds_read_b64_tr_b16 a[250:251], v29 offset:10784
  v_mfma_f32_16x16x32_bf16 v[44:47], a[224:227], a[204:207], v[44:47]
  s_waitcnt lgkmcnt(6)
  v_mfma_f32_16x16x32_bf16 v[48:51], a[232:235], a[192:195], v[48:51]
  ds_read_b64_tr_b16 a[252:253], v29 offset:29792
  v_mfma_f32_16x16x32_bf16 v[48:51], a[236:239], a[208:211], v[48:51]
  v_mfma_f32_16x16x32_bf16 v[52:55], a[236:239], a[212:215], v[52:55]
  ds_read_b64_tr_b16 a[254:255], v29 offset:33376
  v_mfma_f32_16x16x32_bf16 v[52:55], a[232:235], a[196:199], v[52:55]
  buffer_load_dwordx4 a[172:175], v9, s[48:51], s71 offen offset:3072 sc1
  v_mfma_f32_16x16x32_bf16 v[56:59], a[232:235], a[200:203], v[56:59]
  s_add_u32 s72, s68, s12
  ds_read_b64_tr_b16 a[224:225], v29 offset:64
  v_mfma_f32_16x16x32_bf16 v[56:59], a[236:239], a[216:219], v[56:59]
  s_and_b32 s72, s72, 7
  v_mfma_f32_16x16x32_bf16 v[60:63], a[236:239], a[220:223], v[60:63]
  s_and_b32 s73, s68, 0xfffffff8
  ds_read_b64_tr_b16 a[226:227], v29 offset:3648
  v_mfma_f32_16x16x32_bf16 v[60:63], a[232:235], a[204:207], v[60:63]
  s_waitcnt lgkmcnt(6)
  s_or_b32 s72, s72, s73
  v_mfma_f32_16x16x32_bf16 v[64:67], a[240:243], a[192:195], v[64:67]
  s_mul_i32 s69, s72, 229376
  ds_read_b64_tr_b16 a[228:229], v29 offset:22656
  v_mfma_f32_16x16x32_bf16 v[64:67], a[244:247], a[208:211], v[64:67]
  s_lshl_b32 s70, s72, 18
  v_mfma_f32_16x16x32_bf16 v[68:71], a[244:247], a[212:215], v[68:71]
  s_lshl_b32 s71, s72, 12
  ds_read_b64_tr_b16 a[230:231], v29 offset:26240
  v_mfma_f32_16x16x32_bf16 v[68:71], a[240:243], a[196:199], v[68:71]
  s_add_u32 s68, s68, 1
  v_mfma_f32_16x16x32_bf16 v[72:75], a[240:243], a[200:203], v[72:75]
  s_add_u32 m0, s75, 67776
  ds_read_b64_tr_b16 a[232:233], v29 offset:7232
  v_mfma_f32_16x16x32_bf16 v[72:75], a[244:247], a[216:219], v[72:75]
  buffer_load_dwordx4 v5, s[44:47], s70 offen offset:0 lds
  v_mfma_f32_16x16x32_bf16 v[76:79], a[244:247], a[220:223], v[76:79]
  ds_read_b64_tr_b16 a[234:235], v29 offset:10816
  v_mfma_f32_16x16x32_bf16 v[76:79], a[240:243], a[204:207], v[76:79]
  s_waitcnt lgkmcnt(6)
  ds_read_b64_tr_b16 a[236:237], v29 offset:29824
  v_mfma_f32_16x16x32_bf16 v[80:83], a[248:251], a[192:195], v[80:83]
  v_mfma_f32_16x16x32_bf16 v[80:83], a[252:255], a[208:211], v[80:83]
  ds_read_b64_tr_b16 a[238:239], v29 offset:33408
  v_mfma_f32_16x16x32_bf16 v[84:87], a[252:255], a[212:215], v[84:87]
  buffer_load_dwordx4 v6, s[44:47], s70 offen offset:1032 lds
  s_add_u32 m0, s74, 67776
  v_mfma_f32_16x16x32_bf16 v[84:87], a[248:251], a[196:199], v[84:87]
  ds_read_b64_tr_b16 a[240:241], v29 offset:96
  v_mfma_f32_16x16x32_bf16 v[88:91], a[248:251], a[200:203], v[88:91]
  v_mfma_f32_16x16x32_bf16 v[88:91], a[252:255], a[216:219], v[88:91]
  ds_read_b64_tr_b16 a[242:243], v29 offset:3680
  v_mfma_f32_16x16x32_bf16 v[92:95], a[252:255], a[220:223], v[92:95]
  v_mfma_f32_16x16x32_bf16 v[92:95], a[248:251], a[204:207], v[92:95]
  s_waitcnt lgkmcnt(6)
  ds_read_b64_tr_b16 a[244:245], v29 offset:22688
  v_mfma_f32_16x16x32_bf16 v[96:99], a[224:227], a[192:195], v[96:99]
  v_mfma_f32_16x16x32_bf16 v[96:99], a[228:231], a[208:211], v[96:99]
  ds_read_b64_tr_b16 a[246:247], v29 offset:26272
  v_mfma_f32_16x16x32_bf16 v[100:103], a[228:231], a[212:215], v[100:103]
  buffer_load_dwordx4 v7, s[36:39], s69 offen offset:0 lds
  v_mfma_f32_16x16x32_bf16 v[100:103], a[224:227], a[196:199], v[100:103]
  ds_read_b64_tr_b16 a[248:249], v29 offset:7264
  v_mfma_f32_16x16x32_bf16 v[104:107], a[224:227], a[200:203], v[104:107]
  v_mfma_f32_16x16x32_bf16 v[104:107], a[228:231], a[216:219], v[104:107]
  ds_read_b64_tr_b16 a[250:251], v29 offset:10848
  v_mfma_f32_16x16x32_bf16 v[108:111], a[228:231], a[220:223], v[108:111]
  v_mfma_f32_16x16x32_bf16 v[108:111], a[224:227], a[204:207], v[108:111]
  s_waitcnt lgkmcnt(6)
  ds_read_b64_tr_b16 a[252:253], v29 offset:29856
  v_mfma_f32_16x16x32_bf16 v[112:115], a[232:235], a[192:195], v[112:115]
  s_mov_b64 exec, s[86:87]
  buffer_load_dwordx4 v8, s[36:39], s69 offen offset:1024 lds
  s_mov_b64 exec, -1
  v_mfma_f32_16x16x32_bf16 v[112:115], a[236:239], a[208:211], v[112:115]
  s_add_u32 m0, s74, 74944
  ds_read_b64_tr_b16 a[254:255], v29 offset:33440
  v_mfma_f32_16x16x32_bf16 v[116:119], a[236:239], a[212:215], v[116:119]
  v_mfma_f32_16x16x32_bf16 v[116:119], a[232:235], a[196:199], v[116:119]
  ds_read_b64_tr_b16 a[224:225], v29 offset:128
  v_mfma_f32_16x16x32_bf16 v[120:123], a[232:235], a[200:203], v[120:123]
  ds_read_b64_tr_b16 a[226:227], v29 offset:3712
  v_mfma_f32_16x16x32_bf16 v[120:123], a[236:239], a[216:219], v[120:123]
  v_mfma_f32_16x16x32_bf16 v[124:127], a[236:239], a[220:223], v[124:127]
  ds_read_b64_tr_b16 a[228:229], v29 offset:22720
  v_mfma_f32_16x16x32_bf16 v[124:127], a[232:235], a[204:207], v[124:127]
  s_waitcnt lgkmcnt(7)
  v_mfma_f32_16x16x32_bf16 v[128:131], a[240:243], a[192:195], v[128:131]
  ds_read_b64_tr_b16 a[230:231], v29 offset:26304
  v_mfma_f32_16x16x32_bf16 v[128:131], a[244:247], a[208:211], v[128:131]
  buffer_load_dwordx4 v7, s[40:43], s69 offen offset:0 lds
  v_mfma_f32_16x16x32_bf16 v[132:135], a[244:247], a[212:215], v[132:135]
  ds_read_b64_tr_b16 a[232:233], v29 offset:7296
  v_mfma_f32_16x16x32_bf16 v[132:135], a[240:243], a[196:199], v[132:135]
  v_mfma_f32_16x16x32_bf16 v[136:139], a[240:243], a[200:203], v[136:139]
  ds_read_b64_tr_b16 a[234:235], v29 offset:10880
  v_mfma_f32_16x16x32_bf16 v[136:139], a[244:247], a[216:219], v[136:139]
  v_mfma_f32_16x16x32_bf16 v[140:143], a[244:247], a[220:223], v[140:143]
  ds_read_b64_tr_b16 a[236:237], v29 offset:29888
  v_mfma_f32_16x16x32_bf16 v[140:143], a[240:243], a[204:207], v[140:143]
  s_waitcnt lgkmcnt(7)
  v_mfma_f32_16x16x32_bf16 v[144:147], a[248:251], a[192:195], v[144:147]
  ds_read_b64_tr_b16 a[238:239], v29 offset:33472
  v_mfma_f32_16x16x32_bf16 v[144:147], a[252:255], a[208:211], v[144:147]
  s_mov_b64 exec, s[86:87]
  buffer_load_dwordx4 v8, s[40:43], s69 offen offset:1024 lds
  s_mov_b64 exec, -1
  v_mfma_f32_16x16x32_bf16 v[148:151], a[252:255], a[212:215], v[148:151]
  ds_read_b64_tr_b16 a[240:241], v29 offset:160
  v_mfma_f32_16x16x32_bf16 v[148:151], a[248:251], a[196:199], v[148:151]
  v_mfma_f32_16x16x32_bf16 v[152:155], a[248:251], a[200:203], v[152:155]
  ds_read_b64_tr_b16 a[242:243], v29 offset:3744
  v_mfma_f32_16x16x32_bf16 v[152:155], a[252:255], a[216:219], v[152:155]
  v_mfma_f32_16x16x32_bf16 v[156:159], a[252:255], a[220:223], v[156:159]
  ds_read_b64_tr_b16 a[244:245], v29 offset:22752
  buffer_load_dwordx4 a[176:179], v9, s[48:51], s71 offen offset:0 sc1
  v_mfma_f32_16x16x32_bf16 v[156:159], a[248:251], a[204:207], v[156:159]
  s_waitcnt lgkmcnt(7)
  v_mfma_f32_16x16x32_bf16 v[160:163], a[224:227], a[192:195], v[160:163]
  ds_read_b64_tr_b16 a[246:247], v29 offset:26336
  v_mfma_f32_16x16x32_bf16 v[160:163], a[228:231], a[208:211], v[160:163]
  ds_read_b64_tr_b16 a[248:249], v29 offset:7328
  v_mfma_f32_16x16x32_bf16 v[164:167], a[228:231], a[212:215], v[164:167]
  v_mfma_f32_16x16x32_bf16 v[164:167], a[224:227], a[196:199], v[164:167]
  ds_read_b64_tr_b16 a[250:251], v29 offset:10912
  v_mfma_f32_16x16x32_bf16 v[168:171], a[224:227], a[200:203], v[168:171]
  v_mfma_f32_16x16x32_bf16 v[168:171], a[228:231], a[216:219], v[168:171]
  ds_read_b64_tr_b16 a[252:253], v29 offset:29920
  v_mfma_f32_16x16x32_bf16 v[172:175], a[228:231], a[220:223], v[172:175]
  buffer_load_dwordx4 a[180:183], v9, s[48:51], s71 offen offset:1024 sc1
  v_mfma_f32_16x16x32_bf16 v[172:175], a[224:227], a[204:207], v[172:175]
  s_waitcnt lgkmcnt(7)
  ds_read_b64_tr_b16 a[254:255], v29 offset:33504
  v_mfma_f32_16x16x32_bf16 v[176:179], a[232:235], a[192:195], v[176:179]
  v_mfma_f32_16x16x32_bf16 v[176:179], a[236:239], a[208:211], v[176:179]
  ds_read_b64_tr_b16 a[224:225], v29 offset:192
  v_mfma_f32_16x16x32_bf16 v[180:183], a[236:239], a[212:215], v[180:183]
  v_mfma_f32_16x16x32_bf16 v[180:183], a[232:235], a[196:199], v[180:183]
  ds_read_b64_tr_b16 a[226:227], v29 offset:3776
  v_mfma_f32_16x16x32_bf16 v[184:187], a[232:235], a[200:203], v[184:187]
  buffer_load_dwordx4 a[184:187], v9, s[48:51], s71 offen offset:2048 sc1
  v_mfma_f32_16x16x32_bf16 v[184:187], a[236:239], a[216:219], v[184:187]
  ds_read_b64_tr_b16 a[228:229], v29 offset:22784
  v_mfma_f32_16x16x32_bf16 v[188:191], a[236:239], a[220:223], v[188:191]
  v_mfma_f32_16x16x32_bf16 v[188:191], a[232:235], a[204:207], v[188:191]
  s_waitcnt lgkmcnt(7)
  ds_read_b64_tr_b16 a[230:231], v29 offset:26368
  v_mfma_f32_16x16x32_bf16 v[192:195], a[240:243], a[192:195], v[192:195]
  v_mfma_f32_16x16x32_bf16 v[192:195], a[244:247], a[208:211], v[192:195]
  ds_read_b64_tr_b16 a[232:233], v29 offset:7360
  v_mfma_f32_16x16x32_bf16 v[196:199], a[244:247], a[212:215], v[196:199]
  v_mfma_f32_16x16x32_bf16 v[196:199], a[240:243], a[196:199], v[196:199]
  ds_read_b64_tr_b16 a[234:235], v29 offset:10944
  v_mfma_f32_16x16x32_bf16 v[200:203], a[240:243], a[200:203], v[200:203]
  buffer_load_dwordx4 a[188:191], v9, s[48:51], s71 offen offset:3072 sc1
  v_mfma_f32_16x16x32_bf16 v[200:203], a[244:247], a[216:219], v[200:203]
  ds_read_b64_tr_b16 a[236:237], v29 offset:29952
  v_mfma_f32_16x16x32_bf16 v[204:207], a[244:247], a[220:223], v[204:207]
  v_mfma_f32_16x16x32_bf16 v[204:207], a[240:243], a[204:207], v[204:207]
  s_waitcnt lgkmcnt(7)
  ds_read_b64_tr_b16 a[238:239], v29 offset:33536
  v_mfma_f32_16x16x32_bf16 v[208:211], a[248:251], a[192:195], v[208:211]
  v_mfma_f32_16x16x32_bf16 v[208:211], a[252:255], a[208:211], v[208:211]
  v_mfma_f32_16x16x32_bf16 v[212:215], a[252:255], a[212:215], v[212:215]
  v_mfma_f32_16x16x32_bf16 v[212:215], a[248:251], a[196:199], v[212:215]
  v_mfma_f32_16x16x32_bf16 v[216:219], a[248:251], a[200:203], v[216:219]
  v_mfma_f32_16x16x32_bf16 v[216:219], a[252:255], a[216:219], v[216:219]
  v_mfma_f32_16x16x32_bf16 v[220:223], a[252:255], a[220:223], v[220:223]
  v_mfma_f32_16x16x32_bf16 v[220:223], a[248:251], a[204:207], v[220:223]
  s_waitcnt lgkmcnt(4)
  s_waitcnt lgkmcnt(0)
  s_waitcnt vmcnt(20)
  s_barrier
  v_mfma_f32_16x16x32_bf16 v[224:227], a[224:227], a[192:195], v[224:227]
  ds_read_b64_tr_b16 a[240:241], v1 offset:0
  v_mfma_f32_16x16x32_bf16 v[224:227], a[228:231], a[208:211], v[224:227]
  v_mfma_f32_16x16x32_bf16 v[228:231], a[228:231], a[212:215], v[228:231]
  ds_read_b64_tr_b16 a[242:243], v1 offset:4128
  v_mfma_f32_16x16x32_bf16 v[228:231], a[224:227], a[196:199], v[228:231]
  v_mfma_f32_16x16x32_bf16 v[232:235], a[224:227], a[200:203], v[232:235]
  ds_read_b64_tr_b16 a[244:245], v1 offset:22592
  v_mfma_f32_16x16x32_bf16 v[232:235], a[228:231], a[216:219], v[232:235]
  v_mfma_f32_16x16x32_bf16 v[236:239], a[228:231], a[220:223], v[236:239]
  ds_read_b64_tr_b16 a[246:247], v1 offset:26720
  v_mfma_f32_16x16x32_bf16 v[236:239], a[224:227], a[204:207], v[236:239]
  s_waitcnt lgkmcnt(4)
  v_mfma_f32_16x16x32_bf16 v[240:243], a[232:235], a[192:195], v[240:243]
  ds_read_b64_tr_b16 a[248:249], v1 offset:8
  v_mfma_f32_16x16x32_bf16 v[240:243], a[236:239], a[208:211], v[240:243]
  v_mfma_f32_16x16x32_bf16 v[244:247], a[236:239], a[212:215], v[244:247]
  ds_read_b64_tr_b16 a[250:251], v1 offset:4136
  v_mfma_f32_16x16x32_bf16 v[244:247], a[232:235], a[196:199], v[244:247]
  v_mfma_f32_16x16x32_bf16 v[248:251], a[232:235], a[200:203], v[248:251]
  ds_read_b64_tr_b16 a[252:253], v1 offset:22600
  v_mfma_f32_16x16x32_bf16 v[248:251], a[236:239], a[216:219], v[248:251]
  v_mfma_f32_16x16x32_bf16 v[252:255], a[236:239], a[220:223], v[252:255]
  ds_read_b64_tr_b16 a[254:255], v1 offset:26728
  v_mfma_f32_16x16x32_bf16 v[252:255], a[232:235], a[204:207], v[252:255]
  s_waitcnt lgkmcnt(4)
  v_mfma_f32_16x16x32_bf16 a[0:3], a[240:243], a[128:131], a[0:3]
  s_add_u32 s72, s68, s12
  ds_read_b64_tr_b16 a[224:225], v1 offset:256
  v_mfma_f32_16x16x32_bf16 a[0:3], a[244:247], a[144:147], a[0:3]
  s_and_b32 s72, s72, 7
  ds_read_b64_tr_b16 a[226:227], v1 offset:4384
  v_mfma_f32_16x16x32_bf16 a[4:7], a[244:247], a[148:151], a[4:7]
  s_and_b32 s73, s68, 0xfffffff8
  v_mfma_f32_16x16x32_bf16 a[4:7], a[240:243], a[132:135], a[4:7]
  s_or_b32 s72, s72, s73
  ds_read_b64_tr_b16 a[228:229], v1 offset:22848
  v_mfma_f32_16x16x32_bf16 a[8:11], a[240:243], a[136:139], a[8:11]
  s_mul_i32 s69, s72, 229376
  v_mfma_f32_16x16x32_bf16 a[8:11], a[244:247], a[152:155], a[8:11]
  s_lshl_b32 s70, s72, 18
  ds_read_b64_tr_b16 a[230:231], v1 offset:26976
  buffer_load_dwordx4 v[24:27], v21, s[4:7], s3 offen
  v_mfma_f32_16x16x32_bf16 a[12:15], a[244:247], a[156:159], a[12:15]
  s_lshl_b32 s71, s72, 12
  v_mfma_f32_16x16x32_bf16 a[12:15], a[240:243], a[140:143], a[12:15]
  s_waitcnt lgkmcnt(4)
  s_add_u32 s68, s68, 1
  ds_read_b64_tr_b16 a[232:233], v1 offset:264
  v_mfma_f32_16x16x32_bf16 a[16:19], a[248:251], a[128:131], a[16:19]
  s_add_u32 m0, s75, 90368
  v_mfma_f32_16x16x32_bf16 a[16:19], a[252:255], a[144:147], a[16:19]
  ds_read_b64_tr_b16 a[234:235], v1 offset:4392
  v_mfma_f32_16x16x32_bf16 a[20:23], a[252:255], a[148:151], a[20:23]
  buffer_load_dwordx4 v5, s[44:47], s70 offen offset:0 lds
  v_mfma_f32_16x16x32_bf16 a[20:23], a[248:251], a[132:135], a[20:23]
  ds_read_b64_tr_b16 a[236:237], v1 offset:22856
  v_mfma_f32_16x16x32_bf16 a[24:27], a[248:251], a[136:139], a[24:27]
  v_mfma_f32_16x16x32_bf16 a[24:27], a[252:255], a[152:155], a[24:27]
  ds_read_b64_tr_b16 a[238:239], v1 offset:26984
  v_mfma_f32_16x16x32_bf16 a[28:31], a[252:255], a[156:159], a[28:31]
  v_mfma_f32_16x16x32_bf16 a[28:31], a[248:251], a[140:143], a[28:31]
  s_waitcnt lgkmcnt(4)
  ds_read_b64_tr_b16 a[240:241], v1 offset:512
  v_mfma_f32_16x16x32_bf16 a[32:35], a[224:227], a[128:131], a[32:35]
  v_mfma_f32_16x16x32_bf16 a[32:35], a[228:231], a[144:147], a[32:35]
  ds_read_b64_tr_b16 a[242:243], v1 offset:4640
  v_mfma_f32_16x16x32_bf16 a[36:39], a[228:231], a[148:151], a[36:39]
  buffer_load_dwordx4 v6, s[44:47], s70 offen offset:1032 lds
  s_add_u32 m0, s74, 90368
  v_mfma_f32_16x16x32_bf16 a[36:39], a[224:227], a[132:135], a[36:39]
  ds_read_b64_tr_b16 a[244:245], v1 offset:23104
  v_mfma_f32_16x16x32_bf16 a[40:43], a[224:227], a[136:139], a[40:43]
  v_mfma_f32_16x16x32_bf16 a[40:43], a[228:231], a[152:155], a[40:43]
  ds_read_b64_tr_b16 a[246:247], v1 offset:27232
  v_mfma_f32_16x16x32_bf16 a[44:47], a[228:231], a[156:159], a[44:47]
  ds_read_b64_tr_b16 a[248:249], v1 offset:520
  v_mfma_f32_16x16x32_bf16 a[44:47], a[224:227], a[140:143], a[44:47]
  s_waitcnt lgkmcnt(5)
  v_mfma_f32_16x16x32_bf16 a[48:51], a[232:235], a[128:131], a[48:51]
  ds_read_b64_tr_b16 a[250:251], v1 offset:4648
  v_mfma_f32_16x16x32_bf16 a[48:51], a[236:239], a[144:147], a[48:51]
  buffer_load_dwordx4 v7, s[36:39], s69 offen offset:0 lds
  v_mfma_f32_16x16x32_bf16 a[52:55], a[236:239], a[148:151], a[52:55]
  ds_read_b64_tr_b16 a[252:253], v1 offset:23112
  v_mfma_f32_16x16x32_bf16 a[52:55], a[232:235], a[132:135], a[52:55]
  v_mfma_f32_16x16x32_bf16 a[56:59], a[232:235], a[136:139], a[56:59]
  ds_read_b64_tr_b16 a[254:255], v1 offset:27240
  v_mfma_f32_16x16x32_bf16 a[56:59], a[236:239], a[152:155], a[56:59]
  v_mfma_f32_16x16x32_bf16 a[60:63], a[236:239], a[156:159], a[60:63]
  ds_read_b64_tr_b16 a[224:225], v1 offset:768
  v_mfma_f32_16x16x32_bf16 a[60:63], a[232:235], a[140:143], a[60:63]
  s_waitcnt lgkmcnt(5)
  v_mfma_f32_16x16x32_bf16 a[64:67], a[240:243], a[128:131], a[64:67]
  ds_read_b64_tr_b16 a[226:227], v1 offset:4896
  v_mfma_f32_16x16x32_bf16 a[64:67], a[244:247], a[144:147], a[64:67]
  s_mov_b64 exec, s[86:87]
  buffer_load_dwordx4 v8, s[36:39], s69 offen offset:1024 lds
  s_mov_b64 exec, -1
  v_mfma_f32_16x16x32_bf16 a[68:71], a[244:247], a[148:151], a[68:71]
  s_add_u32 m0, s74, 97536
  ds_read_b64_tr_b16 a[228:229], v1 offset:23360
  v_mfma_f32_16x16x32_bf16 a[68:71], a[240:243], a[132:135], a[68:71]
  v_mfma_f32_16x16x32_bf16 a[72:75], a[240:243], a[136:139], a[72:75]
  ds_read_b64_tr_b16 a[230:231], v1 offset:27488
  v_mfma_f32_16x16x32_bf16 a[72:75], a[244:247], a[152:155], a[72:75]
  v_mfma_f32_16x16x32_bf16 a[76:79], a[244:247], a[156:159], a[76:79]
  ds_read_b64_tr_b16 a[232:233], v1 offset:776
  v_mfma_f32_16x16x32_bf16 a[76:79], a[240:243], a[140:143], a[76:79]
  s_waitcnt lgkmcnt(5)
  v_mfma_f32_16x16x32_bf16 a[80:83], a[248:251], a[128:131], a[80:83]
  ds_read_b64_tr_b16 a[234:235], v1 offset:4904
  v_mfma_f32_16x16x32_bf16 a[80:83], a[252:255], a[144:147], a[80:83]
  buffer_load_dwordx4 v7, s[40:43], s69 offen offset:0 lds
  v_mfma_f32_16x16x32_bf16 a[84:87], a[252:255], a[148:151], a[84:87]
  ds_read_b64_tr_b16 a[236:237], v1 offset:23368
  v_mfma_f32_16x16x32_bf16 a[84:87], a[248:251], a[132:135], a[84:87]
  ds_read_b64_tr_b16 a[238:239], v1 offset:27496
  v_mfma_f32_16x16x32_bf16 a[88:91], a[248:251], a[136:139], a[88:91]
  v_mfma_f32_16x16x32_bf16 a[88:91], a[252:255], a[152:155], a[88:91]
  ds_read_b64_tr_b16 a[240:241], v3 offset:0
  v_mfma_f32_16x16x32_bf16 a[92:95], a[252:255], a[156:159], a[92:95]
  s_mov_b64 exec, s[86:87]
  buffer_load_dwordx4 v8, s[40:43], s69 offen offset:1024 lds
  s_mov_b64 exec, -1
  v_mfma_f32_16x16x32_bf16 a[92:95], a[248:251], a[140:143], a[92:95]
  s_waitcnt lgkmcnt(5)
  ds_read_b64_tr_b16 a[242:243], v3 offset:3584
  v_mfma_f32_16x16x32_bf16 a[96:99], a[224:227], a[128:131], a[96:99]
  v_mfma_f32_16x16x32_bf16 a[96:99], a[228:231], a[144:147], a[96:99]
  ds_read_b64_tr_b16 a[244:245], v3 offset:22592
  v_mfma_f32_16x16x32_bf16 a[100:103], a[228:231], a[148:151], a[100:103]
  v_mfma_f32_16x16x32_bf16 a[100:103], a[224:227], a[132:135], a[100:103]
  ds_read_b64_tr_b16 a[246:247], v3 offset:26176
  v_mfma_f32_16x16x32_bf16 a[104:107], a[224:227], a[136:139], a[104:107]
  v_mfma_f32_16x16x32_bf16 a[104:107], a[228:231], a[152:155], a[104:107]
  ds_read_b64_tr_b16 a[248:249], v3 offset:7168
  v_mfma_f32_16x16x32_bf16 a[108:111], a[228:231], a[156:159], a[108:111]
  buffer_load_dwordx4 a[192:195], v9, s[48:51], s71 offen offset:0 sc1
  v_mfma_f32_16x16x32_bf16 a[108:111], a[224:227], a[140:143], a[108:111]
  s_waitcnt lgkmcnt(5)
  ds_read_b64_tr_b16 a[250:251], v3 offset:10752
  v_mfma_f32_16x16x32_bf16 a[112:115], a[232:235], a[128:131], a[112:115]
  v_mfma_f32_16x16x32_bf16 a[112:115], a[236:239], a[144:147], a[112:115]
  ds_read_b64_tr_b16 a[252:253], v3 offset:29760
  v_mfma_f32_16x16x32_bf16 a[116:119], a[236:239], a[148:151], a[116:119]
  v_mfma_f32_16x16x32_bf16 a[116:119], a[232:235], a[132:135], a[116:119]
  ds_read_b64_tr_b16 a[254:255], v3 offset:33344
  v_mfma_f32_16x16x32_bf16 a[120:123], a[232:235], a[136:139], a[120:123]
  buffer_load_dwordx4 a[196:199], v9, s[48:51], s71 offen offset:1024 sc1
  v_mfma_f32_16x16x32_bf16 a[120:123], a[236:239], a[152:155], a[120:123]
  ds_read_b64_tr_b16 a[224:225], v3 offset:32
  v_mfma_f32_16x16x32_bf16 a[124:127], a[236:239], a[156:159], a[124:127]
  v_mfma_f32_16x16x32_bf16 a[124:127], a[232:235], a[140:143], a[124:127]
  s_waitcnt lgkmcnt(5)
  ds_read_b64_tr_b16 a[226:227], v3 offset:3616
  v_mfma_f32_16x16x32_bf16 v[32:35], a[240:243], a[128:131], v[32:35]
  ds_read_b64_tr_b16 a[228:229], v3 offset:22624
  v_mfma_f32_16x16x32_bf16 v[32:35], a[244:247], a[144:147], v[32:35]
  v_mfma_f32_16x16x32_bf16 v[36:39], a[244:247], a[148:151], v[36:39]
  ds_read_b64_tr_b16 a[230:231], v3 offset:26208
  v_mfma_f32_16x16x32_bf16 v[36:39], a[240:243], a[132:135], v[36:39]
  v_mfma_f32_16x16x32_bf16 v[40:43], a[240:243], a[136:139], v[40:43]
  ds_read_b64_tr_b16 a[232:233], v3 offset:7200
  v_mfma_f32_16x16x32_bf16 v[40:43], a[244:247], a[152:155], v[40:43]
  buffer_load_dwordx4 a[200:203], v9, s[48:51], s71 offen offset:2048 sc1
  v_mfma_f32_16x16x32_bf16 v[44:47], a[244:247], a[156:159], v[44:47]
  ds_read_b64_tr_b16 a[234:235], v3 offset:10784
  v_mfma_f32_16x16x32_bf16 v[44:47], a[240:243], a[140:143], v[44:47]
  s_waitcnt lgkmcnt(6)
  v_mfma_f32_16x16x32_bf16 v[48:51], a[248:251], a[128:131], v[48:51]
  ds_read_b64_tr_b16 a[236:237], v3 offset:29792
  v_mfma_f32_16x16x32_bf16 v[48:51], a[252:255], a[144:147], v[48:51]
  v_mfma_f32_16x16x32_bf16 v[52:55], a[252:255], a[148:151], v[52:55]
  ds_read_b64_tr_b16 a[238:239], v3 offset:33376
  v_mfma_f32_16x16x32_bf16 v[52:55], a[248:251], a[132:135], v[52:55]
  buffer_load_dwordx4 a[204:207], v9, s[48:51], s71 offen offset:3072 sc1
  v_mfma_f32_16x16x32_bf16 v[56:59], a[248:251], a[136:139], v[56:59]
  s_add_u32 s72, s68, s12
  ds_read_b64_tr_b16 a[240:241], v3 offset:64
  v_mfma_f32_16x16x32_bf16 v[56:59], a[252:255], a[152:155], v[56:59]
  s_and_b32 s72, s72, 7
  v_mfma_f32_16x16x32_bf16 v[60:63], a[252:255], a[156:159], v[60:63]
  s_and_b32 s73, s68, 0xfffffff8
  ds_read_b64_tr_b16 a[242:243], v3 offset:3648
  v_mfma_f32_16x16x32_bf16 v[60:63], a[248:251], a[140:143], v[60:63]
  s_waitcnt lgkmcnt(6)
  s_or_b32 s72, s72, s73
  v_mfma_f32_16x16x32_bf16 v[64:67], a[224:227], a[128:131], v[64:67]
  s_mul_i32 s69, s72, 229376
  ds_read_b64_tr_b16 a[244:245], v3 offset:22656
  v_mfma_f32_16x16x32_bf16 v[64:67], a[228:231], a[144:147], v[64:67]
  s_lshl_b32 s70, s72, 18
  v_mfma_f32_16x16x32_bf16 v[68:71], a[228:231], a[148:151], v[68:71]
  s_lshl_b32 s71, s72, 12
  ds_read_b64_tr_b16 a[246:247], v3 offset:26240
  v_mfma_f32_16x16x32_bf16 v[68:71], a[224:227], a[132:135], v[68:71]
  s_add_u32 s68, s68, 1
  v_mfma_f32_16x16x32_bf16 v[72:75], a[224:227], a[136:139], v[72:75]
  s_add_u32 m0, s75, 112960
  ds_read_b64_tr_b16 a[248:249], v3 offset:7232
  v_mfma_f32_16x16x32_bf16 v[72:75], a[228:231], a[152:155], v[72:75]
  buffer_load_dwordx4 v5, s[44:47], s70 offen offset:0 lds
  v_mfma_f32_16x16x32_bf16 v[76:79], a[228:231], a[156:159], v[76:79]
  ds_read_b64_tr_b16 a[250:251], v3 offset:10816
  v_mfma_f32_16x16x32_bf16 v[76:79], a[224:227], a[140:143], v[76:79]
  s_waitcnt lgkmcnt(6)
  ds_read_b64_tr_b16 a[252:253], v3 offset:29824
  v_mfma_f32_16x16x32_bf16 v[80:83], a[232:235], a[128:131], v[80:83]
  v_mfma_f32_16x16x32_bf16 v[80:83], a[236:239], a[144:147], v[80:83]
  ds_read_b64_tr_b16 a[254:255], v3 offset:33408
  v_mfma_f32_16x16x32_bf16 v[84:87], a[236:239], a[148:151], v[84:87]
  buffer_load_dwordx4 v6, s[44:47], s70 offen offset:1032 lds
  s_add_u32 m0, s74, 112960
  v_mfma_f32_16x16x32_bf16 v[84:87], a[232:235], a[132:135], v[84:87]
  ds_read_b64_tr_b16 a[224:225], v3 offset:96
  v_mfma_f32_16x16x32_bf16 v[88:91], a[232:235], a[136:139], v[88:91]
  v_mfma_f32_16x16x32_bf16 v[88:91], a[236:239], a[152:155], v[88:91]
  ds_read_b64_tr_b16 a[226:227], v3 offset:3680
  v_mfma_f32_16x16x32_bf16 v[92:95], a[236:239], a[156:159], v[92:95]
  v_mfma_f32_16x16x32_bf16 v[92:95], a[232:235], a[140:143], v[92:95]
  s_waitcnt lgkmcnt(6)
  ds_read_b64_tr_b16 a[228:229], v3 offset:22688
  v_mfma_f32_16x16x32_bf16 v[96:99], a[240:243], a[128:131], v[96:99]
  v_mfma_f32_16x16x32_bf16 v[96:99], a[244:247], a[144:147], v[96:99]
  ds_read_b64_tr_b16 a[230:231], v3 offset:26272
  v_mfma_f32_16x16x32_bf16 v[100:103], a[244:247], a[148:151], v[100:103]
  buffer_load_dwordx4 v7, s[36:39], s69 offen offset:0 lds
  v_mfma_f32_16x16x32_bf16 v[100:103], a[240:243], a[132:135], v[100:103]
  ds_read_b64_tr_b16 a[232:233], v3 offset:7264
  v_mfma_f32_16x16x32_bf16 v[104:107], a[240:243], a[136:139], v[104:107]
  v_mfma_f32_16x16x32_bf16 v[104:107], a[244:247], a[152:155], v[104:107]
  ds_read_b64_tr_b16 a[234:235], v3 offset:10848
  v_mfma_f32_16x16x32_bf16 v[108:111], a[244:247], a[156:159], v[108:111]
  v_mfma_f32_16x16x32_bf16 v[108:111], a[240:243], a[140:143], v[108:111]
  s_waitcnt lgkmcnt(6)
  ds_read_b64_tr_b16 a[236:237], v3 offset:29856
  v_mfma_f32_16x16x32_bf16 v[112:115], a[248:251], a[128:131], v[112:115]
  s_mov_b64 exec, s[86:87]
  buffer_load_dwordx4 v8, s[36:39], s69 offen offset:1024 lds
  s_mov_b64 exec, -1
  v_mfma_f32_16x16x32_bf16 v[112:115], a[252:255], a[144:147], v[112:115]
  s_add_u32 m0, s74, 120128
  ds_read_b64_tr_b16 a[238:239], v3 offset:33440
  v_mfma_f32_16x16x32_bf16 v[116:119], a[252:255], a[148:151], v[116:119]
  v_mfma_f32_16x16x32_bf16 v[116:119], a[248:251], a[132:135], v[116:119]
  ds_read_b64_tr_b16 a[240:241], v3 offset:128
  v_mfma_f32_16x16x32_bf16 v[120:123], a[248:251], a[136:139], v[120:123]
  ds_read_b64_tr_b16 a[242:243], v3 offset:3712
  v_mfma_f32_16x16x32_bf16 v[120:123], a[252:255], a[152:155], v[120:123]
  v_mfma_f32_16x16x32_bf16 v[124:127], a[252:255], a[156:159], v[124:127]
  ds_read_b64_tr_b16 a[244:245], v3 offset:22720
  v_mfma_f32_16x16x32_bf16 v[124:127], a[248:251], a[140:143], v[124:127]
  s_waitcnt lgkmcnt(7)
  v_mfma_f32_16x16x32_bf16 v[128:131], a[224:227], a[128:131], v[128:131]
  ds_read_b64_tr_b16 a[246:247], v3 offset:26304
  v_mfma_f32_16x16x32_bf16 v[128:131], a[228:231], a[144:147], v[128:131]
  buffer_load_dwordx4 v7, s[40:43], s69 offen offset:0 lds
  v_mfma_f32_16x16x32_bf16 v[132:135], a[228:231], a[148:151], v[132:135]
  ds_read_b64_tr_b16 a[248:249], v3 offset:7296
  v_mfma_f32_16x16x32_bf16 v[132:135], a[224:227], a[132:135], v[132:135]
  v_mfma_f32_16x16x32_bf16 v[136:139], a[224:227], a[136:139], v[136:139]
  ds_read_b64_tr_b16 a[250:251], v3 offset:10880
  v_mfma_f32_16x16x32_bf16 v[136:139], a[228:231], a[152:155], v[136:139]
  v_mfma_f32_16x16x32_bf16 v[140:143], a[228:231], a[156:159], v[140:143]
  ds_read_b64_tr_b16 a[252:253], v3 offset:29888
  v_mfma_f32_16x16x32_bf16 v[140:143], a[224:227], a[140:143], v[140:143]
  s_waitcnt lgkmcnt(7)
  v_mfma_f32_16x16x32_bf16 v[144:147], a[232:235], a[128:131], v[144:147]
  ds_read_b64_tr_b16 a[254:255], v3 offset:33472
  v_mfma_f32_16x16x32_bf16 v[144:147], a[236:239], a[144:147], v[144:147]
  s_mov_b64 exec, s[86:87]
  buffer_load_dwordx4 v8, s[40:43], s69 offen offset:1024 lds
  s_mov_b64 exec, -1
  v_mfma_f32_16x16x32_bf16 v[148:151], a[236:239], a[148:151], v[148:151]
  ds_read_b64_tr_b16 a[224:225], v3 offset:160
  v_mfma_f32_16x16x32_bf16 v[148:151], a[232:235], a[132:135], v[148:151]
  v_mfma_f32_16x16x32_bf16 v[152:155], a[232:235], a[136:139], v[152:155]
  ds_read_b64_tr_b16 a[226:227], v3 offset:3744
  v_mfma_f32_16x16x32_bf16 v[152:155], a[236:239], a[152:155], v[152:155]
  v_mfma_f32_16x16x32_bf16 v[156:159], a[236:239], a[156:159], v[156:159]
  ds_read_b64_tr_b16 a[228:229], v3 offset:22752
  buffer_load_dwordx4 a[208:211], v9, s[48:51], s71 offen offset:0 sc1
  v_mfma_f32_16x16x32_bf16 v[156:159], a[232:235], a[140:143], v[156:159]
  s_waitcnt lgkmcnt(7)
  v_mfma_f32_16x16x32_bf16 v[160:163], a[240:243], a[128:131], v[160:163]
  ds_read_b64_tr_b16 a[230:231], v3 offset:26336
  v_mfma_f32_16x16x32_bf16 v[160:163], a[244:247], a[144:147], v[160:163]
  ds_read_b64_tr_b16 a[232:233], v3 offset:7328
  v_mfma_f32_16x16x32_bf16 v[164:167], a[244:247], a[148:151], v[164:167]
  v_mfma_f32_16x16x32_bf16 v[164:167], a[240:243], a[132:135], v[164:167]
  ds_read_b64_tr_b16 a[234:235], v3 offset:10912
  v_mfma_f32_16x16x32_bf16 v[168:171], a[240:243], a[136:139], v[168:171]
  v_mfma_f32_16x16x32_bf16 v[168:171], a[244:247], a[152:155], v[168:171]
  ds_read_b64_tr_b16 a[236:237], v3 offset:29920
  v_mfma_f32_16x16x32_bf16 v[172:175], a[244:247], a[156:159], v[172:175]
  buffer_load_dwordx4 a[212:215], v9, s[48:51], s71 offen offset:1024 sc1
  v_mfma_f32_16x16x32_bf16 v[172:175], a[240:243], a[140:143], v[172:175]
  s_waitcnt lgkmcnt(7)
  ds_read_b64_tr_b16 a[238:239], v3 offset:33504
  v_mfma_f32_16x16x32_bf16 v[176:179], a[248:251], a[128:131], v[176:179]
  v_mfma_f32_16x16x32_bf16 v[176:179], a[252:255], a[144:147], v[176:179]
  ds_read_b64_tr_b16 a[240:241], v3 offset:192
  v_mfma_f32_16x16x32_bf16 v[180:183], a[252:255], a[148:151], v[180:183]
  v_mfma_f32_16x16x32_bf16 v[180:183], a[248:251], a[132:135], v[180:183]
  ds_read_b64_tr_b16 a[242:243], v3 offset:3776
  v_mfma_f32_16x16x32_bf16 v[184:187], a[248:251], a[136:139], v[184:187]
  buffer_load_dwordx4 a[216:219], v9, s[48:51], s71 offen offset:2048 sc1
  v_mfma_f32_16x16x32_bf16 v[184:187], a[252:255], a[152:155], v[184:187]
  ds_read_b64_tr_b16 a[244:245], v3 offset:22784
  v_mfma_f32_16x16x32_bf16 v[188:191], a[252:255], a[156:159], v[188:191]
  v_mfma_f32_16x16x32_bf16 v[188:191], a[248:251], a[140:143], v[188:191]
  s_waitcnt lgkmcnt(7)
  ds_read_b64_tr_b16 a[246:247], v3 offset:26368
  v_mfma_f32_16x16x32_bf16 v[192:195], a[224:227], a[128:131], v[192:195]
  v_mfma_f32_16x16x32_bf16 v[192:195], a[228:231], a[144:147], v[192:195]
  ds_read_b64_tr_b16 a[248:249], v3 offset:7360
  v_mfma_f32_16x16x32_bf16 v[196:199], a[228:231], a[148:151], v[196:199]
  v_mfma_f32_16x16x32_bf16 v[196:199], a[224:227], a[132:135], v[196:199]
  ds_read_b64_tr_b16 a[250:251], v3 offset:10944
  v_mfma_f32_16x16x32_bf16 v[200:203], a[224:227], a[136:139], v[200:203]
  buffer_load_dwordx4 a[220:223], v9, s[48:51], s71 offen offset:3072 sc1
  v_mfma_f32_16x16x32_bf16 v[200:203], a[228:231], a[152:155], v[200:203]
  ds_read_b64_tr_b16 a[252:253], v3 offset:29952
  v_mfma_f32_16x16x32_bf16 v[204:207], a[228:231], a[156:159], v[204:207]
  v_mfma_f32_16x16x32_bf16 v[204:207], a[224:227], a[140:143], v[204:207]
  s_waitcnt lgkmcnt(7)
  ds_read_b64_tr_b16 a[254:255], v3 offset:33536
  v_mfma_f32_16x16x32_bf16 v[208:211], a[232:235], a[128:131], v[208:211]
  v_mfma_f32_16x16x32_bf16 v[208:211], a[236:239], a[144:147], v[208:211]
  v_mfma_f32_16x16x32_bf16 v[212:215], a[236:239], a[148:151], v[212:215]
  v_mfma_f32_16x16x32_bf16 v[212:215], a[232:235], a[132:135], v[212:215]
  v_mfma_f32_16x16x32_bf16 v[216:219], a[232:235], a[136:139], v[216:219]
  v_mfma_f32_16x16x32_bf16 v[216:219], a[236:239], a[152:155], v[216:219]
  v_mfma_f32_16x16x32_bf16 v[220:223], a[236:239], a[156:159], v[220:223]
  v_mfma_f32_16x16x32_bf16 v[220:223], a[232:235], a[140:143], v[220:223]
  s_waitcnt lgkmcnt(4)
  s_waitcnt lgkmcnt(0)
  s_waitcnt vmcnt(21)
  s_barrier
  v_mfma_f32_16x16x32_bf16 v[224:227], a[240:243], a[128:131], v[224:227]
  ds_read_b64_tr_b16 a[224:225], v2 offset:0
  v_mfma_f32_16x16x32_bf16 v[224:227], a[244:247], a[144:147], v[224:227]
  v_mfma_f32_16x16x32_bf16 v[228:231], a[244:247], a[148:151], v[228:231]
  ds_read_b64_tr_b16 a[226:227], v2 offset:4128
  v_mfma_f32_16x16x32_bf16 v[228:231], a[240:243], a[132:135], v[228:231]
  v_mfma_f32_16x16x32_bf16 v[232:235], a[240:243], a[136:139], v[232:235]
  ds_read_b64_tr_b16 a[228:229], v2 offset:22592
  v_mfma_f32_16x16x32_bf16 v[232:235], a[244:247], a[152:155], v[232:235]
  v_mfma_f32_16x16x32_bf16 v[236:239], a[244:247], a[156:159], v[236:239]
  ds_read_b64_tr_b16 a[230:231], v2 offset:26720
  v_mfma_f32_16x16x32_bf16 v[236:239], a[240:243], a[140:143], v[236:239]
  s_waitcnt lgkmcnt(4)
  v_mfma_f32_16x16x32_bf16 v[240:243], a[248:251], a[128:131], v[240:243]
  ds_read_b64_tr_b16 a[232:233], v2 offset:8
  v_mfma_f32_16x16x32_bf16 v[240:243], a[252:255], a[144:147], v[240:243]
  v_mfma_f32_16x16x32_bf16 v[244:247], a[252:255], a[148:151], v[244:247]
  ds_read_b64_tr_b16 a[234:235], v2 offset:4136
  v_mfma_f32_16x16x32_bf16 v[244:247], a[248:251], a[132:135], v[244:247]
  v_mfma_f32_16x16x32_bf16 v[248:251], a[248:251], a[136:139], v[248:251]
  ds_read_b64_tr_b16 a[236:237], v2 offset:22600
  v_mfma_f32_16x16x32_bf16 v[248:251], a[252:255], a[152:155], v[248:251]
  v_mfma_f32_16x16x32_bf16 v[252:255], a[252:255], a[156:159], v[252:255]
  ds_read_b64_tr_b16 a[238:239], v2 offset:26728
  v_mfma_f32_16x16x32_bf16 v[252:255], a[248:251], a[140:143], v[252:255]
  s_waitcnt lgkmcnt(4)
  s_sub_u32 s76, s76, 1
  s_cmp_lg_u32 s76, 0
  s_cbranch_scc1 LB_gloop
  s_mov_b32 s73, 0x3e0293ee
  v_mov_b32 v30, v4
  v_mov_b32 v1, v2
  v_mfma_f32_16x16x32_bf16 a[0:3], a[224:227], a[160:163], a[0:3]
  ds_read_b64_tr_b16 a[240:241], v1 offset:256
  v_mfma_f32_16x16x32_bf16 a[0:3], a[228:231], a[176:179], a[0:3]
  s_add_u32 m0, s77, 0
  v_mfma_f32_16x16x32_bf16 a[4:7], a[228:231], a[180:183], a[4:7]
  ds_read_b64_tr_b16 a[242:243], v1 offset:4384
  v_mfma_f32_16x16x32_bf16 a[4:7], a[224:227], a[164:167], a[4:7]
  buffer_load_dwordx4 v15, s[52:55], s79 offen offset:0 lds
  v_mfma_f32_16x16x32_bf16 a[8:11], a[224:227], a[168:171], a[8:11]
  ds_read_b64_tr_b16 a[244:245], v1 offset:22848
  ds_read_b64_tr_b16 a[248:249], v1 offset:264
  v_mfma_f32_16x16x32_bf16 a[8:11], a[228:231], a[184:187], a[8:11]
  v_mfma_f32_16x16x32_bf16 a[12:15], a[228:231], a[188:191], a[12:15]
  ds_read_b64_tr_b16 a[246:247], v1 offset:26976
  ds_read_b64_tr_b16 a[250:251], v1 offset:4392
  v_mfma_f32_16x16x32_bf16 a[12:15], a[224:227], a[172:175], a[12:15]
  s_waitcnt lgkmcnt(6)
  v_mfma_f32_16x16x32_bf16 a[16:19], a[232:235], a[160:163], a[16:19]
  ds_read_b64_tr_b16 a[252:253], v1 offset:22856
  v_mfma_f32_16x16x32_bf16 a[16:19], a[236:239], a[176:179], a[16:19]
  v_mfma_f32_16x16x32_bf16 a[20:23], a[236:239], a[180:183], a[20:23]
  ds_read_b64_tr_b16 a[254:255], v1 offset:26984
  v_mfma_f32_16x16x32_bf16 a[20:23], a[232:235], a[164:167], a[20:23]
  v_mfma_f32_16x16x32_bf16 a[24:27], a[232:235], a[168:171], a[24:27]
  ds_read_b64_tr_b16 a[224:225], v1 offset:512
  v_mfma_f32_16x16x32_bf16 a[24:27], a[236:239], a[184:187], a[24:27]
  v_mfma_f32_16x16x32_bf16 a[28:31], a[236:239], a[188:191], a[28:31]
  ds_read_b64_tr_b16 a[226:227], v1 offset:4640
  v_mfma_f32_16x16x32_bf16 a[28:31], a[232:235], a[172:175], a[28:31]
  s_waitcnt lgkmcnt(5)
  buffer_load_dwordx4 v16, s[52:55], s79 offen offset:1024 lds
  v_mfma_f32_16x16x32_bf16 a[32:35], a[240:243], a[160:163], a[32:35]
  ds_read_b64_tr_b16 a[228:229], v1 offset:23104
  v_mfma_f32_16x16x32_bf16 a[32:35], a[244:247], a[176:179], a[32:35]
  v_mfma_f32_16x16x32_bf16 a[36:39], a[244:247], a[180:183], a[36:39]
  ds_read_b64_tr_b16 a[230:231], v1 offset:27232
  v_mfma_f32_16x16x32_bf16 a[36:39], a[240:243], a[164:167], a[36:39]
  v_mfma_f32_16x16x32_bf16 a[40:43], a[240:243], a[168:171], a[40:43]
  ds_read_b64_tr_b16 a[232:233], v1 offset:520
  v_mfma_f32_16x16x32_bf16 a[40:43], a[244:247], a[184:187], a[40:43]
  v_mfma_f32_16x16x32_bf16 a[44:47], a[244:247], a[188:191], a[44:47]
  ds_read_b64_tr_b16 a[234:235], v1 offset:4648
  v_mfma_f32_16x16x32_bf16 a[44:47], a[240:243], a[172:175], a[44:47]
  s_waitcnt lgkmcnt(6)
  v_mfma_f32_16x16x32_bf16 a[48:51], a[248:251], a[160:163], a[48:51]
  ds_read_b64_tr_b16 a[236:237], v1 offset:23112
  v_mfma_f32_16x16x32_bf16 a[48:51], a[252:255], a[176:179], a[48:51]
  v_mfma_f32_16x16x32_bf16 a[52:55], a[252:255], a[180:183], a[52:55]
  ds_read_b64_tr_b16 a[238:239], v1 offset:27240
  v_mfma_f32_16x16x32_bf16 a[52:55], a[248:251], a[164:167], a[52:55]
  buffer_load_dwordx4 v17, s[52:55], s79 offen offset:2048 lds
  v_mfma_f32_16x16x32_bf16 a[56:59], a[248:251], a[168:171], a[56:59]
  ds_read_b64_tr_b16 a[240:241], v1 offset:768
  v_mfma_f32_16x16x32_bf16 a[56:59], a[252:255], a[184:187], a[56:59]
  v_mfma_f32_16x16x32_bf16 a[60:63], a[252:255], a[188:191], a[60:63]
  ds_read_b64_tr_b16 a[242:243], v1 offset:4896
  v_mfma_f32_16x16x32_bf16 a[60:63], a[248:251], a[172:175], a[60:63]
  s_waitcnt lgkmcnt(6)
  v_mfma_f32_16x16x32_bf16 a[64:67], a[224:227], a[160:163], a[64:67]
  ds_read_b64_tr_b16 a[244:245], v1 offset:23360
  v_mfma_f32_16x16x32_bf16 a[64:67], a[228:231], a[176:179], a[64:67]
  v_mfma_f32_16x16x32_bf16 a[68:71], a[228:231], a[180:183], a[68:71]
  ds_read_b64_tr_b16 a[246:247], v1 offset:27488
  v_mfma_f32_16x16x32_bf16 a[68:71], a[224:227], a[164:167], a[68:71]
  v_mfma_f32_16x16x32_bf16 a[72:75], a[224:227], a[168:171], a[72:75]
  ds_read_b64_tr_b16 a[248:249], v1 offset:776
  v_mfma_f32_16x16x32_bf16 a[72:75], a[228:231], a[184:187], a[72:75]
  v_mfma_f32_16x16x32_bf16 a[76:79], a[228:231], a[188:191], a[76:79]
  ds_read_b64_tr_b16 a[250:251], v1 offset:4904
  v_mfma_f32_16x16x32_bf16 a[76:79], a[224:227], a[172:175], a[76:79]
  s_waitcnt lgkmcnt(6)
  buffer_load_dwordx4 v18, s[52:55], s79 offen offset:3072 lds
  v_mfma_f32_16x16x32_bf16 a[80:83], a[232:235], a[160:163], a[80:83]
  s_add_u32 s79, s79, 16384
  ds_read_b64_tr_b16 a[252:253], v1 offset:23368
  v_mfma_f32_16x16x32_bf16 a[80:83], a[236:239], a[176:179], a[80:83]
  s_add_u32 m0, s77, 16384
  v_mfma_f32_16x16x32_bf16 a[84:87], a[236:239], a[180:183], a[84:87]
  ds_read_b64_tr_b16 a[254:255], v1 offset:27496
  v_mfma_f32_16x16x32_bf16 a[84:87], a[232:235], a[164:167], a[84:87]
  v_mfma_f32_16x16x32_bf16 a[88:91], a[232:235], a[168:171], a[88:91]
  ds_read_b64_tr_b16 a[224:225], v30 offset:0
  v_mfma_f32_16x16x32_bf16 a[88:91], a[236:239], a[184:187], a[88:91]
  v_mfma_f32_16x16x32_bf16 a[92:95], a[236:239], a[188:191], a[92:95]
  ds_read_b64_tr_b16 a[226:227], v30 offset:3584
  v_mfma_f32_16x16x32_bf16 a[92:95], a[232:235], a[172:175], a[92:95]
  s_waitcnt lgkmcnt(6)
  v_mfma_f32_16x16x32_bf16 a[96:99], a[240:243], a[160:163], a[96:99]
  ds_read_b64_tr_b16 a[228:229], v30 offset:22592
  v_mfma_f32_16x16x32_bf16 a[96:99], a[244:247], a[176:179], a[96:99]
  v_mfma_f32_16x16x32_bf16 a[100:103], a[244:247], a[180:183], a[100:103]
  ds_read_b64_tr_b16 a[230:231], v30 offset:26176
  v_mfma_f32_16x16x32_bf16 a[100:103], a[240:243], a[164:167], a[100:103]
  buffer_load_dwordx4 v15, s[52:55], s79 offen offset:0 lds
  v_mfma_f32_16x16x32_bf16 a[104:107], a[240:243], a[168:171], a[104:107]
  ds_read_b64_tr_b16 a[232:233], v30 offset:7168
  v_mfma_f32_16x16x32_bf16 a[104:107], a[244:247], a[184:187], a[104:107]
  v_mfma_f32_16x16x32_bf16 a[108:111], a[244:247], a[188:191], a[108:111]
  ds_read_b64_tr_b16 a[234:235], v30 offset:10752
  v_mfma_f32_16x16x32_bf16 a[108:111], a[240:243], a[172:175], a[108:111]
  s_waitcnt lgkmcnt(6)
  v_mfma_f32_16x16x32_bf16 a[112:115], a[248:251], a[160:163], a[112:115]
  ds_read_b64_tr_b16 a[236:237], v30 offset:29760
  v_mfma_f32_16x16x32_bf16 a[112:115], a[252:255], a[176:179], a[112:115]
  v_mfma_f32_16x16x32_bf16 a[116:119], a[252:255], a[180:183], a[116:119]
  ds_read_b64_tr_b16 a[238:239], v30 offset:33344
  v_mfma_f32_16x16x32_bf16 a[116:119], a[248:251], a[164:167], a[116:119]
  v_mfma_f32_16x16x32_bf16 a[120:123], a[248:251], a[168:171], a[120:123]
  ds_read_b64_tr_b16 a[240:241], v30 offset:32
  v_mfma_f32_16x16x32_bf16 a[120:123], a[252:255], a[184:187], a[120:123]
  v_mfma_f32_16x16x32_bf16 a[124:127], a[252:255], a[188:191], a[124:127]
  ds_read_b64_tr_b16 a[242:243], v30 offset:3616
  v_mfma_f32_16x16x32_bf16 a[124:127], a[248:251], a[172:175], a[124:127]
  s_waitcnt lgkmcnt(6)
  buffer_load_dwordx4 v16, s[52:55], s79 offen offset:1024 lds
  v_mfma_f32_16x16x32_bf16 v[32:35], a[224:227], a[160:163], v[32:35]
  ds_read_b64_tr_b16 a[244:245], v30 offset:22624
  v_mfma_f32_16x16x32_bf16 v[32:35], a[228:231], a[176:179], v[32:35]
  v_mfma_f32_16x16x32_bf16 v[36:39], a[228:231], a[180:183], v[36:39]
  ds_read_b64_tr_b16 a[246:247], v30 offset:26208
  v_mfma_f32_16x16x32_bf16 v[36:39], a[224:227], a[164:167], v[36:39]
  s_waitcnt vmcnt(6)
  s_barrier
  v_mfma_f32_16x16x32_bf16 v[40:43], a[224:227], a[168:171], v[40:43]
  ds_read_b64_tr_b16 a[248:249], v28 offset:0
  v_mfma_f32_16x16x32_bf16 v[40:43], a[228:231], a[184:187], v[40:43]
  v_mfma_f32_16x16x32_bf16 v[44:47], a[228:231], a[188:191], v[44:47]
  ds_read_b64_tr_b16 a[250:251], v28 offset:4128
  v_mfma_f32_16x16x32_bf16 v[44:47], a[224:227], a[172:175], v[44:47]
  s_waitcnt lgkmcnt(6)
  v_mfma_f32_16x16x32_bf16 v[48:51], a[232:235], a[160:163], v[48:51]
  ds_read_b64_tr_b16 a[252:253], v28 offset:22592
  v_mfma_f32_16x16x32_bf16 v[48:51], a[236:239], a[176:179], v[48:51]
  v_mfma_f32_16x16x32_bf16 v[52:55], a[236:239], a[180:183], v[52:55]
  ds_read_b64_tr_b16 a[254:255], v28 offset:26720
  v_mfma_f32_16x16x32_bf16 v[52:55], a[232:235], a[164:167], v[52:55]
  buffer_load_dwordx4 v17, s[52:55], s79 offen offset:2048 lds
  v_mfma_f32_16x16x32_bf16 v[56:59], a[232:235], a[168:171], v[56:59]
  ds_read_b64_tr_b16 a[224:225], v28 offset:8
  v_mfma_f32_16x16x32_bf16 v[56:59], a[236:239], a[184:187], v[56:59]
  v_mfma_f32_16x16x32_bf16 v[60:63], a[236:239], a[188:191], v[60:63]
  ds_read_b64_tr_b16 a[226:227], v28 offset:4136
  v_mfma_f32_16x16x32_bf16 v[60:63], a[232:235], a[172:175], v[60:63]
  s_waitcnt lgkmcnt(6)
  v_mfma_f32_16x16x32_bf16 v[64:67], a[240:243], a[160:163], v[64:67]
  ds_read_b64_tr_b16 a[228:229], v28 offset:22600
  v_mfma_f32_16x16x32_bf16 v[64:67], a[244:247], a[176:179], v[64:67]
  v_mfma_f32_16x16x32_bf16 v[68:71], a[244:247], a[180:183], v[68:71]
  ds_read_b64_tr_b16 a[230:231], v28 offset:26728
  v_mfma_f32_16x16x32_bf16 v[68:71], a[240:243], a[164:167], v[68:71]
  v_mfma_f32_16x16x32_bf16 v[72:75], a[240:243], a[168:171], v[72:75]
  ds_read_b64_tr_b16 a[232:233], v28 offset:256
  v_mfma_f32_16x16x32_bf16 v[72:75], a[244:247], a[184:187], v[72:75]
  v_mfma_f32_16x16x32_bf16 v[76:79], a[244:247], a[188:191], v[76:79]
  ds_read_b64_tr_b16 a[234:235], v28 offset:4384
  v_mfma_f32_16x16x32_bf16 v[76:79], a[240:243], a[172:175], v[76:79]
  s_waitcnt lgkmcnt(6)
  v_mfma_f32_16x16x32_bf16 a[0:3], a[248:251], a[192:195], a[0:3]
  ds_read_b64_tr_b16 a[236:237], v28 offset:22848
  v_mfma_f32_16x16x32_bf16 a[0:3], a[252:255], a[208:211], a[0:3]
  buffer_load_dwordx4 v18, s[52:55], s79 offen offset:3072 lds
  v_mfma_f32_16x16x32_bf16 a[4:7], a[252:255], a[212:215], a[4:7]
  s_add_u32 s79, s79, 16384
  ds_read_b64_tr_b16 a[238:239], v28 offset:26976
  v_mfma_f32_16x16x32_bf16 a[4:7], a[248:251], a[196:199], a[4:7]
  s_add_u32 m0, s77, 32768
  v_mfma_f32_16x16x32_bf16 a[8:11], a[248:251], a[200:203], a[8:11]
  ds_read_b64_tr_b16 a[240:241], v28 offset:264
  v_mfma_f32_16x16x32_bf16 a[8:11], a[252:255], a[216:219], a[8:11]
  v_mfma_f32_16x16x32_bf16 a[12:15], a[252:255], a[220:223], a[12:15]
  ds_read_b64_tr_b16 a[242:243], v28 offset:4392
  v_mfma_f32_16x16x32_bf16 a[12:15], a[248:251], a[204:207], a[12:15]
  s_waitcnt lgkmcnt(6)
  v_mfma_f32_16x16x32_bf16 a[16:19], a[224:227], a[192:195], a[16:19]
  ds_read_b64_tr_b16 a[244:245], v28 offset:22856
  v_mfma_f32_16x16x32_bf16 a[16:19], a[228:231], a[208:211], a[16:19]
  v_mfma_f32_16x16x32_bf16 a[20:23], a[228:231], a[212:215], a[20:23]
  ds_read_b64_tr_b16 a[246:247], v28 offset:26984
  v_mfma_f32_16x16x32_bf16 a[20:23], a[224:227], a[196:199], a[20:23]
  v_mfma_f32_16x16x32_bf16 a[24:27], a[224:227], a[200:203], a[24:27]
  ds_read_b64_tr_b16 a[248:249], v28 offset:512
  v_mfma_f32_16x16x32_bf16 a[24:27], a[228:231], a[216:219], a[24:27]
  buffer_load_dwordx4 v15, s[52:55], s79 offen offset:0 lds
  v_mfma_f32_16x16x32_bf16 a[28:31], a[228:231], a[220:223], a[28:31]
  ds_read_b64_tr_b16 a[250:251], v28 offset:4640
  v_mfma_f32_16x16x32_bf16 a[28:31], a[224:227], a[204:207], a[28:31]
  s_waitcnt lgkmcnt(6)
  v_mfma_f32_16x16x32_bf16 a[32:35], a[232:235], a[192:195], a[32:35]
  ds_read_b64_tr_b16 a[252:253], v28 offset:23104
  v_mfma_f32_16x16x32_bf16 a[32:35], a[236:239], a[208:211], a[32:35]
  v_mfma_f32_16x16x32_bf16 a[36:39], a[236:239], a[212:215], a[36:39]
  ds_read_b64_tr_b16 a[254:255], v28 offset:27232
  v_mfma_f32_16x16x32_bf16 a[36:39], a[232:235], a[196:199], a[36:39]
  v_mfma_f32_16x16x32_bf16 a[40:43], a[232:235], a[200:203], a[40:43]
  ds_read_b64_tr_b16 a[224:225], v28 offset:520
  v_mfma_f32_16x16x32_bf16 a[40:43], a[236:239], a[216:219], a[40:43]
  v_mfma_f32_16x16x32_bf16 a[44:47], a[236:239], a[220:223], a[44:47]
  ds_read_b64_tr_b16 a[226:227], v28 offset:4648
  v_mfma_f32_16x16x32_bf16 a[44:47], a[232:235], a[204:207], a[44:47]
  s_waitcnt lgkmcnt(6)
  v_mfma_f32_16x16x32_bf16 a[48:51], a[240:243], a[192:195], a[48:51]
  ds_read_b64_tr_b16 a[228:229], v28 offset:23112
  v_mfma_f32_16x16x32_bf16 a[48:51], a[244:247], a[208:211], a[48:51]
  buffer_load_dwordx4 v16, s[52:55], s79 offen offset:1024 lds
  v_mfma_f32_16x16x32_bf16 a[52:55], a[244:247], a[212:215], a[52:55]
  ds_read_b64_tr_b16 a[230:231], v28 offset:27240
  v_mfma_f32_16x16x32_bf16 a[52:55], a[240:243], a[196:199], a[52:55]
  v_mfma_f32_16x16x32_bf16 a[56:59], a[240:243], a[200:203], a[56:59]
  ds_read_b64_tr_b16 a[232:233], v28 offset:768
  v_mfma_f32_16x16x32_bf16 a[56:59], a[244:247], a[216:219], a[56:59]
  v_mfma_f32_16x16x32_bf16 a[60:63], a[244:247], a[220:223], a[60:63]
  ds_read_b64_tr_b16 a[234:235], v28 offset:4896
  v_mfma_f32_16x16x32_bf16 a[60:63], a[240:243], a[204:207], a[60:63]
  s_waitcnt lgkmcnt(6)
  v_mfma_f32_16x16x32_bf16 a[64:67], a[248:251], a[192:195], a[64:67]
  ds_read_b64_tr_b16 a[236:237], v28 offset:23360
  v_mfma_f32_16x16x32_bf16 a[64:67], a[252:255], a[208:211], a[64:67]
  v_mfma_f32_16x16x32_bf16 a[68:71], a[252:255], a[212:215], a[68:71]
  ds_read_b64_tr_b16 a[238:239], v28 offset:27488
  v_mfma_f32_16x16x32_bf16 a[68:71], a[248:251], a[196:199], a[68:71]
  v_mfma_f32_16x16x32_bf16 a[72:75], a[248:251], a[200:203], a[72:75]
  ds_read_b64_tr_b16 a[240:241], v28 offset:776
  v_mfma_f32_16x16x32_bf16 a[72:75], a[252:255], a[216:219], a[72:75]
  buffer_load_dwordx4 v17, s[52:55], s79 offen offset:2048 lds
  v_mfma_f32_16x16x32_bf16 a[76:79], a[252:255], a[220:223], a[76:79]
  ds_read_b64_tr_b16 a[242:243], v28 offset:4904
  v_mfma_f32_16x16x32_bf16 a[76:79], a[248:251], a[204:207], a[76:79]
  s_waitcnt lgkmcnt(6)
  v_mfma_f32_16x16x32_bf16 a[80:83], a[224:227], a[192:195], a[80:83]
  ds_read_b64_tr_b16 a[244:245], v28 offset:23368
  v_mfma_f32_16x16x32_bf16 a[80:83], a[228:231], a[208:211], a[80:83]
  v_mfma_f32_16x16x32_bf16 a[84:87], a[228:231], a[212:215], a[84:87]
  ds_read_b64_tr_b16 a[246:247], v28 offset:27496
  v_mfma_f32_16x16x32_bf16 a[84:87], a[224:227], a[196:199], a[84:87]
  v_mfma_f32_16x16x32_bf16 a[88:91], a[224:227], a[200:203], a[88:91]
  ds_read_b64_tr_b16 a[248:249], v30 offset:7200
  v_mfma_f32_16x16x32_bf16 a[88:91], a[228:231], a[216:219], a[88:91]
  v_mfma_f32_16x16x32_bf16 a[92:95], a[228:231], a[220:223], a[92:95]
  ds_read_b64_tr_b16 a[250:251], v30 offset:10784
  v_mfma_f32_16x16x32_bf16 a[92:95], a[224:227], a[204:207], a[92:95]
  s_waitcnt lgkmcnt(6)
  v_mfma_f32_16x16x32_bf16 a[96:99], a[232:235], a[192:195], a[96:99]
  ds_read_b64_tr_b16 a[252:253], v30 offset:29792
  v_mfma_f32_16x16x32_bf16 a[96:99], a[236:239], a[208:211], a[96:99]
  buffer_load_dwordx4 v18, s[52:55], s79 offen offset:3072 lds
  v_mfma_f32_16x16x32_bf16 a[100:103], a[236:239], a[212:215], a[100:103]
  s_add_u32 s79, s79, 16384
  ds_read_b64_tr_b16 a[254:255], v30 offset:33376
  v_mfma_f32_16x16x32_bf16 a[100:103], a[232:235], a[196:199], a[100:103]
  s_add_u32 m0, s77, 49152
  v_mfma_f32_16x16x32_bf16 a[104:107], a[232:235], a[200:203], a[104:107]
  ds_read_b64_tr_b16 a[224:225], v30 offset:64
  v_mfma_f32_16x16x32_bf16 a[104:107], a[236:239], a[216:219], a[104:107]
  v_mfma_f32_16x16x32_bf16 a[108:111], a[236:239], a[220:223], a[108:111]
  ds_read_b64_tr_b16 a[226:227], v30 offset:3648
  v_mfma_f32_16x16x32_bf16 a[108:111], a[232:235], a[204:207], a[108:111]
  s_waitcnt lgkmcnt(6)
  v_mfma_f32_16x16x32_bf16 a[112:115], a[240:243], a[192:195], a[112:115]
  ds_read_b64_tr_b16 a[228:229], v30 offset:22656
  v_mfma_f32_16x16x32_bf16 a[112:115], a[244:247], a[208:211], a[112:115]
  v_mfma_f32_16x16x32_bf16 a[116:119], a[244:247], a[212:215], a[116:119]
  ds_read_b64_tr_b16 a[230:231], v30 offset:26240
  v_mfma_f32_16x16x32_bf16 a[116:119], a[240:243], a[196:199], a[116:119]
  v_mfma_f32_16x16x32_bf16 a[120:123], a[240:243], a[200:203], a[120:123]
  ds_read_b64_tr_b16 a[232:233], v30 offset:7232
  v_mfma_f32_16x16x32_bf16 a[120:123], a[244:247], a[216:219], a[120:123]
  buffer_load_dwordx4 v15, s[52:55], s79 offen offset:0 lds
  v_mfma_f32_16x16x32_bf16 a[124:127], a[244:247], a[220:223], a[124:127]
  ds_read_b64_tr_b16 a[234:235], v30 offset:10816
  v_mfma_f32_16x16x32_bf16 a[124:127], a[240:243], a[204:207], a[124:127]
  s_waitcnt lgkmcnt(6)
  v_mfma_f32_16x16x32_bf16 v[80:83], a[248:251], a[160:163], v[80:83]
  ds_read_b64_tr_b16 a[236:237], v30 offset:29824
  v_mfma_f32_16x16x32_bf16 v[80:83], a[252:255], a[176:179], v[80:83]
  v_mfma_f32_16x16x32_bf16 v[84:87], a[252:255], a[180:183], v[84:87]
  ds_read_b64_tr_b16 a[238:239], v30 offset:33408
  v_mfma_f32_16x16x32_bf16 v[84:87], a[248:251], a[164:167], v[84:87]
  v_mfma_f32_16x16x32_bf16 v[88:91], a[248:251], a[168:171], v[88:91]
  ds_read_b64_tr_b16 a[240:241], v30 offset:96
  v_mfma_f32_16x16x32_bf16 v[88:91], a[252:255], a[184:187], v[88:91]
  v_mfma_f32_16x16x32_bf16 v[92:95], a[252:255], a[188:191], v[92:95]
  ds_read_b64_tr_b16 a[242:243], v30 offset:3680
  v_accvgpr_read_b32 v2, a0
  v_accvgpr_read_b32 v3, a1
  v_accvgpr_read_b32 v4, a2
  v_mfma_f32_16x16x32_bf16 v[92:95], a[248:251], a[172:175], v[92:95]
  s_waitcnt lgkmcnt(6)
  v_accvgpr_read_b32 v5, a3
  v_accvgpr_read_b32 v6, a16
  v_mfma_f32_16x16x32_bf16 v[96:99], a[224:227], a[160:163], v[96:99]
  ds_read_b64_tr_b16 a[244:245], v30 offset:22688
  v_accvgpr_read_b32 v7, a17
  v_accvgpr_read_b32 v8, a18
  v_mfma_f32_16x16x32_bf16 v[96:99], a[228:231], a[176:179], v[96:99]
  v_accvgpr_read_b32 v9, a19
  v_mul_f32 v2, s73, v2
  v_mul_f32 v3, s73, v3
  v_mfma_f32_16x16x32_bf16 v[100:103], a[228:231], a[180:183], v[100:103]
  ds_read_b64_tr_b16 a[246:247], v30 offset:26272
  v_mul_f32 v4, s73, v4
  v_mul_f32 v5, s73, v5
  v_mfma_f32_16x16x32_bf16 v[100:103], a[224:227], a[164:167], v[100:103]
  buffer_load_dwordx4 v16, s[52:55], s79 offen offset:1024 lds
  v_mul_f32 v6, s73, v6
  v_mul_f32 v7, s73, v7
  v_mfma_f32_16x16x32_bf16 v[104:107], a[224:227], a[168:171], v[104:107]
  ds_read_b64_tr_b16 a[248:249], v30 offset:7264
  v_mul_f32 v8, s73, v8
  v_mul_f32 v9, s73, v9
  v_mfma_f32_16x16x32_bf16 v[104:107], a[228:231], a[184:187], v[104:107]
  v_cvt_pk_bf16_f32 v2, v2, v3
  v_cvt_pk_bf16_f32 v3, v4, v5
  v_cvt_pk_bf16_f32 v4, v6, v7
  v_mfma_f32_16x16x32_bf16 v[108:111], a[228:231], a[188:191], v[108:111]
  ds_read_b64_tr_b16 a[250:251], v30 offset:10848
  v_cvt_pk_bf16_f32 v5, v8, v9
  v_accvgpr_write_b32 a128, v2
  v_mfma_f32_16x16x32_bf16 v[108:111], a[224:227], a[172:175], v[108:111]
  s_waitcnt lgkmcnt(6)
  v_accvgpr_write_b32 a129, v3
  v_accvgpr_write_b32 a130, v4
  v_mfma_f32_16x16x32_bf16 v[112:115], a[232:235], a[160:163], v[112:115]
  ds_read_b64_tr_b16 a[252:253], v30 offset:29856
  v_accvgpr_write_b32 a131, v5
  v_accvgpr_read_b32 v2, a32
  v_accvgpr_read_b32 v3, a33
  v_mfma_f32_16x16x32_bf16 v[112:115], a[236:239], a[176:179], v[112:115]
  v_accvgpr_read_b32 v4, a34
  v_accvgpr_read_b32 v5, a35
  v_mfma_f32_16x16x32_bf16 v[116:119], a[236:239], a[180:183], v[116:119]
  ds_read_b64_tr_b16 a[254:255], v30 offset:33440
  v_accvgpr_read_b32 v6, a48
  v_accvgpr_read_b32 v7, a49
  v_mfma_f32_16x16x32_bf16 v[116:119], a[232:235], a[164:167], v[116:119]
  v_accvgpr_read_b32 v8, a50
  v_accvgpr_read_b32 v9, a51
  v_mfma_f32_16x16x32_bf16 v[120:123], a[232:235], a[168:171], v[120:123]
  ds_read_b64_tr_b16 a[224:225], v30 offset:128
  v_mul_f32 v2, s73, v2
  v_mul_f32 v3, s73, v3
  v_mul_f32 v4, s73, v4
  v_mfma_f32_16x16x32_bf16 v[120:123], a[236:239], a[184:187], v[120:123]
  v_mul_f32 v5, s73, v5
  v_mul_f32 v6, s73, v6
  v_mfma_f32_16x16x32_bf16 v[124:127], a[236:239], a[188:191], v[124:127]
  ds_read_b64_tr_b16 a[226:227], v30 offset:3712
  v_mul_f32 v7, s73, v7
  v_mul_f32 v8, s73, v8
  v_mfma_f32_16x16x32_bf16 v[124:127], a[232:235], a[172:175], v[124:127]
  s_waitcnt lgkmcnt(6)
  buffer_load_dwordx4 v17, s[52:55], s79 offen offset:2048 lds
  v_mul_f32 v9, s73, v9
  v_cvt_pk_bf16_f32 v2, v2, v3
  v_cvt_pk_bf16_f32 v3, v4, v5
  v_mfma_f32_16x16x32_bf16 v[128:131], a[240:243], a[160:163], v[128:131]
  ds_read_b64_tr_b16 a[228:229], v30 offset:22720
  v_cvt_pk_bf16_f32 v4, v6, v7
  v_cvt_pk_bf16_f32 v5, v8, v9
  v_mfma_f32_16x16x32_bf16 v[128:131], a[244:247], a[176:179], v[128:131]
  v_accvgpr_write_b32 a132, v2
  v_accvgpr_write_b32 a133, v3
  v_mfma_f32_16x16x32_bf16 v[132:135], a[244:247], a[180:183], v[132:135]
  ds_read_b64_tr_b16 a[230:231], v30 offset:26304
  v_accvgpr_write_b32 a134, v4
  v_accvgpr_write_b32 a135, v5
  v_mfma_f32_16x16x32_bf16 v[132:135], a[240:243], a[164:167], v[132:135]
  v_accvgpr_read_b32 v2, a64
  v_accvgpr_read_b32 v3, a65
  v_accvgpr_read_b32 v4, a66
  v_mfma_f32_16x16x32_bf16 v[136:139], a[240:243], a[168:171], v[136:139]
  ds_read_b64_tr_b16 a[232:233], v30 offset:7296
  v_accvgpr_read_b32 v5, a67
  v_accvgpr_read_b32 v6, a80
  v_mfma_f32_16x16x32_bf16 v[136:139], a[244:247], a[184:187], v[136:139]
  v_accvgpr_read_b32 v7, a81
  v_accvgpr_read_b32 v8, a82
  v_mfma_f32_16x16x32_bf16 v[140:143], a[244:247], a[188:191], v[140:143]
  ds_read_b64_tr_b16 a[234:235], v30 offset:10880
  v_accvgpr_read_b32 v9, a83
  v_mul_f32 v2, s73, v2
  v_mul_f32 v3, s73, v3
  v_mfma_f32_16x16x32_bf16 v[140:143], a[240:243], a[172:175], v[140:143]
  s_waitcnt lgkmcnt(6)
  v_mul_f32 v4, s73, v4
  v_mul_f32 v5, s73, v5
  v_mfma_f32_16x16x32_bf16 v[144:147], a[248:251], a[160:163], v[144:147]
  ds_read_b64_tr_b16 a[236:237], v30 offset:29888
  v_mul_f32 v6, s73, v6
  v_mul_f32 v7, s73, v7
  v_mfma_f32_16x16x32_bf16 v[144:147], a[252:255], a[176:179], v[144:147]
  v_mul_f32 v8, s73, v8
  v_mul_f32 v9, s73, v9
  v_mfma_f32_16x16x32_bf16 v[148:151], a[252:255], a[180:183], v[148:151]
  ds_read_b64_tr_b16 a[238:239], v30 offset:33472
  v_cvt_pk_bf16_f32 v2, v2, v3
  v_cvt_pk_bf16_f32 v3, v4, v5
  v_cvt_pk_bf16_f32 v4, v6, v7
  v_mfma_f32_16x16x32_bf16 v[148:151], a[248:251], a[164:167], v[148:151]
  buffer_load_dwordx4 v18, s[52:55], s79 offen offset:3072 lds
  v_cvt_pk_bf16_f32 v5, v8, v9
  v_accvgpr_write_b32 a136, v2
  v_mfma_f32_16x16x32_bf16 v[152:155], a[248:251], a[168:171], v[152:155]
  s_add_u32 s79, s79, 16384
  ds_read_b64_tr_b16 a[240:241], v30 offset:160
  v_accvgpr_write_b32 a137, v3
  v_accvgpr_write_b32 a138, v4
  v_mfma_f32_16x16x32_bf16 v[152:155], a[252:255], a[184:187], v[152:155]
  v_accvgpr_write_b32 a139, v5
  v_accvgpr_read_b32 v2, a96
  v_accvgpr_read_b32 v3, a97
  v_mfma_f32_16x16x32_bf16 v[156:159], a[252:255], a[188:191], v[156:159]
  ds_read_b64_tr_b16 a[242:243], v30 offset:3744
  v_accvgpr_read_b32 v4, a98
  v_accvgpr_read_b32 v5, a99
  v_mfma_f32_16x16x32_bf16 v[156:159], a[248:251], a[172:175], v[156:159]
  s_waitcnt lgkmcnt(6)
  v_accvgpr_read_b32 v6, a112
  v_accvgpr_read_b32 v7, a113
  v_mfma_f32_16x16x32_bf16 v[160:163], a[224:227], a[160:163], v[160:163]
  ds_read_b64_tr_b16 a[244:245], v30 offset:22752
  v_accvgpr_read_b32 v8, a114
  v_accvgpr_read_b32 v9, a115
  v_mfma_f32_16x16x32_bf16 v[160:163], a[228:231], a[176:179], v[160:163]
  v_mul_f32 v2, s73, v2
  v_mul_f32 v3, s73, v3
  v_mul_f32 v4, s73, v4
  v_mfma_f32_16x16x32_bf16 v[164:167], a[228:231], a[180:183], v[164:167]
  ds_read_b64_tr_b16 a[246:247], v30 offset:26336
  v_mul_f32 v5, s73, v5
  v_mul_f32 v6, s73, v6
  v_mfma_f32_16x16x32_bf16 v[164:167], a[224:227], a[164:167], v[164:167]
  v_mul_f32 v7, s73, v7
  v_mul_f32 v8, s73, v8
  v_mfma_f32_16x16x32_bf16 v[168:171], a[224:227], a[168:171], v[168:171]
  ds_read_b64_tr_b16 a[248:249], v30 offset:7328
  v_mul_f32 v9, s73, v9
  v_cvt_pk_bf16_f32 v2, v2, v3
  v_cvt_pk_bf16_f32 v3, v4, v5
  v_mfma_f32_16x16x32_bf16 v[168:171], a[228:231], a[184:187], v[168:171]
  v_cvt_pk_bf16_f32 v4, v6, v7
  v_cvt_pk_bf16_f32 v5, v8, v9
  v_mfma_f32_16x16x32_bf16 v[172:175], a[228:231], a[188:191], v[172:175]
  ds_read_b64_tr_b16 a[250:251], v30 offset:10912
  v_accvgpr_write_b32 a140, v2
  v_accvgpr_write_b32 a141, v3
  v_mfma_f32_16x16x32_bf16 v[172:175], a[224:227], a[172:175], v[172:175]
  s_waitcnt lgkmcnt(6)
  v_accvgpr_write_b32 a142, v4
  v_accvgpr_write_b32 a143, v5
  v_mfma_f32_16x16x32_bf16 v[176:179], a[232:235], a[160:163], v[176:179]
  ds_read_b64_tr_b16 a[252:253], v30 offset:29920
  v_accvgpr_read_b32 v2, a4
  v_accvgpr_read_b32 v3, a5
  v_accvgpr_read_b32 v4, a6
  v_mfma_f32_16x16x32_bf16 v[176:179], a[236:239], a[176:179], v[176:179]
  v_accvgpr_read_b32 v5, a7
  v_accvgpr_read_b32 v6, a20
  v_mfma_f32_16x16x32_bf16 v[180:183], a[236:239], a[180:183], v[180:183]
  ds_read_b64_tr_b16 a[254:255], v30 offset:33504
  v_accvgpr_read_b32 v7, a21
  v_accvgpr_read_b32 v8, a22
  v_mfma_f32_16x16x32_bf16 v[180:183], a[232:235], a[164:167], v[180:183]
  v_accvgpr_read_b32 v9, a23
  v_mul_f32 v2, s73, v2
  v_mul_f32 v3, s73, v3
  v_mfma_f32_16x16x32_bf16 v[184:187], a[232:235], a[168:171], v[184:187]
  ds_read_b64_tr_b16 a[224:225], v30 offset:192
  v_mul_f32 v4, s73, v4
  v_mul_f32 v5, s73, v5
  v_mfma_f32_16x16x32_bf16 v[184:187], a[236:239], a[184:187], v[184:187]
  v_mul_f32 v6, s73, v6
  v_mul_f32 v7, s73, v7
  v_mfma_f32_16x16x32_bf16 v[188:191], a[236:239], a[188:191], v[188:191]
  ds_read_b64_tr_b16 a[226:227], v30 offset:3776
  v_mul_f32 v8, s73, v8
  v_mul_f32 v9, s73, v9
  v_mfma_f32_16x16x32_bf16 v[188:191], a[232:235], a[172:175], v[188:191]
  s_waitcnt lgkmcnt(6)
  v_cvt_pk_bf16_f32 v2, v2, v3
  v_cvt_pk_bf16_f32 v3, v4, v5
  v_cvt_pk_bf16_f32 v4, v6, v7
  v_mfma_f32_16x16x32_bf16 v[192:195], a[240:243], a[160:163], v[192:195]
  ds_read_b64_tr_b16 a[228:229], v30 offset:22784
  v_cvt_pk_bf16_f32 v5, v8, v9
  v_accvgpr_write_b32 a144, v2
  v_mfma_f32_16x16x32_bf16 v[192:195], a[244:247], a[176:179], v[192:195]
  v_accvgpr_write_b32 a145, v3
  v_accvgpr_write_b32 a146, v4
  v_mfma_f32_16x16x32_bf16 v[196:199], a[244:247], a[180:183], v[196:199]
  ds_read_b64_tr_b16 a[230:231], v30 offset:26368
  v_accvgpr_write_b32 a147, v5
  v_accvgpr_read_b32 v2, a36
  v_accvgpr_read_b32 v3, a37
  v_mfma_f32_16x16x32_bf16 v[196:199], a[240:243], a[164:167], v[196:199]
  v_accvgpr_read_b32 v4, a38
  v_accvgpr_read_b32 v5, a39
  v_mfma_f32_16x16x32_bf16 v[200:203], a[240:243], a[168:171], v[200:203]
  ds_read_b64_tr_b16 a[232:233], v30 offset:7360
  v_accvgpr_read_b32 v6, a52
  v_accvgpr_read_b32 v7, a53
  v_mfma_f32_16x16x32_bf16 v[200:203], a[244:247], a[184:187], v[200:203]
  v_accvgpr_read_b32 v8, a54
  v_accvgpr_read_b32 v9, a55
  v_mfma_f32_16x16x32_bf16 v[204:207], a[244:247], a[188:191], v[204:207]
  ds_read_b64_tr_b16 a[234:235], v30 offset:10944
  v_mul_f32 v2, s73, v2
  v_mul_f32 v3, s73, v3
  v_mul_f32 v4, s73, v4
  v_mfma_f32_16x16x32_bf16 v[204:207], a[240:243], a[172:175], v[204:207]
  s_waitcnt lgkmcnt(6)
  v_mul_f32 v5, s73, v5
  v_mul_f32 v6, s73, v6
  v_mfma_f32_16x16x32_bf16 v[208:211], a[248:251], a[160:163], v[208:211]
  ds_read_b64_tr_b16 a[236:237], v30 offset:29952
  v_mul_f32 v7, s73, v7
  v_mul_f32 v8, s73, v8
  v_mfma_f32_16x16x32_bf16 v[208:211], a[252:255], a[176:179], v[208:211]
  v_mul_f32 v9, s73, v9
  v_cvt_pk_bf16_f32 v2, v2, v3
  v_cvt_pk_bf16_f32 v3, v4, v5
  v_mfma_f32_16x16x32_bf16 v[212:215], a[252:255], a[180:183], v[212:215]
  ds_read_b64_tr_b16 a[238:239], v30 offset:33536
  v_cvt_pk_bf16_f32 v4, v6, v7
  v_cvt_pk_bf16_f32 v5, v8, v9
  v_mfma_f32_16x16x32_bf16 v[212:215], a[248:251], a[164:167], v[212:215]
  v_accvgpr_write_b32 a148, v2
  v_accvgpr_write_b32 a149, v3
  v_mfma_f32_16x16x32_bf16 v[216:219], a[248:251], a[168:171], v[216:219]
  ds_read_b64_tr_b16 a[240:241], v29 offset:0
  v_accvgpr_write_b32 a150, v4
  v_accvgpr_write_b32 a151, v5
  v_mfma_f32_16x16x32_bf16 v[216:219], a[252:255], a[184:187], v[216:219]
  v_accvgpr_read_b32 v2, a68
  v_accvgpr_read_b32 v3, a69
  v_accvgpr_read_b32 v4, a70
  v_mfma_f32_16x16x32_bf16 v[220:223], a[252:255], a[188:191], v[220:223]
  ds_read_b64_tr_b16 a[242:243], v29 offset:3584
  v_accvgpr_read_b32 v5, a71
  v_accvgpr_read_b32 v6, a84
  v_mfma_f32_16x16x32_bf16 v[220:223], a[248:251], a[172:175], v[220:223]
  s_waitcnt lgkmcnt(6)
  v_accvgpr_read_b32 v7, a85
  v_accvgpr_read_b32 v8, a86
  v_mfma_f32_16x16x32_bf16 v[224:227], a[224:227], a[160:163], v[224:227]
  ds_read_b64_tr_b16 a[244:245], v29 offset:22592
  v_accvgpr_read_b32 v9, a87
  v_mul_f32 v2, s73, v2
  v_mul_f32 v3, s73, v3
  v_mfma_f32_16x16x32_bf16 v[224:227], a[228:231], a[176:179], v[224:227]
  v_mul_f32 v4, s73, v4
  v_mul_f32 v5, s73, v5
  v_mfma_f32_16x16x32_bf16 v[228:231], a[228:231], a[180:183], v[228:231]
  ds_read_b64_tr_b16 a[246:247], v29 offset:26176
  v_mul_f32 v6, s73, v6
  v_mul_f32 v7, s73, v7
  v_mfma_f32_16x16x32_bf16 v[228:231], a[224:227], a[164:167], v[228:231]
  v_mul_f32 v8, s73, v8
  v_mul_f32 v9, s73, v9
  v_mfma_f32_16x16x32_bf16 v[232:235], a[224:227], a[168:171], v[232:235]
  ds_read_b64_tr_b16 a[248:249], v29 offset:7168
  v_cvt_pk_bf16_f32 v2, v2, v3
  v_cvt_pk_bf16_f32 v3, v4, v5
  v_cvt_pk_bf16_f32 v4, v6, v7
  v_mfma_f32_16x16x32_bf16 v[232:235], a[228:231], a[184:187], v[232:235]
  v_cvt_pk_bf16_f32 v5, v8, v9
  v_accvgpr_write_b32 a152, v2
  v_mfma_f32_16x16x32_bf16 v[236:239], a[228:231], a[188:191], v[236:239]
  ds_read_b64_tr_b16 a[250:251], v29 offset:10752
  v_accvgpr_write_b32 a153, v3
  v_accvgpr_write_b32 a154, v4
  v_mfma_f32_16x16x32_bf16 v[236:239], a[224:227], a[172:175], v[236:239]
  s_waitcnt lgkmcnt(6)
  s_waitcnt vmcnt(12)
  s_barrier
  v_accvgpr_write_b32 a155, v5
  v_accvgpr_read_b32 v2, a100
  v_accvgpr_read_b32 v3, a101
  v_mfma_f32_16x16x32_bf16 v[240:243], a[232:235], a[160:163], v[240:243]
  s_add_u32 m0, s78, 0
  ds_read_b64_tr_b16 a[252:253], v29 offset:29760
  v_accvgpr_read_b32 v4, a102
  v_accvgpr_read_b32 v5, a103
  v_mfma_f32_16x16x32_bf16 v[240:243], a[236:239], a[176:179], v[240:243]
  buffer_load_dwordx4 v19, s[56:59], s80 offen offset:0 lds
  v_accvgpr_read_b32 v6, a116
  v_accvgpr_read_b32 v7, a117
  v_mfma_f32_16x16x32_bf16 v[244:247], a[236:239], a[180:183], v[244:247]
  ds_read_b64_tr_b16 a[254:255], v29 offset:33344
  v_accvgpr_read_b32 v8, a118
  v_accvgpr_read_b32 v9, a119
  v_mfma_f32_16x16x32_bf16 v[244:247], a[232:235], a[164:167], v[244:247]
  v_mul_f32 v2, s73, v2
  v_mul_f32 v3, s73, v3
  v_mul_f32 v4, s73, v4
  v_mfma_f32_16x16x32_bf16 v[248:251], a[232:235], a[168:171], v[248:251]
  ds_read_b64_tr_b16 a[224:225], v29 offset:32
  v_mul_f32 v5, s73, v5
  v_mul_f32 v6, s73, v6
  v_mfma_f32_16x16x32_bf16 v[248:251], a[236:239], a[184:187], v[248:251]
  v_mul_f32 v7, s73, v7
  v_mul_f32 v8, s73, v8
  v_mfma_f32_16x16x32_bf16 v[252:255], a[236:239], a[188:191], v[252:255]
  ds_read_b64_tr_b16 a[226:227], v29 offset:3616
  v_mul_f32 v9, s73, v9
  v_cvt_pk_bf16_f32 v2, v2, v3
  v_cvt_pk_bf16_f32 v3, v4, v5
  v_mfma_f32_16x16x32_bf16 v[252:255], a[232:235], a[172:175], v[252:255]
  s_waitcnt lgkmcnt(6)
  v_cvt_pk_bf16_f32 v4, v6, v7
  v_cvt_pk_bf16_f32 v5, v8, v9
  v_mfma_f32_16x16x32_bf16 v[32:35], a[240:243], a[192:195], v[32:35]
  ds_read_b64_tr_b16 a[228:229], v29 offset:22624
  v_accvgpr_write_b32 a156, v2
  v_accvgpr_write_b32 a157, v3
  v_mfma_f32_16x16x32_bf16 v[32:35], a[244:247], a[208:211], v[32:35]
  v_accvgpr_write_b32 a158, v4
  v_accvgpr_write_b32 a159, v5
  v_mfma_f32_16x16x32_bf16 v[36:39], a[244:247], a[212:215], v[36:39]
  ds_read_b64_tr_b16 a[230:231], v29 offset:26208
  v_accvgpr_read_b32 v2, a8
  v_accvgpr_read_b32 v3, a9
  v_mfma_f32_16x16x32_bf16 v[36:39], a[240:243], a[196:199], v[36:39]
  buffer_load_dwordx4 v20, s[56:59], s80 offen offset:1024 lds
  v_accvgpr_read_b32 v4, a10
  v_accvgpr_read_b32 v5, a11
  v_mfma_f32_16x16x32_bf16 v[40:43], a[240:243], a[200:203], v[40:43]
  ds_read_b64_tr_b16 a[232:233], v29 offset:7200
  v_accvgpr_read_b32 v6, a24
  v_accvgpr_read_b32 v7, a25
  v_mfma_f32_16x16x32_bf16 v[40:43], a[244:247], a[216:219], v[40:43]
  v_accvgpr_read_b32 v8, a26
  v_mfma_f32_16x16x32_bf16 v[44:47], a[244:247], a[220:223], v[44:47]
  ds_read_b64_tr_b16 a[234:235], v29 offset:10784
  v_accvgpr_read_b32 v9, a27
  v_mul_f32 v2, s73, v2
  v_mfma_f32_16x16x32_bf16 v[44:47], a[240:243], a[204:207], v[44:47]
  s_waitcnt lgkmcnt(6)
  v_mul_f32 v3, s73, v3
  v_mul_f32 v4, s73, v4
  v_mfma_f32_16x16x32_bf16 v[48:51], a[248:251], a[192:195], v[48:51]
  ds_read_b64_tr_b16 a[236:237], v29 offset:29792
  v_mul_f32 v5, s73, v5
  v_mul_f32 v6, s73, v6
  v_mfma_f32_16x16x32_bf16 v[48:51], a[252:255], a[208:211], v[48:51]
  v_mul_f32 v7, s73, v7
  v_mfma_f32_16x16x32_bf16 v[52:55], a[252:255], a[212:215], v[52:55]
  ds_read_b64_tr_b16 a[238:239], v29 offset:33376
  v_mul_f32 v8, s73, v8
  v_mul_f32 v9, s73, v9
  v_mfma_f32_16x16x32_bf16 v[52:55], a[248:251], a[196:199], v[52:55]
  v_cvt_pk_bf16_f32 v2, v2, v3
  v_cvt_pk_bf16_f32 v3, v4, v5
  v_mfma_f32_16x16x32_bf16 v[56:59], a[248:251], a[200:203], v[56:59]
  ds_read_b64_tr_b16 a[240:241], v29 offset:64
  v_cvt_pk_bf16_f32 v4, v6, v7
  v_cvt_pk_bf16_f32 v5, v8, v9
  v_mfma_f32_16x16x32_bf16 v[56:59], a[252:255], a[216:219], v[56:59]
  buffer_load_dwordx4 v19, s[56:59], s80 offen offset:2048 lds
  v_accvgpr_write_b32 a160, v2
  v_mfma_f32_16x16x32_bf16 v[60:63], a[252:255], a[220:223], v[60:63]
  ds_read_b64_tr_b16 a[242:243], v29 offset:3648
  v_accvgpr_write_b32 a161, v3
  v_accvgpr_write_b32 a162, v4
  v_mfma_f32_16x16x32_bf16 v[60:63], a[248:251], a[204:207], v[60:63]
  s_waitcnt lgkmcnt(6)
  v_accvgpr_write_b32 a163, v5
  v_accvgpr_read_b32 v2, a40
  v_mfma_f32_16x16x32_bf16 v[64:67], a[224:227], a[192:195], v[64:67]
  ds_read_b64_tr_b16 a[244:245], v29 offset:22656
  v_accvgpr_read_b32 v3, a41
  v_accvgpr_read_b32 v4, a42
  v_mfma_f32_16x16x32_bf16 v[64:67], a[228:231], a[208:211], v[64:67]
  v_accvgpr_read_b32 v5, a43
  v_mfma_f32_16x16x32_bf16 v[68:71], a[228:231], a[212:215], v[68:71]
  ds_read_b64_tr_b16 a[246:247], v29 offset:26240
  v_accvgpr_read_b32 v6, a56
  v_accvgpr_read_b32 v7, a57
  v_mfma_f32_16x16x32_bf16 v[68:71], a[224:227], a[196:199], v[68:71]
  v_accvgpr_read_b32 v8, a58
  v_accvgpr_read_b32 v9, a59
  v_mfma_f32_16x16x32_bf16 v[72:75], a[224:227], a[200:203], v[72:75]
  ds_read_b64_tr_b16 a[248:249], v29 offset:7232
  v_mul_f32 v2, s73, v2
  v_mul_f32 v3, s73, v3
  v_mfma_f32_16x16x32_bf16 v[72:75], a[228:231], a[216:219], v[72:75]
  buffer_load_dwordx4 v20, s[56:59], s80 offen offset:3072 lds
  v_mul_f32 v4, s73, v4
  v_mfma_f32_16x16x32_bf16 v[76:79], a[228:231], a[220:223], v[76:79]
  s_add_u32 s80, s80, 16384
  ds_read_b64_tr_b16 a[250:251], v29 offset:10816
  v_mul_f32 v5, s73, v5
  v_mul_f32 v6, s73, v6
  v_mfma_f32_16x16x32_bf16 v[76:79], a[224:227], a[204:207], v[76:79]
  s_waitcnt lgkmcnt(6)
  s_add_u32 m0, s78, 16384
  v_mul_f32 v7, s73, v7
  v_mul_f32 v8, s73, v8
  v_mfma_f32_16x16x32_bf16 v[80:83], a[232:235], a[192:195], v[80:83]
  ds_read_b64_tr_b16 a[252:253], v29 offset:29824
  v_mul_f32 v9, s73, v9
  v_cvt_pk_bf16_f32 v2, v2, v3
  v_mfma_f32_16x16x32_bf16 v[80:83], a[236:239], a[208:211], v[80:83]
  v_cvt_pk_bf16_f32 v3, v4, v5
  v_mfma_f32_16x16x32_bf16 v[84:87], a[236:239], a[212:215], v[84:87]
  ds_read_b64_tr_b16 a[254:255], v29 offset:33408
  v_cvt_pk_bf16_f32 v4, v6, v7
  v_cvt_pk_bf16_f32 v5, v8, v9
  v_mfma_f32_16x16x32_bf16 v[84:87], a[232:235], a[196:199], v[84:87]
  v_accvgpr_write_b32 a164, v2
  v_accvgpr_write_b32 a165, v3
  v_mfma_f32_16x16x32_bf16 v[88:91], a[232:235], a[200:203], v[88:91]
  ds_read_b64_tr_b16 a[224:225], v29 offset:96
  v_accvgpr_write_b32 a166, v4
  v_accvgpr_write_b32 a167, v5
  v_mfma_f32_16x16x32_bf16 v[88:91], a[236:239], a[216:219], v[88:91]
  v_accvgpr_read_b32 v2, a72
  v_mfma_f32_16x16x32_bf16 v[92:95], a[236:239], a[220:223], v[92:95]
  ds_read_b64_tr_b16 a[226:227], v29 offset:3680
  v_accvgpr_read_b32 v3, a73
  v_accvgpr_read_b32 v4, a74
  v_mfma_f32_16x16x32_bf16 v[92:95], a[232:235], a[204:207], v[92:95]
  s_waitcnt lgkmcnt(6)
  buffer_load_dwordx4 v19, s[56:59], s80 offen offset:0 lds
  v_accvgpr_read_b32 v5, a75
  v_accvgpr_read_b32 v6, a88
  v_mfma_f32_16x16x32_bf16 v[96:99], a[240:243], a[192:195], v[96:99]
  ds_read_b64_tr_b16 a[228:229], v29 offset:22688
  v_accvgpr_read_b32 v7, a89
  v_accvgpr_read_b32 v8, a90
  v_mfma_f32_16x16x32_bf16 v[96:99], a[244:247], a[208:211], v[96:99]
  v_accvgpr_read_b32 v9, a91
  v_mfma_f32_16x16x32_bf16 v[100:103], a[244:247], a[212:215], v[100:103]
  ds_read_b64_tr_b16 a[230:231], v29 offset:26272
  v_mul_f32 v2, s73, v2
  v_mul_f32 v3, s73, v3
  v_mfma_f32_16x16x32_bf16 v[100:103], a[240:243], a[196:199], v[100:103]
  v_mul_f32 v4, s73, v4
  v_mul_f32 v5, s73, v5
  v_mfma_f32_16x16x32_bf16 v[104:107], a[240:243], a[200:203], v[104:107]
  ds_read_b64_tr_b16 a[232:233], v29 offset:7264
  v_mul_f32 v6, s73, v6
  v_mul_f32 v7, s73, v7
  v_mfma_f32_16x16x32_bf16 v[104:107], a[244:247], a[216:219], v[104:107]
  v_mul_f32 v8, s73, v8
  v_mfma_f32_16x16x32_bf16 v[108:111], a[244:247], a[220:223], v[108:111]
  ds_read_b64_tr_b16 a[234:235], v29 offset:10848
  v_mul_f32 v9, s73, v9
  v_cvt_pk_bf16_f32 v2, v2, v3
  v_mfma_f32_16x16x32_bf16 v[108:111], a[240:243], a[204:207], v[108:111]
  s_waitcnt lgkmcnt(6)
  v_cvt_pk_bf16_f32 v3, v4, v5
  v_cvt_pk_bf16_f32 v4, v6, v7
  v_mfma_f32_16x16x32_bf16 v[112:115], a[248:251], a[192:195], v[112:115]
  ds_read_b64_tr_b16 a[236:237], v29 offset:29856
  v_cvt_pk_bf16_f32 v5, v8, v9
  v_accvgpr_write_b32 a168, v2
  v_mfma_f32_16x16x32_bf16 v[112:115], a[252:255], a[208:211], v[112:115]
  buffer_load_dwordx4 v20, s[56:59], s80 offen offset:1024 lds
  v_accvgpr_write_b32 a169, v3
  v_mfma_f32_16x16x32_bf16 v[116:119], a[252:255], a[212:215], v[116:119]
  ds_read_b64_tr_b16 a[238:239], v29 offset:33440
  v_accvgpr_write_b32 a170, v4
  v_accvgpr_write_b32 a171, v5
  v_mfma_f32_16x16x32_bf16 v[116:119], a[248:251], a[196:199], v[116:119]
  v_accvgpr_read_b32 v2, a104
  v_accvgpr_read_b32 v3, a105
  v_mfma_f32_16x16x32_bf16 v[120:123], a[248:251], a[200:203], v[120:123]
  ds_read_b64_tr_b16 a[240:241], v29 offset:128
  v_accvgpr_read_b32 v4, a106
  v_accvgpr_read_b32 v5, a107
  v_mfma_f32_16x16x32_bf16 v[120:123], a[252:255], a[216:219], v[120:123]
  v_accvgpr_read_b32 v6, a120
  v_mfma_f32_16x16x32_bf16 v[124:127], a[252:255], a[220:223], v[124:127]
  ds_read_b64_tr_b16 a[242:243], v29 offset:3712
  v_accvgpr_read_b32 v7, a121
  v_accvgpr_read_b32 v8, a122
  v_mfma_f32_16x16x32_bf16 v[124:127], a[248:251], a[204:207], v[124:127]
  s_waitcnt lgkmcnt(6)
  v_accvgpr_read_b32 v9, a123
  v_mul_f32 v2, s73, v2
  v_mfma_f32_16x16x32_bf16 v[128:131], a[224:227], a[192:195], v[128:131]
  ds_read_b64_tr_b16 a[244:245], v29 offset:22720
  v_mul_f32 v3, s73, v3
  v_mul_f32 v4, s73, v4
  v_mfma_f32_16x16x32_bf16 v[128:131], a[228:231], a[208:211], v[128:131]
  v_mul_f32 v5, s73, v5
  v_mfma_f32_16x16x32_bf16 v[132:135], a[228:231], a[212:215], v[132:135]
  ds_read_b64_tr_b16 a[246:247], v29 offset:26304
  v_mul_f32 v6, s73, v6
  v_mul_f32 v7, s73, v7
  v_mfma_f32_16x16x32_bf16 v[132:135], a[224:227], a[196:199], v[132:135]
  buffer_load_dwordx4 v19, s[56:59], s80 offen offset:2048 lds
  v_mul_f32 v8, s73, v8
  v_mul_f32 v9, s73, v9
  v_mfma_f32_16x16x32_bf16 v[136:139], a[224:227], a[200:203], v[136:139]
  ds_read_b64_tr_b16 a[248:249], v29 offset:7296
  v_cvt_pk_bf16_f32 v2, v2, v3
  v_cvt_pk_bf16_f32 v3, v4, v5
  v_mfma_f32_16x16x32_bf16 v[136:139], a[228:231], a[216:219], v[136:139]
  v_cvt_pk_bf16_f32 v4, v6, v7
  v_mfma_f32_16x16x32_bf16 v[140:143], a[228:231], a[220:223], v[140:143]
  ds_read_b64_tr_b16 a[250:251], v29 offset:10880
  v_cvt_pk_bf16_f32 v5, v8, v9
  v_accvgpr_write_b32 a172, v2
  v_mfma_f32_16x16x32_bf16 v[140:143], a[224:227], a[204:207], v[140:143]
  s_waitcnt lgkmcnt(6)
  v_accvgpr_write_b32 a173, v3
  v_accvgpr_write_b32 a174, v4
  v_mfma_f32_16x16x32_bf16 v[144:147], a[232:235], a[192:195], v[144:147]
  ds_read_b64_tr_b16 a[252:253], v29 offset:29888
  v_accvgpr_write_b32 a175, v5
  v_mfma_f32_16x16x32_bf16 v[144:147], a[236:239], a[208:211], v[144:147]
  v_accvgpr_read_b32 v2, a12
  v_accvgpr_read_b32 v3, a13
  v_mfma_f32_16x16x32_bf16 v[148:151], a[236:239], a[212:215], v[148:151]
  ds_read_b64_tr_b16 a[254:255], v29 offset:33472
  v_accvgpr_read_b32 v4, a14
  v_accvgpr_read_b32 v5, a15
  v_mfma_f32_16x16x32_bf16 v[148:151], a[232:235], a[196:199], v[148:151]
  buffer_load_dwordx4 v20, s[56:59], s80 offen offset:3072 lds
  v_accvgpr_read_b32 v6, a28
  v_accvgpr_read_b32 v7, a29
  v_mfma_f32_16x16x32_bf16 v[152:155], a[232:235], a[200:203], v[152:155]
  s_add_u32 s80, s80, 16384
  ds_read_b64_tr_b16 a[224:225], v29 offset:160
  v_accvgpr_read_b32 v8, a30
  v_mfma_f32_16x16x32_bf16 v[152:155], a[236:239], a[216:219], v[152:155]
  s_add_u32 m0, s78, 32768
  v_accvgpr_read_b32 v9, a31
  v_mul_f32 v2, s73, v2
  v_mfma_f32_16x16x32_bf16 v[156:159], a[236:239], a[220:223], v[156:159]
  ds_read_b64_tr_b16 a[226:227], v29 offset:3744
  v_mul_f32 v3, s73, v3
  v_mul_f32 v4, s73, v4
  v_mfma_f32_16x16x32_bf16 v[156:159], a[232:235], a[204:207], v[156:159]
  s_waitcnt lgkmcnt(6)
  v_mul_f32 v5, s73, v5
  v_mul_f32 v6, s73, v6
  v_mfma_f32_16x16x32_bf16 v[160:163], a[240:243], a[192:195], v[160:163]
  ds_read_b64_tr_b16 a[228:229], v29 offset:22752
  v_mul_f32 v7, s73, v7
  v_mfma_f32_16x16x32_bf16 v[160:163], a[244:247], a[208:211], v[160:163]
  v_mul_f32 v8, s73, v8
  v_mul_f32 v9, s73, v9
  v_mfma_f32_16x16x32_bf16 v[164:167], a[244:247], a[212:215], v[164:167]
  ds_read_b64_tr_b16 a[230:231], v29 offset:26336
  v_cvt_pk_bf16_f32 v2, v2, v3
  v_cvt_pk_bf16_f32 v3, v4, v5
  v_mfma_f32_16x16x32_bf16 v[164:167], a[240:243], a[196:199], v[164:167]
  v_cvt_pk_bf16_f32 v4, v6, v7
  v_cvt_pk_bf16_f32 v5, v8, v9
  v_mfma_f32_16x16x32_bf16 v[168:171], a[240:243], a[200:203], v[168:171]
  ds_read_b64_tr_b16 a[232:233], v29 offset:7328
  v_accvgpr_write_b32 a176, v2
  v_mfma_f32_16x16x32_bf16 v[168:171], a[244:247], a[216:219], v[168:171]
  buffer_load_dwordx4 v19, s[56:59], s80 offen offset:0 lds
  v_accvgpr_write_b32 a177, v3
  v_accvgpr_write_b32 a178, v4
  v_mfma_f32_16x16x32_bf16 v[172:175], a[244:247], a[220:223], v[172:175]
  ds_read_b64_tr_b16 a[234:235], v29 offset:10912
  v_accvgpr_write_b32 a179, v5
  v_accvgpr_read_b32 v2, a44
  v_mfma_f32_16x16x32_bf16 v[172:175], a[240:243], a[204:207], v[172:175]
  s_waitcnt lgkmcnt(6)
  v_accvgpr_read_b32 v3, a45
  v_accvgpr_read_b32 v4, a46
  v_mfma_f32_16x16x32_bf16 v[176:179], a[248:251], a[192:195], v[176:179]
  ds_read_b64_tr_b16 a[236:237], v29 offset:29920
  v_accvgpr_read_b32 v5, a47
  v_mfma_f32_16x16x32_bf16 v[176:179], a[252:255], a[208:211], v[176:179]
  v_accvgpr_read_b32 v6, a60
  v_accvgpr_read_b32 v7, a61
  v_mfma_f32_16x16x32_bf16 v[180:183], a[252:255], a[212:215], v[180:183]
  ds_read_b64_tr_b16 a[238:239], v29 offset:33504
  v_accvgpr_read_b32 v8, a62
  v_accvgpr_read_b32 v9, a63
  v_mfma_f32_16x16x32_bf16 v[180:183], a[248:251], a[196:199], v[180:183]
  v_mul_f32 v2, s73, v2
  v_mul_f32 v3, s73, v3
  v_mfma_f32_16x16x32_bf16 v[184:187], a[248:251], a[200:203], v[184:187]
  ds_read_b64_tr_b16 a[240:241], v29 offset:192
  v_mul_f32 v4, s73, v4
  v_mfma_f32_16x16x32_bf16 v[184:187], a[252:255], a[216:219], v[184:187]
  v_mul_f32 v5, s73, v5
  v_mul_f32 v6, s73, v6
  v_mfma_f32_16x16x32_bf16 v[188:191], a[252:255], a[220:223], v[188:191]
  ds_read_b64_tr_b16 a[242:243], v29 offset:3776
  v_mul_f32 v7, s73, v7
  v_mul_f32 v8, s73, v8
  v_mfma_f32_16x16x32_bf16 v[188:191], a[248:251], a[204:207], v[188:191]
  s_waitcnt lgkmcnt(6)
  buffer_load_dwordx4 v20, s[56:59], s80 offen offset:1024 lds
  v_mul_f32 v9, s73, v9
  v_cvt_pk_bf16_f32 v2, v2, v3
  v_mfma_f32_16x16x32_bf16 v[192:195], a[224:227], a[192:195], v[192:195]
  ds_read_b64_tr_b16 a[244:245], v29 offset:22784
  v_cvt_pk_bf16_f32 v3, v4, v5
  v_mfma_f32_16x16x32_bf16 v[192:195], a[228:231], a[208:211], v[192:195]
  v_cvt_pk_bf16_f32 v4, v6, v7
  v_cvt_pk_bf16_f32 v5, v8, v9
  v_mfma_f32_16x16x32_bf16 v[196:199], a[228:231], a[212:215], v[196:199]
  ds_read_b64_tr_b16 a[246:247], v29 offset:26368
  v_accvgpr_write_b32 a180, v2
  v_accvgpr_write_b32 a181, v3
  v_mfma_f32_16x16x32_bf16 v[196:199], a[224:227], a[196:199], v[196:199]
  v_accvgpr_write_b32 a182, v4
  v_accvgpr_write_b32 a183, v5
  v_mfma_f32_16x16x32_bf16 v[200:203], a[224:227], a[200:203], v[200:203]
  ds_read_b64_tr_b16 a[248:249], v29 offset:7360
  v_accvgpr_read_b32 v2, a76
  v_mfma_f32_16x16x32_bf16 v[200:203], a[228:231], a[216:219], v[200:203]
  v_accvgpr_read_b32 v3, a77
  v_accvgpr_read_b32 v4, a78
  v_mfma_f32_16x16x32_bf16 v[204:207], a[228:231], a[220:223], v[204:207]
  ds_read_b64_tr_b16 a[250:251], v29 offset:10944
  v_accvgpr_read_b32 v5, a79
  v_accvgpr_read_b32 v6, a92
  v_mfma_f32_16x16x32_bf16 v[204:207], a[224:227], a[204:207], v[204:207]
  s_waitcnt lgkmcnt(6)
  v_accvgpr_read_b32 v7, a93
  v_accvgpr_read_b32 v8, a94
  v_mfma_f32_16x16x32_bf16 v[208:211], a[232:235], a[192:195], v[208:211]
  ds_read_b64_tr_b16 a[252:253], v29 offset:29952
  v_accvgpr_read_b32 v9, a95
  v_mfma_f32_16x16x32_bf16 v[208:211], a[236:239], a[208:211], v[208:211]
  buffer_load_dwordx4 v19, s[56:59], s80 offen offset:2048 lds
  v_mul_f32 v2, s73, v2
  v_mul_f32 v3, s73, v3
  v_mfma_f32_16x16x32_bf16 v[212:215], a[236:239], a[212:215], v[212:215]
  ds_read_b64_tr_b16 a[254:255], v29 offset:33536
  v_mul_f32 v4, s73, v4
  v_mul_f32 v5, s73, v5
  v_mfma_f32_16x16x32_bf16 v[212:215], a[232:235], a[196:199], v[212:215]
  v_mul_f32 v6, s73, v6
  v_mul_f32 v7, s73, v7
  v_mfma_f32_16x16x32_bf16 v[216:219], a[232:235], a[200:203], v[216:219]
  v_mul_f32 v8, s73, v8
  v_mfma_f32_16x16x32_bf16 v[216:219], a[236:239], a[216:219], v[216:219]
  v_mul_f32 v9, s73, v9
  v_cvt_pk_bf16_f32 v2, v2, v3
  v_mfma_f32_16x16x32_bf16 v[220:223], a[236:239], a[220:223], v[220:223]
  v_cvt_pk_bf16_f32 v3, v4, v5
  v_cvt_pk_bf16_f32 v4, v6, v7
  v_mfma_f32_16x16x32_bf16 v[220:223], a[232:235], a[204:207], v[220:223]
  s_waitcnt lgkmcnt(4)
  v_cvt_pk_bf16_f32 v5, v8, v9
  v_accvgpr_write_b32 a184, v2
  v_mfma_f32_16x16x32_bf16 v[224:227], a[240:243], a[192:195], v[224:227]
  v_accvgpr_write_b32 a185, v3
  v_mfma_f32_16x16x32_bf16 v[224:227], a[244:247], a[208:211], v[224:227]
  buffer_load_dwordx4 v20, s[56:59], s80 offen offset:3072 lds
  v_accvgpr_write_b32 a186, v4
  v_accvgpr_write_b32 a187, v5
  v_mfma_f32_16x16x32_bf16 v[228:231], a[244:247], a[212:215], v[228:231]
  s_add_u32 s80, s80, 16384
  v_accvgpr_read_b32 v2, a108
  v_accvgpr_read_b32 v3, a109
  v_mfma_f32_16x16x32_bf16 v[228:231], a[240:243], a[196:199], v[228:231]
  v_accvgpr_read_b32 v4, a110
  v_accvgpr_read_b32 v5, a111
  v_mfma_f32_16x16x32_bf16 v[232:235], a[240:243], a[200:203], v[232:235]
  v_accvgpr_read_b32 v6, a124
  v_mfma_f32_16x16x32_bf16 v[232:235], a[244:247], a[216:219], v[232:235]
  v_accvgpr_read_b32 v7, a125
  v_accvgpr_read_b32 v8, a126
  v_mfma_f32_16x16x32_bf16 v[236:239], a[244:247], a[220:223], v[236:239]
  v_accvgpr_read_b32 v9, a127
  v_mul_f32 v2, s73, v2
  v_mfma_f32_16x16x32_bf16 v[236:239], a[240:243], a[204:207], v[236:239]
  s_waitcnt lgkmcnt(0)
  v_mul_f32 v3, s73, v3
  v_mul_f32 v4, s73, v4
  v_mfma_f32_16x16x32_bf16 v[240:243], a[248:251], a[192:195], v[240:243]
  v_mul_f32 v5, s73, v5
  v_mfma_f32_16x16x32_bf16 v[240:243], a[252:255], a[208:211], v[240:243]
  v_mul_f32 v6, s73, v6
  v_mul_f32 v7, s73, v7
  v_mfma_f32_16x16x32_bf16 v[244:247], a[252:255], a[212:215], v[244:247]
  v_mul_f32 v8, s73, v8
  v_mul_f32 v9, s73, v9
  v_mfma_f32_16x16x32_bf16 v[244:247], a[248:251], a[196:199], v[244:247]
  v_cvt_pk_bf16_f32 v2, v2, v3
  v_cvt_pk_bf16_f32 v3, v4, v5
  v_mfma_f32_16x16x32_bf16 v[248:251], a[248:251], a[200:203], v[248:251]
  v_cvt_pk_bf16_f32 v4, v6, v7
  v_mfma_f32_16x16x32_bf16 v[248:251], a[252:255], a[216:219], v[248:251]
  v_cvt_pk_bf16_f32 v5, v8, v9
  v_accvgpr_write_b32 a188, v2
  v_mfma_f32_16x16x32_bf16 v[252:255], a[252:255], a[220:223], v[252:255]
  v_accvgpr_write_b32 a189, v3
  v_accvgpr_write_b32 a190, v4
  v_mfma_f32_16x16x32_bf16 v[252:255], a[248:251], a[204:207], v[252:255]
  v_accvgpr_write_b32 a191, v5
  s_lshl_b32 s72, s8, 15
  s_mul_i32 s73, s8, 28672
  s_add_u32 s36, s20, s72
  s_addc_u32 s37, s21, 0
  s_and_b32 s37, s37, 0xffff
  s_mov_b32 s38, 32768
  s_mov_b32 s39, 0x20000
  s_add_u32 s40, s22, s73
  s_addc_u32 s41, s23, 0
  s_and_b32 s41, s41, 0xffff
  s_mov_b32 s42, 28672
  s_mov_b32 s43, 0x20000
  s_mov_b32 s69, 0
  s_mov_b32 s44, s24
  s_and_b32 s45, s25, 0xffff
  s_mov_b32 s46, 77824
  s_mov_b32 s47, 0x20000
  ds_read_b128 a[192:195], v12 offset:0
  ds_read_b128 a[196:199], v12 offset:512
  ds_read_b128 a[200:203], v12 offset:1024
  ds_read_b128 a[204:207], v12 offset:1536
  ds_read_b128 a[208:211], v12 offset:4096
  ds_read_b128 a[212:215], v12 offset:4608
  ds_read_b128 a[216:219], v12 offset:5120
  ds_read_b128 a[220:223], v12 offset:5632
  v_max_f32 v96, 0, v96
  v_max_f32 v97, 0, v97
  v_max_f32 v98, 0, v98
  v_max_f32 v99, 0, v99
  v_pk_mul_f32 v[96:97], v[96:97], v[112:113]
  v_pk_mul_f32 v[98:99], v[98:99], v[114:115]
  v_cvt_pk_bf16_f32 v112, v96, v97
  v_cvt_pk_bf16_f32 v113, v98, v99
  v_max_f32 v128, 0, v128
  v_max_f32 v129, 0, v129
  v_max_f32 v130, 0, v130
  v_max_f32 v131, 0, v131
  v_pk_mul_f32 v[128:129], v[128:129], v[144:145]
  v_pk_mul_f32 v[130:131], v[130:131], v[146:147]
  v_cvt_pk_bf16_f32 v114, v128, v129
  v_cvt_pk_bf16_f32 v115, v130, v131
  v_max_f32 v100, 0, v100
  v_max_f32 v101, 0, v101
  v_max_f32 v102, 0, v102
  v_max_f32 v103, 0, v103
  v_pk_mul_f32 v[100:101], v[100:101], v[116:117]
  v_pk_mul_f32 v[102:103], v[102:103], v[118:119]
  v_cvt_pk_bf16_f32 v116, v100, v101
  v_cvt_pk_bf16_f32 v117, v102, v103
  v_max_f32 v132, 0, v132
  v_max_f32 v133, 0, v133
  v_max_f32 v134, 0, v134
  v_max_f32 v135, 0, v135
  v_pk_mul_f32 v[132:133], v[132:133], v[148:149]
  v_pk_mul_f32 v[134:135], v[134:135], v[150:151]
  v_cvt_pk_bf16_f32 v118, v132, v133
  v_cvt_pk_bf16_f32 v119, v134, v135
  v_max_f32 v104, 0, v104
  v_max_f32 v105, 0, v105
  v_max_f32 v106, 0, v106
  v_max_f32 v107, 0, v107
  v_pk_mul_f32 v[104:105], v[104:105], v[120:121]
  v_pk_mul_f32 v[106:107], v[106:107], v[122:123]
  v_cvt_pk_bf16_f32 v120, v104, v105
  v_cvt_pk_bf16_f32 v121, v106, v107
  v_max_f32 v136, 0, v136
  v_max_f32 v137, 0, v137
  v_max_f32 v138, 0, v138
  v_max_f32 v139, 0, v139
  v_pk_mul_f32 v[136:137], v[136:137], v[152:153]
  v_pk_mul_f32 v[138:139], v[138:139], v[154:155]
  v_cvt_pk_bf16_f32 v122, v136, v137
  v_cvt_pk_bf16_f32 v123, v138, v139
  v_max_f32 v108, 0, v108
  v_max_f32 v109, 0, v109
  v_max_f32 v110, 0, v110
  v_max_f32 v111, 0, v111
  v_pk_mul_f32 v[108:109], v[108:109], v[124:125]
  v_pk_mul_f32 v[110:111], v[110:111], v[126:127]
  v_cvt_pk_bf16_f32 v124, v108, v109
  v_cvt_pk_bf16_f32 v125, v110, v111
  v_max_f32 v140, 0, v140
  v_max_f32 v141, 0, v141
  v_max_f32 v142, 0, v142
  v_max_f32 v143, 0, v143
  v_pk_mul_f32 v[140:141], v[140:141], v[156:157]
  v_pk_mul_f32 v[142:143], v[142:143], v[158:159]
  v_cvt_pk_bf16_f32 v126, v140, v141
  v_cvt_pk_bf16_f32 v127, v142, v143
  v_max_f32 v32, 0, v32
  v_max_f32 v33, 0, v33
  v_max_f32 v34, 0, v34
  v_max_f32 v35, 0, v35
  v_pk_mul_f32 v[32:33], v[32:33], v[48:49]
  v_pk_mul_f32 v[34:35], v[34:35], v[50:51]
  v_cvt_pk_bf16_f32 v96, v32, v33
  v_cvt_pk_bf16_f32 v97, v34, v35
  v_max_f32 v64, 0, v64
  v_max_f32 v65, 0, v65
  v_max_f32 v66, 0, v66
  v_max_f32 v67, 0, v67
  v_pk_mul_f32 v[64:65], v[64:65], v[80:81]
  v_pk_mul_f32 v[66:67], v[66:67], v[82:83]
  v_cvt_pk_bf16_f32 v98, v64, v65
  v_cvt_pk_bf16_f32 v99, v66, v67
  v_max_f32 v36, 0, v36
  v_max_f32 v37, 0, v37
  v_max_f32 v38, 0, v38
  v_max_f32 v39, 0, v39
  v_pk_mul_f32 v[36:37], v[36:37], v[52:53]
  v_pk_mul_f32 v[38:39], v[38:39], v[54:55]
  v_cvt_pk_bf16_f32 v100, v36, v37
  v_cvt_pk_bf16_f32 v101, v38, v39
  v_max_f32 v68, 0, v68
  v_max_f32 v69, 0, v69
  v_max_f32 v70, 0, v70
  v_max_f32 v71, 0, v71
  v_pk_mul_f32 v[68:69], v[68:69], v[84:85]
  v_pk_mul_f32 v[70:71], v[70:71], v[86:87]
  v_cvt_pk_bf16_f32 v102, v68, v69
  v_cvt_pk_bf16_f32 v103, v70, v71
  v_max_f32 v40, 0, v40
  v_max_f32 v41, 0, v41
  v_max_f32 v42, 0, v42
  v_max_f32 v43, 0, v43
  v_pk_mul_f32 v[40:41], v[40:41], v[56:57]
  v_pk_mul_f32 v[42:43], v[42:43], v[58:59]
  v_cvt_pk_bf16_f32 v104, v40, v41
  v_cvt_pk_bf16_f32 v105, v42, v43
  v_max_f32 v72, 0, v72
  v_max_f32 v73, 0, v73
  v_max_f32 v74, 0, v74
  v_max_f32 v75, 0, v75
  v_pk_mul_f32 v[72:73], v[72:73], v[88:89]
  v_pk_mul_f32 v[74:75], v[74:75], v[90:91]
  v_cvt_pk_bf16_f32 v106, v72, v73
  v_cvt_pk_bf16_f32 v107, v74, v75
  v_max_f32 v44, 0, v44
  v_max_f32 v45, 0, v45
  v_max_f32 v46, 0, v46
  v_max_f32 v47, 0, v47
  v_pk_mul_f32 v[44:45], v[44:45], v[60:61]
  v_pk_mul_f32 v[46:47], v[46:47], v[62:63]
  v_cvt_pk_bf16_f32 v108, v44, v45
  v_cvt_pk_bf16_f32 v109, v46, v47
  v_max_f32 v76, 0, v76
  v_max_f32 v77, 0, v77
  v_max_f32 v78, 0, v78
  v_max_f32 v79, 0, v79
  v_pk_mul_f32 v[76:77], v[76:77], v[92:93]
  v_pk_mul_f32 v[78:79], v[78:79], v[94:95]
  v_cvt_pk_bf16_f32 v110, v76, v77
  v_cvt_pk_bf16_f32 v111, v78, v79
  v_max_f32 v160, 0, v160
  v_max_f32 v161, 0, v161
  v_max_f32 v162, 0, v162
  v_max_f32 v163, 0, v163
  v_pk_mul_f32 v[160:161], v[160:161], v[176:177]
  v_pk_mul_f32 v[162:163], v[162:163], v[178:179]
  v_cvt_pk_bf16_f32 v128, v160, v161
  v_cvt_pk_bf16_f32 v129, v162, v163
  v_max_f32 v192, 0, v192
  v_max_f32 v193, 0, v193
  v_max_f32 v194, 0, v194
  v_max_f32 v195, 0, v195
  v_pk_mul_f32 v[192:193], v[192:193], v[208:209]
  v_pk_mul_f32 v[194:195], v[194:195], v[210:211]
  v_cvt_pk_bf16_f32 v130, v192, v193
  v_cvt_pk_bf16_f32 v131, v194, v195
  v_max_f32 v164, 0, v164
  v_max_f32 v165, 0, v165
  v_max_f32 v166, 0, v166
  v_max_f32 v167, 0, v167
  v_pk_mul_f32 v[164:165], v[164:165], v[180:181]
  v_pk_mul_f32 v[166:167], v[166:167], v[182:183]
  v_cvt_pk_bf16_f32 v132, v164, v165
  v_cvt_pk_bf16_f32 v133, v166, v167
  v_max_f32 v196, 0, v196
  v_max_f32 v197, 0, v197
  v_max_f32 v198, 0, v198
  v_max_f32 v199, 0, v199
  v_pk_mul_f32 v[196:197], v[196:197], v[212:213]
  v_pk_mul_f32 v[198:199], v[198:199], v[214:215]
  v_cvt_pk_bf16_f32 v134, v196, v197
  v_cvt_pk_bf16_f32 v135, v198, v199
  v_max_f32 v168, 0, v168
  v_max_f32 v169, 0, v169
  v_max_f32 v170, 0, v170
  v_max_f32 v171, 0, v171
  v_pk_mul_f32 v[168:169], v[168:169], v[184:185]
  v_pk_mul_f32 v[170:171], v[170:171], v[186:187]
  v_cvt_pk_bf16_f32 v136, v168, v169
  v_cvt_pk_bf16_f32 v137, v170, v171
  v_max_f32 v200, 0, v200
  v_max_f32 v201, 0, v201
  v_max_f32 v202, 0, v202
  v_max_f32 v203, 0, v203
  v_pk_mul_f32 v[200:201], v[200:201], v[216:217]
  v_pk_mul_f32 v[202:203], v[202:203], v[218:219]
  v_cvt_pk_bf16_f32 v138, v200, v201
  v_cvt_pk_bf16_f32 v139, v202, v203
  v_max_f32 v172, 0, v172
  v_max_f32 v173, 0, v173
  v_max_f32 v174, 0, v174
  v_max_f32 v175, 0, v175
  v_pk_mul_f32 v[172:173], v[172:173], v[188:189]
  v_pk_mul_f32 v[174:175], v[174:175], v[190:191]
  v_cvt_pk_bf16_f32 v140, v172, v173
  v_cvt_pk_bf16_f32 v141, v174, v175
  v_max_f32 v204, 0, v204
  v_max_f32 v205, 0, v205
  v_max_f32 v206, 0, v206
  v_max_f32 v207, 0, v207
  v_pk_mul_f32 v[204:205], v[204:205], v[220:221]
  v_pk_mul_f32 v[206:207], v[206:207], v[222:223]
  v_cvt_pk_bf16_f32 v142, v204, v205
  v_cvt_pk_bf16_f32 v143, v206, v207
  v_max_f32 v224, 0, v224
  v_max_f32 v225, 0, v225
  v_max_f32 v226, 0, v226
  v_max_f32 v227, 0, v227
  v_pk_mul_f32 v[224:225], v[224:225], v[240:241]
  v_pk_mul_f32 v[226:227], v[226:227], v[242:243]
  v_cvt_pk_bf16_f32 v144, v224, v225
  v_cvt_pk_bf16_f32 v145, v226, v227
  v_max_f32 v228, 0, v228
  v_max_f32 v229, 0, v229
  v_max_f32 v230, 0, v230
  v_max_f32 v231, 0, v231
  v_pk_mul_f32 v[228:229], v[228:229], v[244:245]
  v_pk_mul_f32 v[230:231], v[230:231], v[246:247]
  v_cvt_pk_bf16_f32 v148, v228, v229
  v_cvt_pk_bf16_f32 v149, v230, v231
  v_max_f32 v232, 0, v232
  v_max_f32 v233, 0, v233
  v_max_f32 v234, 0, v234
  v_max_f32 v235, 0, v235
  v_pk_mul_f32 v[232:233], v[232:233], v[248:249]
  v_pk_mul_f32 v[234:235], v[234:235], v[250:251]
  v_cvt_pk_bf16_f32 v152, v232, v233
  v_cvt_pk_bf16_f32 v153, v234, v235
  v_max_f32 v236, 0, v236
  v_max_f32 v237, 0, v237
  v_max_f32 v238, 0, v238
  v_max_f32 v239, 0, v239
  v_pk_mul_f32 v[236:237], v[236:237], v[252:253]
  v_pk_mul_f32 v[238:239], v[238:239], v[254:255]
  v_cvt_pk_bf16_f32 v156, v236, v237
  v_cvt_pk_bf16_f32 v157, v238, v239
  v_mov_b32 v23, 0
  v_mov_b32 v24, 0
  v_mov_b32 v25, 0
  v_mov_b32 v26, 0
  s_mov_b32 s76, 7
  s_waitcnt lgkmcnt(4)
  v_mfma_f32_16x16x32_bf16 v[32:35], a[192:195], a[128:131], 0
  v_accvgpr_write_b32 a0, 0
  v_accvgpr_write_b32 a1, 0
  v_mfma_f32_16x16x32_bf16 v[32:35], a[196:199], a[132:135], v[32:35]
  ds_read_b128 a[224:227], v12 offset:8192
  v_accvgpr_write_b32 a2, 0
  v_accvgpr_write_b32 a3, 0
  v_mfma_f32_16x16x32_bf16 v[32:35], a[200:203], a[136:139], v[32:35]
  buffer_store_dwordx4 v[112:115], v10, s[60:63], s83 offen offset:0 sc1
  v_accvgpr_write_b32 a4, 0
  v_accvgpr_write_b32 a5, 0
  v_mfma_f32_16x16x32_bf16 v[32:35], a[204:207], a[140:143], v[32:35]
  v_accvgpr_write_b32 a6, 0
  v_mfma_f32_16x16x32_bf16 v[36:39], a[204:207], a[156:159], 0
  v_accvgpr_write_b32 a7, 0
  v_accvgpr_write_b32 a8, 0
  v_mfma_f32_16x16x32_bf16 v[36:39], a[200:203], a[152:155], v[36:39]
  ds_read_b128 a[228:231], v12 offset:8704
  v_accvgpr_write_b32 a9, 0
  v_accvgpr_write_b32 a10, 0
  v_mfma_f32_16x16x32_bf16 v[36:39], a[196:199], a[148:151], v[36:39]
  v_accvgpr_write_b32 a11, 0
  v_accvgpr_write_b32 a12, 0
  v_mfma_f32_16x16x32_bf16 v[36:39], a[192:195], a[144:147], v[36:39]
  v_accvgpr_write_b32 a13, 0
  v_mfma_f32_16x16x32_bf16 v[40:43], a[192:195], a[160:163], 0
  buffer_store_dwordx4 v[116:119], v10, s[60:63], s83 offen offset:1024 sc1
  v_accvgpr_write_b32 a14, 0
  v_accvgpr_write_b32 a15, 0
  v_mfma_f32_16x16x32_bf16 v[40:43], a[196:199], a[164:167], v[40:43]
  ds_read_b128 a[232:235], v12 offset:9216
  v_accvgpr_write_b32 a16, 0
  v_accvgpr_write_b32 a17, 0
  v_mfma_f32_16x16x32_bf16 v[40:43], a[200:203], a[168:171], v[40:43]
  v_accvgpr_write_b32 a18, 0
  v_accvgpr_write_b32 a19, 0
  v_mfma_f32_16x16x32_bf16 v[40:43], a[204:207], a[172:175], v[40:43]
  v_accvgpr_write_b32 a20, 0
  v_mfma_f32_16x16x32_bf16 v[44:47], a[204:207], a[188:191], 0
  v_accvgpr_write_b32 a21, 0
  v_accvgpr_write_b32 a22, 0
  v_mfma_f32_16x16x32_bf16 v[44:47], a[200:203], a[184:187], v[44:47]
  ds_read_b128 a[236:239], v12 offset:9728
  v_accvgpr_write_b32 a23, 0
  v_accvgpr_write_b32 a24, 0
  v_mfma_f32_16x16x32_bf16 v[44:47], a[196:199], a[180:183], v[44:47]
  buffer_store_dwordx4 v[120:123], v10, s[60:63], s83 offen offset:2048 sc1
  v_accvgpr_write_b32 a25, 0
  v_accvgpr_write_b32 a26, 0
  v_mfma_f32_16x16x32_bf16 v[44:47], a[192:195], a[176:179], v[44:47]
  s_waitcnt lgkmcnt(4)
  v_accvgpr_write_b32 a27, 0
  s_nop 7
  v_max3_f32 v27, v32, v33, v34
  v_max_f32 v27, v27, v35
  v_max3_f32 v28, v36, v37, v38
  v_max_f32 v28, v28, v39
  v_max3_f32 v29, v40, v41, v42
  v_max_f32 v29, v29, v43
  v_max3_f32 v30, v44, v45, v46
  v_max_f32 v30, v30, v47
  v_mov_b32 v254, v27
  s_nop 1
  v_permlane32_swap_b32 v27, v254
  v_max_f32 v27, v27, v254
  v_mov_b32 v254, v27
  s_nop 1
  v_permlane16_swap_b32 v27, v254
  v_max_f32 v27, v27, v254
  v_mov_b32 v254, v28
  s_nop 1
  v_permlane32_swap_b32 v28, v254
  v_max_f32 v28, v28, v254
  v_mov_b32 v254, v28
  s_nop 1
  v_permlane16_swap_b32 v28, v254
  v_max_f32 v28, v28, v254
  v_mov_b32 v254, v29
  s_nop 1
  v_permlane32_swap_b32 v29, v254
  v_max_f32 v29, v29, v254
  v_mov_b32 v254, v29
  s_nop 1
  v_permlane16_swap_b32 v29, v254
  v_max_f32 v29, v29, v254
  v_mov_b32 v254, v30
  s_nop 1
  v_permlane32_swap_b32 v30, v254
  v_max_f32 v30, v30, v254
  v_mov_b32 v254, v30
  s_nop 1
  v_permlane16_swap_b32 v30, v254
  v_max_f32 v30, v30, v254
  v_sub_f32 v224, 0, v27
  v_sub_f32 v225, 0, v27
  v_sub_f32 v226, 0, v27
  v_sub_f32 v227, 0, v27
  v_sub_f32 v228, 0, v28
  v_sub_f32 v229, 0, v28
  v_sub_f32 v230, 0, v28
  v_sub_f32 v231, 0, v28
  v_sub_f32 v232, 0, v29
  v_sub_f32 v233, 0, v29
  v_sub_f32 v234, 0, v29
  v_sub_f32 v235, 0, v29
  v_sub_f32 v236, 0, v30
  v_sub_f32 v237, 0, v30
  v_sub_f32 v238, 0, v30
  v_sub_f32 v239, 0, v30
  v_sub_f32 v32, v32, v27
  v_sub_f32 v33, v33, v27
  v_sub_f32 v34, v34, v27
  v_sub_f32 v35, v35, v27
  v_sub_f32 v36, v36, v28
  v_sub_f32 v37, v37, v28
  v_sub_f32 v38, v38, v28
  v_sub_f32 v39, v39, v28
  v_sub_f32 v40, v40, v29
  v_sub_f32 v41, v41, v29
  v_sub_f32 v42, v42, v29
  v_sub_f32 v43, v43, v29
  v_sub_f32 v44, v44, v30
  v_sub_f32 v45, v45, v30
  v_sub_f32 v46, v46, v30
  v_sub_f32 v47, v47, v30
  v_mfma_f32_16x16x32_bf16 v[48:51], a[208:211], a[128:131], v[224:227]
  v_accvgpr_write_b32 a28, 0
  v_accvgpr_write_b32 a29, 0
  v_mfma_f32_16x16x32_bf16 v[48:51], a[212:215], a[132:135], v[48:51]
  ds_read_b128 a[240:243], v12 offset:12288
  v_accvgpr_write_b32 a30, 0
  v_accvgpr_write_b32 a31, 0
  v_mfma_f32_16x16x32_bf16 v[48:51], a[216:219], a[136:139], v[48:51]
  v_accvgpr_write_b32 a32, 0
  v_accvgpr_write_b32 a33, 0
  v_mfma_f32_16x16x32_bf16 v[48:51], a[220:223], a[140:143], v[48:51]
  v_accvgpr_write_b32 a34, 0
  v_mfma_f32_16x16x32_bf16 v[52:55], a[220:223], a[156:159], v[228:231]
  buffer_store_dwordx4 v[124:127], v10, s[60:63], s83 offen offset:3072 sc1
  v_accvgpr_write_b32 a35, 0
  v_accvgpr_write_b32 a36, 0
  v_mfma_f32_16x16x32_bf16 v[52:55], a[216:219], a[152:155], v[52:55]
  ds_read_b128 a[244:247], v12 offset:12800
  v_accvgpr_write_b32 a37, 0
  v_accvgpr_write_b32 a38, 0
  v_mfma_f32_16x16x32_bf16 v[52:55], a[212:215], a[148:151], v[52:55]
  v_accvgpr_write_b32 a39, 0
  v_accvgpr_write_b32 a40, 0
  v_mfma_f32_16x16x32_bf16 v[52:55], a[208:211], a[144:147], v[52:55]
  v_accvgpr_write_b32 a41, 0
  v_mfma_f32_16x16x32_bf16 v[56:59], a[208:211], a[160:163], v[232:235]
  v_accvgpr_write_b32 a42, 0
  v_accvgpr_write_b32 a43, 0
  v_mfma_f32_16x16x32_bf16 v[56:59], a[212:215], a[164:167], v[56:59]
  ds_read_b128 a[248:251], v12 offset:13312
  v_accvgpr_write_b32 a44, 0
  v_accvgpr_write_b32 a45, 0
  v_mfma_f32_16x16x32_bf16 v[56:59], a[216:219], a[168:171], v[56:59]
  buffer_store_dwordx4 v[96:99], v10, s[60:63], 0 offen offset:0 sc1
  v_accvgpr_write_b32 a46, 0
  v_accvgpr_write_b32 a47, 0
  v_mfma_f32_16x16x32_bf16 v[56:59], a[220:223], a[172:175], v[56:59]
  v_accvgpr_write_b32 a48, 0
  v_mfma_f32_16x16x32_bf16 v[60:63], a[220:223], a[188:191], v[236:239]
  v_accvgpr_write_b32 a49, 0
  v_accvgpr_write_b32 a50, 0
  v_mfma_f32_16x16x32_bf16 v[60:63], a[216:219], a[184:187], v[60:63]
  ds_read_b128 a[252:255], v12 offset:13824
  v_accvgpr_write_b32 a51, 0
  v_accvgpr_write_b32 a52, 0
  v_mfma_f32_16x16x32_bf16 v[60:63], a[212:215], a[180:183], v[60:63]
  v_accvgpr_write_b32 a53, 0
  v_accvgpr_write_b32 a54, 0
  v_mfma_f32_16x16x32_bf16 v[60:63], a[208:211], a[176:179], v[60:63]
  s_waitcnt lgkmcnt(4)
  v_accvgpr_write_b32 a55, 0
  v_mfma_f32_16x16x32_bf16 v[64:67], a[224:227], a[128:131], v[224:227]
  buffer_store_dwordx4 v[100:103], v10, s[60:63], 0 offen offset:1024 sc1
  v_accvgpr_write_b32 a56, 0
  v_accvgpr_write_b32 a57, 0
  v_mfma_f32_16x16x32_bf16 v[64:67], a[228:231], a[132:135], v[64:67]
  v_accvgpr_write_b32 a58, 0
  v_accvgpr_write_b32 a59, 0
  v_mfma_f32_16x16x32_bf16 v[64:67], a[232:235], a[136:139], v[64:67]
  v_accvgpr_write_b32 a60, 0
  v_accvgpr_write_b32 a61, 0
  v_mfma_f32_16x16x32_bf16 v[64:67], a[236:239], a[140:143], v[64:67]
  v_accvgpr_write_b32 a62, 0
  v_mfma_f32_16x16x32_bf16 v[68:71], a[236:239], a[156:159], v[228:231]
  v_accvgpr_write_b32 a63, 0
  v_accvgpr_write_b32 a64, 0
  v_mfma_f32_16x16x32_bf16 v[68:71], a[232:235], a[152:155], v[68:71]
  v_accvgpr_write_b32 a65, 0
  v_accvgpr_write_b32 a66, 0
  v_mfma_f32_16x16x32_bf16 v[68:71], a[228:231], a[148:151], v[68:71]
  buffer_store_dwordx4 v[104:107], v10, s[60:63], 0 offen offset:2048 sc1
  v_accvgpr_write_b32 a67, 0
  v_accvgpr_write_b32 a68, 0
  v_mfma_f32_16x16x32_bf16 v[68:71], a[224:227], a[144:147], v[68:71]
  v_accvgpr_write_b32 a69, 0
  v_mfma_f32_16x16x32_bf16 v[72:75], a[224:227], a[160:163], v[232:235]
  v_accvgpr_write_b32 a70, 0
  v_accvgpr_write_b32 a71, 0
  v_mfma_f32_16x16x32_bf16 v[72:75], a[228:231], a[164:167], v[72:75]
  v_accvgpr_write_b32 a72, 0
  v_accvgpr_write_b32 a73, 0
  v_mfma_f32_16x16x32_bf16 v[72:75], a[232:235], a[168:171], v[72:75]
  v_accvgpr_write_b32 a74, 0
  v_accvgpr_write_b32 a75, 0
  v_mfma_f32_16x16x32_bf16 v[72:75], a[236:239], a[172:175], v[72:75]
  v_accvgpr_write_b32 a76, 0
  v_mfma_f32_16x16x32_bf16 v[76:79], a[236:239], a[188:191], v[236:239]
  buffer_store_dwordx4 v[108:111], v10, s[60:63], 0 offen offset:3072 sc1
  v_accvgpr_write_b32 a77, 0
  v_accvgpr_write_b32 a78, 0
  v_mfma_f32_16x16x32_bf16 v[76:79], a[232:235], a[184:187], v[76:79]
  v_accvgpr_write_b32 a79, 0
  v_accvgpr_write_b32 a80, 0
  v_mfma_f32_16x16x32_bf16 v[76:79], a[228:231], a[180:183], v[76:79]
  v_accvgpr_write_b32 a81, 0
  v_accvgpr_write_b32 a82, 0
  v_mfma_f32_16x16x32_bf16 v[76:79], a[224:227], a[176:179], v[76:79]
  s_waitcnt lgkmcnt(0)
  v_accvgpr_write_b32 a83, 0
  v_mfma_f32_16x16x32_bf16 v[80:83], a[240:243], a[128:131], v[224:227]
  v_accvgpr_write_b32 a84, 0
  v_accvgpr_write_b32 a85, 0
  v_mfma_f32_16x16x32_bf16 v[80:83], a[244:247], a[132:135], v[80:83]
  v_accvgpr_write_b32 a86, 0
  v_accvgpr_write_b32 a87, 0
  v_mfma_f32_16x16x32_bf16 v[80:83], a[248:251], a[136:139], v[80:83]
  buffer_store_dwordx4 v[128:131], v10, s[60:63], s84 offen offset:0 sc1
  v_accvgpr_write_b32 a88, 0
  v_accvgpr_write_b32 a89, 0
  v_mfma_f32_16x16x32_bf16 v[80:83], a[252:255], a[140:143], v[80:83]
  v_accvgpr_write_b32 a90, 0
  v_mfma_f32_16x16x32_bf16 v[84:87], a[252:255], a[156:159], v[228:231]
  v_accvgpr_write_b32 a91, 0
  v_accvgpr_write_b32 a92, 0
  v_mfma_f32_16x16x32_bf16 v[84:87], a[248:251], a[152:155], v[84:87]
  v_accvgpr_write_b32 a93, 0
  v_accvgpr_write_b32 a94, 0
  v_mfma_f32_16x16x32_bf16 v[84:87], a[244:247], a[148:151], v[84:87]
  v_accvgpr_write_b32 a95, 0
  v_mfma_f32_16x16x32_bf16 v[84:87], a[240:243], a[144:147], v[84:87]
  v_mfma_f32_16x16x32_bf16 v[88:91], a[240:243], a[160:163], v[232:235]
  buffer_store_dwordx4 v[132:135], v10, s[60:63], s84 offen offset:1024 sc1
  v_exp_f32 v240, v32
  v_mfma_f32_16x16x32_bf16 v[88:91], a[244:247], a[164:167], v[88:91]
  v_mfma_f32_16x16x32_bf16 v[88:91], a[248:251], a[168:171], v[88:91]
  v_exp_f32 v241, v33
  v_mfma_f32_16x16x32_bf16 v[88:91], a[252:255], a[172:175], v[88:91]
  v_add_f32 v246, v240, v241
  v_cvt_pk_bf16_f32 v160, v240, v241
  v_mfma_f32_16x16x32_bf16 v[92:95], a[252:255], a[188:191], v[236:239]
  v_exp_f32 v242, v34
  v_mfma_f32_16x16x32_bf16 v[92:95], a[248:251], a[184:187], v[92:95]
  v_add_f32 v246, v246, v242
  v_mfma_f32_16x16x32_bf16 v[92:95], a[244:247], a[180:183], v[92:95]
  v_exp_f32 v243, v35
  v_mfma_f32_16x16x32_bf16 v[92:95], a[240:243], a[176:179], v[92:95]
  v_add_f32 v246, v246, v243
  v_cvt_pk_bf16_f32 v161, v242, v243
  v_accvgpr_write_b32 a96, 0
  v_accvgpr_write_b32 a97, 0
  v_accvgpr_write_b32 a98, 0
  v_accvgpr_write_b32 a99, 0
  v_accvgpr_write_b32 a100, 0
  v_accvgpr_write_b32 a101, 0
  v_accvgpr_write_b32 a102, 0
  v_accvgpr_write_b32 a103, 0
  v_accvgpr_write_b32 a104, 0
  v_accvgpr_write_b32 a105, 0
  v_accvgpr_write_b32 a106, 0
  v_accvgpr_write_b32 a107, 0
  v_accvgpr_write_b32 a108, 0
  v_accvgpr_write_b32 a109, 0
  v_accvgpr_write_b32 a110, 0
  v_accvgpr_write_b32 a111, 0
  v_accvgpr_write_b32 a112, 0
  v_accvgpr_write_b32 a113, 0
  v_accvgpr_write_b32 a114, 0
  v_accvgpr_write_b32 a115, 0
  v_accvgpr_write_b32 a116, 0
  v_accvgpr_write_b32 a117, 0
  v_accvgpr_write_b32 a118, 0
  v_accvgpr_write_b32 a119, 0
  v_accvgpr_write_b32 a120, 0
  v_accvgpr_write_b32 a121, 0
  v_accvgpr_write_b32 a122, 0
  v_accvgpr_write_b32 a123, 0
  v_accvgpr_write_b32 a124, 0
  v_accvgpr_write_b32 a125, 0
  v_accvgpr_write_b32 a126, 0
  v_accvgpr_write_b32 a127, 0
  v_exp_f32 v244, v36
  buffer_store_dwordx4 v[136:139], v10, s[60:63], s84 offen offset:2048 sc1
  v_exp_f32 v245, v37
  v_exp_f32 v240, v38
  v_add_f32 v247, v244, v245
  v_cvt_pk_bf16_f32 v168, v244, v245
  v_exp_f32 v241, v39
  buffer_store_dwordx4 v[140:143], v10, s[60:63], s84 offen offset:3072 sc1
  v_add_f32 v247, v247, v240
  v_exp_f32 v242, v40
  v_add_f32 v247, v247, v241
  v_cvt_pk_bf16_f32 v169, v240, v241
  v_exp_f32 v243, v41
  buffer_store_dwordx2 v[144:145], v11, s[60:63], 0 offen offset:0 sc1
  v_exp_f32 v244, v42
  v_add_f32 v248, v242, v243
  v_cvt_pk_bf16_f32 v176, v242, v243
  v_exp_f32 v245, v43
  v_add_f32 v248, v248, v244
  v_exp_f32 v240, v44
  buffer_store_dwordx2 v[148:149], v11, s[60:63], 0 offen offset:1024 sc1
  v_add_f32 v248, v248, v245
  v_cvt_pk_bf16_f32 v177, v244, v245
  v_exp_f32 v241, v45
  v_exp_f32 v242, v46
  v_add_f32 v249, v240, v241
  v_cvt_pk_bf16_f32 v184, v240, v241
  buffer_store_dwordx2 v[152:153], v11, s[60:63], 0 offen offset:2048 sc1
  v_exp_f32 v243, v47
  v_add_f32 v249, v249, v242
  v_exp_f32 v244, v48
  v_add_f32 v249, v249, v243
  v_cvt_pk_bf16_f32 v185, v242, v243
  v_exp_f32 v245, v49
  buffer_store_dwordx2 v[156:157], v11, s[60:63], 0 offen offset:3072 sc1
  v_add_f32 v246, v246, v244
  v_exp_f32 v240, v50
  v_add_f32 v246, v246, v245
  v_cvt_pk_bf16_f32 v162, v244, v245
  v_exp_f32 v241, v51
  v_add_f32 v246, v246, v240
  s_waitcnt vmcnt(24)
  s_barrier
  v_exp_f32 v242, v52
  ds_read_b128 a[192:195], v12 offset:16384
  v_add_f32 v246, v246, v241
  v_cvt_pk_bf16_f32 v163, v240, v241
  v_exp_f32 v243, v53
  v_add_f32 v247, v247, v242
  v_exp_f32 v244, v54
  ds_read_b128 a[196:199], v12 offset:16896
  v_add_f32 v247, v247, v243
  v_cvt_pk_bf16_f32 v170, v242, v243
  v_exp_f32 v245, v55
  v_add_f32 v247, v247, v244
  v_exp_f32 v240, v56
  ds_read_b128 a[200:203], v12 offset:17408
  v_add_f32 v247, v247, v245
  v_cvt_pk_bf16_f32 v171, v244, v245
  v_exp_f32 v241, v57
  v_add_f32 v248, v248, v240
  v_exp_f32 v242, v58
  ds_read_b128 a[204:207], v12 offset:17920
  v_add_f32 v248, v248, v241
  v_cvt_pk_bf16_f32 v178, v240, v241
  v_exp_f32 v243, v59
  v_add_f32 v248, v248, v242
  v_exp_f32 v244, v60
  ds_read_b128 a[208:211], v12 offset:20480
  v_add_f32 v248, v248, v243
  v_cvt_pk_bf16_f32 v179, v242, v243
  v_exp_f32 v245, v61
  v_add_f32 v249, v249, v244
  v_exp_f32 v240, v62
  ds_read_b128 a[212:215], v12 offset:20992
  v_add_f32 v249, v249, v245
  v_cvt_pk_bf16_f32 v186, v244, v245
  v_exp_f32 v241, v63
  v_add_f32 v249, v249, v240
  v_exp_f32 v242, v64
  ds_read_b128 a[216:219], v12 offset:21504
  v_add_f32 v249, v249, v241
  v_cvt_pk_bf16_f32 v187, v240, v241
  v_exp_f32 v243, v65
  v_add_f32 v246, v246, v242
  v_exp_f32 v244, v66
  ds_read_b128 a[220:223], v12 offset:22016
  v_add_f32 v246, v246, v243
  v_cvt_pk_bf16_f32 v164, v242, v243
  v_exp_f32 v245, v67
  s_waitcnt lgkmcnt(4)
  v_add_f32 v246, v246, v244
  v_add_f32 v246, v246, v245
  v_cvt_pk_bf16_f32 v165, v244, v245
  v_and_b32 v192, 63, v0
  v_lshrrev_b32 v193, 4, v192
  v_bfe_u32 v194, v192, 2, 2
  v_and_b32 v195, 3, v192
  v_lshlrev_b32 v196, 4, v192
  s_lshl_b32 s72, s9, 15
  s_lshl_b32 s73, s14, 8
  s_add_u32 s72, s72, s73
  v_lshlrev_b32 v197, 11, v194
  v_lshl_add_u32 v197, v193, 6, v197
  v_lshl_add_u32 v197, v195, 4, v197
  v_add_u32 v1, s72, v197
  v_add_u32 v2, 7160, v1
  v_add_u32 v3, 14320, v1
  v_add_u32 v4, 21480, v1
  s_lshl_b32 s72, s15, 2
  s_add_u32 s72, s72, s9
  s_mul_i32 s72, s72, 458752
  s_add_u32 s73, s13, 0
  s_and_b32 s73, s73, 3
  s_lshl_b32 s73, s73, 10
  s_add_u32 s73, s73, s72
  v_add_u32 v5, s73, v196
  s_add_u32 s73, s13, 1
  s_and_b32 s73, s73, 3
  s_lshl_b32 s73, s73, 10
  s_add_u32 s73, s73, s72
  v_add_u32 v6, s73, v196
  s_add_u32 s73, s13, 2
  s_and_b32 s73, s73, 3
  s_lshl_b32 s73, s73, 10
  s_add_u32 s73, s73, s72
  v_add_u32 v7, s73, v196
  s_add_u32 s73, s13, 3
  s_and_b32 s73, s73, 3
  s_lshl_b32 s73, s73, 10
  s_add_u32 s73, s73, s72
  v_add_u32 v8, s73, v196
  s_lshl_b32 s72, s15, 8
  s_add_u32 s72, s72, 65536
  v_mov_b32 v9, s72
  v_mov_b32 v11, 1
  s_mul_i32 s74, s9, 4128
  s_mul_i32 s86, s13, 14
  s_mul_i32 s87, s13, 14
  s_add_u32 s88, s16, 0
  s_and_b32 s88, s88, 3
  s_add_u32 s89, s16, 1
  s_and_b32 s89, s89, 3
  s_add_u32 s90, s16, 2
  s_and_b32 s90, s90, 3
  s_add_u32 s91, s16, 3
  s_and_b32 s91, s91, 3
  s_add_u32 s92, s16, 0
  s_and_b32 s92, s92, 1
  s_add_u32 s93, s16, 1
  s_and_b32 s93, s93, 1
  s_mov_b32 s48, s22
  s_and_b32 s49, s23, 0xffff
  s_mov_b32 s50, 7340032
  s_mov_b32 s51, 0x20000
  v_mfma_f32_16x16x32_bf16 v[96:99], a[192:195], a[128:131], v[224:227]
  v_exp_f32 v240, v68
  v_mfma_f32_16x16x32_bf16 v[96:99], a[196:199], a[132:135], v[96:99]
  ds_read_b128 a[224:227], v12 offset:24576
  v_add_f32 v247, v247, v240
  v_mfma_f32_16x16x32_bf16 v[96:99], a[200:203], a[136:139], v[96:99]
  v_exp_f32 v241, v69
  v_mfma_f32_16x16x32_bf16 v[96:99], a[204:207], a[140:143], v[96:99]
  v_add_f32 v247, v247, v241
  v_cvt_pk_bf16_f32 v172, v240, v241
  v_mfma_f32_16x16x32_bf16 v[100:103], a[204:207], a[156:159], v[228:231]
  v_exp_f32 v242, v70
  v_mfma_f32_16x16x32_bf16 v[100:103], a[200:203], a[152:155], v[100:103]
  ds_read_b128 a[228:231], v12 offset:25088
  v_add_f32 v247, v247, v242
  v_mfma_f32_16x16x32_bf16 v[100:103], a[196:199], a[148:151], v[100:103]
  v_exp_f32 v243, v71
  v_mfma_f32_16x16x32_bf16 v[100:103], a[192:195], a[144:147], v[100:103]
  v_add_f32 v247, v247, v243
  v_cvt_pk_bf16_f32 v173, v242, v243
  v_mfma_f32_16x16x32_bf16 v[104:107], a[192:195], a[160:163], v[232:235]
  v_exp_f32 v244, v72
  v_mfma_f32_16x16x32_bf16 v[104:107], a[196:199], a[164:167], v[104:107]
  ds_read_b128 a[232:235], v12 offset:25600
  v_add_f32 v248, v248, v244
  v_mfma_f32_16x16x32_bf16 v[104:107], a[200:203], a[168:171], v[104:107]
  v_exp_f32 v245, v73
  v_mfma_f32_16x16x32_bf16 v[104:107], a[204:207], a[172:175], v[104:107]
  v_add_f32 v248, v248, v245
  v_cvt_pk_bf16_f32 v180, v244, v245
  v_mfma_f32_16x16x32_bf16 v[108:111], a[204:207], a[188:191], v[236:239]
  v_exp_f32 v240, v74
  v_mfma_f32_16x16x32_bf16 v[108:111], a[200:203], a[184:187], v[108:111]
  ds_read_b128 a[236:239], v12 offset:26112
  v_add_f32 v248, v248, v240
  v_mfma_f32_16x16x32_bf16 v[108:111], a[196:199], a[180:183], v[108:111]
  v_exp_f32 v241, v75
  v_mfma_f32_16x16x32_bf16 v[108:111], a[192:195], a[176:179], v[108:111]
  s_waitcnt lgkmcnt(4)
  v_add_f32 v248, v248, v241
  v_cvt_pk_bf16_f32 v181, v240, v241
  v_mfma_f32_16x16x32_bf16 v[112:115], a[208:211], a[128:131], v[224:227]
  v_exp_f32 v242, v76
  v_mfma_f32_16x16x32_bf16 v[112:115], a[212:215], a[132:135], v[112:115]
  ds_read_b128 a[240:243], v12 offset:28672
  v_add_f32 v249, v249, v242
  v_mfma_f32_16x16x32_bf16 v[112:115], a[216:219], a[136:139], v[112:115]
  v_exp_f32 v243, v77
  v_mfma_f32_16x16x32_bf16 v[112:115], a[220:223], a[140:143], v[112:115]
  v_add_f32 v249, v249, v243
  v_cvt_pk_bf16_f32 v188, v242, v243
  v_mfma_f32_16x16x32_bf16 v[116:119], a[220:223], a[156:159], v[228:231]
  v_exp_f32 v244, v78
  v_mfma_f32_16x16x32_bf16 v[116:119], a[216:219], a[152:155], v[116:119]
  ds_read_b128 a[244:247], v12 offset:29184
  v_add_f32 v249, v249, v244
  v_mfma_f32_16x16x32_bf16 v[116:119], a[212:215], a[148:151], v[116:119]
  v_exp_f32 v245, v79
  v_mfma_f32_16x16x32_bf16 v[116:119], a[208:211], a[144:147], v[116:119]
  v_add_f32 v249, v249, v245
  v_cvt_pk_bf16_f32 v189, v244, v245
  v_mfma_f32_16x16x32_bf16 v[120:123], a[208:211], a[160:163], v[232:235]
  v_exp_f32 v240, v80
  v_mfma_f32_16x16x32_bf16 v[120:123], a[212:215], a[164:167], v[120:123]
  ds_read_b128 a[248:251], v12 offset:29696
  v_add_f32 v246, v246, v240
  v_mfma_f32_16x16x32_bf16 v[120:123], a[216:219], a[168:171], v[120:123]
  v_exp_f32 v241, v81
  v_mfma_f32_16x16x32_bf16 v[120:123], a[220:223], a[172:175], v[120:123]
  v_add_f32 v246, v246, v241
  v_cvt_pk_bf16_f32 v166, v240, v241
  v_mfma_f32_16x16x32_bf16 v[124:127], a[220:223], a[188:191], v[236:239]
  v_exp_f32 v242, v82
  v_mfma_f32_16x16x32_bf16 v[124:127], a[216:219], a[184:187], v[124:127]
  ds_read_b128 a[252:255], v12 offset:30208
  v_add_f32 v246, v246, v242
  v_mfma_f32_16x16x32_bf16 v[124:127], a[212:215], a[180:183], v[124:127]
  s_add_u32 m0, s77, 131072
  v_exp_f32 v243, v83
  v_mfma_f32_16x16x32_bf16 v[124:127], a[208:211], a[176:179], v[124:127]
  s_waitcnt lgkmcnt(4)
  buffer_load_dwordx4 v21, s[36:39], s69 offen lds
  v_add_f32 v246, v246, v243
  v_cvt_pk_bf16_f32 v167, v242, v243
  v_mfma_f32_16x16x32_bf16 v[128:131], a[224:227], a[128:131], v[224:227]
  v_exp_f32 v244, v84
  v_mfma_f32_16x16x32_bf16 v[128:131], a[228:231], a[132:135], v[128:131]
  ds_read_b64_tr_b16 a[192:193], v13 offset:0
  buffer_load_dwordx4 v21, s[4:7], s3 offen lds
  v_add_f32 v247, v247, v244
  v_mfma_f32_16x16x32_bf16 v[128:131], a[232:235], a[136:139], v[128:131]
  v_exp_f32 v245, v85
  v_mfma_f32_16x16x32_bf16 v[128:131], a[236:239], a[140:143], v[128:131]
  ds_read_b64_tr_b16 a[194:195], v13 offset:4096
  v_add_f32 v247, v247, v245
  v_cvt_pk_bf16_f32 v174, v244, v245
  v_mfma_f32_16x16x32_bf16 v[132:135], a[236:239], a[156:159], v[228:231]
  v_exp_f32 v240, v86
  v_mfma_f32_16x16x32_bf16 v[132:135], a[232:235], a[152:155], v[132:135]
  ds_read_b64_tr_b16 a[196:197], v13 offset:8192
  v_add_f32 v247, v247, v240
  v_mfma_f32_16x16x32_bf16 v[132:135], a[228:231], a[148:151], v[132:135]
  v_exp_f32 v241, v87
  v_mfma_f32_16x16x32_bf16 v[132:135], a[224:227], a[144:147], v[132:135]
  ds_read_b64_tr_b16 a[198:199], v13 offset:12288
  v_add_f32 v247, v247, v241
  v_cvt_pk_bf16_f32 v175, v240, v241
  v_mfma_f32_16x16x32_bf16 v[136:139], a[224:227], a[160:163], v[232:235]
  v_exp_f32 v242, v88
  v_mfma_f32_16x16x32_bf16 v[136:139], a[228:231], a[164:167], v[136:139]
  ds_read_b64_tr_b16 a[200:201], v14 offset:0
  v_add_f32 v248, v248, v242
  v_mfma_f32_16x16x32_bf16 v[136:139], a[232:235], a[168:171], v[136:139]
  v_exp_f32 v243, v89
  v_mfma_f32_16x16x32_bf16 v[136:139], a[236:239], a[172:175], v[136:139]
  ds_read_b64_tr_b16 a[202:203], v14 offset:4096
  v_add_f32 v248, v248, v243
  v_cvt_pk_bf16_f32 v182, v242, v243
  v_mfma_f32_16x16x32_bf16 v[140:143], a[236:239], a[188:191], v[236:239]
  v_exp_f32 v244, v90
  v_mfma_f32_16x16x32_bf16 v[140:143], a[232:235], a[184:187], v[140:143]
  ds_read_b64_tr_b16 a[204:205], v14 offset:8192
  v_add_f32 v248, v248, v244
  v_mfma_f32_16x16x32_bf16 v[140:143], a[228:231], a[180:183], v[140:143]
  v_exp_f32 v245, v91
  v_mfma_f32_16x16x32_bf16 v[140:143], a[224:227], a[176:179], v[140:143]
  s_waitcnt lgkmcnt(7)
  ds_read_b64_tr_b16 a[206:207], v14 offset:12288
  v_add_f32 v248, v248, v245
  v_cvt_pk_bf16_f32 v183, v244, v245
  v_mfma_f32_16x16x32_bf16 v[144:147], a[240:243], a[128:131], v[224:227]
  v_exp_f32 v240, v92
  v_mfma_f32_16x16x32_bf16 v[144:147], a[244:247], a[132:135], v[144:147]
  ds_read_b64_tr_b16 a[208:209], v13 offset:512
  v_add_f32 v249, v249, v240
  v_mfma_f32_16x16x32_bf16 v[144:147], a[248:251], a[136:139], v[144:147]
  v_exp_f32 v241, v93
  v_mfma_f32_16x16x32_bf16 v[144:147], a[252:255], a[140:143], v[144:147]
  ds_read_b64_tr_b16 a[210:211], v13 offset:4608
  v_add_f32 v249, v249, v241
  v_cvt_pk_bf16_f32 v190, v240, v241
  v_mfma_f32_16x16x32_bf16 v[148:151], a[252:255], a[156:159], v[228:231]
  v_exp_f32 v242, v94
  v_mfma_f32_16x16x32_bf16 v[148:151], a[248:251], a[152:155], v[148:151]
  ds_read_b64_tr_b16 a[212:213], v13 offset:8704
  v_add_f32 v249, v249, v242
  v_mfma_f32_16x16x32_bf16 v[148:151], a[244:247], a[148:151], v[148:151]
  v_exp_f32 v243, v95
  v_mfma_f32_16x16x32_bf16 v[148:151], a[240:243], a[144:147], v[148:151]
  ds_read_b64_tr_b16 a[214:215], v13 offset:12800
  v_add_f32 v249, v249, v243
  v_cvt_pk_bf16_f32 v191, v242, v243
  v_mfma_f32_16x16x32_bf16 v[152:155], a[240:243], a[160:163], v[232:235]
  v_exp_f32 v240, v96
  v_mfma_f32_16x16x32_bf16 v[152:155], a[244:247], a[164:167], v[152:155]
  ds_read_b64_tr_b16 a[216:217], v14 offset:512
  v_max3_f32 v254, v246, v247, v248
  v_mfma_f32_16x16x32_bf16 v[152:155], a[248:251], a[168:171], v[152:155]
  v_exp_f32 v241, v97
  v_mfma_f32_16x16x32_bf16 v[152:155], a[252:255], a[172:175], v[152:155]
  ds_read_b64_tr_b16 a[218:219], v14 offset:4608
  v_add_f32 v250, v240, v241
  v_cvt_pk_bf16_f32 v192, v240, v241
  v_max_f32 v254, v254, v249
  v_mfma_f32_16x16x32_bf16 v[156:159], a[252:255], a[188:191], v[236:239]
  v_exp_f32 v242, v98
  v_mfma_f32_16x16x32_bf16 v[156:159], a[248:251], a[184:187], v[156:159]
  ds_read_b64_tr_b16 a[220:221], v14 offset:8704
  v_add_f32 v250, v250, v242
  v_cmp_nge_f32 vcc, s82, v254
  v_mfma_f32_16x16x32_bf16 v[156:159], a[244:247], a[180:183], v[156:159]
  v_exp_f32 v243, v99
  v_mfma_f32_16x16x32_bf16 v[156:159], a[240:243], a[176:179], v[156:159]
  s_waitcnt lgkmcnt(11)
  ds_read_b64_tr_b16 a[222:223], v14 offset:12800
  v_add_f32 v250, v250, v243
  v_cvt_pk_bf16_f32 v193, v242, v243
  s_cbranch_vccnz LB_stub5
LB_back5:
  v_mfma_f32_16x16x32_bf16 a[0:3], a[192:195], v[160:163], a[0:3]
  v_exp_f32 v244, v100
  v_mfma_f32_16x16x32_bf16 a[0:3], a[196:199], v[164:167], a[0:3]
  ds_read_b64_tr_b16 a[224:225], v13 offset:1024
  v_mfma_f32_16x16x32_bf16 a[4:7], a[196:199], v[172:175], a[4:7]
  v_exp_f32 v245, v101
  v_mfma_f32_16x16x32_bf16 a[4:7], a[192:195], v[168:171], a[4:7]
  ds_read_b64_tr_b16 a[226:227], v13 offset:5120
  v_add_f32 v251, v244, v245
  v_cvt_pk_bf16_f32 v200, v244, v245
  v_mfma_f32_16x16x32_bf16 a[8:11], a[192:195], v[176:179], a[8:11]
  v_exp_f32 v240, v102
  v_mfma_f32_16x16x32_bf16 a[8:11], a[196:199], v[180:183], a[8:11]
  ds_read_b64_tr_b16 a[228:229], v13 offset:9216
  v_add_f32 v23, v23, v246
  v_add_f32 v251, v251, v240
  v_mfma_f32_16x16x32_bf16 a[12:15], a[196:199], v[188:191], a[12:15]
  v_exp_f32 v241, v103
  v_mfma_f32_16x16x32_bf16 a[12:15], a[192:195], v[184:187], a[12:15]
  s_waitcnt lgkmcnt(11)
  ds_read_b64_tr_b16 a[230:231], v13 offset:13312
  v_add_f32 v251, v251, v241
  v_cvt_pk_bf16_f32 v201, v240, v241
  v_mfma_f32_16x16x32_bf16 a[16:19], a[200:203], v[160:163], a[16:19]
  v_exp_f32 v242, v104
  v_mfma_f32_16x16x32_bf16 a[16:19], a[204:207], v[164:167], a[16:19]
  ds_read_b64_tr_b16 a[232:233], v14 offset:1024
  v_add_f32 v24, v24, v247
  v_mfma_f32_16x16x32_bf16 a[20:23], a[204:207], v[172:175], a[20:23]
  v_exp_f32 v243, v105
  v_mfma_f32_16x16x32_bf16 a[20:23], a[200:203], v[168:171], a[20:23]
  ds_read_b64_tr_b16 a[234:235], v14 offset:5120
  v_add_f32 v252, v242, v243
  v_cvt_pk_bf16_f32 v208, v242, v243
  v_mfma_f32_16x16x32_bf16 a[24:27], a[200:203], v[176:179], a[24:27]
  v_exp_f32 v244, v106
  v_mfma_f32_16x16x32_bf16 a[24:27], a[204:207], v[180:183], a[24:27]
  ds_read_b64_tr_b16 a[236:237], v14 offset:9216
  v_add_f32 v25, v25, v248
  v_add_f32 v252, v252, v244
  v_mfma_f32_16x16x32_bf16 a[28:31], a[204:207], v[188:191], a[28:31]
  v_exp_f32 v245, v107
  v_mfma_f32_16x16x32_bf16 a[28:31], a[200:203], v[184:187], a[28:31]
  s_waitcnt lgkmcnt(11)
  ds_read_b64_tr_b16 a[238:239], v14 offset:13312
  v_add_f32 v252, v252, v245
  v_cvt_pk_bf16_f32 v209, v244, v245
  v_mfma_f32_16x16x32_bf16 a[32:35], a[208:211], v[160:163], a[32:35]
  v_exp_f32 v240, v108
  v_mfma_f32_16x16x32_bf16 a[32:35], a[212:215], v[164:167], a[32:35]
  ds_read_b64_tr_b16 a[240:241], v13 offset:1536
  v_add_f32 v26, v26, v249
  v_mfma_f32_16x16x32_bf16 a[36:39], a[212:215], v[172:175], a[36:39]
  v_exp_f32 v241, v109
  v_mfma_f32_16x16x32_bf16 a[36:39], a[208:211], v[168:171], a[36:39]
  ds_read_b64_tr_b16 a[242:243], v13 offset:5632
  v_add_f32 v253, v240, v241
  v_cvt_pk_bf16_f32 v216, v240, v241
  v_mfma_f32_16x16x32_bf16 a[40:43], a[208:211], v[176:179], a[40:43]
  v_exp_f32 v242, v110
  v_mfma_f32_16x16x32_bf16 a[40:43], a[212:215], v[180:183], a[40:43]
  ds_read_b64_tr_b16 a[244:245], v13 offset:9728
  v_add_f32 v253, v253, v242
  v_mfma_f32_16x16x32_bf16 a[44:47], a[212:215], v[188:191], a[44:47]
  v_exp_f32 v243, v111
  v_mfma_f32_16x16x32_bf16 a[44:47], a[208:211], v[184:187], a[44:47]
  s_waitcnt lgkmcnt(11)
  ds_read_b64_tr_b16 a[246:247], v13 offset:13824
  v_add_f32 v253, v253, v243
  v_cvt_pk_bf16_f32 v217, v242, v243
  v_mfma_f32_16x16x32_bf16 a[48:51], a[216:219], v[160:163], a[48:51]
  v_exp_f32 v244, v112
  v_mfma_f32_16x16x32_bf16 a[48:51], a[220:223], v[164:167], a[48:51]
  ds_read_b64_tr_b16 a[248:249], v14 offset:1536
  v_add_f32 v250, v250, v244
  v_mfma_f32_16x16x32_bf16 a[52:55], a[220:223], v[172:175], a[52:55]
  v_exp_f32 v245, v113
  v_mfma_f32_16x16x32_bf16 a[52:55], a[216:219], v[168:171], a[52:55]
  ds_read_b64_tr_b16 a[250:251], v14 offset:5632
  v_add_f32 v250, v250, v245
  v_cvt_pk_bf16_f32 v194, v244, v245
  v_mfma_f32_16x16x32_bf16 a[56:59], a[216:219], v[176:179], a[56:59]
  v_exp_f32 v240, v114
  v_mfma_f32_16x16x32_bf16 a[56:59], a[220:223], v[180:183], a[56:59]
  ds_read_b64_tr_b16 a[252:253], v14 offset:9728
  v_add_f32 v250, v250, v240
  v_mfma_f32_16x16x32_bf16 a[60:63], a[220:223], v[188:191], a[60:63]
  v_exp_f32 v241, v115
  v_mfma_f32_16x16x32_bf16 a[60:63], a[216:219], v[184:187], a[60:63]
  s_waitcnt lgkmcnt(11)
  ds_read_b64_tr_b16 a[254:255], v14 offset:13824
  v_add_f32 v250, v250, v241
  v_cvt_pk_bf16_f32 v195, v240, v241
  v_mfma_f32_16x16x32_bf16 a[64:67], a[224:227], v[160:163], a[64:67]
  s_waitcnt vmcnt(22)
  s_barrier
  v_exp_f32 v242, v116
  v_mfma_f32_16x16x32_bf16 a[64:67], a[228:231], v[164:167], a[64:67]
  ds_read_b128 a[192:195], v12 offset:32768
  v_add_f32 v251, v251, v242
  v_mfma_f32_16x16x32_bf16 a[68:71], a[228:231], v[172:175], a[68:71]
  s_add_u32 m0, s77, 0
  v_exp_f32 v243, v117
  v_mfma_f32_16x16x32_bf16 a[68:71], a[224:227], v[168:171], a[68:71]
  buffer_load_dwordx4 v15, s[52:55], s79 offen offset:0 lds
  v_add_f32 v251, v251, v243
  v_cvt_pk_bf16_f32 v202, v242, v243
  v_mfma_f32_16x16x32_bf16 a[72:75], a[224:227], v[176:179], a[72:75]
  v_exp_f32 v244, v118
  v_mfma_f32_16x16x32_bf16 a[72:75], a[228:231], v[180:183], a[72:75]
  ds_read_b128 a[196:199], v12 offset:33280
  v_add_f32 v251, v251, v244
  v_mfma_f32_16x16x32_bf16 a[76:79], a[228:231], v[188:191], a[76:79]
  v_exp_f32 v245, v119
  v_mfma_f32_16x16x32_bf16 a[76:79], a[224:227], v[184:187], a[76:79]
  s_waitcnt lgkmcnt(10)
  v_add_f32 v251, v251, v245
  v_cvt_pk_bf16_f32 v203, v244, v245
  v_mfma_f32_16x16x32_bf16 a[80:83], a[232:235], v[160:163], a[80:83]
  v_exp_f32 v240, v120
  v_mfma_f32_16x16x32_bf16 a[80:83], a[236:239], v[164:167], a[80:83]
  ds_read_b128 a[200:203], v12 offset:33792
  v_add_f32 v252, v252, v240
  v_mfma_f32_16x16x32_bf16 a[84:87], a[236:239], v[172:175], a[84:87]
  v_exp_f32 v241, v121
  v_mfma_f32_16x16x32_bf16 a[84:87], a[232:235], v[168:171], a[84:87]
  buffer_load_dwordx4 v16, s[52:55], s79 offen offset:1024 lds
  v_add_f32 v252, v252, v241
  v_cvt_pk_bf16_f32 v210, v240, v241
  v_mfma_f32_16x16x32_bf16 a[88:91], a[232:235], v[176:179], a[88:91]
  v_exp_f32 v242, v122
  v_mfma_f32_16x16x32_bf16 a[88:91], a[236:239], v[180:183], a[88:91]
  ds_read_b128 a[204:207], v12 offset:34304
  v_add_f32 v252, v252, v242
  v_mfma_f32_16x16x32_bf16 a[92:95], a[236:239], v[188:191], a[92:95]
  v_exp_f32 v243, v123
  v_mfma_f32_16x16x32_bf16 a[92:95], a[232:235], v[184:187], a[92:95]
  s_waitcnt lgkmcnt(8)
  v_add_f32 v252, v252, v243
  v_cvt_pk_bf16_f32 v211, v242, v243
  v_mfma_f32_16x16x32_bf16 a[96:99], a[240:243], v[160:163], a[96:99]
  v_exp_f32 v244, v124
  v_mfma_f32_16x16x32_bf16 a[96:99], a[244:247], v[164:167], a[96:99]
  ds_read_b128 a[208:211], v12 offset:36864
  v_add_f32 v253, v253, v244
  v_mfma_f32_16x16x32_bf16 a[100:103], a[244:247], v[172:175], a[100:103]
  v_exp_f32 v245, v125
  v_mfma_f32_16x16x32_bf16 a[100:103], a[240:243], v[168:171], a[100:103]
  buffer_load_dwordx4 v17, s[52:55], s79 offen offset:2048 lds
  v_add_f32 v253, v253, v245
  v_cvt_pk_bf16_f32 v218, v244, v245
  v_mfma_f32_16x16x32_bf16 a[104:107], a[240:243], v[176:179], a[104:107]
  v_exp_f32 v240, v126
  v_mfma_f32_16x16x32_bf16 a[104:107], a[244:247], v[180:183], a[104:107]
  ds_read_b128 a[212:215], v12 offset:37376
  v_add_f32 v253, v253, v240
  v_mfma_f32_16x16x32_bf16 a[108:111], a[244:247], v[188:191], a[108:111]
  v_exp_f32 v241, v127
  v_mfma_f32_16x16x32_bf16 a[108:111], a[240:243], v[184:187], a[108:111]
  s_waitcnt lgkmcnt(6)
  v_add_f32 v253, v253, v241
  v_cvt_pk_bf16_f32 v219, v240, v241
  v_mfma_f32_16x16x32_bf16 a[112:115], a[248:251], v[160:163], a[112:115]
  v_exp_f32 v242, v128
  v_mfma_f32_16x16x32_bf16 a[112:115], a[252:255], v[164:167], a[112:115]
  ds_read_b128 a[216:219], v12 offset:37888
  v_add_f32 v250, v250, v242
  v_mfma_f32_16x16x32_bf16 a[116:119], a[252:255], v[172:175], a[116:119]
  v_exp_f32 v243, v129
  v_mfma_f32_16x16x32_bf16 a[116:119], a[248:251], v[168:171], a[116:119]
  buffer_load_dwordx4 v18, s[52:55], s79 offen offset:3072 lds
  v_add_f32 v250, v250, v243
  v_cvt_pk_bf16_f32 v196, v242, v243
  v_mfma_f32_16x16x32_bf16 a[120:123], a[248:251], v[176:179], a[120:123]
  s_add_u32 s79, s79, 16384
  v_exp_f32 v244, v130
  v_mfma_f32_16x16x32_bf16 a[120:123], a[252:255], v[180:183], a[120:123]
  ds_read_b128 a[220:223], v12 offset:38400
  v_add_f32 v250, v250, v244
  v_mfma_f32_16x16x32_bf16 a[124:127], a[252:255], v[188:191], a[124:127]
  v_exp_f32 v245, v131
  v_mfma_f32_16x16x32_bf16 a[124:127], a[248:251], v[184:187], a[124:127]
  s_waitcnt lgkmcnt(4)
  v_add_f32 v250, v250, v245
  v_cvt_pk_bf16_f32 v197, v244, v245
  v_mfma_f32_16x16x32_bf16 v[32:35], a[192:195], a[128:131], v[224:227]
  v_exp_f32 v240, v132
  v_mfma_f32_16x16x32_bf16 v[32:35], a[196:199], a[132:135], v[32:35]
  ds_read_b128 a[224:227], v12 offset:40960
  v_add_f32 v251, v251, v240
  v_mfma_f32_16x16x32_bf16 v[32:35], a[200:203], a[136:139], v[32:35]
  s_add_u32 m0, s78, 49152
  v_exp_f32 v241, v133
  v_mfma_f32_16x16x32_bf16 v[32:35], a[204:207], a[140:143], v[32:35]
  buffer_load_dwordx4 v19, s[56:59], s80 offen offset:0 lds
  v_add_f32 v251, v251, v241
  v_cvt_pk_bf16_f32 v204, v240, v241
  v_mfma_f32_16x16x32_bf16 v[36:39], a[204:207], a[156:159], v[228:231]
  v_exp_f32 v242, v134
  v_mfma_f32_16x16x32_bf16 v[36:39], a[200:203], a[152:155], v[36:39]
  ds_read_b128 a[228:231], v12 offset:41472
  v_add_f32 v251, v251, v242
  v_mfma_f32_16x16x32_bf16 v[36:39], a[196:199], a[148:151], v[36:39]
  v_exp_f32 v243, v135
  v_mfma_f32_16x16x32_bf16 v[36:39], a[192:195], a[144:147], v[36:39]
  v_add_f32 v251, v251, v243
  v_cvt_pk_bf16_f32 v205, v242, v243
  v_mfma_f32_16x16x32_bf16 v[40:43], a[192:195], a[160:163], v[232:235]
  v_exp_f32 v244, v136
  v_mfma_f32_16x16x32_bf16 v[40:43], a[196:199], a[164:167], v[40:43]
  ds_read_b128 a[232:235], v12 offset:41984
  v_add_f32 v252, v252, v244
  v_mfma_f32_16x16x32_bf16 v[40:43], a[200:203], a[168:171], v[40:43]
  v_exp_f32 v245, v137
  v_mfma_f32_16x16x32_bf16 v[40:43], a[204:207], a[172:175], v[40:43]
  buffer_load_dwordx4 v20, s[56:59], s80 offen offset:1024 lds
  v_add_f32 v252, v252, v245
  v_cvt_pk_bf16_f32 v212, v244, v245
  v_mfma_f32_16x16x32_bf16 v[44:47], a[204:207], a[188:191], v[236:239]
  v_exp_f32 v240, v138
  v_mfma_f32_16x16x32_bf16 v[44:47], a[200:203], a[184:187], v[44:47]
  ds_read_b128 a[236:239], v12 offset:42496
  v_add_f32 v252, v252, v240
  v_mfma_f32_16x16x32_bf16 v[44:47], a[196:199], a[180:183], v[44:47]
  v_exp_f32 v241, v139
  v_mfma_f32_16x16x32_bf16 v[44:47], a[192:195], a[176:179], v[44:47]
  s_waitcnt lgkmcnt(4)
  v_add_f32 v252, v252, v241
  v_cvt_pk_bf16_f32 v213, v240, v241
  v_mfma_f32_16x16x32_bf16 v[48:51], a[208:211], a[128:131], v[224:227]
  v_exp_f32 v242, v140
  v_mfma_f32_16x16x32_bf16 v[48:51], a[212:215], a[132:135], v[48:51]
  ds_read_b128 a[240:243], v12 offset:45056
  v_add_f32 v253, v253, v242
  v_mfma_f32_16x16x32_bf16 v[48:51], a[216:219], a[136:139], v[48:51]
  v_exp_f32 v243, v141
  v_mfma_f32_16x16x32_bf16 v[48:51], a[220:223], a[140:143], v[48:51]
  buffer_load_dwordx4 v19, s[56:59], s80 offen offset:2048 lds
  v_add_f32 v253, v253, v243
  v_cvt_pk_bf16_f32 v220, v242, v243
  v_mfma_f32_16x16x32_bf16 v[52:55], a[220:223], a[156:159], v[228:231]
  v_exp_f32 v244, v142
  v_mfma_f32_16x16x32_bf16 v[52:55], a[216:219], a[152:155], v[52:55]
  ds_read_b128 a[244:247], v12 offset:45568
  v_add_f32 v253, v253, v244
  v_mfma_f32_16x16x32_bf16 v[52:55], a[212:215], a[148:151], v[52:55]
  v_exp_f32 v245, v143
  v_mfma_f32_16x16x32_bf16 v[52:55], a[208:211], a[144:147], v[52:55]
  v_add_f32 v253, v253, v245
  v_cvt_pk_bf16_f32 v221, v244, v245
  v_mfma_f32_16x16x32_bf16 v[56:59], a[208:211], a[160:163], v[232:235]
  v_exp_f32 v240, v144
  v_mfma_f32_16x16x32_bf16 v[56:59], a[212:215], a[164:167], v[56:59]
  ds_read_b128 a[248:251], v12 offset:46080
  v_add_f32 v250, v250, v240
  v_mfma_f32_16x16x32_bf16 v[56:59], a[216:219], a[168:171], v[56:59]
  v_exp_f32 v241, v145
  v_mfma_f32_16x16x32_bf16 v[56:59], a[220:223], a[172:175], v[56:59]
  buffer_load_dwordx4 v20, s[56:59], s80 offen offset:3072 lds
  v_add_f32 v250, v250, v241
  v_cvt_pk_bf16_f32 v198, v240, v241
  v_mfma_f32_16x16x32_bf16 v[60:63], a[220:223], a[188:191], v[236:239]
  s_add_u32 s80, s80, 16384
  v_exp_f32 v242, v146
  v_mfma_f32_16x16x32_bf16 v[60:63], a[216:219], a[184:187], v[60:63]
  ds_read_b128 a[252:255], v12 offset:46592
  v_add_f32 v250, v250, v242
  v_mfma_f32_16x16x32_bf16 v[60:63], a[212:215], a[180:183], v[60:63]
  s_add_u32 m0, s77, 131072
  v_exp_f32 v243, v147
  v_mfma_f32_16x16x32_bf16 v[60:63], a[208:211], a[176:179], v[60:63]
  s_waitcnt lgkmcnt(4)
  buffer_load_dwordx4 v21, s[40:43], s69 offen lds
  v_add_f32 v250, v250, v243
  v_cvt_pk_bf16_f32 v199, v242, v243
  v_mfma_f32_16x16x32_bf16 v[64:67], a[224:227], a[128:131], v[224:227]
  s_add_u32 s69, s69, 4096
  v_exp_f32 v244, v148
  v_mfma_f32_16x16x32_bf16 v[64:67], a[228:231], a[132:135], v[64:67]
  ds_read_b64_tr_b16 a[192:193], v13 offset:16384
  v_add_f32 v251, v251, v244
  v_mfma_f32_16x16x32_bf16 v[64:67], a[232:235], a[136:139], v[64:67]
  v_exp_f32 v245, v149
  v_mfma_f32_16x16x32_bf16 v[64:67], a[236:239], a[140:143], v[64:67]
  ds_read_b64_tr_b16 a[194:195], v13 offset:20480
  v_add_f32 v251, v251, v245
  v_cvt_pk_bf16_f32 v206, v244, v245
  v_mfma_f32_16x16x32_bf16 v[68:71], a[236:239], a[156:159], v[228:231]
  v_exp_f32 v240, v150
  v_mfma_f32_16x16x32_bf16 v[68:71], a[232:235], a[152:155], v[68:71]
  ds_read_b64_tr_b16 a[196:197], v13 offset:24576
  v_add_f32 v251, v251, v240
  v_mfma_f32_16x16x32_bf16 v[68:71], a[228:231], a[148:151], v[68:71]
  v_exp_f32 v241, v151
  v_mfma_f32_16x16x32_bf16 v[68:71], a[224:227], a[144:147], v[68:71]
  ds_read_b64_tr_b16 a[198:199], v13 offset:28672
  v_add_f32 v251, v251, v241
  v_cvt_pk_bf16_f32 v207, v240, v241
  v_mfma_f32_16x16x32_bf16 v[72:75], a[224:227], a[160:163], v[232:235]
  v_exp_f32 v242, v152
  v_mfma_f32_16x16x32_bf16 v[72:75], a[228:231], a[164:167], v[72:75]
  ds_read_b64_tr_b16 a[200:201], v14 offset:16384
  v_add_f32 v252, v252, v242
  v_mfma_f32_16x16x32_bf16 v[72:75], a[232:235], a[168:171], v[72:75]
  v_exp_f32 v243, v153
  v_mfma_f32_16x16x32_bf16 v[72:75], a[236:239], a[172:175], v[72:75]
  ds_read_b64_tr_b16 a[202:203], v14 offset:20480
  v_add_f32 v252, v252, v243
  v_cvt_pk_bf16_f32 v214, v242, v243
  v_mfma_f32_16x16x32_bf16 v[76:79], a[236:239], a[188:191], v[236:239]
  v_exp_f32 v244, v154
  v_mfma_f32_16x16x32_bf16 v[76:79], a[232:235], a[184:187], v[76:79]
  ds_read_b64_tr_b16 a[204:205], v14 offset:24576
  v_add_f32 v252, v252, v244
  v_mfma_f32_16x16x32_bf16 v[76:79], a[228:231], a[180:183], v[76:79]
  v_exp_f32 v245, v155
  v_mfma_f32_16x16x32_bf16 v[76:79], a[224:227], a[176:179], v[76:79]
  s_waitcnt lgkmcnt(7)
  ds_read_b64_tr_b16 a[206:207], v14 offset:28672
  v_add_f32 v252, v252, v245
  v_cvt_pk_bf16_f32 v215, v244, v245
  v_mfma_f32_16x16x32_bf16 v[80:83], a[240:243], a[128:131], v[224:227]
  v_exp_f32 v240, v156
  v_mfma_f32_16x16x32_bf16 v[80:83], a[244:247], a[132:135], v[80:83]
  ds_read_b64_tr_b16 a[208:209], v13 offset:16896
  v_add_f32 v253, v253, v240
  v_mfma_f32_16x16x32_bf16 v[80:83], a[248:251], a[136:139], v[80:83]
  v_exp_f32 v241, v157
  v_mfma_f32_16x16x32_bf16 v[80:83], a[252:255], a[140:143], v[80:83]
  ds_read_b64_tr_b16 a[210:211], v13 offset:20992
  v_add_f32 v253, v253, v241
  v_cvt_pk_bf16_f32 v222, v240, v241
  v_mfma_f32_16x16x32_bf16 v[84:87], a[252:255], a[156:159], v[228:231]
  v_exp_f32 v242, v158
  v_mfma_f32_16x16x32_bf16 v[84:87], a[248:251], a[152:155], v[84:87]
  ds_read_b64_tr_b16 a[212:213], v13 offset:25088
  v_add_f32 v253, v253, v242
  v_mfma_f32_16x16x32_bf16 v[84:87], a[244:247], a[148:151], v[84:87]
  v_exp_f32 v243, v159
  v_mfma_f32_16x16x32_bf16 v[84:87], a[240:243], a[144:147], v[84:87]
  ds_read_b64_tr_b16 a[214:215], v13 offset:29184
  v_add_f32 v253, v253, v243
  v_cvt_pk_bf16_f32 v223, v242, v243
  v_mfma_f32_16x16x32_bf16 v[88:91], a[240:243], a[160:163], v[232:235]
  v_exp_f32 v240, v32
  v_mfma_f32_16x16x32_bf16 v[88:91], a[244:247], a[164:167], v[88:91]
  ds_read_b64_tr_b16 a[216:217], v14 offset:16896
  v_max3_f32 v254, v250, v251, v252
  v_mfma_f32_16x16x32_bf16 v[88:91], a[248:251], a[168:171], v[88:91]
  v_exp_f32 v241, v33
  v_mfma_f32_16x16x32_bf16 v[88:91], a[252:255], a[172:175], v[88:91]
  ds_read_b64_tr_b16 a[218:219], v14 offset:20992
  v_add_f32 v246, v240, v241
  v_cvt_pk_bf16_f32 v160, v240, v241
  v_max_f32 v254, v254, v253
  v_mfma_f32_16x16x32_bf16 v[92:95], a[252:255], a[188:191], v[236:239]
  v_exp_f32 v242, v34
  v_mfma_f32_16x16x32_bf16 v[92:95], a[248:251], a[184:187], v[92:95]
  ds_read_b64_tr_b16 a[220:221], v14 offset:25088
  v_add_f32 v246, v246, v242
  v_cmp_nge_f32 vcc, s82, v254
  v_mfma_f32_16x16x32_bf16 v[92:95], a[244:247], a[180:183], v[92:95]
  v_exp_f32 v243, v35
  v_mfma_f32_16x16x32_bf16 v[92:95], a[240:243], a[176:179], v[92:95]
  s_waitcnt lgkmcnt(11)
  ds_read_b64_tr_b16 a[222:223], v14 offset:29184
  v_add_f32 v246, v246, v243
  v_cvt_pk_bf16_f32 v161, v242, v243
  s_cbranch_vccnz LB_stub6
LB_back6:
  v_mfma_f32_16x16x32_bf16 a[0:3], a[192:195], v[192:195], a[0:3]
  v_exp_f32 v244, v36
  v_mfma_f32_16x16x32_bf16 a[0:3], a[196:199], v[196:199], a[0:3]
  ds_read_b64_tr_b16 a[224:225], v13 offset:17408
  v_mfma_f32_16x16x32_bf16 a[4:7], a[196:199], v[204:207], a[4:7]
  v_exp_f32 v245, v37
  v_mfma_f32_16x16x32_bf16 a[4:7], a[192:195], v[200:203], a[4:7]
  ds_read_b64_tr_b16 a[226:227], v13 offset:21504
  v_add_f32 v247, v244, v245
  v_cvt_pk_bf16_f32 v168, v244, v245
  v_mfma_f32_16x16x32_bf16 a[8:11], a[192:195], v[208:211], a[8:11]
  v_exp_f32 v240, v38
  v_mfma_f32_16x16x32_bf16 a[8:11], a[196:199], v[212:215], a[8:11]
  ds_read_b64_tr_b16 a[228:229], v13 offset:25600
  v_add_f32 v23, v23, v250
  v_add_f32 v247, v247, v240
  v_mfma_f32_16x16x32_bf16 a[12:15], a[196:199], v[220:223], a[12:15]
  v_exp_f32 v241, v39
  v_mfma_f32_16x16x32_bf16 a[12:15], a[192:195], v[216:219], a[12:15]
  s_waitcnt lgkmcnt(11)
  ds_read_b64_tr_b16 a[230:231], v13 offset:29696
  v_add_f32 v247, v247, v241
  v_cvt_pk_bf16_f32 v169, v240, v241
  v_mfma_f32_16x16x32_bf16 a[16:19], a[200:203], v[192:195], a[16:19]
  v_exp_f32 v242, v40
  v_mfma_f32_16x16x32_bf16 a[16:19], a[204:207], v[196:199], a[16:19]
  ds_read_b64_tr_b16 a[232:233], v14 offset:17408
  v_add_f32 v24, v24, v251
  v_mfma_f32_16x16x32_bf16 a[20:23], a[204:207], v[204:207], a[20:23]
  v_exp_f32 v243, v41
  v_mfma_f32_16x16x32_bf16 a[20:23], a[200:203], v[200:203], a[20:23]
  ds_read_b64_tr_b16 a[234:235], v14 offset:21504
  v_add_f32 v248, v242, v243
  v_cvt_pk_bf16_f32 v176, v242, v243
  v_mfma_f32_16x16x32_bf16 a[24:27], a[200:203], v[208:211], a[24:27]
  v_exp_f32 v244, v42
  v_mfma_f32_16x16x32_bf16 a[24:27], a[204:207], v[212:215], a[24:27]
  ds_read_b64_tr_b16 a[236:237], v14 offset:25600
  v_add_f32 v25, v25, v252
  v_add_f32 v248, v248, v244
  v_mfma_f32_16x16x32_bf16 a[28:31], a[204:207], v[220:223], a[28:31]
  v_exp_f32 v245, v43
  v_mfma_f32_16x16x32_bf16 a[28:31], a[200:203], v[216:219], a[28:31]
  s_waitcnt lgkmcnt(11)
  ds_read_b64_tr_b16 a[238:239], v14 offset:29696
  v_add_f32 v248, v248, v245
  v_cvt_pk_bf16_f32 v177, v244, v245
  v_mfma_f32_16x16x32_bf16 a[32:35], a[208:211], v[192:195], a[32:35]
  v_exp_f32 v240, v44
  v_mfma_f32_16x16x32_bf16 a[32:35], a[212:215], v[196:199], a[32:35]
  ds_read_b64_tr_b16 a[240:241], v13 offset:17920
  v_add_f32 v26, v26, v253
  v_mfma_f32_16x16x32_bf16 a[36:39], a[212:215], v[204:207], a[36:39]
  v_exp_f32 v241, v45
  v_mfma_f32_16x16x32_bf16 a[36:39], a[208:211], v[200:203], a[36:39]
  ds_read_b64_tr_b16 a[242:243], v13 offset:22016
  v_add_f32 v249, v240, v241
  v_cvt_pk_bf16_f32 v184, v240, v241
  v_mfma_f32_16x16x32_bf16 a[40:43], a[208:211], v[208:211], a[40:43]
  v_exp_f32 v242, v46
  v_mfma_f32_16x16x32_bf16 a[40:43], a[212:215], v[212:215], a[40:43]
  ds_read_b64_tr_b16 a[244:245], v13 offset:26112
  v_add_f32 v249, v249, v242
  v_mfma_f32_16x16x32_bf16 a[44:47], a[212:215], v[220:223], a[44:47]
  v_exp_f32 v243, v47
  v_mfma_f32_16x16x32_bf16 a[44:47], a[208:211], v[216:219], a[44:47]
  s_waitcnt lgkmcnt(11)
  ds_read_b64_tr_b16 a[246:247], v13 offset:30208
  v_add_f32 v249, v249, v243
  v_cvt_pk_bf16_f32 v185, v242, v243
  v_mfma_f32_16x16x32_bf16 a[48:51], a[216:219], v[192:195], a[48:51]
  v_exp_f32 v244, v48
  v_mfma_f32_16x16x32_bf16 a[48:51], a[220:223], v[196:199], a[48:51]
  ds_read_b64_tr_b16 a[248:249], v14 offset:17920
  v_add_f32 v246, v246, v244
  v_mfma_f32_16x16x32_bf16 a[52:55], a[220:223], v[204:207], a[52:55]
  v_exp_f32 v245, v49
  v_mfma_f32_16x16x32_bf16 a[52:55], a[216:219], v[200:203], a[52:55]
  ds_read_b64_tr_b16 a[250:251], v14 offset:22016
  v_add_f32 v246, v246, v245
  v_cvt_pk_bf16_f32 v162, v244, v245
  v_mfma_f32_16x16x32_bf16 a[56:59], a[216:219], v[208:211], a[56:59]
  v_exp_f32 v240, v50
  v_mfma_f32_16x16x32_bf16 a[56:59], a[220:223], v[212:215], a[56:59]
  ds_read_b64_tr_b16 a[252:253], v14 offset:26112
  v_add_f32 v246, v246, v240
  v_mfma_f32_16x16x32_bf16 a[60:63], a[220:223], v[220:223], a[60:63]
  v_exp_f32 v241, v51
  v_mfma_f32_16x16x32_bf16 a[60:63], a[216:219], v[216:219], a[60:63]
  s_waitcnt lgkmcnt(11)
  ds_read_b64_tr_b16 a[254:255], v14 offset:30208
  v_add_f32 v246, v246, v241
  v_cvt_pk_bf16_f32 v163, v240, v241
  v_mfma_f32_16x16x32_bf16 a[64:67], a[224:227], v[192:195], a[64:67]
  s_waitcnt vmcnt(27)
  s_barrier
  v_exp_f32 v242, v52
  v_mfma_f32_16x16x32_bf16 a[64:67], a[228:231], v[196:199], a[64:67]
  ds_read_b128 a[192:195], v12 offset:49152
  v_add_f32 v247, v247, v242
  v_mfma_f32_16x16x32_bf16 a[68:71], a[228:231], v[204:207], a[68:71]
  s_add_u32 m0, s77, 16384
  v_exp_f32 v243, v53
  v_mfma_f32_16x16x32_bf16 a[68:71], a[224:227], v[200:203], a[68:71]
  buffer_load_dwordx4 v15, s[52:55], s79 offen offset:0 lds
  v_add_f32 v247, v247, v243
  v_cvt_pk_bf16_f32 v170, v242, v243
  v_mfma_f32_16x16x32_bf16 a[72:75], a[224:227], v[208:211], a[72:75]
  v_exp_f32 v244, v54
  v_mfma_f32_16x16x32_bf16 a[72:75], a[228:231], v[212:215], a[72:75]
  ds_read_b128 a[196:199], v12 offset:49664
  v_add_f32 v247, v247, v244
  v_mfma_f32_16x16x32_bf16 a[76:79], a[228:231], v[220:223], a[76:79]
  v_exp_f32 v245, v55
  v_mfma_f32_16x16x32_bf16 a[76:79], a[224:227], v[216:219], a[76:79]
  s_waitcnt lgkmcnt(10)
  v_add_f32 v247, v247, v245
  v_cvt_pk_bf16_f32 v171, v244, v245
  v_mfma_f32_16x16x32_bf16 a[80:83], a[232:235], v[192:195], a[80:83]
  v_exp_f32 v240, v56
  v_mfma_f32_16x16x32_bf16 a[80:83], a[236:239], v[196:199], a[80:83]
  ds_read_b128 a[200:203], v12 offset:50176
  v_add_f32 v248, v248, v240
  v_mfma_f32_16x16x32_bf16 a[84:87], a[236:239], v[204:207], a[84:87]
  v_exp_f32 v241, v57
  v_mfma_f32_16x16x32_bf16 a[84:87], a[232:235], v[200:203], a[84:87]
  buffer_load_dwordx4 v16, s[52:55], s79 offen offset:1024 lds
  v_add_f32 v248, v248, v241
  v_cvt_pk_bf16_f32 v178, v240, v241
  v_mfma_f32_16x16x32_bf16 a[88:91], a[232:235], v[208:211], a[88:91]
  v_exp_f32 v242, v58
  v_mfma_f32_16x16x32_bf16 a[88:91], a[236:239], v[212:215], a[88:91]
  ds_read_b128 a[204:207], v12 offset:50688
  v_add_f32 v248, v248, v242
  v_mfma_f32_16x16x32_bf16 a[92:95], a[236:239], v[220:223], a[92:95]
  v_exp_f32 v243, v59
  v_mfma_f32_16x16x32_bf16 a[92:95], a[232:235], v[216:219], a[92:95]
  s_waitcnt lgkmcnt(8)
  v_add_f32 v248, v248, v243
  v_cvt_pk_bf16_f32 v179, v242, v243
  v_mfma_f32_16x16x32_bf16 a[96:99], a[240:243], v[192:195], a[96:99]
  v_exp_f32 v244, v60
  v_mfma_f32_16x16x32_bf16 a[96:99], a[244:247], v[196:199], a[96:99]
  ds_read_b128 a[208:211], v12 offset:53248
  v_add_f32 v249, v249, v244
  v_mfma_f32_16x16x32_bf16 a[100:103], a[244:247], v[204:207], a[100:103]
  v_exp_f32 v245, v61
  v_mfma_f32_16x16x32_bf16 a[100:103], a[240:243], v[200:203], a[100:103]
  buffer_load_dwordx4 v17, s[52:55], s79 offen offset:2048 lds
  v_add_f32 v249, v249, v245
  v_cvt_pk_bf16_f32 v186, v244, v245
  v_mfma_f32_16x16x32_bf16 a[104:107], a[240:243], v[208:211], a[104:107]
  v_exp_f32 v240, v62
  v_mfma_f32_16x16x32_bf16 a[104:107], a[244:247], v[212:215], a[104:107]
  ds_read_b128 a[212:215], v12 offset:53760
  v_add_f32 v249, v249, v240
  v_mfma_f32_16x16x32_bf16 a[108:111], a[244:247], v[220:223], a[108:111]
  v_exp_f32 v241, v63
  v_mfma_f32_16x16x32_bf16 a[108:111], a[240:243], v[216:219], a[108:111]
  s_waitcnt lgkmcnt(6)
  v_add_f32 v249, v249, v241
  v_cvt_pk_bf16_f32 v187, v240, v241
  v_mfma_f32_16x16x32_bf16 a[112:115], a[248:251], v[192:195], a[112:115]
  v_exp_f32 v242, v64
  v_mfma_f32_16x16x32_bf16 a[112:115], a[252:255], v[196:199], a[112:115]
  ds_read_b128 a[216:219], v12 offset:54272
  v_add_f32 v246, v246, v242
  v_mfma_f32_16x16x32_bf16 a[116:119], a[252:255], v[204:207], a[116:119]
  v_exp_f32 v243, v65
  v_mfma_f32_16x16x32_bf16 a[116:119], a[248:251], v[200:203], a[116:119]
  buffer_load_dwordx4 v18, s[52:55], s79 offen offset:3072 lds
  v_add_f32 v246, v246, v243
  v_cvt_pk_bf16_f32 v164, v242, v243
  v_mfma_f32_16x16x32_bf16 a[120:123], a[248:251], v[208:211], a[120:123]
  s_add_u32 s79, s79, 16384
  v_exp_f32 v244, v66
  v_mfma_f32_16x16x32_bf16 a[120:123], a[252:255], v[212:215], a[120:123]
  ds_read_b128 a[220:223], v12 offset:54784
  v_add_f32 v246, v246, v244
  v_mfma_f32_16x16x32_bf16 a[124:127], a[252:255], v[220:223], a[124:127]
  v_exp_f32 v245, v67
  v_mfma_f32_16x16x32_bf16 a[124:127], a[248:251], v[216:219], a[124:127]
  s_waitcnt lgkmcnt(4)
  v_add_f32 v246, v246, v245
  v_cvt_pk_bf16_f32 v165, v244, v245
  v_mfma_f32_16x16x32_bf16 v[96:99], a[192:195], a[128:131], v[224:227]
  v_exp_f32 v240, v68
  v_mfma_f32_16x16x32_bf16 v[96:99], a[196:199], a[132:135], v[96:99]
  ds_read_b128 a[224:227], v12 offset:57344
  v_add_f32 v247, v247, v240
  v_mfma_f32_16x16x32_bf16 v[96:99], a[200:203], a[136:139], v[96:99]
  s_add_u32 m0, s78, 0
  v_exp_f32 v241, v69
  v_mfma_f32_16x16x32_bf16 v[96:99], a[204:207], a[140:143], v[96:99]
  buffer_load_dwordx4 v19, s[56:59], s80 offen offset:0 lds
  v_add_f32 v247, v247, v241
  v_cvt_pk_bf16_f32 v172, v240, v241
  v_mfma_f32_16x16x32_bf16 v[100:103], a[204:207], a[156:159], v[228:231]
  v_exp_f32 v242, v70
  v_mfma_f32_16x16x32_bf16 v[100:103], a[200:203], a[152:155], v[100:103]
  ds_read_b128 a[228:231], v12 offset:57856
  v_add_f32 v247, v247, v242
  v_mfma_f32_16x16x32_bf16 v[100:103], a[196:199], a[148:151], v[100:103]
  v_exp_f32 v243, v71
  v_mfma_f32_16x16x32_bf16 v[100:103], a[192:195], a[144:147], v[100:103]
  v_add_f32 v247, v247, v243
  v_cvt_pk_bf16_f32 v173, v242, v243
  v_mfma_f32_16x16x32_bf16 v[104:107], a[192:195], a[160:163], v[232:235]
  v_exp_f32 v244, v72
  v_mfma_f32_16x16x32_bf16 v[104:107], a[196:199], a[164:167], v[104:107]
  ds_read_b128 a[232:235], v12 offset:58368
  v_add_f32 v248, v248, v244
  v_mfma_f32_16x16x32_bf16 v[104:107], a[200:203], a[168:171], v[104:107]
  v_exp_f32 v245, v73
  v_mfma_f32_16x16x32_bf16 v[104:107], a[204:207], a[172:175], v[104:107]
  buffer_load_dwordx4 v20, s[56:59], s80 offen offset:1024 lds
  v_add_f32 v248, v248, v245
  v_cvt_pk_bf16_f32 v180, v244, v245
  v_mfma_f32_16x16x32_bf16 v[108:111], a[204:207], a[188:191], v[236:239]
  v_exp_f32 v240, v74
  v_mfma_f32_16x16x32_bf16 v[108:111], a[200:203], a[184:187], v[108:111]
  ds_read_b128 a[236:239], v12 offset:58880
  v_add_f32 v248, v248, v240
  v_mfma_f32_16x16x32_bf16 v[108:111], a[196:199], a[180:183], v[108:111]
  v_exp_f32 v241, v75
  v_mfma_f32_16x16x32_bf16 v[108:111], a[192:195], a[176:179], v[108:111]
  s_waitcnt lgkmcnt(4)
  v_add_f32 v248, v248, v241
  v_cvt_pk_bf16_f32 v181, v240, v241
  v_mfma_f32_16x16x32_bf16 v[112:115], a[208:211], a[128:131], v[224:227]
  v_exp_f32 v242, v76
  v_mfma_f32_16x16x32_bf16 v[112:115], a[212:215], a[132:135], v[112:115]
  ds_read_b128 a[240:243], v12 offset:61440
  v_add_f32 v249, v249, v242
  v_mfma_f32_16x16x32_bf16 v[112:115], a[216:219], a[136:139], v[112:115]
  v_exp_f32 v243, v77
  v_mfma_f32_16x16x32_bf16 v[112:115], a[220:223], a[140:143], v[112:115]
  buffer_load_dwordx4 v19, s[56:59], s80 offen offset:2048 lds
  v_add_f32 v249, v249, v243
  v_cvt_pk_bf16_f32 v188, v242, v243
  v_mfma_f32_16x16x32_bf16 v[116:119], a[220:223], a[156:159], v[228:231]
  v_exp_f32 v244, v78
  v_mfma_f32_16x16x32_bf16 v[116:119], a[216:219], a[152:155], v[116:119]
  ds_read_b128 a[244:247], v12 offset:61952
  v_add_f32 v249, v249, v244
  v_mfma_f32_16x16x32_bf16 v[116:119], a[212:215], a[148:151], v[116:119]
  v_exp_f32 v245, v79
  v_mfma_f32_16x16x32_bf16 v[116:119], a[208:211], a[144:147], v[116:119]
  v_add_f32 v249, v249, v245
  v_cvt_pk_bf16_f32 v189, v244, v245
  v_mfma_f32_16x16x32_bf16 v[120:123], a[208:211], a[160:163], v[232:235]
  v_exp_f32 v240, v80
  v_mfma_f32_16x16x32_bf16 v[120:123], a[212:215], a[164:167], v[120:123]
  ds_read_b128 a[248:251], v12 offset:62464
  v_add_f32 v246, v246, v240
  v_mfma_f32_16x16x32_bf16 v[120:123], a[216:219], a[168:171], v[120:123]
  v_exp_f32 v241, v81
  v_mfma_f32_16x16x32_bf16 v[120:123], a[220:223], a[172:175], v[120:123]
  buffer_load_dwordx4 v20, s[56:59], s80 offen offset:3072 lds
  v_add_f32 v246, v246, v241
  v_cvt_pk_bf16_f32 v166, v240, v241
  v_mfma_f32_16x16x32_bf16 v[124:127], a[220:223], a[188:191], v[236:239]
  s_add_u32 s80, s80, 16384
  v_exp_f32 v242, v82
  v_mfma_f32_16x16x32_bf16 v[124:127], a[216:219], a[184:187], v[124:127]
  ds_read_b128 a[252:255], v12 offset:62976
  v_add_f32 v246, v246, v242
  v_mfma_f32_16x16x32_bf16 v[124:127], a[212:215], a[180:183], v[124:127]
  s_add_u32 m0, s77, 131072
  v_exp_f32 v243, v83
  v_mfma_f32_16x16x32_bf16 v[124:127], a[208:211], a[176:179], v[124:127]
  s_waitcnt lgkmcnt(4)
  buffer_load_dwordx4 v21, s[36:39], s69 offen lds
  v_add_f32 v246, v246, v243
  v_cvt_pk_bf16_f32 v167, v242, v243
  v_mfma_f32_16x16x32_bf16 v[128:131], a[224:227], a[128:131], v[224:227]
  v_exp_f32 v244, v84
  v_mfma_f32_16x16x32_bf16 v[128:131], a[228:231], a[132:135], v[128:131]
  ds_read_b64_tr_b16 a[192:193], v13 offset:32768
  v_add_f32 v247, v247, v244
  v_mfma_f32_16x16x32_bf16 v[128:131], a[232:235], a[136:139], v[128:131]
  v_exp_f32 v245, v85
  v_mfma_f32_16x16x32_bf16 v[128:131], a[236:239], a[140:143], v[128:131]
  ds_read_b64_tr_b16 a[194:195], v13 offset:36864
  v_add_f32 v247, v247, v245
  v_cvt_pk_bf16_f32 v174, v244, v245
  v_mfma_f32_16x16x32_bf16 v[132:135], a[236:239], a[156:159], v[228:231]
  v_exp_f32 v240, v86
  v_mfma_f32_16x16x32_bf16 v[132:135], a[232:235], a[152:155], v[132:135]
  ds_read_b64_tr_b16 a[196:197], v13 offset:40960
  v_add_f32 v247, v247, v240
  v_mfma_f32_16x16x32_bf16 v[132:135], a[228:231], a[148:151], v[132:135]
  v_exp_f32 v241, v87
  v_mfma_f32_16x16x32_bf16 v[132:135], a[224:227], a[144:147], v[132:135]
  ds_read_b64_tr_b16 a[198:199], v13 offset:45056
  v_add_f32 v247, v247, v241
  v_cvt_pk_bf16_f32 v175, v240, v241
  v_mfma_f32_16x16x32_bf16 v[136:139], a[224:227], a[160:163], v[232:235]
  v_exp_f32 v242, v88
  v_mfma_f32_16x16x32_bf16 v[136:139], a[228:231], a[164:167], v[136:139]
  ds_read_b64_tr_b16 a[200:201], v14 offset:32768
  v_add_f32 v248, v248, v242
  v_mfma_f32_16x16x32_bf16 v[136:139], a[232:235], a[168:171], v[136:139]
  v_exp_f32 v243, v89
  v_mfma_f32_16x16x32_bf16 v[136:139], a[236:239], a[172:175], v[136:139]
  ds_read_b64_tr_b16 a[202:203], v14 offset:36864
  v_add_f32 v248, v248, v243
  v_cvt_pk_bf16_f32 v182, v242, v243
  v_mfma_f32_16x16x32_bf16 v[140:143], a[236:239], a[188:191], v[236:239]
  v_exp_f32 v244, v90
  v_mfma_f32_16x16x32_bf16 v[140:143], a[232:235], a[184:187], v[140:143]
  ds_read_b64_tr_b16 a[204:205], v14 offset:40960
  v_add_f32 v248, v248, v244
  v_mfma_f32_16x16x32_bf16 v[140:143], a[228:231], a[180:183], v[140:143]
  v_exp_f32 v245, v91
  v_mfma_f32_16x16x32_bf16 v[140:143], a[224:227], a[176:179], v[140:143]
  s_waitcnt lgkmcnt(7)
  ds_read_b64_tr_b16 a[206:207], v14 offset:45056
  v_add_f32 v248, v248, v245
  v_cvt_pk_bf16_f32 v183, v244, v245
  v_mfma_f32_16x16x32_bf16 v[144:147], a[240:243], a[128:131], v[224:227]
  v_exp_f32 v240, v92
  v_mfma_f32_16x16x32_bf16 v[144:147], a[244:247], a[132:135], v[144:147]
  ds_read_b64_tr_b16 a[208:209], v13 offset:33280
  v_add_f32 v249, v249, v240
  v_mfma_f32_16x16x32_bf16 v[144:147], a[248:251], a[136:139], v[144:147]
  v_exp_f32 v241, v93
  v_mfma_f32_16x16x32_bf16 v[144:147], a[252:255], a[140:143], v[144:147]
  ds_read_b64_tr_b16 a[210:211], v13 offset:37376
  v_add_f32 v249, v249, v241
  v_cvt_pk_bf16_f32 v190, v240, v241
  v_mfma_f32_16x16x32_bf16 v[148:151], a[252:255], a[156:159], v[228:231]
  v_exp_f32 v242, v94
  v_mfma_f32_16x16x32_bf16 v[148:151], a[248:251], a[152:155], v[148:151]
  ds_read_b64_tr_b16 a[212:213], v13 offset:41472
  v_add_f32 v249, v249, v242
  v_mfma_f32_16x16x32_bf16 v[148:151], a[244:247], a[148:151], v[148:151]
  v_exp_f32 v243, v95
  v_mfma_f32_16x16x32_bf16 v[148:151], a[240:243], a[144:147], v[148:151]
  ds_read_b64_tr_b16 a[214:215], v13 offset:45568
  v_add_f32 v249, v249, v243
  v_cvt_pk_bf16_f32 v191, v242, v243
  v_mfma_f32_16x16x32_bf16 v[152:155], a[240:243], a[160:163], v[232:235]
  v_exp_f32 v240, v96
  v_mfma_f32_16x16x32_bf16 v[152:155], a[244:247], a[164:167], v[152:155]
  ds_read_b64_tr_b16 a[216:217], v14 offset:33280
  v_max3_f32 v254, v246, v247, v248
  v_mfma_f32_16x16x32_bf16 v[152:155], a[248:251], a[168:171], v[152:155]
  v_exp_f32 v241, v97
  v_mfma_f32_16x16x32_bf16 v[152:155], a[252:255], a[172:175], v[152:155]
  ds_read_b64_tr_b16 a[218:219], v14 offset:37376
  v_add_f32 v250, v240, v241
  v_cvt_pk_bf16_f32 v192, v240, v241
  v_max_f32 v254, v254, v249
  v_mfma_f32_16x16x32_bf16 v[156:159], a[252:255], a[188:191], v[236:239]
  v_exp_f32 v242, v98
  v_mfma_f32_16x16x32_bf16 v[156:159], a[248:251], a[184:187], v[156:159]
  ds_read_b64_tr_b16 a[220:221], v14 offset:41472
  v_add_f32 v250, v250, v242
  v_cmp_nge_f32 vcc, s82, v254
  v_mfma_f32_16x16x32_bf16 v[156:159], a[244:247], a[180:183], v[156:159]
  v_exp_f32 v243, v99
  v_mfma_f32_16x16x32_bf16 v[156:159], a[240:243], a[176:179], v[156:159]
  s_waitcnt lgkmcnt(11)
  ds_read_b64_tr_b16 a[222:223], v14 offset:45568
  v_add_f32 v250, v250, v243
  v_cvt_pk_bf16_f32 v193, v242, v243
  s_cbranch_vccnz LB_stub7
LB_back7:
  v_mfma_f32_16x16x32_bf16 a[0:3], a[192:195], v[160:163], a[0:3]
  v_exp_f32 v244, v100
  v_mfma_f32_16x16x32_bf16 a[0:3], a[196:199], v[164:167], a[0:3]
  ds_read_b64_tr_b16 a[224:225], v13 offset:33792
  v_mfma_f32_16x16x32_bf16 a[4:7], a[196:199], v[172:175], a[4:7]
  v_exp_f32 v245, v101
  v_mfma_f32_16x16x32_bf16 a[4:7], a[192:195], v[168:171], a[4:7]
  ds_read_b64_tr_b16 a[226:227], v13 offset:37888
  v_add_f32 v251, v244, v245
  v_cvt_pk_bf16_f32 v200, v244, v245
  v_mfma_f32_16x16x32_bf16 a[8:11], a[192:195], v[176:179], a[8:11]
  v_exp_f32 v240, v102
  v_mfma_f32_16x16x32_bf16 a[8:11], a[196:199], v[180:183], a[8:11]
  ds_read_b64_tr_b16 a[228:229], v13 offset:41984
  v_add_f32 v23, v23, v246
  v_add_f32 v251, v251, v240
  v_mfma_f32_16x16x32_bf16 a[12:15], a[196:199], v[188:191], a[12:15]
  v_exp_f32 v241, v103
  v_mfma_f32_16x16x32_bf16 a[12:15], a[192:195], v[184:187], a[12:15]
  s_waitcnt lgkmcnt(11)
  ds_read_b64_tr_b16 a[230:231], v13 offset:46080
  v_add_f32 v251, v251, v241
  v_cvt_pk_bf16_f32 v201, v240, v241
  v_mfma_f32_16x16x32_bf16 a[16:19], a[200:203], v[160:163], a[16:19]
  v_exp_f32 v242, v104
  v_mfma_f32_16x16x32_bf16 a[16:19], a[204:207], v[164:167], a[16:19]
  ds_read_b64_tr_b16 a[232:233], v14 offset:33792
  v_add_f32 v24, v24, v247
  v_mfma_f32_16x16x32_bf16 a[20:23], a[204:207], v[172:175], a[20:23]
  v_exp_f32 v243, v105
  v_mfma_f32_16x16x32_bf16 a[20:23], a[200:203], v[168:171], a[20:23]
  ds_read_b64_tr_b16 a[234:235], v14 offset:37888
  v_add_f32 v252, v242, v243
  v_cvt_pk_bf16_f32 v208, v242, v243
  v_mfma_f32_16x16x32_bf16 a[24:27], a[200:203], v[176:179], a[24:27]
  v_exp_f32 v244, v106
  v_mfma_f32_16x16x32_bf16 a[24:27], a[204:207], v[180:183], a[24:27]
  ds_read_b64_tr_b16 a[236:237], v14 offset:41984
  v_add_f32 v25, v25, v248
  v_add_f32 v252, v252, v244
  v_mfma_f32_16x16x32_bf16 a[28:31], a[204:207], v[188:191], a[28:31]
  v_exp_f32 v245, v107
  v_mfma_f32_16x16x32_bf16 a[28:31], a[200:203], v[184:187], a[28:31]
  s_waitcnt lgkmcnt(11)
  ds_read_b64_tr_b16 a[238:239], v14 offset:46080
  v_add_f32 v252, v252, v245
  v_cvt_pk_bf16_f32 v209, v244, v245
  v_mfma_f32_16x16x32_bf16 a[32:35], a[208:211], v[160:163], a[32:35]
  v_exp_f32 v240, v108
  v_mfma_f32_16x16x32_bf16 a[32:35], a[212:215], v[164:167], a[32:35]
  ds_read_b64_tr_b16 a[240:241], v13 offset:34304
  v_add_f32 v26, v26, v249
  v_mfma_f32_16x16x32_bf16 a[36:39], a[212:215], v[172:175], a[36:39]
  v_exp_f32 v241, v109
  v_mfma_f32_16x16x32_bf16 a[36:39], a[208:211], v[168:171], a[36:39]
  ds_read_b64_tr_b16 a[242:243], v13 offset:38400
  v_add_f32 v253, v240, v241
  v_cvt_pk_bf16_f32 v216, v240, v241
  v_mfma_f32_16x16x32_bf16 a[40:43], a[208:211], v[176:179], a[40:43]
  v_exp_f32 v242, v110
  v_mfma_f32_16x16x32_bf16 a[40:43], a[212:215], v[180:183], a[40:43]
  ds_read_b64_tr_b16 a[244:245], v13 offset:42496
  v_add_f32 v253, v253, v242
  v_mfma_f32_16x16x32_bf16 a[44:47], a[212:215], v[188:191], a[44:47]
  v_exp_f32 v243, v111
  v_mfma_f32_16x16x32_bf16 a[44:47], a[208:211], v[184:187], a[44:47]
  s_waitcnt lgkmcnt(11)
  ds_read_b64_tr_b16 a[246:247], v13 offset:46592
  v_add_f32 v253, v253, v243
  v_cvt_pk_bf16_f32 v217, v242, v243
  v_mfma_f32_16x16x32_bf16 a[48:51], a[216:219], v[160:163], a[48:51]
  v_exp_f32 v244, v112
  v_mfma_f32_16x16x32_bf16 a[48:51], a[220:223], v[164:167], a[48:51]
  ds_read_b64_tr_b16 a[248:249], v14 offset:34304
  v_add_f32 v250, v250, v244
  v_mfma_f32_16x16x32_bf16 a[52:55], a[220:223], v[172:175], a[52:55]
  v_exp_f32 v245, v113
  v_mfma_f32_16x16x32_bf16 a[52:55], a[216:219], v[168:171], a[52:55]
  ds_read_b64_tr_b16 a[250:251], v14 offset:38400
  v_add_f32 v250, v250, v245
  v_cvt_pk_bf16_f32 v194, v244, v245
  v_mfma_f32_16x16x32_bf16 a[56:59], a[216:219], v[176:179], a[56:59]
  v_exp_f32 v240, v114
  v_mfma_f32_16x16x32_bf16 a[56:59], a[220:223], v[180:183], a[56:59]
  ds_read_b64_tr_b16 a[252:253], v14 offset:42496
  v_add_f32 v250, v250, v240
  v_mfma_f32_16x16x32_bf16 a[60:63], a[220:223], v[188:191], a[60:63]
  v_exp_f32 v241, v115
  v_mfma_f32_16x16x32_bf16 a[60:63], a[216:219], v[184:187], a[60:63]
  s_waitcnt lgkmcnt(11)
  ds_read_b64_tr_b16 a[254:255], v14 offset:46592
  v_add_f32 v250, v250, v241
  v_cvt_pk_bf16_f32 v195, v240, v241
  v_mfma_f32_16x16x32_bf16 a[64:67], a[224:227], v[160:163], a[64:67]
  s_waitcnt vmcnt(10)
  s_barrier
  v_exp_f32 v242, v116
  v_mfma_f32_16x16x32_bf16 a[64:67], a[228:231], v[164:167], a[64:67]
  ds_read_b128 a[192:195], v12 offset:0
  s_lshl_b32 s72, s11, 8
  s_add_u32 s72, s72, 65536
  v_mov_b32 v254, 1
  v_mov_b32 v255, s72
  s_mov_b64 exec, 1
  buffer_atomic_add v254, v255, s[44:47], 0 offen sc1
  s_mov_b64 exec, -1
  v_add_f32 v251, v251, v242
  v_mfma_f32_16x16x32_bf16 a[68:71], a[228:231], v[172:175], a[68:71]
  s_add_u32 m0, s77, 32768
  v_exp_f32 v243, v117
  v_mfma_f32_16x16x32_bf16 a[68:71], a[224:227], v[168:171], a[68:71]
  buffer_load_dwordx4 v15, s[52:55], s79 offen offset:0 lds
  v_add_f32 v251, v251, v243
  v_cvt_pk_bf16_f32 v202, v242, v243
  v_mfma_f32_16x16x32_bf16 a[72:75], a[224:227], v[176:179], a[72:75]
  v_exp_f32 v244, v118
  v_mfma_f32_16x16x32_bf16 a[72:75], a[228:231], v[180:183], a[72:75]
  ds_read_b128 a[196:199], v12 offset:512
  v_add_f32 v251, v251, v244
  v_mfma_f32_16x16x32_bf16 a[76:79], a[228:231], v[188:191], a[76:79]
  v_exp_f32 v245, v119
  v_mfma_f32_16x16x32_bf16 a[76:79], a[224:227], v[184:187], a[76:79]
  s_waitcnt lgkmcnt(10)
  v_add_f32 v251, v251, v245
  v_cvt_pk_bf16_f32 v203, v244, v245
  v_mfma_f32_16x16x32_bf16 a[80:83], a[232:235], v[160:163], a[80:83]
  v_exp_f32 v240, v120
  v_mfma_f32_16x16x32_bf16 a[80:83], a[236:239], v[164:167], a[80:83]
  ds_read_b128 a[200:203], v12 offset:1024
  v_add_f32 v252, v252, v240
  v_mfma_f32_16x16x32_bf16 a[84:87], a[236:239], v[172:175], a[84:87]
  v_exp_f32 v241, v121
  v_mfma_f32_16x16x32_bf16 a[84:87], a[232:235], v[168:171], a[84:87]
  buffer_load_dwordx4 v16, s[52:55], s79 offen offset:1024 lds
  v_add_f32 v252, v252, v241
  v_cvt_pk_bf16_f32 v210, v240, v241
  v_mfma_f32_16x16x32_bf16 a[88:91], a[232:235], v[176:179], a[88:91]
  v_exp_f32 v242, v122
  v_mfma_f32_16x16x32_bf16 a[88:91], a[236:239], v[180:183], a[88:91]
  ds_read_b128 a[204:207], v12 offset:1536
  v_add_f32 v252, v252, v242
  v_mfma_f32_16x16x32_bf16 a[92:95], a[236:239], v[188:191], a[92:95]
  v_exp_f32 v243, v123
  v_mfma_f32_16x16x32_bf16 a[92:95], a[232:235], v[184:187], a[92:95]
  s_waitcnt lgkmcnt(8)
  v_add_f32 v252, v252, v243
  v_cvt_pk_bf16_f32 v211, v242, v243
  v_mfma_f32_16x16x32_bf16 a[96:99], a[240:243], v[160:163], a[96:99]
  v_exp_f32 v244, v124
  v_mfma_f32_16x16x32_bf16 a[96:99], a[244:247], v[164:167], a[96:99]
  ds_read_b128 a[208:211], v12 offset:4096
  v_add_f32 v253, v253, v244
  v_mfma_f32_16x16x32_bf16 a[100:103], a[244:247], v[172:175], a[100:103]
  v_exp_f32 v245, v125
  v_mfma_f32_16x16x32_bf16 a[100:103], a[240:243], v[168:171], a[100:103]
  buffer_load_dwordx4 v17, s[52:55], s79 offen offset:2048 lds
  v_add_f32 v253, v253, v245
  v_cvt_pk_bf16_f32 v218, v244, v245
  v_mfma_f32_16x16x32_bf16 a[104:107], a[240:243], v[176:179], a[104:107]
  v_exp_f32 v240, v126
  v_mfma_f32_16x16x32_bf16 a[104:107], a[244:247], v[180:183], a[104:107]
  ds_read_b128 a[212:215], v12 offset:4608
  v_add_f32 v253, v253, v240
  v_mfma_f32_16x16x32_bf16 a[108:111], a[244:247], v[188:191], a[108:111]
  v_exp_f32 v241, v127
  v_mfma_f32_16x16x32_bf16 a[108:111], a[240:243], v[184:187], a[108:111]
  s_waitcnt lgkmcnt(6)
  v_add_f32 v253, v253, v241
  v_cvt_pk_bf16_f32 v219, v240, v241
  v_mfma_f32_16x16x32_bf16 a[112:115], a[248:251], v[160:163], a[112:115]
  v_exp_f32 v242, v128
  v_mfma_f32_16x16x32_bf16 a[112:115], a[252:255], v[164:167], a[112:115]
  ds_read_b128 a[216:219], v12 offset:5120
  v_add_f32 v250, v250, v242
  v_mfma_f32_16x16x32_bf16 a[116:119], a[252:255], v[172:175], a[116:119]
  v_exp_f32 v243, v129
  v_mfma_f32_16x16x32_bf16 a[116:119], a[248:251], v[168:171], a[116:119]
  buffer_load_dwordx4 v18, s[52:55], s79 offen offset:3072 lds
  v_add_f32 v250, v250, v243
  v_cvt_pk_bf16_f32 v196, v242, v243
  v_mfma_f32_16x16x32_bf16 a[120:123], a[248:251], v[176:179], a[120:123]
  s_add_u32 s79, s79, 16384
  v_exp_f32 v244, v130
  v_mfma_f32_16x16x32_bf16 a[120:123], a[252:255], v[180:183], a[120:123]
  ds_read_b128 a[220:223], v12 offset:5632
  v_add_f32 v250, v250, v244
  v_mfma_f32_16x16x32_bf16 a[124:127], a[252:255], v[188:191], a[124:127]
  v_exp_f32 v245, v131
  v_mfma_f32_16x16x32_bf16 a[124:127], a[248:251], v[184:187], a[124:127]
  s_waitcnt lgkmcnt(4)
  v_add_f32 v250, v250, v245
  v_cvt_pk_bf16_f32 v197, v244, v245
  v_mfma_f32_16x16x32_bf16 v[32:35], a[192:195], a[128:131], v[224:227]
  v_exp_f32 v240, v132
  v_mfma_f32_16x16x32_bf16 v[32:35], a[196:199], a[132:135], v[32:35]
  ds_read_b128 a[224:227], v12 offset:8192
  v_add_f32 v251, v251, v240
  v_mfma_f32_16x16x32_bf16 v[32:35], a[200:203], a[136:139], v[32:35]
  s_add_u32 m0, s78, 16384
  v_exp_f32 v241, v133
  v_mfma_f32_16x16x32_bf16 v[32:35], a[204:207], a[140:143], v[32:35]
  buffer_load_dwordx4 v19, s[56:59], s80 offen offset:0 lds
  v_add_f32 v251, v251, v241
  v_cvt_pk_bf16_f32 v204, v240, v241
  v_mfma_f32_16x16x32_bf16 v[36:39], a[204:207], a[156:159], v[228:231]
  v_exp_f32 v242, v134
  v_mfma_f32_16x16x32_bf16 v[36:39], a[200:203], a[152:155], v[36:39]
  ds_read_b128 a[228:231], v12 offset:8704
  v_add_f32 v251, v251, v242
  v_mfma_f32_16x16x32_bf16 v[36:39], a[196:199], a[148:151], v[36:39]
  v_exp_f32 v243, v135
  v_mfma_f32_16x16x32_bf16 v[36:39], a[192:195], a[144:147], v[36:39]
  v_add_f32 v251, v251, v243
  v_cvt_pk_bf16_f32 v205, v242, v243
  v_mfma_f32_16x16x32_bf16 v[40:43], a[192:195], a[160:163], v[232:235]
  v_exp_f32 v244, v136
  v_mfma_f32_16x16x32_bf16 v[40:43], a[196:199], a[164:167], v[40:43]
  ds_read_b128 a[232:235], v12 offset:9216
  v_add_f32 v252, v252, v244
  v_mfma_f32_16x16x32_bf16 v[40:43], a[200:203], a[168:171], v[40:43]
  v_exp_f32 v245, v137
  v_mfma_f32_16x16x32_bf16 v[40:43], a[204:207], a[172:175], v[40:43]
  buffer_load_dwordx4 v20, s[56:59], s80 offen offset:1024 lds
  v_add_f32 v252, v252, v245
  v_cvt_pk_bf16_f32 v212, v244, v245
  v_mfma_f32_16x16x32_bf16 v[44:47], a[204:207], a[188:191], v[236:239]
  v_exp_f32 v240, v138
  v_mfma_f32_16x16x32_bf16 v[44:47], a[200:203], a[184:187], v[44:47]
  ds_read_b128 a[236:239], v12 offset:9728
  v_add_f32 v252, v252, v240
  v_mfma_f32_16x16x32_bf16 v[44:47], a[196:199], a[180:183], v[44:47]
  v_exp_f32 v241, v139
  v_mfma_f32_16x16x32_bf16 v[44:47], a[192:195], a[176:179], v[44:47]
  s_waitcnt lgkmcnt(4)
  v_add_f32 v252, v252, v241
  v_cvt_pk_bf16_f32 v213, v240, v241
  v_mfma_f32_16x16x32_bf16 v[48:51], a[208:211], a[128:131], v[224:227]
  v_exp_f32 v242, v140
  v_mfma_f32_16x16x32_bf16 v[48:51], a[212:215], a[132:135], v[48:51]
  ds_read_b128 a[240:243], v12 offset:12288
  v_add_f32 v253, v253, v242
  v_mfma_f32_16x16x32_bf16 v[48:51], a[216:219], a[136:139], v[48:51]
  v_exp_f32 v243, v141
  v_mfma_f32_16x16x32_bf16 v[48:51], a[220:223], a[140:143], v[48:51]
  buffer_load_dwordx4 v19, s[56:59], s80 offen offset:2048 lds
  v_add_f32 v253, v253, v243
  v_cvt_pk_bf16_f32 v220, v242, v243
  v_mfma_f32_16x16x32_bf16 v[52:55], a[220:223], a[156:159], v[228:231]
  v_exp_f32 v244, v142
  v_mfma_f32_16x16x32_bf16 v[52:55], a[216:219], a[152:155], v[52:55]
  ds_read_b128 a[244:247], v12 offset:12800
  v_add_f32 v253, v253, v244
  v_mfma_f32_16x16x32_bf16 v[52:55], a[212:215], a[148:151], v[52:55]
  v_exp_f32 v245, v143
  v_mfma_f32_16x16x32_bf16 v[52:55], a[208:211], a[144:147], v[52:55]
  v_add_f32 v253, v253, v245
  v_cvt_pk_bf16_f32 v221, v244, v245
  v_mfma_f32_16x16x32_bf16 v[56:59], a[208:211], a[160:163], v[232:235]
  v_exp_f32 v240, v144
  v_mfma_f32_16x16x32_bf16 v[56:59], a[212:215], a[164:167], v[56:59]
  ds_read_b128 a[248:251], v12 offset:13312
  v_add_f32 v250, v250, v240
  v_mfma_f32_16x16x32_bf16 v[56:59], a[216:219], a[168:171], v[56:59]
  v_exp_f32 v241, v145
  v_mfma_f32_16x16x32_bf16 v[56:59], a[220:223], a[172:175], v[56:59]
  buffer_load_dwordx4 v20, s[56:59], s80 offen offset:3072 lds
  v_add_f32 v250, v250, v241
  v_cvt_pk_bf16_f32 v198, v240, v241
  v_mfma_f32_16x16x32_bf16 v[60:63], a[220:223], a[188:191], v[236:239]
  s_add_u32 s80, s80, 16384
  v_exp_f32 v242, v146
  v_mfma_f32_16x16x32_bf16 v[60:63], a[216:219], a[184:187], v[60:63]
  ds_read_b128 a[252:255], v12 offset:13824
  v_add_f32 v250, v250, v242
  v_mfma_f32_16x16x32_bf16 v[60:63], a[212:215], a[180:183], v[60:63]
  s_add_u32 m0, s77, 131072
  v_exp_f32 v243, v147
  v_mfma_f32_16x16x32_bf16 v[60:63], a[208:211], a[176:179], v[60:63]
  s_waitcnt lgkmcnt(4)
  buffer_load_dwordx4 v21, s[40:43], s69 offen lds
  v_add_f32 v250, v250, v243
  v_cvt_pk_bf16_f32 v199, v242, v243
  v_mfma_f32_16x16x32_bf16 v[64:67], a[224:227], a[128:131], v[224:227]
  s_add_u32 s69, s69, 4096
  v_exp_f32 v244, v148
  v_mfma_f32_16x16x32_bf16 v[64:67], a[228:231], a[132:135], v[64:67]
  ds_read_b64_tr_b16 a[192:193], v13 offset:49152
  v_add_f32 v251, v251, v244
  v_mfma_f32_16x16x32_bf16 v[64:67], a[232:235], a[136:139], v[64:67]
  v_exp_f32 v245, v149
  v_mfma_f32_16x16x32_bf16 v[64:67], a[236:239], a[140:143], v[64:67]
  ds_read_b64_tr_b16 a[194:195], v13 offset:53248
  v_add_f32 v251, v251, v245
  v_cvt_pk_bf16_f32 v206, v244, v245
  v_mfma_f32_16x16x32_bf16 v[68:71], a[236:239], a[156:159], v[228:231]
  v_exp_f32 v240, v150
  v_mfma_f32_16x16x32_bf16 v[68:71], a[232:235], a[152:155], v[68:71]
  ds_read_b64_tr_b16 a[196:197], v13 offset:57344
  v_add_f32 v251, v251, v240
  v_mfma_f32_16x16x32_bf16 v[68:71], a[228:231], a[148:151], v[68:71]
  v_exp_f32 v241, v151
  v_mfma_f32_16x16x32_bf16 v[68:71], a[224:227], a[144:147], v[68:71]
  ds_read_b64_tr_b16 a[198:199], v13 offset:61440
  v_add_f32 v251, v251, v241
  v_cvt_pk_bf16_f32 v207, v240, v241
  v_mfma_f32_16x16x32_bf16 v[72:75], a[224:227], a[160:163], v[232:235]
  v_exp_f32 v242, v152
  v_mfma_f32_16x16x32_bf16 v[72:75], a[228:231], a[164:167], v[72:75]
  ds_read_b64_tr_b16 a[200:201], v14 offset:49152
  v_add_f32 v252, v252, v242
  v_mfma_f32_16x16x32_bf16 v[72:75], a[232:235], a[168:171], v[72:75]
  v_exp_f32 v243, v153
  v_mfma_f32_16x16x32_bf16 v[72:75], a[236:239], a[172:175], v[72:75]
  ds_read_b64_tr_b16 a[202:203], v14 offset:53248
  v_add_f32 v252, v252, v243
  v_cvt_pk_bf16_f32 v214, v242, v243
  v_mfma_f32_16x16x32_bf16 v[76:79], a[236:239], a[188:191], v[236:239]
  v_exp_f32 v244, v154
  v_mfma_f32_16x16x32_bf16 v[76:79], a[232:235], a[184:187], v[76:79]
  ds_read_b64_tr_b16 a[204:205], v14 offset:57344
  v_add_f32 v252, v252, v244
  v_mfma_f32_16x16x32_bf16 v[76:79], a[228:231], a[180:183], v[76:79]
  v_exp_f32 v245, v155
  v_mfma_f32_16x16x32_bf16 v[76:79], a[224:227], a[176:179], v[76:79]
  s_waitcnt lgkmcnt(7)
  ds_read_b64_tr_b16 a[206:207], v14 offset:61440
  v_add_f32 v252, v252, v245
  v_cvt_pk_bf16_f32 v215, v244, v245
  v_mfma_f32_16x16x32_bf16 v[80:83], a[240:243], a[128:131], v[224:227]
  v_exp_f32 v240, v156
  v_mfma_f32_16x16x32_bf16 v[80:83], a[244:247], a[132:135], v[80:83]
  ds_read_b64_tr_b16 a[208:209], v13 offset:49664
  v_add_f32 v253, v253, v240
  v_mfma_f32_16x16x32_bf16 v[80:83], a[248:251], a[136:139], v[80:83]
  v_exp_f32 v241, v157
  v_mfma_f32_16x16x32_bf16 v[80:83], a[252:255], a[140:143], v[80:83]
  ds_read_b64_tr_b16 a[210:211], v13 offset:53760
  v_add_f32 v253, v253, v241
  v_cvt_pk_bf16_f32 v222, v240, v241
  v_mfma_f32_16x16x32_bf16 v[84:87], a[252:255], a[156:159], v[228:231]
  v_exp_f32 v242, v158
  v_mfma_f32_16x16x32_bf16 v[84:87], a[248:251], a[152:155], v[84:87]
  ds_read_b64_tr_b16 a[212:213], v13 offset:57856
  v_add_f32 v253, v253, v242
  v_mfma_f32_16x16x32_bf16 v[84:87], a[244:247], a[148:151], v[84:87]
  v_exp_f32 v243, v159
  v_mfma_f32_16x16x32_bf16 v[84:87], a[240:243], a[144:147], v[84:87]
  ds_read_b64_tr_b16 a[214:215], v13 offset:61952
  v_add_f32 v253, v253, v243
  v_cvt_pk_bf16_f32 v223, v242, v243
  v_mfma_f32_16x16x32_bf16 v[88:91], a[240:243], a[160:163], v[232:235]
  v_exp_f32 v240, v32
  v_mfma_f32_16x16x32_bf16 v[88:91], a[244:247], a[164:167], v[88:91]
  ds_read_b64_tr_b16 a[216:217], v14 offset:49664
  v_max3_f32 v254, v250, v251, v252
  v_mfma_f32_16x16x32_bf16 v[88:91], a[248:251], a[168:171], v[88:91]
  v_exp_f32 v241, v33
  v_mfma_f32_16x16x32_bf16 v[88:91], a[252:255], a[172:175], v[88:91]
  ds_read_b64_tr_b16 a[218:219], v14 offset:53760
  v_add_f32 v246, v240, v241
  v_cvt_pk_bf16_f32 v160, v240, v241
  v_max_f32 v254, v254, v253
  v_mfma_f32_16x16x32_bf16 v[92:95], a[252:255], a[188:191], v[236:239]
  v_exp_f32 v242, v34
  v_mfma_f32_16x16x32_bf16 v[92:95], a[248:251], a[184:187], v[92:95]
  ds_read_b64_tr_b16 a[220:221], v14 offset:57856
  v_add_f32 v246, v246, v242
  v_cmp_nge_f32 vcc, s82, v254
  v_mfma_f32_16x16x32_bf16 v[92:95], a[244:247], a[180:183], v[92:95]
  v_exp_f32 v243, v35
  v_mfma_f32_16x16x32_bf16 v[92:95], a[240:243], a[176:179], v[92:95]
  s_waitcnt lgkmcnt(11)
  ds_read_b64_tr_b16 a[222:223], v14 offset:61952
  v_add_f32 v246, v246, v243
  v_cvt_pk_bf16_f32 v161, v242, v243
  s_cbranch_vccnz LB_stub8
LB_back8:
  v_mfma_f32_16x16x32_bf16 a[0:3], a[192:195], v[192:195], a[0:3]
  v_exp_f32 v244, v36
  v_mfma_f32_16x16x32_bf16 a[0:3], a[196:199], v[196:199], a[0:3]
  ds_read_b64_tr_b16 a[224:225], v13 offset:50176
  v_mfma_f32_16x16x32_bf16 a[4:7], a[196:199], v[204:207], a[4:7]
  v_exp_f32 v245, v37
  v_mfma_f32_16x16x32_bf16 a[4:7], a[192:195], v[200:203], a[4:7]
  ds_read_b64_tr_b16 a[226:227], v13 offset:54272
  v_add_f32 v247, v244, v245
  v_cvt_pk_bf16_f32 v168, v244, v245
  v_mfma_f32_16x16x32_bf16 a[8:11], a[192:195], v[208:211], a[8:11]
  v_exp_f32 v240, v38
  v_mfma_f32_16x16x32_bf16 a[8:11], a[196:199], v[212:215], a[8:11]
  ds_read_b64_tr_b16 a[228:229], v13 offset:58368
  v_add_f32 v23, v23, v250
  v_add_f32 v247, v247, v240
  v_mfma_f32_16x16x32_bf16 a[12:15], a[196:199], v[220:223], a[12:15]
  v_exp_f32 v241, v39
  v_mfma_f32_16x16x32_bf16 a[12:15], a[192:195], v[216:219], a[12:15]
  s_waitcnt lgkmcnt(11)
  ds_read_b64_tr_b16 a[230:231], v13 offset:62464
  v_add_f32 v247, v247, v241
  v_cvt_pk_bf16_f32 v169, v240, v241
  v_mfma_f32_16x16x32_bf16 a[16:19], a[200:203], v[192:195], a[16:19]
  v_exp_f32 v242, v40
  v_mfma_f32_16x16x32_bf16 a[16:19], a[204:207], v[196:199], a[16:19]
  ds_read_b64_tr_b16 a[232:233], v14 offset:50176
  v_add_f32 v24, v24, v251
  v_mfma_f32_16x16x32_bf16 a[20:23], a[204:207], v[204:207], a[20:23]
  v_exp_f32 v243, v41
  v_mfma_f32_16x16x32_bf16 a[20:23], a[200:203], v[200:203], a[20:23]
  ds_read_b64_tr_b16 a[234:235], v14 offset:54272
  v_add_f32 v248, v242, v243
  v_cvt_pk_bf16_f32 v176, v242, v243
  v_mfma_f32_16x16x32_bf16 a[24:27], a[200:203], v[208:211], a[24:27]
  v_exp_f32 v244, v42
  v_mfma_f32_16x16x32_bf16 a[24:27], a[204:207], v[212:215], a[24:27]
  ds_read_b64_tr_b16 a[236:237], v14 offset:58368
  v_add_f32 v25, v25, v252
  v_add_f32 v248, v248, v244
  v_mfma_f32_16x16x32_bf16 a[28:31], a[204:207], v[220:223], a[28:31]
  v_exp_f32 v245, v43
  v_mfma_f32_16x16x32_bf16 a[28:31], a[200:203], v[216:219], a[28:31]
  s_waitcnt lgkmcnt(11)
  ds_read_b64_tr_b16 a[238:239], v14 offset:62464
  v_add_f32 v248, v248, v245
  v_cvt_pk_bf16_f32 v177, v244, v245
  v_mfma_f32_16x16x32_bf16 a[32:35], a[208:211], v[192:195], a[32:35]
  v_exp_f32 v240, v44
  v_mfma_f32_16x16x32_bf16 a[32:35], a[212:215], v[196:199], a[32:35]
  ds_read_b64_tr_b16 a[240:241], v13 offset:50688
  v_add_f32 v26, v26, v253
  v_mfma_f32_16x16x32_bf16 a[36:39], a[212:215], v[204:207], a[36:39]
  v_exp_f32 v241, v45
  v_mfma_f32_16x16x32_bf16 a[36:39], a[208:211], v[200:203], a[36:39]
  ds_read_b64_tr_b16 a[242:243], v13 offset:54784
  v_add_f32 v249, v240, v241
  v_cvt_pk_bf16_f32 v184, v240, v241
  v_mfma_f32_16x16x32_bf16 a[40:43], a[208:211], v[208:211], a[40:43]
  v_exp_f32 v242, v46
  v_mfma_f32_16x16x32_bf16 a[40:43], a[212:215], v[212:215], a[40:43]
  ds_read_b64_tr_b16 a[244:245], v13 offset:58880
  v_add_f32 v249, v249, v242
  v_mfma_f32_16x16x32_bf16 a[44:47], a[212:215], v[220:223], a[44:47]
  v_exp_f32 v243, v47
  v_mfma_f32_16x16x32_bf16 a[44:47], a[208:211], v[216:219], a[44:47]
  s_waitcnt lgkmcnt(11)
  ds_read_b64_tr_b16 a[246:247], v13 offset:62976
  v_add_f32 v249, v249, v243
  v_cvt_pk_bf16_f32 v185, v242, v243
  v_mfma_f32_16x16x32_bf16 a[48:51], a[216:219], v[192:195], a[48:51]
  v_exp_f32 v244, v48
  v_mfma_f32_16x16x32_bf16 a[48:51], a[220:223], v[196:199], a[48:51]
  ds_read_b64_tr_b16 a[248:249], v14 offset:50688
  v_add_f32 v246, v246, v244
  v_mfma_f32_16x16x32_bf16 a[52:55], a[220:223], v[204:207], a[52:55]
  v_exp_f32 v245, v49
  v_mfma_f32_16x16x32_bf16 a[52:55], a[216:219], v[200:203], a[52:55]
  ds_read_b64_tr_b16 a[250:251], v14 offset:54784
  v_add_f32 v246, v246, v245
  v_cvt_pk_bf16_f32 v162, v244, v245
  v_mfma_f32_16x16x32_bf16 a[56:59], a[216:219], v[208:211], a[56:59]
  v_exp_f32 v240, v50
  v_mfma_f32_16x16x32_bf16 a[56:59], a[220:223], v[212:215], a[56:59]
  ds_read_b64_tr_b16 a[252:253], v14 offset:58880
  v_add_f32 v246, v246, v240
  v_mfma_f32_16x16x32_bf16 a[60:63], a[220:223], v[220:223], a[60:63]
  v_exp_f32 v241, v51
  v_mfma_f32_16x16x32_bf16 a[60:63], a[216:219], v[216:219], a[60:63]
  s_waitcnt lgkmcnt(11)
  ds_read_b64_tr_b16 a[254:255], v14 offset:62976
  v_add_f32 v246, v246, v241
  v_cvt_pk_bf16_f32 v163, v240, v241
  v_mfma_f32_16x16x32_bf16 a[64:67], a[224:227], v[192:195], a[64:67]
  s_waitcnt vmcnt(11)
  s_barrier
  v_exp_f32 v242, v52
  v_mfma_f32_16x16x32_bf16 a[64:67], a[228:231], v[196:199], a[64:67]
  ds_read_b128 a[192:195], v12 offset:16384
  v_add_f32 v247, v247, v242
  v_mfma_f32_16x16x32_bf16 a[68:71], a[228:231], v[204:207], a[68:71]
  s_add_u32 m0, s77, 49152
  v_exp_f32 v243, v53
  v_mfma_f32_16x16x32_bf16 a[68:71], a[224:227], v[200:203], a[68:71]
  buffer_load_dwordx4 v15, s[52:55], s79 offen offset:0 lds
  v_add_f32 v247, v247, v243
  v_cvt_pk_bf16_f32 v170, v242, v243
  v_mfma_f32_16x16x32_bf16 a[72:75], a[224:227], v[208:211], a[72:75]
  v_exp_f32 v244, v54
  v_mfma_f32_16x16x32_bf16 a[72:75], a[228:231], v[212:215], a[72:75]
  ds_read_b128 a[196:199], v12 offset:16896
  v_add_f32 v247, v247, v244
  v_mfma_f32_16x16x32_bf16 a[76:79], a[228:231], v[220:223], a[76:79]
  v_exp_f32 v245, v55
  v_mfma_f32_16x16x32_bf16 a[76:79], a[224:227], v[216:219], a[76:79]
  s_waitcnt lgkmcnt(10)
  v_add_f32 v247, v247, v245
  v_cvt_pk_bf16_f32 v171, v244, v245
  v_mfma_f32_16x16x32_bf16 a[80:83], a[232:235], v[192:195], a[80:83]
  v_exp_f32 v240, v56
  v_mfma_f32_16x16x32_bf16 a[80:83], a[236:239], v[196:199], a[80:83]
  ds_read_b128 a[200:203], v12 offset:17408
  v_add_f32 v248, v248, v240
  v_mfma_f32_16x16x32_bf16 a[84:87], a[236:239], v[204:207], a[84:87]
  v_exp_f32 v241, v57
  v_mfma_f32_16x16x32_bf16 a[84:87], a[232:235], v[200:203], a[84:87]
  buffer_load_dwordx4 v16, s[52:55], s79 offen offset:1024 lds
  v_add_f32 v248, v248, v241
  v_cvt_pk_bf16_f32 v178, v240, v241
  v_mfma_f32_16x16x32_bf16 a[88:91], a[232:235], v[208:211], a[88:91]
  v_exp_f32 v242, v58
  v_mfma_f32_16x16x32_bf16 a[88:91], a[236:239], v[212:215], a[88:91]
  ds_read_b128 a[204:207], v12 offset:17920
  v_add_f32 v248, v248, v242
  v_mfma_f32_16x16x32_bf16 a[92:95], a[236:239], v[220:223], a[92:95]
  v_exp_f32 v243, v59
  v_mfma_f32_16x16x32_bf16 a[92:95], a[232:235], v[216:219], a[92:95]
  s_waitcnt lgkmcnt(8)
  v_add_f32 v248, v248, v243
  v_cvt_pk_bf16_f32 v179, v242, v243
  v_mfma_f32_16x16x32_bf16 a[96:99], a[240:243], v[192:195], a[96:99]
  v_exp_f32 v244, v60
  v_mfma_f32_16x16x32_bf16 a[96:99], a[244:247], v[196:199], a[96:99]
  ds_read_b128 a[208:211], v12 offset:20480
  v_add_f32 v249, v249, v244
  v_mfma_f32_16x16x32_bf16 a[100:103], a[244:247], v[204:207], a[100:103]
  v_exp_f32 v245, v61
  v_mfma_f32_16x16x32_bf16 a[100:103], a[240:243], v[200:203], a[100:103]
  buffer_load_dwordx4 v17, s[52:55], s79 offen offset:2048 lds
  v_add_f32 v249, v249, v245
  v_cvt_pk_bf16_f32 v186, v244, v245
  v_mfma_f32_16x16x32_bf16 a[104:107], a[240:243], v[208:211], a[104:107]
  v_exp_f32 v240, v62
  v_mfma_f32_16x16x32_bf16 a[104:107], a[244:247], v[212:215], a[104:107]
  ds_read_b128 a[212:215], v12 offset:20992
  v_add_f32 v249, v249, v240
  v_mfma_f32_16x16x32_bf16 a[108:111], a[244:247], v[220:223], a[108:111]
  v_exp_f32 v241, v63
  v_mfma_f32_16x16x32_bf16 a[108:111], a[240:243], v[216:219], a[108:111]
  s_waitcnt lgkmcnt(6)
  v_add_f32 v249, v249, v241
  v_cvt_pk_bf16_f32 v187, v240, v241
  v_mfma_f32_16x16x32_bf16 a[112:115], a[248:251], v[192:195], a[112:115]
  v_exp_f32 v242, v64
  v_mfma_f32_16x16x32_bf16 a[112:115], a[252:255], v[196:199], a[112:115]
  ds_read_b128 a[216:219], v12 offset:21504
  v_add_f32 v246, v246, v242
  v_mfma_f32_16x16x32_bf16 a[116:119], a[252:255], v[204:207], a[116:119]
  v_exp_f32 v243, v65
  v_mfma_f32_16x16x32_bf16 a[116:119], a[248:251], v[200:203], a[116:119]
  buffer_load_dwordx4 v18, s[52:55], s79 offen offset:3072 lds
  v_add_f32 v246, v246, v243
  v_cvt_pk_bf16_f32 v164, v242, v243
  v_mfma_f32_16x16x32_bf16 a[120:123], a[248:251], v[208:211], a[120:123]
  s_add_u32 s79, s79, 16384
  v_exp_f32 v244, v66
  v_mfma_f32_16x16x32_bf16 a[120:123], a[252:255], v[212:215], a[120:123]
  ds_read_b128 a[220:223], v12 offset:22016
  v_add_f32 v246, v246, v244
  v_mfma_f32_16x16x32_bf16 a[124:127], a[252:255], v[220:223], a[124:127]
  v_exp_f32 v245, v67
  v_mfma_f32_16x16x32_bf16 a[124:127], a[248:251], v[216:219], a[124:127]
  s_waitcnt lgkmcnt(4)
  v_add_f32 v246, v246, v245
  v_cvt_pk_bf16_f32 v165, v244, v245
LB_aloop:
  v_mfma_f32_16x16x32_bf16 v[96:99], a[192:195], a[128:131], v[224:227]
  v_exp_f32 v240, v68
  v_mfma_f32_16x16x32_bf16 v[96:99], a[196:199], a[132:135], v[96:99]
  ds_read_b128 a[224:227], v12 offset:24576
  v_add_f32 v247, v247, v240
  v_mfma_f32_16x16x32_bf16 v[96:99], a[200:203], a[136:139], v[96:99]
  s_add_u32 m0, s78, 32768
  v_exp_f32 v241, v69
  v_mfma_f32_16x16x32_bf16 v[96:99], a[204:207], a[140:143], v[96:99]
  buffer_load_dwordx4 v19, s[56:59], s80 offen offset:0 lds
  v_add_f32 v247, v247, v241
  v_cvt_pk_bf16_f32 v172, v240, v241
  v_mfma_f32_16x16x32_bf16 v[100:103], a[204:207], a[156:159], v[228:231]
  v_exp_f32 v242, v70
  v_mfma_f32_16x16x32_bf16 v[100:103], a[200:203], a[152:155], v[100:103]
  ds_read_b128 a[228:231], v12 offset:25088
  v_add_f32 v247, v247, v242
  v_mfma_f32_16x16x32_bf16 v[100:103], a[196:199], a[148:151], v[100:103]
  v_exp_f32 v243, v71
  v_mfma_f32_16x16x32_bf16 v[100:103], a[192:195], a[144:147], v[100:103]
  v_add_f32 v247, v247, v243
  v_cvt_pk_bf16_f32 v173, v242, v243
  v_mfma_f32_16x16x32_bf16 v[104:107], a[192:195], a[160:163], v[232:235]
  v_exp_f32 v244, v72
  v_mfma_f32_16x16x32_bf16 v[104:107], a[196:199], a[164:167], v[104:107]
  ds_read_b128 a[232:235], v12 offset:25600
  v_add_f32 v248, v248, v244
  v_mfma_f32_16x16x32_bf16 v[104:107], a[200:203], a[168:171], v[104:107]
  v_exp_f32 v245, v73
  v_mfma_f32_16x16x32_bf16 v[104:107], a[204:207], a[172:175], v[104:107]
  buffer_load_dwordx4 v20, s[56:59], s80 offen offset:1024 lds
  v_add_f32 v248, v248, v245
  v_cvt_pk_bf16_f32 v180, v244, v245
  v_mfma_f32_16x16x32_bf16 v[108:111], a[204:207], a[188:191], v[236:239]
  v_exp_f32 v240, v74
  v_mfma_f32_16x16x32_bf16 v[108:111], a[200:203], a[184:187], v[108:111]
  ds_read_b128 a[236:239], v12 offset:26112
  v_add_f32 v248, v248, v240
  v_mfma_f32_16x16x32_bf16 v[108:111], a[196:199], a[180:183], v[108:111]
  v_exp_f32 v241, v75
  v_mfma_f32_16x16x32_bf16 v[108:111], a[192:195], a[176:179], v[108:111]
  s_waitcnt lgkmcnt(4)
  v_add_f32 v248, v248, v241
  v_cvt_pk_bf16_f32 v181, v240, v241
  v_mfma_f32_16x16x32_bf16 v[112:115], a[208:211], a[128:131], v[224:227]
  v_exp_f32 v242, v76
  v_mfma_f32_16x16x32_bf16 v[112:115], a[212:215], a[132:135], v[112:115]
  ds_read_b128 a[240:243], v12 offset:28672
  v_add_f32 v249, v249, v242
  v_mfma_f32_16x16x32_bf16 v[112:115], a[216:219], a[136:139], v[112:115]
  v_exp_f32 v243, v77
  v_mfma_f32_16x16x32_bf16 v[112:115], a[220:223], a[140:143], v[112:115]
  buffer_load_dwordx4 v19, s[56:59], s80 offen offset:2048 lds
  v_add_f32 v249, v249, v243
  v_cvt_pk_bf16_f32 v188, v242, v243
  v_mfma_f32_16x16x32_bf16 v[116:119], a[220:223], a[156:159], v[228:231]
  v_exp_f32 v244, v78
  v_mfma_f32_16x16x32_bf16 v[116:119], a[216:219], a[152:155], v[116:119]
  ds_read_b128 a[244:247], v12 offset:29184
  v_add_f32 v249, v249, v244
  v_mfma_f32_16x16x32_bf16 v[116:119], a[212:215], a[148:151], v[116:119]
  v_exp_f32 v245, v79
  v_mfma_f32_16x16x32_bf16 v[116:119], a[208:211], a[144:147], v[116:119]
  v_add_f32 v249, v249, v245
  v_cvt_pk_bf16_f32 v189, v244, v245
  v_mfma_f32_16x16x32_bf16 v[120:123], a[208:211], a[160:163], v[232:235]
  v_exp_f32 v240, v80
  v_mfma_f32_16x16x32_bf16 v[120:123], a[212:215], a[164:167], v[120:123]
  ds_read_b128 a[248:251], v12 offset:29696
  v_add_f32 v246, v246, v240
  v_mfma_f32_16x16x32_bf16 v[120:123], a[216:219], a[168:171], v[120:123]
  v_exp_f32 v241, v81
  v_mfma_f32_16x16x32_bf16 v[120:123], a[220:223], a[172:175], v[120:123]
  buffer_load_dwordx4 v20, s[56:59], s80 offen offset:3072 lds
  v_add_f32 v246, v246, v241
  v_cvt_pk_bf16_f32 v166, v240, v241
  v_mfma_f32_16x16x32_bf16 v[124:127], a[220:223], a[188:191], v[236:239]
  s_add_u32 s80, s80, 16384
  v_exp_f32 v242, v82
  v_mfma_f32_16x16x32_bf16 v[124:127], a[216:219], a[184:187], v[124:127]
  ds_read_b128 a[252:255], v12 offset:30208
  v_add_f32 v246, v246, v242
  v_mfma_f32_16x16x32_bf16 v[124:127], a[212:215], a[180:183], v[124:127]
  s_add_u32 m0, s77, 131072
  v_exp_f32 v243, v83
  v_mfma_f32_16x16x32_bf16 v[124:127], a[208:211], a[176:179], v[124:127]
  s_waitcnt lgkmcnt(4)
  buffer_load_dwordx4 v21, s[36:39], s69 offen lds
  v_add_f32 v246, v246, v243
  v_cvt_pk_bf16_f32 v167, v242, v243
  v_mfma_f32_16x16x32_bf16 v[128:131], a[224:227], a[128:131], v[224:227]
  v_exp_f32 v244, v84
  v_mfma_f32_16x16x32_bf16 v[128:131], a[228:231], a[132:135], v[128:131]
  ds_read_b64_tr_b16 a[192:193], v13 offset:0
  buffer_load_dwordx4 v21, s[4:7], s3 offen lds
  v_add_f32 v247, v247, v244
  v_mfma_f32_16x16x32_bf16 v[128:131], a[232:235], a[136:139], v[128:131]
  v_exp_f32 v245, v85
  v_mfma_f32_16x16x32_bf16 v[128:131], a[236:239], a[140:143], v[128:131]
  ds_read_b64_tr_b16 a[194:195], v13 offset:4096
  v_add_f32 v247, v247, v245
  v_cvt_pk_bf16_f32 v174, v244, v245
  v_mfma_f32_16x16x32_bf16 v[132:135], a[236:239], a[156:159], v[228:231]
  v_exp_f32 v240, v86
  v_mfma_f32_16x16x32_bf16 v[132:135], a[232:235], a[152:155], v[132:135]
  ds_read_b64_tr_b16 a[196:197], v13 offset:8192
  v_add_f32 v247, v247, v240
  v_mfma_f32_16x16x32_bf16 v[132:135], a[228:231], a[148:151], v[132:135]
  v_exp_f32 v241, v87
  v_mfma_f32_16x16x32_bf16 v[132:135], a[224:227], a[144:147], v[132:135]
  ds_read_b64_tr_b16 a[198:199], v13 offset:12288
  v_add_f32 v247, v247, v241
  v_cvt_pk_bf16_f32 v175, v240, v241
  v_mfma_f32_16x16x32_bf16 v[136:139], a[224:227], a[160:163], v[232:235]
  v_exp_f32 v242, v88
  v_mfma_f32_16x16x32_bf16 v[136:139], a[228:231], a[164:167], v[136:139]
  ds_read_b64_tr_b16 a[200:201], v14 offset:0
  v_add_f32 v248, v248, v242
  v_mfma_f32_16x16x32_bf16 v[136:139], a[232:235], a[168:171], v[136:139]
  v_exp_f32 v243, v89
  v_mfma_f32_16x16x32_bf16 v[136:139], a[236:239], a[172:175], v[136:139]
  ds_read_b64_tr_b16 a[202:203], v14 offset:4096
  v_add_f32 v248, v248, v243
  v_cvt_pk_bf16_f32 v182, v242, v243
  v_mfma_f32_16x16x32_bf16 v[140:143], a[236:239], a[188:191], v[236:239]
  v_exp_f32 v244, v90
  v_mfma_f32_16x16x32_bf16 v[140:143], a[232:235], a[184:187], v[140:143]
  ds_read_b64_tr_b16 a[204:205], v14 offset:8192
  v_add_f32 v248, v248, v244
  v_mfma_f32_16x16x32_bf16 v[140:143], a[228:231], a[180:183], v[140:143]
  v_exp_f32 v245, v91
  v_mfma_f32_16x16x32_bf16 v[140:143], a[224:227], a[176:179], v[140:143]
  s_waitcnt lgkmcnt(7)
  ds_read_b64_tr_b16 a[206:207], v14 offset:12288
  v_add_f32 v248, v248, v245
  v_cvt_pk_bf16_f32 v183, v244, v245
  v_mfma_f32_16x16x32_bf16 v[144:147], a[240:243], a[128:131], v[224:227]
  v_exp_f32 v240, v92
  v_mfma_f32_16x16x32_bf16 v[144:147], a[244:247], a[132:135], v[144:147]
  ds_read_b64_tr_b16 a[208:209], v13 offset:512
  v_add_f32 v249, v249, v240
  v_mfma_f32_16x16x32_bf16 v[144:147], a[248:251], a[136:139], v[144:147]
  v_exp_f32 v241, v93
  v_mfma_f32_16x16x32_bf16 v[144:147], a[252:255], a[140:143], v[144:147]
  ds_read_b64_tr_b16 a[210:211], v13 offset:4608
  v_add_f32 v249, v249, v241
  v_cvt_pk_bf16_f32 v190, v240, v241
  v_mfma_f32_16x16x32_bf16 v[148:151], a[252:255], a[156:159], v[228:231]
  v_exp_f32 v242, v94
  v_mfma_f32_16x16x32_bf16 v[148:151], a[248:251], a[152:155], v[148:151]
  ds_read_b64_tr_b16 a[212:213], v13 offset:8704
  v_add_f32 v249, v249, v242
  v_mfma_f32_16x16x32_bf16 v[148:151], a[244:247], a[148:151], v[148:151]
  v_exp_f32 v243, v95
  v_mfma_f32_16x16x32_bf16 v[148:151], a[240:243], a[144:147], v[148:151]
  ds_read_b64_tr_b16 a[214:215], v13 offset:12800
  v_add_f32 v249, v249, v243
  v_cvt_pk_bf16_f32 v191, v242, v243
  v_mfma_f32_16x16x32_bf16 v[152:155], a[240:243], a[160:163], v[232:235]
  v_exp_f32 v240, v96
  v_mfma_f32_16x16x32_bf16 v[152:155], a[244:247], a[164:167], v[152:155]
  ds_read_b64_tr_b16 a[216:217], v14 offset:512
  v_max3_f32 v254, v246, v247, v248
  v_mfma_f32_16x16x32_bf16 v[152:155], a[248:251], a[168:171], v[152:155]
  v_exp_f32 v241, v97
  v_mfma_f32_16x16x32_bf16 v[152:155], a[252:255], a[172:175], v[152:155]
  ds_read_b64_tr_b16 a[218:219], v14 offset:4608
  v_add_f32 v250, v240, v241
  v_cvt_pk_bf16_f32 v192, v240, v241
  v_max_f32 v254, v254, v249
  v_mfma_f32_16x16x32_bf16 v[156:159], a[252:255], a[188:191], v[236:239]
  v_exp_f32 v242, v98
  v_mfma_f32_16x16x32_bf16 v[156:159], a[248:251], a[184:187], v[156:159]
  ds_read_b64_tr_b16 a[220:221], v14 offset:8704
  v_add_f32 v250, v250, v242
  v_cmp_nge_f32 vcc, s82, v254
  v_mfma_f32_16x16x32_bf16 v[156:159], a[244:247], a[180:183], v[156:159]
  v_exp_f32 v243, v99
  v_mfma_f32_16x16x32_bf16 v[156:159], a[240:243], a[176:179], v[156:159]
  s_waitcnt lgkmcnt(11)
  ds_read_b64_tr_b16 a[222:223], v14 offset:12800
  v_add_f32 v250, v250, v243
  v_cvt_pk_bf16_f32 v193, v242, v243
  s_cbranch_vccnz LB_stub1
LB_back1:
  v_mfma_f32_16x16x32_bf16 a[0:3], a[192:195], v[160:163], a[0:3]
  v_exp_f32 v244, v100
  v_mfma_f32_16x16x32_bf16 a[0:3], a[196:199], v[164:167], a[0:3]
  ds_read_b64_tr_b16 a[224:225], v13 offset:1024
  v_mfma_f32_16x16x32_bf16 a[4:7], a[196:199], v[172:175], a[4:7]
  v_exp_f32 v245, v101
  v_mfma_f32_16x16x32_bf16 a[4:7], a[192:195], v[168:171], a[4:7]
  ds_read_b64_tr_b16 a[226:227], v13 offset:5120
  v_add_f32 v251, v244, v245
  v_cvt_pk_bf16_f32 v200, v244, v245
  v_mfma_f32_16x16x32_bf16 a[8:11], a[192:195], v[176:179], a[8:11]
  v_exp_f32 v240, v102
  v_mfma_f32_16x16x32_bf16 a[8:11], a[196:199], v[180:183], a[8:11]
  ds_read_b64_tr_b16 a[228:229], v13 offset:9216
  v_add_f32 v23, v23, v246
  v_add_f32 v251, v251, v240
  v_mfma_f32_16x16x32_bf16 a[12:15], a[196:199], v[188:191], a[12:15]
  v_exp_f32 v241, v103
  v_mfma_f32_16x16x32_bf16 a[12:15], a[192:195], v[184:187], a[12:15]
  s_waitcnt lgkmcnt(11)
  ds_read_b64_tr_b16 a[230:231], v13 offset:13312
  v_add_f32 v251, v251, v241
  v_cvt_pk_bf16_f32 v201, v240, v241
  v_mfma_f32_16x16x32_bf16 a[16:19], a[200:203], v[160:163], a[16:19]
  v_exp_f32 v242, v104
  v_mfma_f32_16x16x32_bf16 a[16:19], a[204:207], v[164:167], a[16:19]
  ds_read_b64_tr_b16 a[232:233], v14 offset:1024
  v_add_f32 v24, v24, v247
  v_mfma_f32_16x16x32_bf16 a[20:23], a[204:207], v[172:175], a[20:23]
  v_exp_f32 v243, v105
  v_mfma_f32_16x16x32_bf16 a[20:23], a[200:203], v[168:171], a[20:23]
  ds_read_b64_tr_b16 a[234:235], v14 offset:5120
  v_add_f32 v252, v242, v243
  v_cvt_pk_bf16_f32 v208, v242, v243
  v_mfma_f32_16x16x32_bf16 a[24:27], a[200:203], v[176:179], a[24:27]
  v_exp_f32 v244, v106
  v_mfma_f32_16x16x32_bf16 a[24:27], a[204:207], v[180:183], a[24:27]
  ds_read_b64_tr_b16 a[236:237], v14 offset:9216
  v_add_f32 v25, v25, v248
  v_add_f32 v252, v252, v244
  v_mfma_f32_16x16x32_bf16 a[28:31], a[204:207], v[188:191], a[28:31]
  v_exp_f32 v245, v107
  v_mfma_f32_16x16x32_bf16 a[28:31], a[200:203], v[184:187], a[28:31]
  s_waitcnt lgkmcnt(11)
  ds_read_b64_tr_b16 a[238:239], v14 offset:13312
  v_add_f32 v252, v252, v245
  v_cvt_pk_bf16_f32 v209, v244, v245
  v_mfma_f32_16x16x32_bf16 a[32:35], a[208:211], v[160:163], a[32:35]
  v_exp_f32 v240, v108
  v_mfma_f32_16x16x32_bf16 a[32:35], a[212:215], v[164:167], a[32:35]
  ds_read_b64_tr_b16 a[240:241], v13 offset:1536
  v_add_f32 v26, v26, v249
  v_mfma_f32_16x16x32_bf16 a[36:39], a[212:215], v[172:175], a[36:39]
  v_exp_f32 v241, v109
  v_mfma_f32_16x16x32_bf16 a[36:39], a[208:211], v[168:171], a[36:39]
  ds_read_b64_tr_b16 a[242:243], v13 offset:5632
  v_add_f32 v253, v240, v241
  v_cvt_pk_bf16_f32 v216, v240, v241
  v_mfma_f32_16x16x32_bf16 a[40:43], a[208:211], v[176:179], a[40:43]
  v_exp_f32 v242, v110
  v_mfma_f32_16x16x32_bf16 a[40:43], a[212:215], v[180:183], a[40:43]
  ds_read_b64_tr_b16 a[244:245], v13 offset:9728
  v_add_f32 v253, v253, v242
  v_mfma_f32_16x16x32_bf16 a[44:47], a[212:215], v[188:191], a[44:47]
  v_exp_f32 v243, v111
  v_mfma_f32_16x16x32_bf16 a[44:47], a[208:211], v[184:187], a[44:47]
  s_waitcnt lgkmcnt(11)
  ds_read_b64_tr_b16 a[246:247], v13 offset:13824
  v_add_f32 v253, v253, v243
  v_cvt_pk_bf16_f32 v217, v242, v243
  v_mfma_f32_16x16x32_bf16 a[48:51], a[216:219], v[160:163], a[48:51]
  v_exp_f32 v244, v112
  v_mfma_f32_16x16x32_bf16 a[48:51], a[220:223], v[164:167], a[48:51]
  ds_read_b64_tr_b16 a[248:249], v14 offset:1536
  v_add_f32 v250, v250, v244
  v_mfma_f32_16x16x32_bf16 a[52:55], a[220:223], v[172:175], a[52:55]
  v_exp_f32 v245, v113
  v_mfma_f32_16x16x32_bf16 a[52:55], a[216:219], v[168:171], a[52:55]
  ds_read_b64_tr_b16 a[250:251], v14 offset:5632
  v_add_f32 v250, v250, v245
  v_cvt_pk_bf16_f32 v194, v244, v245
  v_mfma_f32_16x16x32_bf16 a[56:59], a[216:219], v[176:179], a[56:59]
  v_exp_f32 v240, v114
  v_mfma_f32_16x16x32_bf16 a[56:59], a[220:223], v[180:183], a[56:59]
  ds_read_b64_tr_b16 a[252:253], v14 offset:9728
  v_add_f32 v250, v250, v240
  v_mfma_f32_16x16x32_bf16 a[60:63], a[220:223], v[188:191], a[60:63]
  v_exp_f32 v241, v115
  v_mfma_f32_16x16x32_bf16 a[60:63], a[216:219], v[184:187], a[60:63]
  s_waitcnt lgkmcnt(11)
  ds_read_b64_tr_b16 a[254:255], v14 offset:13824
  v_add_f32 v250, v250, v241
  v_cvt_pk_bf16_f32 v195, v240, v241
  v_mfma_f32_16x16x32_bf16 a[64:67], a[224:227], v[160:163], a[64:67]
  s_waitcnt vmcnt(11)
  s_barrier
  v_exp_f32 v242, v116
  v_mfma_f32_16x16x32_bf16 a[64:67], a[228:231], v[164:167], a[64:67]
  ds_read_b128 a[192:195], v12 offset:32768
  v_add_f32 v251, v251, v242
  v_mfma_f32_16x16x32_bf16 a[68:71], a[228:231], v[172:175], a[68:71]
  s_add_u32 m0, s77, 0
  v_exp_f32 v243, v117
  v_mfma_f32_16x16x32_bf16 a[68:71], a[224:227], v[168:171], a[68:71]
  buffer_load_dwordx4 v15, s[52:55], s79 offen offset:0 lds
  v_add_f32 v251, v251, v243
  v_cvt_pk_bf16_f32 v202, v242, v243
  v_mfma_f32_16x16x32_bf16 a[72:75], a[224:227], v[176:179], a[72:75]
  v_exp_f32 v244, v118
  v_mfma_f32_16x16x32_bf16 a[72:75], a[228:231], v[180:183], a[72:75]
  ds_read_b128 a[196:199], v12 offset:33280
  v_add_f32 v251, v251, v244
  v_mfma_f32_16x16x32_bf16 a[76:79], a[228:231], v[188:191], a[76:79]
  v_exp_f32 v245, v119
  v_mfma_f32_16x16x32_bf16 a[76:79], a[224:227], v[184:187], a[76:79]
  s_waitcnt lgkmcnt(10)
  v_add_f32 v251, v251, v245
  v_cvt_pk_bf16_f32 v203, v244, v245
  v_mfma_f32_16x16x32_bf16 a[80:83], a[232:235], v[160:163], a[80:83]
  v_exp_f32 v240, v120
  v_mfma_f32_16x16x32_bf16 a[80:83], a[236:239], v[164:167], a[80:83]
  ds_read_b128 a[200:203], v12 offset:33792
  v_add_f32 v252, v252, v240
  v_mfma_f32_16x16x32_bf16 a[84:87], a[236:239], v[172:175], a[84:87]
  v_exp_f32 v241, v121
  v_mfma_f32_16x16x32_bf16 a[84:87], a[232:235], v[168:171], a[84:87]
  buffer_load_dwordx4 v16, s[52:55], s79 offen offset:1024 lds
  v_add_f32 v252, v252, v241
  v_cvt_pk_bf16_f32 v210, v240, v241
  v_mfma_f32_16x16x32_bf16 a[88:91], a[232:235], v[176:179], a[88:91]
  v_exp_f32 v242, v122
  v_mfma_f32_16x16x32_bf16 a[88:91], a[236:239], v[180:183], a[88:91]
  ds_read_b128 a[204:207], v12 offset:34304
  v_add_f32 v252, v252, v242
  v_mfma_f32_16x16x32_bf16 a[92:95], a[236:239], v[188:191], a[92:95]
  v_exp_f32 v243, v123
  v_mfma_f32_16x16x32_bf16 a[92:95], a[232:235], v[184:187], a[92:95]
  s_waitcnt lgkmcnt(8)
  v_add_f32 v252, v252, v243
  v_cvt_pk_bf16_f32 v211, v242, v243
  v_mfma_f32_16x16x32_bf16 a[96:99], a[240:243], v[160:163], a[96:99]
  v_exp_f32 v244, v124
  v_mfma_f32_16x16x32_bf16 a[96:99], a[244:247], v[164:167], a[96:99]
  ds_read_b128 a[208:211], v12 offset:36864
  v_add_f32 v253, v253, v244
  v_mfma_f32_16x16x32_bf16 a[100:103], a[244:247], v[172:175], a[100:103]
  v_exp_f32 v245, v125
  v_mfma_f32_16x16x32_bf16 a[100:103], a[240:243], v[168:171], a[100:103]
  buffer_load_dwordx4 v17, s[52:55], s79 offen offset:2048 lds
  v_add_f32 v253, v253, v245
  v_cvt_pk_bf16_f32 v218, v244, v245
  v_mfma_f32_16x16x32_bf16 a[104:107], a[240:243], v[176:179], a[104:107]
  v_exp_f32 v240, v126
  v_mfma_f32_16x16x32_bf16 a[104:107], a[244:247], v[180:183], a[104:107]
  ds_read_b128 a[212:215], v12 offset:37376
  v_add_f32 v253, v253, v240
  v_mfma_f32_16x16x32_bf16 a[108:111], a[244:247], v[188:191], a[108:111]
  v_exp_f32 v241, v127
  v_mfma_f32_16x16x32_bf16 a[108:111], a[240:243], v[184:187], a[108:111]
  s_waitcnt lgkmcnt(6)
  v_add_f32 v253, v253, v241
  v_cvt_pk_bf16_f32 v219, v240, v241
  v_mfma_f32_16x16x32_bf16 a[112:115], a[248:251], v[160:163], a[112:115]
  v_exp_f32 v242, v128
  v_mfma_f32_16x16x32_bf16 a[112:115], a[252:255], v[164:167], a[112:115]
  ds_read_b128 a[216:219], v12 offset:37888
  v_add_f32 v250, v250, v242
  v_mfma_f32_16x16x32_bf16 a[116:119], a[252:255], v[172:175], a[116:119]
  v_exp_f32 v243, v129
  v_mfma_f32_16x16x32_bf16 a[116:119], a[248:251], v[168:171], a[116:119]
  buffer_load_dwordx4 v18, s[52:55], s79 offen offset:3072 lds
  v_add_f32 v250, v250, v243
  v_cvt_pk_bf16_f32 v196, v242, v243
  v_mfma_f32_16x16x32_bf16 a[120:123], a[248:251], v[176:179], a[120:123]
  s_add_u32 s79, s79, 16384
  v_exp_f32 v244, v130
  v_mfma_f32_16x16x32_bf16 a[120:123], a[252:255], v[180:183], a[120:123]
  ds_read_b128 a[220:223], v12 offset:38400
  v_add_f32 v250, v250, v244
  v_mfma_f32_16x16x32_bf16 a[124:127], a[252:255], v[188:191], a[124:127]
  v_exp_f32 v245, v131
  v_mfma_f32_16x16x32_bf16 a[124:127], a[248:251], v[184:187], a[124:127]
  s_waitcnt lgkmcnt(4)
  v_add_f32 v250, v250, v245
  v_cvt_pk_bf16_f32 v197, v244, v245
  v_mfma_f32_16x16x32_bf16 v[32:35], a[192:195], a[128:131], v[224:227]
  v_exp_f32 v240, v132
  v_mfma_f32_16x16x32_bf16 v[32:35], a[196:199], a[132:135], v[32:35]
  ds_read_b128 a[224:227], v12 offset:40960
  v_add_f32 v251, v251, v240
  v_mfma_f32_16x16x32_bf16 v[32:35], a[200:203], a[136:139], v[32:35]
  s_add_u32 m0, s78, 49152
  v_exp_f32 v241, v133
  v_mfma_f32_16x16x32_bf16 v[32:35], a[204:207], a[140:143], v[32:35]
  buffer_load_dwordx4 v19, s[56:59], s80 offen offset:0 lds
  v_add_f32 v251, v251, v241
  v_cvt_pk_bf16_f32 v204, v240, v241
  v_mfma_f32_16x16x32_bf16 v[36:39], a[204:207], a[156:159], v[228:231]
  v_exp_f32 v242, v134
  v_mfma_f32_16x16x32_bf16 v[36:39], a[200:203], a[152:155], v[36:39]
  ds_read_b128 a[228:231], v12 offset:41472
  v_add_f32 v251, v251, v242
  v_mfma_f32_16x16x32_bf16 v[36:39], a[196:199], a[148:151], v[36:39]
  v_exp_f32 v243, v135
  v_mfma_f32_16x16x32_bf16 v[36:39], a[192:195], a[144:147], v[36:39]
  v_add_f32 v251, v251, v243
  v_cvt_pk_bf16_f32 v205, v242, v243
  v_mfma_f32_16x16x32_bf16 v[40:43], a[192:195], a[160:163], v[232:235]
  v_exp_f32 v244, v136
  v_mfma_f32_16x16x32_bf16 v[40:43], a[196:199], a[164:167], v[40:43]
  ds_read_b128 a[232:235], v12 offset:41984
  v_add_f32 v252, v252, v244
  v_mfma_f32_16x16x32_bf16 v[40:43], a[200:203], a[168:171], v[40:43]
  v_exp_f32 v245, v137
  v_mfma_f32_16x16x32_bf16 v[40:43], a[204:207], a[172:175], v[40:43]
  buffer_load_dwordx4 v20, s[56:59], s80 offen offset:1024 lds
  v_add_f32 v252, v252, v245
  v_cvt_pk_bf16_f32 v212, v244, v245
  v_mfma_f32_16x16x32_bf16 v[44:47], a[204:207], a[188:191], v[236:239]
  v_exp_f32 v240, v138
  v_mfma_f32_16x16x32_bf16 v[44:47], a[200:203], a[184:187], v[44:47]
  ds_read_b128 a[236:239], v12 offset:42496
  v_add_f32 v252, v252, v240
  v_mfma_f32_16x16x32_bf16 v[44:47], a[196:199], a[180:183], v[44:47]
  v_exp_f32 v241, v139
  v_mfma_f32_16x16x32_bf16 v[44:47], a[192:195], a[176:179], v[44:47]
  s_waitcnt lgkmcnt(4)
  v_add_f32 v252, v252, v241
  v_cvt_pk_bf16_f32 v213, v240, v241
  v_mfma_f32_16x16x32_bf16 v[48:51], a[208:211], a[128:131], v[224:227]
  v_exp_f32 v242, v140
  v_mfma_f32_16x16x32_bf16 v[48:51], a[212:215], a[132:135], v[48:51]
  ds_read_b128 a[240:243], v12 offset:45056
  v_add_f32 v253, v253, v242
  v_mfma_f32_16x16x32_bf16 v[48:51], a[216:219], a[136:139], v[48:51]
  v_exp_f32 v243, v141
  v_mfma_f32_16x16x32_bf16 v[48:51], a[220:223], a[140:143], v[48:51]
  buffer_load_dwordx4 v19, s[56:59], s80 offen offset:2048 lds
  v_add_f32 v253, v253, v243
  v_cvt_pk_bf16_f32 v220, v242, v243
  v_mfma_f32_16x16x32_bf16 v[52:55], a[220:223], a[156:159], v[228:231]
  v_exp_f32 v244, v142
  v_mfma_f32_16x16x32_bf16 v[52:55], a[216:219], a[152:155], v[52:55]
  ds_read_b128 a[244:247], v12 offset:45568
  v_add_f32 v253, v253, v244
  v_mfma_f32_16x16x32_bf16 v[52:55], a[212:215], a[148:151], v[52:55]
  v_exp_f32 v245, v143
  v_mfma_f32_16x16x32_bf16 v[52:55], a[208:211], a[144:147], v[52:55]
  v_add_f32 v253, v253, v245
  v_cvt_pk_bf16_f32 v221, v244, v245
  v_mfma_f32_16x16x32_bf16 v[56:59], a[208:211], a[160:163], v[232:235]
  v_exp_f32 v240, v144
  v_mfma_f32_16x16x32_bf16 v[56:59], a[212:215], a[164:167], v[56:59]
  ds_read_b128 a[248:251], v12 offset:46080
  v_add_f32 v250, v250, v240
  v_mfma_f32_16x16x32_bf16 v[56:59], a[216:219], a[168:171], v[56:59]
  v_exp_f32 v241, v145
  v_mfma_f32_16x16x32_bf16 v[56:59], a[220:223], a[172:175], v[56:59]
  buffer_load_dwordx4 v20, s[56:59], s80 offen offset:3072 lds
  v_add_f32 v250, v250, v241
  v_cvt_pk_bf16_f32 v198, v240, v241
  v_mfma_f32_16x16x32_bf16 v[60:63], a[220:223], a[188:191], v[236:239]
  s_add_u32 s80, s80, 16384
  v_exp_f32 v242, v146
  v_mfma_f32_16x16x32_bf16 v[60:63], a[216:219], a[184:187], v[60:63]
  ds_read_b128 a[252:255], v12 offset:46592
  v_add_f32 v250, v250, v242
  v_mfma_f32_16x16x32_bf16 v[60:63], a[212:215], a[180:183], v[60:63]
  s_add_u32 m0, s77, 131072
  v_exp_f32 v243, v147
  v_mfma_f32_16x16x32_bf16 v[60:63], a[208:211], a[176:179], v[60:63]
  s_waitcnt lgkmcnt(4)
  buffer_load_dwordx4 v21, s[40:43], s69 offen lds
  v_add_f32 v250, v250, v243
  v_cvt_pk_bf16_f32 v199, v242, v243
  v_mfma_f32_16x16x32_bf16 v[64:67], a[224:227], a[128:131], v[224:227]
  s_add_u32 s69, s69, 4096
  v_exp_f32 v244, v148
  v_mfma_f32_16x16x32_bf16 v[64:67], a[228:231], a[132:135], v[64:67]
  ds_read_b64_tr_b16 a[192:193], v13 offset:16384
  v_add_f32 v251, v251, v244
  v_mfma_f32_16x16x32_bf16 v[64:67], a[232:235], a[136:139], v[64:67]
  v_exp_f32 v245, v149
  v_mfma_f32_16x16x32_bf16 v[64:67], a[236:239], a[140:143], v[64:67]
  ds_read_b64_tr_b16 a[194:195], v13 offset:20480
  v_add_f32 v251, v251, v245
  v_cvt_pk_bf16_f32 v206, v244, v245
  v_mfma_f32_16x16x32_bf16 v[68:71], a[236:239], a[156:159], v[228:231]
  v_exp_f32 v240, v150
  v_mfma_f32_16x16x32_bf16 v[68:71], a[232:235], a[152:155], v[68:71]
  ds_read_b64_tr_b16 a[196:197], v13 offset:24576
  v_add_f32 v251, v251, v240
  v_mfma_f32_16x16x32_bf16 v[68:71], a[228:231], a[148:151], v[68:71]
  v_exp_f32 v241, v151
  v_mfma_f32_16x16x32_bf16 v[68:71], a[224:227], a[144:147], v[68:71]
  ds_read_b64_tr_b16 a[198:199], v13 offset:28672
  v_add_f32 v251, v251, v241
  v_cvt_pk_bf16_f32 v207, v240, v241
  v_mfma_f32_16x16x32_bf16 v[72:75], a[224:227], a[160:163], v[232:235]
  v_exp_f32 v242, v152
  v_mfma_f32_16x16x32_bf16 v[72:75], a[228:231], a[164:167], v[72:75]
  ds_read_b64_tr_b16 a[200:201], v14 offset:16384
  v_add_f32 v252, v252, v242
  v_mfma_f32_16x16x32_bf16 v[72:75], a[232:235], a[168:171], v[72:75]
  v_exp_f32 v243, v153
  v_mfma_f32_16x16x32_bf16 v[72:75], a[236:239], a[172:175], v[72:75]
  ds_read_b64_tr_b16 a[202:203], v14 offset:20480
  v_add_f32 v252, v252, v243
  v_cvt_pk_bf16_f32 v214, v242, v243
  v_mfma_f32_16x16x32_bf16 v[76:79], a[236:239], a[188:191], v[236:239]
  v_exp_f32 v244, v154
  v_mfma_f32_16x16x32_bf16 v[76:79], a[232:235], a[184:187], v[76:79]
  ds_read_b64_tr_b16 a[204:205], v14 offset:24576
  v_add_f32 v252, v252, v244
  v_mfma_f32_16x16x32_bf16 v[76:79], a[228:231], a[180:183], v[76:79]
  v_exp_f32 v245, v155
  v_mfma_f32_16x16x32_bf16 v[76:79], a[224:227], a[176:179], v[76:79]
  s_waitcnt lgkmcnt(7)
  ds_read_b64_tr_b16 a[206:207], v14 offset:28672
  v_add_f32 v252, v252, v245
  v_cvt_pk_bf16_f32 v215, v244, v245
  v_mfma_f32_16x16x32_bf16 v[80:83], a[240:243], a[128:131], v[224:227]
  v_exp_f32 v240, v156
  v_mfma_f32_16x16x32_bf16 v[80:83], a[244:247], a[132:135], v[80:83]
  ds_read_b64_tr_b16 a[208:209], v13 offset:16896
  v_add_f32 v253, v253, v240
  v_mfma_f32_16x16x32_bf16 v[80:83], a[248:251], a[136:139], v[80:83]
  v_exp_f32 v241, v157
  v_mfma_f32_16x16x32_bf16 v[80:83], a[252:255], a[140:143], v[80:83]
  ds_read_b64_tr_b16 a[210:211], v13 offset:20992
  v_add_f32 v253, v253, v241
  v_cvt_pk_bf16_f32 v222, v240, v241
  v_mfma_f32_16x16x32_bf16 v[84:87], a[252:255], a[156:159], v[228:231]
  v_exp_f32 v242, v158
  v_mfma_f32_16x16x32_bf16 v[84:87], a[248:251], a[152:155], v[84:87]
  ds_read_b64_tr_b16 a[212:213], v13 offset:25088
  v_add_f32 v253, v253, v242
  v_mfma_f32_16x16x32_bf16 v[84:87], a[244:247], a[148:151], v[84:87]
  v_exp_f32 v243, v159
  v_mfma_f32_16x16x32_bf16 v[84:87], a[240:243], a[144:147], v[84:87]
  ds_read_b64_tr_b16 a[214:215], v13 offset:29184
  v_add_f32 v253, v253, v243
  v_cvt_pk_bf16_f32 v223, v242, v243
  v_mfma_f32_16x16x32_bf16 v[88:91], a[240:243], a[160:163], v[232:235]
  v_exp_f32 v240, v32
  v_mfma_f32_16x16x32_bf16 v[88:91], a[244:247], a[164:167], v[88:91]
  ds_read_b64_tr_b16 a[216:217], v14 offset:16896
  v_max3_f32 v254, v250, v251, v252
  v_mfma_f32_16x16x32_bf16 v[88:91], a[248:251], a[168:171], v[88:91]
  v_exp_f32 v241, v33
  v_mfma_f32_16x16x32_bf16 v[88:91], a[252:255], a[172:175], v[88:91]
  ds_read_b64_tr_b16 a[218:219], v14 offset:20992
  v_add_f32 v246, v240, v241
  v_cvt_pk_bf16_f32 v160, v240, v241
  v_max_f32 v254, v254, v253
  v_mfma_f32_16x16x32_bf16 v[92:95], a[252:255], a[188:191], v[236:239]
  v_exp_f32 v242, v34
  v_mfma_f32_16x16x32_bf16 v[92:95], a[248:251], a[184:187], v[92:95]
  ds_read_b64_tr_b16 a[220:221], v14 offset:25088
  v_add_f32 v246, v246, v242
  v_cmp_nge_f32 vcc, s82, v254
  v_mfma_f32_16x16x32_bf16 v[92:95], a[244:247], a[180:183], v[92:95]
  v_exp_f32 v243, v35
  v_mfma_f32_16x16x32_bf16 v[92:95], a[240:243], a[176:179], v[92:95]
  s_waitcnt lgkmcnt(11)
  ds_read_b64_tr_b16 a[222:223], v14 offset:29184
  v_add_f32 v246, v246, v243
  v_cvt_pk_bf16_f32 v161, v242, v243
  s_cbranch_vccnz LB_stub2
LB_back2:
  v_mfma_f32_16x16x32_bf16 a[0:3], a[192:195], v[192:195], a[0:3]
  v_exp_f32 v244, v36
  v_mfma_f32_16x16x32_bf16 a[0:3], a[196:199], v[196:199], a[0:3]
  ds_read_b64_tr_b16 a[224:225], v13 offset:17408
  v_mfma_f32_16x16x32_bf16 a[4:7], a[196:199], v[204:207], a[4:7]
  v_exp_f32 v245, v37
  v_mfma_f32_16x16x32_bf16 a[4:7], a[192:195], v[200:203], a[4:7]
  ds_read_b64_tr_b16 a[226:227], v13 offset:21504
  v_add_f32 v247, v244, v245
  v_cvt_pk_bf16_f32 v168, v244, v245
  v_mfma_f32_16x16x32_bf16 a[8:11], a[192:195], v[208:211], a[8:11]
  v_exp_f32 v240, v38
  v_mfma_f32_16x16x32_bf16 a[8:11], a[196:199], v[212:215], a[8:11]
  ds_read_b64_tr_b16 a[228:229], v13 offset:25600
  v_add_f32 v23, v23, v250
  v_add_f32 v247, v247, v240
  v_mfma_f32_16x16x32_bf16 a[12:15], a[196:199], v[220:223], a[12:15]
  v_exp_f32 v241, v39
  v_mfma_f32_16x16x32_bf16 a[12:15], a[192:195], v[216:219], a[12:15]
  s_waitcnt lgkmcnt(11)
  ds_read_b64_tr_b16 a[230:231], v13 offset:29696
  v_add_f32 v247, v247, v241
  v_cvt_pk_bf16_f32 v169, v240, v241
  v_mfma_f32_16x16x32_bf16 a[16:19], a[200:203], v[192:195], a[16:19]
  v_exp_f32 v242, v40
  v_mfma_f32_16x16x32_bf16 a[16:19], a[204:207], v[196:199], a[16:19]
  ds_read_b64_tr_b16 a[232:233], v14 offset:17408
  v_add_f32 v24, v24, v251
  v_mfma_f32_16x16x32_bf16 a[20:23], a[204:207], v[204:207], a[20:23]
  v_exp_f32 v243, v41
  v_mfma_f32_16x16x32_bf16 a[20:23], a[200:203], v[200:203], a[20:23]
  ds_read_b64_tr_b16 a[234:235], v14 offset:21504
  v_add_f32 v248, v242, v243
  v_cvt_pk_bf16_f32 v176, v242, v243
  v_mfma_f32_16x16x32_bf16 a[24:27], a[200:203], v[208:211], a[24:27]
  v_exp_f32 v244, v42
  v_mfma_f32_16x16x32_bf16 a[24:27], a[204:207], v[212:215], a[24:27]
  ds_read_b64_tr_b16 a[236:237], v14 offset:25600
  v_add_f32 v25, v25, v252
  v_add_f32 v248, v248, v244
  v_mfma_f32_16x16x32_bf16 a[28:31], a[204:207], v[220:223], a[28:31]
  v_exp_f32 v245, v43
  v_mfma_f32_16x16x32_bf16 a[28:31], a[200:203], v[216:219], a[28:31]
  s_waitcnt lgkmcnt(11)
  ds_read_b64_tr_b16 a[238:239], v14 offset:29696
  v_add_f32 v248, v248, v245
  v_cvt_pk_bf16_f32 v177, v244, v245
  v_mfma_f32_16x16x32_bf16 a[32:35], a[208:211], v[192:195], a[32:35]
  v_exp_f32 v240, v44
  v_mfma_f32_16x16x32_bf16 a[32:35], a[212:215], v[196:199], a[32:35]
  ds_read_b64_tr_b16 a[240:241], v13 offset:17920
  v_add_f32 v26, v26, v253
  v_mfma_f32_16x16x32_bf16 a[36:39], a[212:215], v[204:207], a[36:39]
  v_exp_f32 v241, v45
  v_mfma_f32_16x16x32_bf16 a[36:39], a[208:211], v[200:203], a[36:39]
  ds_read_b64_tr_b16 a[242:243], v13 offset:22016
  v_add_f32 v249, v240, v241
  v_cvt_pk_bf16_f32 v184, v240, v241
  v_mfma_f32_16x16x32_bf16 a[40:43], a[208:211], v[208:211], a[40:43]
  v_exp_f32 v242, v46
  v_mfma_f32_16x16x32_bf16 a[40:43], a[212:215], v[212:215], a[40:43]
  ds_read_b64_tr_b16 a[244:245], v13 offset:26112
  v_add_f32 v249, v249, v242
  v_mfma_f32_16x16x32_bf16 a[44:47], a[212:215], v[220:223], a[44:47]
  v_exp_f32 v243, v47
  v_mfma_f32_16x16x32_bf16 a[44:47], a[208:211], v[216:219], a[44:47]
  s_waitcnt lgkmcnt(11)
  ds_read_b64_tr_b16 a[246:247], v13 offset:30208
  v_add_f32 v249, v249, v243
  v_cvt_pk_bf16_f32 v185, v242, v243
  v_mfma_f32_16x16x32_bf16 a[48:51], a[216:219], v[192:195], a[48:51]
  v_exp_f32 v244, v48
  v_mfma_f32_16x16x32_bf16 a[48:51], a[220:223], v[196:199], a[48:51]
  ds_read_b64_tr_b16 a[248:249], v14 offset:17920
  v_add_f32 v246, v246, v244
  v_mfma_f32_16x16x32_bf16 a[52:55], a[220:223], v[204:207], a[52:55]
  v_exp_f32 v245, v49
  v_mfma_f32_16x16x32_bf16 a[52:55], a[216:219], v[200:203], a[52:55]
  ds_read_b64_tr_b16 a[250:251], v14 offset:22016
  v_add_f32 v246, v246, v245
  v_cvt_pk_bf16_f32 v162, v244, v245
  v_mfma_f32_16x16x32_bf16 a[56:59], a[216:219], v[208:211], a[56:59]
  v_exp_f32 v240, v50
  v_mfma_f32_16x16x32_bf16 a[56:59], a[220:223], v[212:215], a[56:59]
  ds_read_b64_tr_b16 a[252:253], v14 offset:26112
  v_add_f32 v246, v246, v240
  v_mfma_f32_16x16x32_bf16 a[60:63], a[220:223], v[220:223], a[60:63]
  v_exp_f32 v241, v51
  v_mfma_f32_16x16x32_bf16 a[60:63], a[216:219], v[216:219], a[60:63]
  s_waitcnt lgkmcnt(11)
  ds_read_b64_tr_b16 a[254:255], v14 offset:30208
  v_add_f32 v246, v246, v241
  v_cvt_pk_bf16_f32 v163, v240, v241
  v_mfma_f32_16x16x32_bf16 a[64:67], a[224:227], v[192:195], a[64:67]
  s_waitcnt vmcnt(11)
  s_barrier
  v_exp_f32 v242, v52
  v_mfma_f32_16x16x32_bf16 a[64:67], a[228:231], v[196:199], a[64:67]
  ds_read_b128 a[192:195], v12 offset:49152
  v_add_f32 v247, v247, v242
  v_mfma_f32_16x16x32_bf16 a[68:71], a[228:231], v[204:207], a[68:71]
  s_add_u32 m0, s77, 16384
  v_exp_f32 v243, v53
  v_mfma_f32_16x16x32_bf16 a[68:71], a[224:227], v[200:203], a[68:71]
  buffer_load_dwordx4 v15, s[52:55], s79 offen offset:0 lds
  v_add_f32 v247, v247, v243
  v_cvt_pk_bf16_f32 v170, v242, v243
  v_mfma_f32_16x16x32_bf16 a[72:75], a[224:227], v[208:211], a[72:75]
  v_exp_f32 v244, v54
  v_mfma_f32_16x16x32_bf16 a[72:75], a[228:231], v[212:215], a[72:75]
  ds_read_b128 a[196:199], v12 offset:49664
  v_add_f32 v247, v247, v244
  v_mfma_f32_16x16x32_bf16 a[76:79], a[228:231], v[220:223], a[76:79]
  v_exp_f32 v245, v55
  v_mfma_f32_16x16x32_bf16 a[76:79], a[224:227], v[216:219], a[76:79]
  s_waitcnt lgkmcnt(10)
  v_add_f32 v247, v247, v245
  v_cvt_pk_bf16_f32 v171, v244, v245
  v_mfma_f32_16x16x32_bf16 a[80:83], a[232:235], v[192:195], a[80:83]
  v_exp_f32 v240, v56
  v_mfma_f32_16x16x32_bf16 a[80:83], a[236:239], v[196:199], a[80:83]
  ds_read_b128 a[200:203], v12 offset:50176
  v_add_f32 v248, v248, v240
  v_mfma_f32_16x16x32_bf16 a[84:87], a[236:239], v[204:207], a[84:87]
  v_exp_f32 v241, v57
  v_mfma_f32_16x16x32_bf16 a[84:87], a[232:235], v[200:203], a[84:87]
  buffer_load_dwordx4 v16, s[52:55], s79 offen offset:1024 lds
  v_add_f32 v248, v248, v241
  v_cvt_pk_bf16_f32 v178, v240, v241
  v_mfma_f32_16x16x32_bf16 a[88:91], a[232:235], v[208:211], a[88:91]
  v_exp_f32 v242, v58
  v_mfma_f32_16x16x32_bf16 a[88:91], a[236:239], v[212:215], a[88:91]
  ds_read_b128 a[204:207], v12 offset:50688
  v_add_f32 v248, v248, v242
  v_mfma_f32_16x16x32_bf16 a[92:95], a[236:239], v[220:223], a[92:95]
  v_exp_f32 v243, v59
  v_mfma_f32_16x16x32_bf16 a[92:95], a[232:235], v[216:219], a[92:95]
  s_waitcnt lgkmcnt(8)
  v_add_f32 v248, v248, v243
  v_cvt_pk_bf16_f32 v179, v242, v243
  v_mfma_f32_16x16x32_bf16 a[96:99], a[240:243], v[192:195], a[96:99]
  v_exp_f32 v244, v60
  v_mfma_f32_16x16x32_bf16 a[96:99], a[244:247], v[196:199], a[96:99]
  ds_read_b128 a[208:211], v12 offset:53248
  v_add_f32 v249, v249, v244
  v_mfma_f32_16x16x32_bf16 a[100:103], a[244:247], v[204:207], a[100:103]
  v_exp_f32 v245, v61
  v_mfma_f32_16x16x32_bf16 a[100:103], a[240:243], v[200:203], a[100:103]
  buffer_load_dwordx4 v17, s[52:55], s79 offen offset:2048 lds
  v_add_f32 v249, v249, v245
  v_cvt_pk_bf16_f32 v186, v244, v245
  v_mfma_f32_16x16x32_bf16 a[104:107], a[240:243], v[208:211], a[104:107]
  v_exp_f32 v240, v62
  v_mfma_f32_16x16x32_bf16 a[104:107], a[244:247], v[212:215], a[104:107]
  ds_read_b128 a[212:215], v12 offset:53760
  v_add_f32 v249, v249, v240
  v_mfma_f32_16x16x32_bf16 a[108:111], a[244:247], v[220:223], a[108:111]
  v_exp_f32 v241, v63
  v_mfma_f32_16x16x32_bf16 a[108:111], a[240:243], v[216:219], a[108:111]
  s_waitcnt lgkmcnt(6)
  v_add_f32 v249, v249, v241
  v_cvt_pk_bf16_f32 v187, v240, v241
  v_mfma_f32_16x16x32_bf16 a[112:115], a[248:251], v[192:195], a[112:115]
  v_exp_f32 v242, v64
  v_mfma_f32_16x16x32_bf16 a[112:115], a[252:255], v[196:199], a[112:115]
  ds_read_b128 a[216:219], v12 offset:54272
  v_add_f32 v246, v246, v242
  v_mfma_f32_16x16x32_bf16 a[116:119], a[252:255], v[204:207], a[116:119]
  v_exp_f32 v243, v65
  v_mfma_f32_16x16x32_bf16 a[116:119], a[248:251], v[200:203], a[116:119]
  buffer_load_dwordx4 v18, s[52:55], s79 offen offset:3072 lds
  v_add_f32 v246, v246, v243
  v_cvt_pk_bf16_f32 v164, v242, v243
  v_mfma_f32_16x16x32_bf16 a[120:123], a[248:251], v[208:211], a[120:123]
  s_add_u32 s79, s79, 16384
  v_exp_f32 v244, v66
  v_mfma_f32_16x16x32_bf16 a[120:123], a[252:255], v[212:215], a[120:123]
  ds_read_b128 a[220:223], v12 offset:54784
  v_add_f32 v246, v246, v244
  v_mfma_f32_16x16x32_bf16 a[124:127], a[252:255], v[220:223], a[124:127]
  v_exp_f32 v245, v67
  v_mfma_f32_16x16x32_bf16 a[124:127], a[248:251], v[216:219], a[124:127]
  s_waitcnt lgkmcnt(4)
  v_add_f32 v246, v246, v245
  v_cvt_pk_bf16_f32 v165, v244, v245
  v_mfma_f32_16x16x32_bf16 v[96:99], a[192:195], a[128:131], v[224:227]
  v_exp_f32 v240, v68
  v_mfma_f32_16x16x32_bf16 v[96:99], a[196:199], a[132:135], v[96:99]
  ds_read_b128 a[224:227], v12 offset:57344
  v_add_f32 v247, v247, v240
  v_mfma_f32_16x16x32_bf16 v[96:99], a[200:203], a[136:139], v[96:99]
  s_add_u32 m0, s78, 0
  v_exp_f32 v241, v69
  v_mfma_f32_16x16x32_bf16 v[96:99], a[204:207], a[140:143], v[96:99]
  buffer_load_dwordx4 v19, s[56:59], s80 offen offset:0 lds
  v_add_f32 v247, v247, v241
  v_cvt_pk_bf16_f32 v172, v240, v241
  v_mfma_f32_16x16x32_bf16 v[100:103], a[204:207], a[156:159], v[228:231]
  v_exp_f32 v242, v70
  v_mfma_f32_16x16x32_bf16 v[100:103], a[200:203], a[152:155], v[100:103]
  ds_read_b128 a[228:231], v12 offset:57856
  v_add_f32 v247, v247, v242
  v_mfma_f32_16x16x32_bf16 v[100:103], a[196:199], a[148:151], v[100:103]
  v_exp_f32 v243, v71
  v_mfma_f32_16x16x32_bf16 v[100:103], a[192:195], a[144:147], v[100:103]
  v_add_f32 v247, v247, v243
  v_cvt_pk_bf16_f32 v173, v242, v243
  v_mfma_f32_16x16x32_bf16 v[104:107], a[192:195], a[160:163], v[232:235]
  v_exp_f32 v244, v72
  v_mfma_f32_16x16x32_bf16 v[104:107], a[196:199], a[164:167], v[104:107]
  ds_read_b128 a[232:235], v12 offset:58368
  v_add_f32 v248, v248, v244
  v_mfma_f32_16x16x32_bf16 v[104:107], a[200:203], a[168:171], v[104:107]
  v_exp_f32 v245, v73
  v_mfma_f32_16x16x32_bf16 v[104:107], a[204:207], a[172:175], v[104:107]
  buffer_load_dwordx4 v20, s[56:59], s80 offen offset:1024 lds
  v_add_f32 v248, v248, v245
  v_cvt_pk_bf16_f32 v180, v244, v245
  v_mfma_f32_16x16x32_bf16 v[108:111], a[204:207], a[188:191], v[236:239]
  v_exp_f32 v240, v74
  v_mfma_f32_16x16x32_bf16 v[108:111], a[200:203], a[184:187], v[108:111]
  ds_read_b128 a[236:239], v12 offset:58880
  v_add_f32 v248, v248, v240
  v_mfma_f32_16x16x32_bf16 v[108:111], a[196:199], a[180:183], v[108:111]
  v_exp_f32 v241, v75
  v_mfma_f32_16x16x32_bf16 v[108:111], a[192:195], a[176:179], v[108:111]
  s_waitcnt lgkmcnt(4)
  v_add_f32 v248, v248, v241
  v_cvt_pk_bf16_f32 v181, v240, v241
  v_mfma_f32_16x16x32_bf16 v[112:115], a[208:211], a[128:131], v[224:227]
  v_exp_f32 v242, v76
  v_mfma_f32_16x16x32_bf16 v[112:115], a[212:215], a[132:135], v[112:115]
  ds_read_b128 a[240:243], v12 offset:61440
  v_add_f32 v249, v249, v242
  v_mfma_f32_16x16x32_bf16 v[112:115], a[216:219], a[136:139], v[112:115]
  v_exp_f32 v243, v77
  v_mfma_f32_16x16x32_bf16 v[112:115], a[220:223], a[140:143], v[112:115]
  buffer_load_dwordx4 v19, s[56:59], s80 offen offset:2048 lds
  v_add_f32 v249, v249, v243
  v_cvt_pk_bf16_f32 v188, v242, v243
  v_mfma_f32_16x16x32_bf16 v[116:119], a[220:223], a[156:159], v[228:231]
  v_exp_f32 v244, v78
  v_mfma_f32_16x16x32_bf16 v[116:119], a[216:219], a[152:155], v[116:119]
  ds_read_b128 a[244:247], v12 offset:61952
  v_add_f32 v249, v249, v244
  v_mfma_f32_16x16x32_bf16 v[116:119], a[212:215], a[148:151], v[116:119]
  v_exp_f32 v245, v79
  v_mfma_f32_16x16x32_bf16 v[116:119], a[208:211], a[144:147], v[116:119]
  v_add_f32 v249, v249, v245
  v_cvt_pk_bf16_f32 v189, v244, v245
  v_mfma_f32_16x16x32_bf16 v[120:123], a[208:211], a[160:163], v[232:235]
  v_exp_f32 v240, v80
  v_mfma_f32_16x16x32_bf16 v[120:123], a[212:215], a[164:167], v[120:123]
  ds_read_b128 a[248:251], v12 offset:62464
  v_add_f32 v246, v246, v240
  v_mfma_f32_16x16x32_bf16 v[120:123], a[216:219], a[168:171], v[120:123]
  v_exp_f32 v241, v81
  v_mfma_f32_16x16x32_bf16 v[120:123], a[220:223], a[172:175], v[120:123]
  buffer_load_dwordx4 v20, s[56:59], s80 offen offset:3072 lds
  v_add_f32 v246, v246, v241
  v_cvt_pk_bf16_f32 v166, v240, v241
  v_mfma_f32_16x16x32_bf16 v[124:127], a[220:223], a[188:191], v[236:239]
  s_add_u32 s80, s80, 16384
  v_exp_f32 v242, v82
  v_mfma_f32_16x16x32_bf16 v[124:127], a[216:219], a[184:187], v[124:127]
  ds_read_b128 a[252:255], v12 offset:62976
  v_add_f32 v246, v246, v242
  v_mfma_f32_16x16x32_bf16 v[124:127], a[212:215], a[180:183], v[124:127]
  s_add_u32 m0, s77, 131072
  v_exp_f32 v243, v83
  v_mfma_f32_16x16x32_bf16 v[124:127], a[208:211], a[176:179], v[124:127]
  s_waitcnt lgkmcnt(4)
  buffer_load_dwordx4 v21, s[36:39], s69 offen lds
  v_add_f32 v246, v246, v243
  v_cvt_pk_bf16_f32 v167, v242, v243
  v_mfma_f32_16x16x32_bf16 v[128:131], a[224:227], a[128:131], v[224:227]
  v_exp_f32 v244, v84
  v_mfma_f32_16x16x32_bf16 v[128:131], a[228:231], a[132:135], v[128:131]
  ds_read_b64_tr_b16 a[192:193], v13 offset:32768
  v_add_f32 v247, v247, v244
  v_mfma_f32_16x16x32_bf16 v[128:131], a[232:235], a[136:139], v[128:131]
  v_exp_f32 v245, v85
  v_mfma_f32_16x16x32_bf16 v[128:131], a[236:239], a[140:143], v[128:131]
  ds_read_b64_tr_b16 a[194:195], v13 offset:36864
  v_add_f32 v247, v247, v245
  v_cvt_pk_bf16_f32 v174, v244, v245
  v_mfma_f32_16x16x32_bf16 v[132:135], a[236:239], a[156:159], v[228:231]
  v_exp_f32 v240, v86
  v_mfma_f32_16x16x32_bf16 v[132:135], a[232:235], a[152:155], v[132:135]
  ds_read_b64_tr_b16 a[196:197], v13 offset:40960
  v_add_f32 v247, v247, v240
  v_mfma_f32_16x16x32_bf16 v[132:135], a[228:231], a[148:151], v[132:135]
  v_exp_f32 v241, v87
  v_mfma_f32_16x16x32_bf16 v[132:135], a[224:227], a[144:147], v[132:135]
  ds_read_b64_tr_b16 a[198:199], v13 offset:45056
  v_add_f32 v247, v247, v241
  v_cvt_pk_bf16_f32 v175, v240, v241
  v_mfma_f32_16x16x32_bf16 v[136:139], a[224:227], a[160:163], v[232:235]
  v_exp_f32 v242, v88
  v_mfma_f32_16x16x32_bf16 v[136:139], a[228:231], a[164:167], v[136:139]
  ds_read_b64_tr_b16 a[200:201], v14 offset:32768
  v_add_f32 v248, v248, v242
  v_mfma_f32_16x16x32_bf16 v[136:139], a[232:235], a[168:171], v[136:139]
  v_exp_f32 v243, v89
  v_mfma_f32_16x16x32_bf16 v[136:139], a[236:239], a[172:175], v[136:139]
  ds_read_b64_tr_b16 a[202:203], v14 offset:36864
  v_add_f32 v248, v248, v243
  v_cvt_pk_bf16_f32 v182, v242, v243
  v_mfma_f32_16x16x32_bf16 v[140:143], a[236:239], a[188:191], v[236:239]
  v_exp_f32 v244, v90
  v_mfma_f32_16x16x32_bf16 v[140:143], a[232:235], a[184:187], v[140:143]
  ds_read_b64_tr_b16 a[204:205], v14 offset:40960
  v_add_f32 v248, v248, v244
  v_mfma_f32_16x16x32_bf16 v[140:143], a[228:231], a[180:183], v[140:143]
  v_exp_f32 v245, v91
  v_mfma_f32_16x16x32_bf16 v[140:143], a[224:227], a[176:179], v[140:143]
  s_waitcnt lgkmcnt(7)
  ds_read_b64_tr_b16 a[206:207], v14 offset:45056
  v_add_f32 v248, v248, v245
  v_cvt_pk_bf16_f32 v183, v244, v245
  v_mfma_f32_16x16x32_bf16 v[144:147], a[240:243], a[128:131], v[224:227]
  v_exp_f32 v240, v92
  v_mfma_f32_16x16x32_bf16 v[144:147], a[244:247], a[132:135], v[144:147]
  ds_read_b64_tr_b16 a[208:209], v13 offset:33280
  v_add_f32 v249, v249, v240
  v_mfma_f32_16x16x32_bf16 v[144:147], a[248:251], a[136:139], v[144:147]
  v_exp_f32 v241, v93
  v_mfma_f32_16x16x32_bf16 v[144:147], a[252:255], a[140:143], v[144:147]
  ds_read_b64_tr_b16 a[210:211], v13 offset:37376
  v_add_f32 v249, v249, v241
  v_cvt_pk_bf16_f32 v190, v240, v241
  v_mfma_f32_16x16x32_bf16 v[148:151], a[252:255], a[156:159], v[228:231]
  v_exp_f32 v242, v94
  v_mfma_f32_16x16x32_bf16 v[148:151], a[248:251], a[152:155], v[148:151]
  ds_read_b64_tr_b16 a[212:213], v13 offset:41472
  v_add_f32 v249, v249, v242
  v_mfma_f32_16x16x32_bf16 v[148:151], a[244:247], a[148:151], v[148:151]
  v_exp_f32 v243, v95
  v_mfma_f32_16x16x32_bf16 v[148:151], a[240:243], a[144:147], v[148:151]
  ds_read_b64_tr_b16 a[214:215], v13 offset:45568
  v_add_f32 v249, v249, v243
  v_cvt_pk_bf16_f32 v191, v242, v243
  v_mfma_f32_16x16x32_bf16 v[152:155], a[240:243], a[160:163], v[232:235]
  v_exp_f32 v240, v96
  v_mfma_f32_16x16x32_bf16 v[152:155], a[244:247], a[164:167], v[152:155]
  ds_read_b64_tr_b16 a[216:217], v14 offset:33280
  v_max3_f32 v254, v246, v247, v248
  v_mfma_f32_16x16x32_bf16 v[152:155], a[248:251], a[168:171], v[152:155]
  v_exp_f32 v241, v97
  v_mfma_f32_16x16x32_bf16 v[152:155], a[252:255], a[172:175], v[152:155]
  ds_read_b64_tr_b16 a[218:219], v14 offset:37376
  v_add_f32 v250, v240, v241
  v_cvt_pk_bf16_f32 v192, v240, v241
  v_max_f32 v254, v254, v249
  v_mfma_f32_16x16x32_bf16 v[156:159], a[252:255], a[188:191], v[236:239]
  v_exp_f32 v242, v98
  v_mfma_f32_16x16x32_bf16 v[156:159], a[248:251], a[184:187], v[156:159]
  ds_read_b64_tr_b16 a[220:221], v14 offset:41472
  v_add_f32 v250, v250, v242
  v_cmp_nge_f32 vcc, s82, v254
  v_mfma_f32_16x16x32_bf16 v[156:159], a[244:247], a[180:183], v[156:159]
  v_exp_f32 v243, v99
  v_mfma_f32_16x16x32_bf16 v[156:159], a[240:243], a[176:179], v[156:159]
  s_waitcnt lgkmcnt(11)
  ds_read_b64_tr_b16 a[222:223], v14 offset:45568
  v_add_f32 v250, v250, v243
  v_cvt_pk_bf16_f32 v193, v242, v243
  s_cbranch_vccnz LB_stub3
LB_back3:
  v_mfma_f32_16x16x32_bf16 a[0:3], a[192:195], v[160:163], a[0:3]
  v_exp_f32 v244, v100
  v_mfma_f32_16x16x32_bf16 a[0:3], a[196:199], v[164:167], a[0:3]
  ds_read_b64_tr_b16 a[224:225], v13 offset:33792
  v_mfma_f32_16x16x32_bf16 a[4:7], a[196:199], v[172:175], a[4:7]
  v_exp_f32 v245, v101
  v_mfma_f32_16x16x32_bf16 a[4:7], a[192:195], v[168:171], a[4:7]
  ds_read_b64_tr_b16 a[226:227], v13 offset:37888
  v_add_f32 v251, v244, v245
  v_cvt_pk_bf16_f32 v200, v244, v245
  v_mfma_f32_16x16x32_bf16 a[8:11], a[192:195], v[176:179], a[8:11]
  v_exp_f32 v240, v102
  v_mfma_f32_16x16x32_bf16 a[8:11], a[196:199], v[180:183], a[8:11]
  ds_read_b64_tr_b16 a[228:229], v13 offset:41984
  v_add_f32 v23, v23, v246
  v_add_f32 v251, v251, v240
  v_mfma_f32_16x16x32_bf16 a[12:15], a[196:199], v[188:191], a[12:15]
  v_exp_f32 v241, v103
  v_mfma_f32_16x16x32_bf16 a[12:15], a[192:195], v[184:187], a[12:15]
  s_waitcnt lgkmcnt(11)
  ds_read_b64_tr_b16 a[230:231], v13 offset:46080
  v_add_f32 v251, v251, v241
  v_cvt_pk_bf16_f32 v201, v240, v241
  v_mfma_f32_16x16x32_bf16 a[16:19], a[200:203], v[160:163], a[16:19]
  v_exp_f32 v242, v104
  v_mfma_f32_16x16x32_bf16 a[16:19], a[204:207], v[164:167], a[16:19]
  ds_read_b64_tr_b16 a[232:233], v14 offset:33792
  v_add_f32 v24, v24, v247
  v_mfma_f32_16x16x32_bf16 a[20:23], a[204:207], v[172:175], a[20:23]
  v_exp_f32 v243, v105
  v_mfma_f32_16x16x32_bf16 a[20:23], a[200:203], v[168:171], a[20:23]
  ds_read_b64_tr_b16 a[234:235], v14 offset:37888
  v_add_f32 v252, v242, v243
  v_cvt_pk_bf16_f32 v208, v242, v243
  v_mfma_f32_16x16x32_bf16 a[24:27], a[200:203], v[176:179], a[24:27]
  v_exp_f32 v244, v106
  v_mfma_f32_16x16x32_bf16 a[24:27], a[204:207], v[180:183], a[24:27]
  ds_read_b64_tr_b16 a[236:237], v14 offset:41984
  v_add_f32 v25, v25, v248
  v_add_f32 v252, v252, v244
  v_mfma_f32_16x16x32_bf16 a[28:31], a[204:207], v[188:191], a[28:31]
  v_exp_f32 v245, v107
  v_mfma_f32_16x16x32_bf16 a[28:31], a[200:203], v[184:187], a[28:31]
  s_waitcnt lgkmcnt(11)
  ds_read_b64_tr_b16 a[238:239], v14 offset:46080
  v_add_f32 v252, v252, v245
  v_cvt_pk_bf16_f32 v209, v244, v245
  v_mfma_f32_16x16x32_bf16 a[32:35], a[208:211], v[160:163], a[32:35]
  v_exp_f32 v240, v108
  v_mfma_f32_16x16x32_bf16 a[32:35], a[212:215], v[164:167], a[32:35]
  ds_read_b64_tr_b16 a[240:241], v13 offset:34304
  v_add_f32 v26, v26, v249
  v_mfma_f32_16x16x32_bf16 a[36:39], a[212:215], v[172:175], a[36:39]
  v_exp_f32 v241, v109
  v_mfma_f32_16x16x32_bf16 a[36:39], a[208:211], v[168:171], a[36:39]
  ds_read_b64_tr_b16 a[242:243], v13 offset:38400
  v_add_f32 v253, v240, v241
  v_cvt_pk_bf16_f32 v216, v240, v241
  v_mfma_f32_16x16x32_bf16 a[40:43], a[208:211], v[176:179], a[40:43]
  v_exp_f32 v242, v110
  v_mfma_f32_16x16x32_bf16 a[40:43], a[212:215], v[180:183], a[40:43]
  ds_read_b64_tr_b16 a[244:245], v13 offset:42496
  v_add_f32 v253, v253, v242
  v_mfma_f32_16x16x32_bf16 a[44:47], a[212:215], v[188:191], a[44:47]
  v_exp_f32 v243, v111
  v_mfma_f32_16x16x32_bf16 a[44:47], a[208:211], v[184:187], a[44:47]
  s_waitcnt lgkmcnt(11)
  ds_read_b64_tr_b16 a[246:247], v13 offset:46592
  v_add_f32 v253, v253, v243
  v_cvt_pk_bf16_f32 v217, v242, v243
  v_mfma_f32_16x16x32_bf16 a[48:51], a[216:219], v[160:163], a[48:51]
  v_exp_f32 v244, v112
  v_mfma_f32_16x16x32_bf16 a[48:51], a[220:223], v[164:167], a[48:51]
  ds_read_b64_tr_b16 a[248:249], v14 offset:34304
  v_add_f32 v250, v250, v244
  v_mfma_f32_16x16x32_bf16 a[52:55], a[220:223], v[172:175], a[52:55]
  v_exp_f32 v245, v113
  v_mfma_f32_16x16x32_bf16 a[52:55], a[216:219], v[168:171], a[52:55]
  ds_read_b64_tr_b16 a[250:251], v14 offset:38400
  v_add_f32 v250, v250, v245
  v_cvt_pk_bf16_f32 v194, v244, v245
  v_mfma_f32_16x16x32_bf16 a[56:59], a[216:219], v[176:179], a[56:59]
  v_exp_f32 v240, v114
  v_mfma_f32_16x16x32_bf16 a[56:59], a[220:223], v[180:183], a[56:59]
  ds_read_b64_tr_b16 a[252:253], v14 offset:42496
  v_add_f32 v250, v250, v240
  v_mfma_f32_16x16x32_bf16 a[60:63], a[220:223], v[188:191], a[60:63]
  v_exp_f32 v241, v115
  v_mfma_f32_16x16x32_bf16 a[60:63], a[216:219], v[184:187], a[60:63]
  s_waitcnt lgkmcnt(11)
  ds_read_b64_tr_b16 a[254:255], v14 offset:46592
  v_add_f32 v250, v250, v241
  v_cvt_pk_bf16_f32 v195, v240, v241
  v_mfma_f32_16x16x32_bf16 a[64:67], a[224:227], v[160:163], a[64:67]
  s_waitcnt vmcnt(10)
  s_barrier
  v_exp_f32 v242, v116
  v_mfma_f32_16x16x32_bf16 a[64:67], a[228:231], v[164:167], a[64:67]
  ds_read_b128 a[192:195], v12 offset:0
  v_add_f32 v251, v251, v242
  v_mfma_f32_16x16x32_bf16 a[68:71], a[228:231], v[172:175], a[68:71]
  s_add_u32 m0, s77, 32768
  v_exp_f32 v243, v117
  v_mfma_f32_16x16x32_bf16 a[68:71], a[224:227], v[168:171], a[68:71]
  buffer_load_dwordx4 v15, s[52:55], s79 offen offset:0 lds
  v_add_f32 v251, v251, v243
  v_cvt_pk_bf16_f32 v202, v242, v243
  v_mfma_f32_16x16x32_bf16 a[72:75], a[224:227], v[176:179], a[72:75]
  v_exp_f32 v244, v118
  v_mfma_f32_16x16x32_bf16 a[72:75], a[228:231], v[180:183], a[72:75]
  ds_read_b128 a[196:199], v12 offset:512
  v_add_f32 v251, v251, v244
  v_mfma_f32_16x16x32_bf16 a[76:79], a[228:231], v[188:191], a[76:79]
  v_exp_f32 v245, v119
  v_mfma_f32_16x16x32_bf16 a[76:79], a[224:227], v[184:187], a[76:79]
  s_waitcnt lgkmcnt(10)
  v_add_f32 v251, v251, v245
  v_cvt_pk_bf16_f32 v203, v244, v245
  v_mfma_f32_16x16x32_bf16 a[80:83], a[232:235], v[160:163], a[80:83]
  v_exp_f32 v240, v120
  v_mfma_f32_16x16x32_bf16 a[80:83], a[236:239], v[164:167], a[80:83]
  ds_read_b128 a[200:203], v12 offset:1024
  v_add_f32 v252, v252, v240
  v_mfma_f32_16x16x32_bf16 a[84:87], a[236:239], v[172:175], a[84:87]
  v_exp_f32 v241, v121
  v_mfma_f32_16x16x32_bf16 a[84:87], a[232:235], v[168:171], a[84:87]
  buffer_load_dwordx4 v16, s[52:55], s79 offen offset:1024 lds
  v_add_f32 v252, v252, v241
  v_cvt_pk_bf16_f32 v210, v240, v241
  v_mfma_f32_16x16x32_bf16 a[88:91], a[232:235], v[176:179], a[88:91]
  v_exp_f32 v242, v122
  v_mfma_f32_16x16x32_bf16 a[88:91], a[236:239], v[180:183], a[88:91]
  ds_read_b128 a[204:207], v12 offset:1536
  v_add_f32 v252, v252, v242
  v_mfma_f32_16x16x32_bf16 a[92:95], a[236:239], v[188:191], a[92:95]
  v_exp_f32 v243, v123
  v_mfma_f32_16x16x32_bf16 a[92:95], a[232:235], v[184:187], a[92:95]
  s_waitcnt lgkmcnt(8)
  v_add_f32 v252, v252, v243
  v_cvt_pk_bf16_f32 v211, v242, v243
  v_mfma_f32_16x16x32_bf16 a[96:99], a[240:243], v[160:163], a[96:99]
  v_exp_f32 v244, v124
  v_mfma_f32_16x16x32_bf16 a[96:99], a[244:247], v[164:167], a[96:99]
  ds_read_b128 a[208:211], v12 offset:4096
  v_add_f32 v253, v253, v244
  v_mfma_f32_16x16x32_bf16 a[100:103], a[244:247], v[172:175], a[100:103]
  v_exp_f32 v245, v125
  v_mfma_f32_16x16x32_bf16 a[100:103], a[240:243], v[168:171], a[100:103]
  buffer_load_dwordx4 v17, s[52:55], s79 offen offset:2048 lds
  v_add_f32 v253, v253, v245
  v_cvt_pk_bf16_f32 v218, v244, v245
  v_mfma_f32_16x16x32_bf16 a[104:107], a[240:243], v[176:179], a[104:107]
  v_exp_f32 v240, v126
  v_mfma_f32_16x16x32_bf16 a[104:107], a[244:247], v[180:183], a[104:107]
  ds_read_b128 a[212:215], v12 offset:4608
  v_add_f32 v253, v253, v240
  v_mfma_f32_16x16x32_bf16 a[108:111], a[244:247], v[188:191], a[108:111]
  v_exp_f32 v241, v127
  v_mfma_f32_16x16x32_bf16 a[108:111], a[240:243], v[184:187], a[108:111]
  s_waitcnt lgkmcnt(6)
  v_add_f32 v253, v253, v241
  v_cvt_pk_bf16_f32 v219, v240, v241
  v_mfma_f32_16x16x32_bf16 a[112:115], a[248:251], v[160:163], a[112:115]
  v_exp_f32 v242, v128
  v_mfma_f32_16x16x32_bf16 a[112:115], a[252:255], v[164:167], a[112:115]
  ds_read_b128 a[216:219], v12 offset:5120
  v_add_f32 v250, v250, v242
  v_mfma_f32_16x16x32_bf16 a[116:119], a[252:255], v[172:175], a[116:119]
  v_exp_f32 v243, v129
  v_mfma_f32_16x16x32_bf16 a[116:119], a[248:251], v[168:171], a[116:119]
  buffer_load_dwordx4 v18, s[52:55], s79 offen offset:3072 lds
  v_add_f32 v250, v250, v243
  v_cvt_pk_bf16_f32 v196, v242, v243
  v_mfma_f32_16x16x32_bf16 a[120:123], a[248:251], v[176:179], a[120:123]
  s_add_u32 s79, s79, 16384
  v_exp_f32 v244, v130
  v_mfma_f32_16x16x32_bf16 a[120:123], a[252:255], v[180:183], a[120:123]
  ds_read_b128 a[220:223], v12 offset:5632
  v_add_f32 v250, v250, v244
  v_mfma_f32_16x16x32_bf16 a[124:127], a[252:255], v[188:191], a[124:127]
  v_exp_f32 v245, v131
  v_mfma_f32_16x16x32_bf16 a[124:127], a[248:251], v[184:187], a[124:127]
  s_waitcnt lgkmcnt(4)
  v_add_f32 v250, v250, v245
  v_cvt_pk_bf16_f32 v197, v244, v245
  s_sub_u32 s76, s76, 1
  s_cmp_eq_u32 s76, 0
  s_cbranch_scc1 LB_drain
  v_mfma_f32_16x16x32_bf16 v[32:35], a[192:195], a[128:131], v[224:227]
  v_exp_f32 v240, v132
  v_mfma_f32_16x16x32_bf16 v[32:35], a[196:199], a[132:135], v[32:35]
  ds_read_b128 a[224:227], v12 offset:8192
  v_add_f32 v251, v251, v240
  v_mfma_f32_16x16x32_bf16 v[32:35], a[200:203], a[136:139], v[32:35]
  s_add_u32 m0, s78, 16384
  v_exp_f32 v241, v133
  v_mfma_f32_16x16x32_bf16 v[32:35], a[204:207], a[140:143], v[32:35]
  buffer_load_dwordx4 v19, s[56:59], s80 offen offset:0 lds
  v_add_f32 v251, v251, v241
  v_cvt_pk_bf16_f32 v204, v240, v241
  v_mfma_f32_16x16x32_bf16 v[36:39], a[204:207], a[156:159], v[228:231]
  v_exp_f32 v242, v134
  v_mfma_f32_16x16x32_bf16 v[36:39], a[200:203], a[152:155], v[36:39]
  ds_read_b128 a[228:231], v12 offset:8704
  v_add_f32 v251, v251, v242
  v_mfma_f32_16x16x32_bf16 v[36:39], a[196:199], a[148:151], v[36:39]
  v_exp_f32 v243, v135
  v_mfma_f32_16x16x32_bf16 v[36:39], a[192:195], a[144:147], v[36:39]
  v_add_f32 v251, v251, v243
  v_cvt_pk_bf16_f32 v205, v242, v243
  v_mfma_f32_16x16x32_bf16 v[40:43], a[192:195], a[160:163], v[232:235]
  v_exp_f32 v244, v136
  v_mfma_f32_16x16x32_bf16 v[40:43], a[196:199], a[164:167], v[40:43]
  ds_read_b128 a[232:235], v12 offset:9216
  v_add_f32 v252, v252, v244
  v_mfma_f32_16x16x32_bf16 v[40:43], a[200:203], a[168:171], v[40:43]
  v_exp_f32 v245, v137
  v_mfma_f32_16x16x32_bf16 v[40:43], a[204:207], a[172:175], v[40:43]
  buffer_load_dwordx4 v20, s[56:59], s80 offen offset:1024 lds
  v_add_f32 v252, v252, v245
  v_cvt_pk_bf16_f32 v212, v244, v245
  v_mfma_f32_16x16x32_bf16 v[44:47], a[204:207], a[188:191], v[236:239]
  v_exp_f32 v240, v138
  v_mfma_f32_16x16x32_bf16 v[44:47], a[200:203], a[184:187], v[44:47]
  ds_read_b128 a[236:239], v12 offset:9728
  v_add_f32 v252, v252, v240
  v_mfma_f32_16x16x32_bf16 v[44:47], a[196:199], a[180:183], v[44:47]
  v_exp_f32 v241, v139
  v_mfma_f32_16x16x32_bf16 v[44:47], a[192:195], a[176:179], v[44:47]
  s_waitcnt lgkmcnt(4)
  v_add_f32 v252, v252, v241
  v_cvt_pk_bf16_f32 v213, v240, v241
  v_mfma_f32_16x16x32_bf16 v[48:51], a[208:211], a[128:131], v[224:227]
  v_exp_f32 v242, v140
  v_mfma_f32_16x16x32_bf16 v[48:51], a[212:215], a[132:135], v[48:51]
  ds_read_b128 a[240:243], v12 offset:12288
  v_add_f32 v253, v253, v242
  v_mfma_f32_16x16x32_bf16 v[48:51], a[216:219], a[136:139], v[48:51]
  v_exp_f32 v243, v141
  v_mfma_f32_16x16x32_bf16 v[48:51], a[220:223], a[140:143], v[48:51]
  buffer_load_dwordx4 v19, s[56:59], s80 offen offset:2048 lds
  v_add_f32 v253, v253, v243
  v_cvt_pk_bf16_f32 v220, v242, v243
  v_mfma_f32_16x16x32_bf16 v[52:55], a[220:223], a[156:159], v[228:231]
  v_exp_f32 v244, v142
  v_mfma_f32_16x16x32_bf16 v[52:55], a[216:219], a[152:155], v[52:55]
  ds_read_b128 a[244:247], v12 offset:12800
  v_add_f32 v253, v253, v244
  v_mfma_f32_16x16x32_bf16 v[52:55], a[212:215], a[148:151], v[52:55]
  v_exp_f32 v245, v143
  v_mfma_f32_16x16x32_bf16 v[52:55], a[208:211], a[144:147], v[52:55]
  v_add_f32 v253, v253, v245
  v_cvt_pk_bf16_f32 v221, v244, v245
  v_mfma_f32_16x16x32_bf16 v[56:59], a[208:211], a[160:163], v[232:235]
  v_exp_f32 v240, v144
  v_mfma_f32_16x16x32_bf16 v[56:59], a[212:215], a[164:167], v[56:59]
  ds_read_b128 a[248:251], v12 offset:13312
  v_add_f32 v250, v250, v240
  v_mfma_f32_16x16x32_bf16 v[56:59], a[216:219], a[168:171], v[56:59]
  v_exp_f32 v241, v145
  v_mfma_f32_16x16x32_bf16 v[56:59], a[220:223], a[172:175], v[56:59]
  buffer_load_dwordx4 v20, s[56:59], s80 offen offset:3072 lds
  v_add_f32 v250, v250, v241
  v_cvt_pk_bf16_f32 v198, v240, v241
  v_mfma_f32_16x16x32_bf16 v[60:63], a[220:223], a[188:191], v[236:239]
  s_add_u32 s80, s80, 16384
  v_exp_f32 v242, v146
  v_mfma_f32_16x16x32_bf16 v[60:63], a[216:219], a[184:187], v[60:63]
  ds_read_b128 a[252:255], v12 offset:13824
  v_add_f32 v250, v250, v242
  v_mfma_f32_16x16x32_bf16 v[60:63], a[212:215], a[180:183], v[60:63]
  s_add_u32 m0, s77, 131072
  v_exp_f32 v243, v147
  v_mfma_f32_16x16x32_bf16 v[60:63], a[208:211], a[176:179], v[60:63]
  s_waitcnt lgkmcnt(4)
  buffer_load_dwordx4 v21, s[40:43], s69 offen lds
  v_add_f32 v250, v250, v243
  v_cvt_pk_bf16_f32 v199, v242, v243
  v_mfma_f32_16x16x32_bf16 v[64:67], a[224:227], a[128:131], v[224:227]
  s_add_u32 s69, s69, 4096
  v_exp_f32 v244, v148
  v_mfma_f32_16x16x32_bf16 v[64:67], a[228:231], a[132:135], v[64:67]
  ds_read_b64_tr_b16 a[192:193], v13 offset:49152
  v_add_f32 v251, v251, v244
  v_mfma_f32_16x16x32_bf16 v[64:67], a[232:235], a[136:139], v[64:67]
  v_exp_f32 v245, v149
  v_mfma_f32_16x16x32_bf16 v[64:67], a[236:239], a[140:143], v[64:67]
  ds_read_b64_tr_b16 a[194:195], v13 offset:53248
  v_add_f32 v251, v251, v245
  v_cvt_pk_bf16_f32 v206, v244, v245
  v_mfma_f32_16x16x32_bf16 v[68:71], a[236:239], a[156:159], v[228:231]
  v_exp_f32 v240, v150
  v_mfma_f32_16x16x32_bf16 v[68:71], a[232:235], a[152:155], v[68:71]
  ds_read_b64_tr_b16 a[196:197], v13 offset:57344
  v_add_f32 v251, v251, v240
  v_mfma_f32_16x16x32_bf16 v[68:71], a[228:231], a[148:151], v[68:71]
  v_exp_f32 v241, v151
  v_mfma_f32_16x16x32_bf16 v[68:71], a[224:227], a[144:147], v[68:71]
  ds_read_b64_tr_b16 a[198:199], v13 offset:61440
  v_add_f32 v251, v251, v241
  v_cvt_pk_bf16_f32 v207, v240, v241
  v_mfma_f32_16x16x32_bf16 v[72:75], a[224:227], a[160:163], v[232:235]
  v_exp_f32 v242, v152
  v_mfma_f32_16x16x32_bf16 v[72:75], a[228:231], a[164:167], v[72:75]
  ds_read_b64_tr_b16 a[200:201], v14 offset:49152
  v_add_f32 v252, v252, v242
  v_mfma_f32_16x16x32_bf16 v[72:75], a[232:235], a[168:171], v[72:75]
  v_exp_f32 v243, v153
  v_mfma_f32_16x16x32_bf16 v[72:75], a[236:239], a[172:175], v[72:75]
  ds_read_b64_tr_b16 a[202:203], v14 offset:53248
  v_add_f32 v252, v252, v243
  v_cvt_pk_bf16_f32 v214, v242, v243
  v_mfma_f32_16x16x32_bf16 v[76:79], a[236:239], a[188:191], v[236:239]
  v_exp_f32 v244, v154
  v_mfma_f32_16x16x32_bf16 v[76:79], a[232:235], a[184:187], v[76:79]
  ds_read_b64_tr_b16 a[204:205], v14 offset:57344
  v_add_f32 v252, v252, v244
  v_mfma_f32_16x16x32_bf16 v[76:79], a[228:231], a[180:183], v[76:79]
  v_exp_f32 v245, v155
  v_mfma_f32_16x16x32_bf16 v[76:79], a[224:227], a[176:179], v[76:79]
  s_waitcnt lgkmcnt(7)
  ds_read_b64_tr_b16 a[206:207], v14 offset:61440
  v_add_f32 v252, v252, v245
  v_cvt_pk_bf16_f32 v215, v244, v245
  v_mfma_f32_16x16x32_bf16 v[80:83], a[240:243], a[128:131], v[224:227]
  v_exp_f32 v240, v156
  v_mfma_f32_16x16x32_bf16 v[80:83], a[244:247], a[132:135], v[80:83]
  ds_read_b64_tr_b16 a[208:209], v13 offset:49664
  v_add_f32 v253, v253, v240
  v_mfma_f32_16x16x32_bf16 v[80:83], a[248:251], a[136:139], v[80:83]
  v_exp_f32 v241, v157
  v_mfma_f32_16x16x32_bf16 v[80:83], a[252:255], a[140:143], v[80:83]
  ds_read_b64_tr_b16 a[210:211], v13 offset:53760
  v_add_f32 v253, v253, v241
  v_cvt_pk_bf16_f32 v222, v240, v241
  v_mfma_f32_16x16x32_bf16 v[84:87], a[252:255], a[156:159], v[228:231]
  v_exp_f32 v242, v158
  v_mfma_f32_16x16x32_bf16 v[84:87], a[248:251], a[152:155], v[84:87]
  ds_read_b64_tr_b16 a[212:213], v13 offset:57856
  v_add_f32 v253, v253, v242
  v_mfma_f32_16x16x32_bf16 v[84:87], a[244:247], a[148:151], v[84:87]
  v_exp_f32 v243, v159
  v_mfma_f32_16x16x32_bf16 v[84:87], a[240:243], a[144:147], v[84:87]
  ds_read_b64_tr_b16 a[214:215], v13 offset:61952
  v_add_f32 v253, v253, v243
  v_cvt_pk_bf16_f32 v223, v242, v243
  v_mfma_f32_16x16x32_bf16 v[88:91], a[240:243], a[160:163], v[232:235]
  v_exp_f32 v240, v32
  v_mfma_f32_16x16x32_bf16 v[88:91], a[244:247], a[164:167], v[88:91]
  ds_read_b64_tr_b16 a[216:217], v14 offset:49664
  v_max3_f32 v254, v250, v251, v252
  v_mfma_f32_16x16x32_bf16 v[88:91], a[248:251], a[168:171], v[88:91]
  v_exp_f32 v241, v33
  v_mfma_f32_16x16x32_bf16 v[88:91], a[252:255], a[172:175], v[88:91]
  ds_read_b64_tr_b16 a[218:219], v14 offset:53760
  v_add_f32 v246, v240, v241
  v_cvt_pk_bf16_f32 v160, v240, v241
  v_max_f32 v254, v254, v253
  v_mfma_f32_16x16x32_bf16 v[92:95], a[252:255], a[188:191], v[236:239]
  v_exp_f32 v242, v34
  v_mfma_f32_16x16x32_bf16 v[92:95], a[248:251], a[184:187], v[92:95]
  ds_read_b64_tr_b16 a[220:221], v14 offset:57856
  v_add_f32 v246, v246, v242
  v_cmp_nge_f32 vcc, s82, v254
  v_mfma_f32_16x16x32_bf16 v[92:95], a[244:247], a[180:183], v[92:95]
  v_exp_f32 v243, v35
  v_mfma_f32_16x16x32_bf16 v[92:95], a[240:243], a[176:179], v[92:95]
  s_waitcnt lgkmcnt(11)
  ds_read_b64_tr_b16 a[222:223], v14 offset:61952
  v_add_f32 v246, v246, v243
  v_cvt_pk_bf16_f32 v161, v242, v243
  s_cbranch_vccnz LB_stub0
LB_back0:
  v_mfma_f32_16x16x32_bf16 a[0:3], a[192:195], v[192:195], a[0:3]
  v_exp_f32 v244, v36
  v_mfma_f32_16x16x32_bf16 a[0:3], a[196:199], v[196:199], a[0:3]
  ds_read_b64_tr_b16 a[224:225], v13 offset:50176
  v_mfma_f32_16x16x32_bf16 a[4:7], a[196:199], v[204:207], a[4:7]
  v_exp_f32 v245, v37
  v_mfma_f32_16x16x32_bf16 a[4:7], a[192:195], v[200:203], a[4:7]
  ds_read_b64_tr_b16 a[226:227], v13 offset:54272
  v_add_f32 v247, v244, v245
  v_cvt_pk_bf16_f32 v168, v244, v245
  v_mfma_f32_16x16x32_bf16 a[8:11], a[192:195], v[208:211], a[8:11]
  v_exp_f32 v240, v38
  v_mfma_f32_16x16x32_bf16 a[8:11], a[196:199], v[212:215], a[8:11]
  ds_read_b64_tr_b16 a[228:229], v13 offset:58368
  v_add_f32 v23, v23, v250
  v_add_f32 v247, v247, v240
  v_mfma_f32_16x16x32_bf16 a[12:15], a[196:199], v[220:223], a[12:15]
  v_exp_f32 v241, v39
  v_mfma_f32_16x16x32_bf16 a[12:15], a[192:195], v[216:219], a[12:15]
  s_waitcnt lgkmcnt(11)
  ds_read_b64_tr_b16 a[230:231], v13 offset:62464
  v_add_f32 v247, v247, v241
  v_cvt_pk_bf16_f32 v169, v240, v241
  v_mfma_f32_16x16x32_bf16 a[16:19], a[200:203], v[192:195], a[16:19]
  v_exp_f32 v242, v40
  v_mfma_f32_16x16x32_bf16 a[16:19], a[204:207], v[196:199], a[16:19]
  ds_read_b64_tr_b16 a[232:233], v14 offset:50176
  v_add_f32 v24, v24, v251
  v_mfma_f32_16x16x32_bf16 a[20:23], a[204:207], v[204:207], a[20:23]
  v_exp_f32 v243, v41
  v_mfma_f32_16x16x32_bf16 a[20:23], a[200:203], v[200:203], a[20:23]
  ds_read_b64_tr_b16 a[234:235], v14 offset:54272
  v_add_f32 v248, v242, v243
  v_cvt_pk_bf16_f32 v176, v242, v243
  v_mfma_f32_16x16x32_bf16 a[24:27], a[200:203], v[208:211], a[24:27]
  v_exp_f32 v244, v42
  v_mfma_f32_16x16x32_bf16 a[24:27], a[204:207], v[212:215], a[24:27]
  ds_read_b64_tr_b16 a[236:237], v14 offset:58368
  v_add_f32 v25, v25, v252
  v_add_f32 v248, v248, v244
  v_mfma_f32_16x16x32_bf16 a[28:31], a[204:207], v[220:223], a[28:31]
  v_exp_f32 v245, v43
  v_mfma_f32_16x16x32_bf16 a[28:31], a[200:203], v[216:219], a[28:31]
  s_waitcnt lgkmcnt(11)
  ds_read_b64_tr_b16 a[238:239], v14 offset:62464
  v_add_f32 v248, v248, v245
  v_cvt_pk_bf16_f32 v177, v244, v245
  v_mfma_f32_16x16x32_bf16 a[32:35], a[208:211], v[192:195], a[32:35]
  v_exp_f32 v240, v44
  v_mfma_f32_16x16x32_bf16 a[32:35], a[212:215], v[196:199], a[32:35]
  ds_read_b64_tr_b16 a[240:241], v13 offset:50688
  v_add_f32 v26, v26, v253
  v_mfma_f32_16x16x32_bf16 a[36:39], a[212:215], v[204:207], a[36:39]
  v_exp_f32 v241, v45
  v_mfma_f32_16x16x32_bf16 a[36:39], a[208:211], v[200:203], a[36:39]
  ds_read_b64_tr_b16 a[242:243], v13 offset:54784
  v_add_f32 v249, v240, v241
  v_cvt_pk_bf16_f32 v184, v240, v241
  v_mfma_f32_16x16x32_bf16 a[40:43], a[208:211], v[208:211], a[40:43]
  v_exp_f32 v242, v46
  v_mfma_f32_16x16x32_bf16 a[40:43], a[212:215], v[212:215], a[40:43]
  ds_read_b64_tr_b16 a[244:245], v13 offset:58880
  v_add_f32 v249, v249, v242
  v_mfma_f32_16x16x32_bf16 a[44:47], a[212:215], v[220:223], a[44:47]
  v_exp_f32 v243, v47
  v_mfma_f32_16x16x32_bf16 a[44:47], a[208:211], v[216:219], a[44:47]
  s_waitcnt lgkmcnt(11)
  ds_read_b64_tr_b16 a[246:247], v13 offset:62976
  v_add_f32 v249, v249, v243
  v_cvt_pk_bf16_f32 v185, v242, v243
  v_mfma_f32_16x16x32_bf16 a[48:51], a[216:219], v[192:195], a[48:51]
  v_exp_f32 v244, v48
  v_mfma_f32_16x16x32_bf16 a[48:51], a[220:223], v[196:199], a[48:51]
  ds_read_b64_tr_b16 a[248:249], v14 offset:50688
  v_add_f32 v246, v246, v244
  v_mfma_f32_16x16x32_bf16 a[52:55], a[220:223], v[204:207], a[52:55]
  v_exp_f32 v245, v49
  v_mfma_f32_16x16x32_bf16 a[52:55], a[216:219], v[200:203], a[52:55]
  ds_read_b64_tr_b16 a[250:251], v14 offset:54784
  v_add_f32 v246, v246, v245
  v_cvt_pk_bf16_f32 v162, v244, v245
  v_mfma_f32_16x16x32_bf16 a[56:59], a[216:219], v[208:211], a[56:59]
  v_exp_f32 v240, v50
  v_mfma_f32_16x16x32_bf16 a[56:59], a[220:223], v[212:215], a[56:59]
  ds_read_b64_tr_b16 a[252:253], v14 offset:58880
  v_add_f32 v246, v246, v240
  v_mfma_f32_16x16x32_bf16 a[60:63], a[220:223], v[220:223], a[60:63]
  v_exp_f32 v241, v51
  v_mfma_f32_16x16x32_bf16 a[60:63], a[216:219], v[216:219], a[60:63]
  s_waitcnt lgkmcnt(11)
  ds_read_b64_tr_b16 a[254:255], v14 offset:62976
  v_add_f32 v246, v246, v241
  v_cvt_pk_bf16_f32 v163, v240, v241
  v_mfma_f32_16x16x32_bf16 a[64:67], a[224:227], v[192:195], a[64:67]
  s_waitcnt vmcnt(10)
  s_barrier
  v_exp_f32 v242, v52
  v_mfma_f32_16x16x32_bf16 a[64:67], a[228:231], v[196:199], a[64:67]
  ds_read_b128 a[192:195], v12 offset:16384
  v_add_f32 v247, v247, v242
  v_mfma_f32_16x16x32_bf16 a[68:71], a[228:231], v[204:207], a[68:71]
  s_add_u32 m0, s77, 49152
  v_exp_f32 v243, v53
  v_mfma_f32_16x16x32_bf16 a[68:71], a[224:227], v[200:203], a[68:71]
  buffer_load_dwordx4 v15, s[52:55], s79 offen offset:0 lds
  v_add_f32 v247, v247, v243
  v_cvt_pk_bf16_f32 v170, v242, v243
  v_mfma_f32_16x16x32_bf16 a[72:75], a[224:227], v[208:211], a[72:75]
  v_exp_f32 v244, v54
  v_mfma_f32_16x16x32_bf16 a[72:75], a[228:231], v[212:215], a[72:75]
  ds_read_b128 a[196:199], v12 offset:16896
  v_add_f32 v247, v247, v244
  v_mfma_f32_16x16x32_bf16 a[76:79], a[228:231], v[220:223], a[76:79]
  v_exp_f32 v245, v55
  v_mfma_f32_16x16x32_bf16 a[76:79], a[224:227], v[216:219], a[76:79]
  s_waitcnt lgkmcnt(10)
  v_add_f32 v247, v247, v245
  v_cvt_pk_bf16_f32 v171, v244, v245
  v_mfma_f32_16x16x32_bf16 a[80:83], a[232:235], v[192:195], a[80:83]
  v_exp_f32 v240, v56
  v_mfma_f32_16x16x32_bf16 a[80:83], a[236:239], v[196:199], a[80:83]
  ds_read_b128 a[200:203], v12 offset:17408
  v_add_f32 v248, v248, v240
  v_mfma_f32_16x16x32_bf16 a[84:87], a[236:239], v[204:207], a[84:87]
  v_exp_f32 v241, v57
  v_mfma_f32_16x16x32_bf16 a[84:87], a[232:235], v[200:203], a[84:87]
  buffer_load_dwordx4 v16, s[52:55], s79 offen offset:1024 lds
  v_add_f32 v248, v248, v241
  v_cvt_pk_bf16_f32 v178, v240, v241
  v_mfma_f32_16x16x32_bf16 a[88:91], a[232:235], v[208:211], a[88:91]
  v_exp_f32 v242, v58
  v_mfma_f32_16x16x32_bf16 a[88:91], a[236:239], v[212:215], a[88:91]
  ds_read_b128 a[204:207], v12 offset:17920
  v_add_f32 v248, v248, v242
  v_mfma_f32_16x16x32_bf16 a[92:95], a[236:239], v[220:223], a[92:95]
  v_exp_f32 v243, v59
  v_mfma_f32_16x16x32_bf16 a[92:95], a[232:235], v[216:219], a[92:95]
  s_waitcnt lgkmcnt(8)
  v_add_f32 v248, v248, v243
  v_cvt_pk_bf16_f32 v179, v242, v243
  v_mfma_f32_16x16x32_bf16 a[96:99], a[240:243], v[192:195], a[96:99]
  v_exp_f32 v244, v60
  v_mfma_f32_16x16x32_bf16 a[96:99], a[244:247], v[196:199], a[96:99]
  ds_read_b128 a[208:211], v12 offset:20480
  v_add_f32 v249, v249, v244
  v_mfma_f32_16x16x32_bf16 a[100:103], a[244:247], v[204:207], a[100:103]
  v_exp_f32 v245, v61
  v_mfma_f32_16x16x32_bf16 a[100:103], a[240:243], v[200:203], a[100:103]
  buffer_load_dwordx4 v17, s[52:55], s79 offen offset:2048 lds
  v_add_f32 v249, v249, v245
  v_cvt_pk_bf16_f32 v186, v244, v245
  v_mfma_f32_16x16x32_bf16 a[104:107], a[240:243], v[208:211], a[104:107]
  v_exp_f32 v240, v62
  v_mfma_f32_16x16x32_bf16 a[104:107], a[244:247], v[212:215], a[104:107]
  ds_read_b128 a[212:215], v12 offset:20992
  v_add_f32 v249, v249, v240
  v_mfma_f32_16x16x32_bf16 a[108:111], a[244:247], v[220:223], a[108:111]
  v_exp_f32 v241, v63
  v_mfma_f32_16x16x32_bf16 a[108:111], a[240:243], v[216:219], a[108:111]
  s_waitcnt lgkmcnt(6)
  v_add_f32 v249, v249, v241
  v_cvt_pk_bf16_f32 v187, v240, v241
  v_mfma_f32_16x16x32_bf16 a[112:115], a[248:251], v[192:195], a[112:115]
  v_exp_f32 v242, v64
  v_mfma_f32_16x16x32_bf16 a[112:115], a[252:255], v[196:199], a[112:115]
  ds_read_b128 a[216:219], v12 offset:21504
  v_add_f32 v246, v246, v242
  v_mfma_f32_16x16x32_bf16 a[116:119], a[252:255], v[204:207], a[116:119]
  v_exp_f32 v243, v65
  v_mfma_f32_16x16x32_bf16 a[116:119], a[248:251], v[200:203], a[116:119]
  buffer_load_dwordx4 v18, s[52:55], s79 offen offset:3072 lds
  v_add_f32 v246, v246, v243
  v_cvt_pk_bf16_f32 v164, v242, v243
  v_mfma_f32_16x16x32_bf16 a[120:123], a[248:251], v[208:211], a[120:123]
  s_add_u32 s79, s79, 16384
  v_exp_f32 v244, v66
  v_mfma_f32_16x16x32_bf16 a[120:123], a[252:255], v[212:215], a[120:123]
  ds_read_b128 a[220:223], v12 offset:22016
  v_add_f32 v246, v246, v244
  v_mfma_f32_16x16x32_bf16 a[124:127], a[252:255], v[220:223], a[124:127]
  v_exp_f32 v245, v67
  v_mfma_f32_16x16x32_bf16 a[124:127], a[248:251], v[216:219], a[124:127]
  s_waitcnt lgkmcnt(4)
  v_add_f32 v246, v246, v245
  v_cvt_pk_bf16_f32 v165, v244, v245
  s_branch LB_aloop
LB_stub0:
  s_mov_b32 s81, 0
  s_branch LB_rare1
LB_stub1:
  s_mov_b32 s81, 1
  s_branch LB_rare0
LB_stub2:
  s_mov_b32 s81, 2
  s_branch LB_rare1
LB_stub3:
  s_mov_b32 s81, 3
  s_branch LB_rare0
LB_stub4:
  s_mov_b32 s81, 4
  s_branch LB_rare1
LB_stub5:
  s_mov_b32 s81, 5
  s_branch LB_rare0
LB_stub6:
  s_mov_b32 s81, 6
  s_branch LB_rare1
LB_stub7:
  s_mov_b32 s81, 7
  s_branch LB_rare0
LB_stub8:
  s_mov_b32 s81, 8
  s_branch LB_rare1
LB_rare0:
  s_nop 7
  s_nop 7
  v_max3_f32 v27, v32, v33, v34
  v_max3_f32 v27, v27, v35, v48
  v_max3_f32 v27, v27, v49, v50
  v_max3_f32 v27, v27, v51, v64
  v_max3_f32 v27, v27, v65, v66
  v_max3_f32 v27, v27, v67, v80
  v_max3_f32 v27, v27, v81, v82
  v_max_f32 v27, v27, v83
  v_max3_f32 v28, v36, v37, v38
  v_max3_f32 v28, v28, v39, v52
  v_max3_f32 v28, v28, v53, v54
  v_max3_f32 v28, v28, v55, v68
  v_max3_f32 v28, v28, v69, v70
  v_max3_f32 v28, v28, v71, v84
  v_max3_f32 v28, v28, v85, v86
  v_max_f32 v28, v28, v87
  v_max3_f32 v29, v40, v41, v42
  v_max3_f32 v29, v29, v43, v56
  v_max3_f32 v29, v29, v57, v58
  v_max3_f32 v29, v29, v59, v72
  v_max3_f32 v29, v29, v73, v74
  v_max3_f32 v29, v29, v75, v88
  v_max3_f32 v29, v29, v89, v90
  v_max_f32 v29, v29, v91
  v_max3_f32 v30, v44, v45, v46
  v_max3_f32 v30, v30, v47, v60
  v_max3_f32 v30, v30, v61, v62
  v_max3_f32 v30, v30, v63, v76
  v_max3_f32 v30, v30, v77, v78
  v_max3_f32 v30, v30, v79, v92
  v_max3_f32 v30, v30, v93, v94
  v_max_f32 v30, v30, v95
  v_mov_b32 v254, v27
  s_nop 1
  v_permlane32_swap_b32 v27, v254
  v_max_f32 v27, v27, v254
  v_mov_b32 v254, v27
  s_nop 1
  v_permlane16_swap_b32 v27, v254
  v_max_f32 v27, v27, v254
  v_max_f32 v27, 0, v27
  v_mov_b32 v254, v28
  s_nop 1
  v_permlane32_swap_b32 v28, v254
  v_max_f32 v28, v28, v254
  v_mov_b32 v254, v28
  s_nop 1
  v_permlane16_swap_b32 v28, v254
  v_max_f32 v28, v28, v254
  v_max_f32 v28, 0, v28
  v_mov_b32 v254, v29
  s_nop 1
  v_permlane32_swap_b32 v29, v254
  v_max_f32 v29, v29, v254
  v_mov_b32 v254, v29
  s_nop 1
  v_permlane16_swap_b32 v29, v254
  v_max_f32 v29, v29, v254
  v_max_f32 v29, 0, v29
  v_mov_b32 v254, v30
  s_nop 1
  v_permlane32_swap_b32 v30, v254
  v_max_f32 v30, v30, v254
  v_mov_b32 v254, v30
  s_nop 1
  v_permlane16_swap_b32 v30, v254
  v_max_f32 v30, v30, v254
  v_max_f32 v30, 0, v30
  v_sub_f32 v224, v224, v27
  v_sub_f32 v225, v225, v27
  v_sub_f32 v226, v226, v27
  v_sub_f32 v227, v227, v27
  v_sub_f32 v32, v32, v27
  v_sub_f32 v96, v96, v27
  v_sub_f32 v33, v33, v27
  v_sub_f32 v97, v97, v27
  v_sub_f32 v34, v34, v27
  v_sub_f32 v98, v98, v27
  v_sub_f32 v35, v35, v27
  v_sub_f32 v99, v99, v27
  v_sub_f32 v48, v48, v27
  v_sub_f32 v112, v112, v27
  v_sub_f32 v49, v49, v27
  v_sub_f32 v113, v113, v27
  v_sub_f32 v50, v50, v27
  v_sub_f32 v114, v114, v27
  v_sub_f32 v51, v51, v27
  v_sub_f32 v115, v115, v27
  v_sub_f32 v64, v64, v27
  v_sub_f32 v128, v128, v27
  v_sub_f32 v65, v65, v27
  v_sub_f32 v129, v129, v27
  v_sub_f32 v66, v66, v27
  v_sub_f32 v130, v130, v27
  v_sub_f32 v67, v67, v27
  v_sub_f32 v131, v131, v27
  v_sub_f32 v80, v80, v27
  v_sub_f32 v144, v144, v27
  v_sub_f32 v81, v81, v27
  v_sub_f32 v145, v145, v27
  v_sub_f32 v82, v82, v27
  v_sub_f32 v146, v146, v27
  v_sub_f32 v83, v83, v27
  v_sub_f32 v147, v147, v27
  v_sub_f32 v228, v228, v28
  v_sub_f32 v229, v229, v28
  v_sub_f32 v230, v230, v28
  v_sub_f32 v231, v231, v28
  v_sub_f32 v36, v36, v28
  v_sub_f32 v100, v100, v28
  v_sub_f32 v37, v37, v28
  v_sub_f32 v101, v101, v28
  v_sub_f32 v38, v38, v28
  v_sub_f32 v102, v102, v28
  v_sub_f32 v39, v39, v28
  v_sub_f32 v103, v103, v28
  v_sub_f32 v52, v52, v28
  v_sub_f32 v116, v116, v28
  v_sub_f32 v53, v53, v28
  v_sub_f32 v117, v117, v28
  v_sub_f32 v54, v54, v28
  v_sub_f32 v118, v118, v28
  v_sub_f32 v55, v55, v28
  v_sub_f32 v119, v119, v28
  v_sub_f32 v68, v68, v28
  v_sub_f32 v132, v132, v28
  v_sub_f32 v69, v69, v28
  v_sub_f32 v133, v133, v28
  v_sub_f32 v70, v70, v28
  v_sub_f32 v134, v134, v28
  v_sub_f32 v71, v71, v28
  v_sub_f32 v135, v135, v28
  v_sub_f32 v84, v84, v28
  v_sub_f32 v148, v148, v28
  v_sub_f32 v85, v85, v28
  v_sub_f32 v149, v149, v28
  v_sub_f32 v86, v86, v28
  v_sub_f32 v150, v150, v28
  v_sub_f32 v87, v87, v28
  v_sub_f32 v151, v151, v28
  v_sub_f32 v232, v232, v29
  v_sub_f32 v233, v233, v29
  v_sub_f32 v234, v234, v29
  v_sub_f32 v235, v235, v29
  v_sub_f32 v40, v40, v29
  v_sub_f32 v104, v104, v29
  v_sub_f32 v41, v41, v29
  v_sub_f32 v105, v105, v29
  v_sub_f32 v42, v42, v29
  v_sub_f32 v106, v106, v29
  v_sub_f32 v43, v43, v29
  v_sub_f32 v107, v107, v29
  v_sub_f32 v56, v56, v29
  v_sub_f32 v120, v120, v29
  v_sub_f32 v57, v57, v29
  v_sub_f32 v121, v121, v29
  v_sub_f32 v58, v58, v29
  v_sub_f32 v122, v122, v29
  v_sub_f32 v59, v59, v29
  v_sub_f32 v123, v123, v29
  v_sub_f32 v72, v72, v29
  v_sub_f32 v136, v136, v29
  v_sub_f32 v73, v73, v29
  v_sub_f32 v137, v137, v29
  v_sub_f32 v74, v74, v29
  v_sub_f32 v138, v138, v29
  v_sub_f32 v75, v75, v29
  v_sub_f32 v139, v139, v29
  v_sub_f32 v88, v88, v29
  v_sub_f32 v152, v152, v29
  v_sub_f32 v89, v89, v29
  v_sub_f32 v153, v153, v29
  v_sub_f32 v90, v90, v29
  v_sub_f32 v154, v154, v29
  v_sub_f32 v91, v91, v29
  v_sub_f32 v155, v155, v29
  v_sub_f32 v236, v236, v30
  v_sub_f32 v237, v237, v30
  v_sub_f32 v238, v238, v30
  v_sub_f32 v239, v239, v30
  v_sub_f32 v44, v44, v30
  v_sub_f32 v108, v108, v30
  v_sub_f32 v45, v45, v30
  v_sub_f32 v109, v109, v30
  v_sub_f32 v46, v46, v30
  v_sub_f32 v110, v110, v30
  v_sub_f32 v47, v47, v30
  v_sub_f32 v111, v111, v30
  v_sub_f32 v60, v60, v30
  v_sub_f32 v124, v124, v30
  v_sub_f32 v61, v61, v30
  v_sub_f32 v125, v125, v30
  v_sub_f32 v62, v62, v30
  v_sub_f32 v126, v126, v30
  v_sub_f32 v63, v63, v30
  v_sub_f32 v127, v127, v30
  v_sub_f32 v76, v76, v30
  v_sub_f32 v140, v140, v30
  v_sub_f32 v77, v77, v30
  v_sub_f32 v141, v141, v30
  v_sub_f32 v78, v78, v30
  v_sub_f32 v142, v142, v30
  v_sub_f32 v79, v79, v30
  v_sub_f32 v143, v143, v30
  v_sub_f32 v92, v92, v30
  v_sub_f32 v156, v156, v30
  v_sub_f32 v93, v93, v30
  v_sub_f32 v157, v157, v30
  v_sub_f32 v94, v94, v30
  v_sub_f32 v158, v158, v30
  v_sub_f32 v95, v95, v30
  v_sub_f32 v159, v159, v30
  v_sub_f32 v27, 0, v27
  v_sub_f32 v28, 0, v28
  v_sub_f32 v29, 0, v29
  v_sub_f32 v30, 0, v30
  v_exp_f32 v27, v27
  v_exp_f32 v28, v28
  v_exp_f32 v29, v29
  v_exp_f32 v30, v30
  s_nop 0
  v_mul_f32 v23, v23, v27
  v_mul_f32 v24, v24, v28
  v_mul_f32 v25, v25, v29
  v_mul_f32 v26, v26, v30
  v_accvgpr_read_b32 v240, a0
  v_accvgpr_read_b32 v241, a1
  v_accvgpr_read_b32 v242, a2
  v_accvgpr_read_b32 v243, a3
  v_mul_f32 v240, v240, v27
  v_mul_f32 v241, v241, v27
  v_mul_f32 v242, v242, v27
  v_mul_f32 v243, v243, v27
  v_accvgpr_write_b32 a0, v240
  v_accvgpr_write_b32 a1, v241
  v_accvgpr_write_b32 a2, v242
  v_accvgpr_write_b32 a3, v243
  v_accvgpr_read_b32 v240, a4
  v_accvgpr_read_b32 v241, a5
  v_accvgpr_read_b32 v242, a6
  v_accvgpr_read_b32 v243, a7
  v_mul_f32 v240, v240, v28
  v_mul_f32 v241, v241, v28
  v_mul_f32 v242, v242, v28
  v_mul_f32 v243, v243, v28
  v_accvgpr_write_b32 a4, v240
  v_accvgpr_write_b32 a5, v241
  v_accvgpr_write_b32 a6, v242
  v_accvgpr_write_b32 a7, v243
  v_accvgpr_read_b32 v240, a8
  v_accvgpr_read_b32 v241, a9
  v_accvgpr_read_b32 v242, a10
  v_accvgpr_read_b32 v243, a11
  v_mul_f32 v240, v240, v29
  v_mul_f32 v241, v241, v29
  v_mul_f32 v242, v242, v29
  v_mul_f32 v243, v243, v29
  v_accvgpr_write_b32 a8, v240
  v_accvgpr_write_b32 a9, v241
  v_accvgpr_write_b32 a10, v242
  v_accvgpr_write_b32 a11, v243
  v_accvgpr_read_b32 v240, a12
  v_accvgpr_read_b32 v241, a13
  v_accvgpr_read_b32 v242, a14
  v_accvgpr_read_b32 v243, a15
  v_mul_f32 v240, v240, v30
  v_mul_f32 v241, v241, v30
  v_mul_f32 v242, v242, v30
  v_mul_f32 v243, v243, v30
  v_accvgpr_write_b32 a12, v240
  v_accvgpr_write_b32 a13, v241
  v_accvgpr_write_b32 a14, v242
  v_accvgpr_write_b32 a15, v243
  v_accvgpr_read_b32 v240, a16
  v_accvgpr_read_b32 v241, a17
  v_accvgpr_read_b32 v242, a18
  v_accvgpr_read_b32 v243, a19
  v_mul_f32 v240, v240, v27
  v_mul_f32 v241, v241, v27
  v_mul_f32 v242, v242, v27
  v_mul_f32 v243, v243, v27
  v_accvgpr_write_b32 a16, v240
  v_accvgpr_write_b32 a17, v241
  v_accvgpr_write_b32 a18, v242
  v_accvgpr_write_b32 a19, v243
  v_accvgpr_read_b32 v240, a20
  v_accvgpr_read_b32 v241, a21
  v_accvgpr_read_b32 v242, a22
  v_accvgpr_read_b32 v243, a23
  v_mul_f32 v240, v240, v28
  v_mul_f32 v241, v241, v28
  v_mul_f32 v242, v242, v28
  v_mul_f32 v243, v243, v28
  v_accvgpr_write_b32 a20, v240
  v_accvgpr_write_b32 a21, v241
  v_accvgpr_write_b32 a22, v242
  v_accvgpr_write_b32 a23, v243
  v_accvgpr_read_b32 v240, a24
  v_accvgpr_read_b32 v241, a25
  v_accvgpr_read_b32 v242, a26
  v_accvgpr_read_b32 v243, a27
  v_mul_f32 v240, v240, v29
  v_mul_f32 v241, v241, v29
  v_mul_f32 v242, v242, v29
  v_mul_f32 v243, v243, v29
  v_accvgpr_write_b32 a24, v240
  v_accvgpr_write_b32 a25, v241
  v_accvgpr_write_b32 a26, v242
  v_accvgpr_write_b32 a27, v243
  v_accvgpr_read_b32 v240, a28
  v_accvgpr_read_b32 v241, a29
  v_accvgpr_read_b32 v242, a30
  v_accvgpr_read_b32 v243, a31
  v_mul_f32 v240, v240, v30
  v_mul_f32 v241, v241, v30
  v_mul_f32 v242, v242, v30
  v_mul_f32 v243, v243, v30
  v_accvgpr_write_b32 a28, v240
  v_accvgpr_write_b32 a29, v241
  v_accvgpr_write_b32 a30, v242
  v_accvgpr_write_b32 a31, v243
  v_accvgpr_read_b32 v240, a32
  v_accvgpr_read_b32 v241, a33
  v_accvgpr_read_b32 v242, a34
  v_accvgpr_read_b32 v243, a35
  v_mul_f32 v240, v240, v27
  v_mul_f32 v241, v241, v27
  v_mul_f32 v242, v242, v27
  v_mul_f32 v243, v243, v27
  v_accvgpr_write_b32 a32, v240
  v_accvgpr_write_b32 a33, v241
  v_accvgpr_write_b32 a34, v242
  v_accvgpr_write_b32 a35, v243
  v_accvgpr_read_b32 v240, a36
  v_accvgpr_read_b32 v241, a37
  v_accvgpr_read_b32 v242, a38
  v_accvgpr_read_b32 v243, a39
  v_mul_f32 v240, v240, v28
  v_mul_f32 v241, v241, v28
  v_mul_f32 v242, v242, v28
  v_mul_f32 v243, v243, v28
  v_accvgpr_write_b32 a36, v240
  v_accvgpr_write_b32 a37, v241
  v_accvgpr_write_b32 a38, v242
  v_accvgpr_write_b32 a39, v243
  v_accvgpr_read_b32 v240, a40
  v_accvgpr_read_b32 v241, a41
  v_accvgpr_read_b32 v242, a42
  v_accvgpr_read_b32 v243, a43
  v_mul_f32 v240, v240, v29
  v_mul_f32 v241, v241, v29
  v_mul_f32 v242, v242, v29
  v_mul_f32 v243, v243, v29
  v_accvgpr_write_b32 a40, v240
  v_accvgpr_write_b32 a41, v241
  v_accvgpr_write_b32 a42, v242
  v_accvgpr_write_b32 a43, v243
  v_accvgpr_read_b32 v240, a44
  v_accvgpr_read_b32 v241, a45
  v_accvgpr_read_b32 v242, a46
  v_accvgpr_read_b32 v243, a47
  v_mul_f32 v240, v240, v30
  v_mul_f32 v241, v241, v30
  v_mul_f32 v242, v242, v30
  v_mul_f32 v243, v243, v30
  v_accvgpr_write_b32 a44, v240
  v_accvgpr_write_b32 a45, v241
  v_accvgpr_write_b32 a46, v242
  v_accvgpr_write_b32 a47, v243
  v_accvgpr_read_b32 v240, a48
  v_accvgpr_read_b32 v241, a49
  v_accvgpr_read_b32 v242, a50
  v_accvgpr_read_b32 v243, a51
  v_mul_f32 v240, v240, v27
  v_mul_f32 v241, v241, v27
  v_mul_f32 v242, v242, v27
  v_mul_f32 v243, v243, v27
  v_accvgpr_write_b32 a48, v240
  v_accvgpr_write_b32 a49, v241
  v_accvgpr_write_b32 a50, v242
  v_accvgpr_write_b32 a51, v243
  v_accvgpr_read_b32 v240, a52
  v_accvgpr_read_b32 v241, a53
  v_accvgpr_read_b32 v242, a54
  v_accvgpr_read_b32 v243, a55
  v_mul_f32 v240, v240, v28
  v_mul_f32 v241, v241, v28
  v_mul_f32 v242, v242, v28
  v_mul_f32 v243, v243, v28
  v_accvgpr_write_b32 a52, v240
  v_accvgpr_write_b32 a53, v241
  v_accvgpr_write_b32 a54, v242
  v_accvgpr_write_b32 a55, v243
  v_accvgpr_read_b32 v240, a56
  v_accvgpr_read_b32 v241, a57
  v_accvgpr_read_b32 v242, a58
  v_accvgpr_read_b32 v243, a59
  v_mul_f32 v240, v240, v29
  v_mul_f32 v241, v241, v29
  v_mul_f32 v242, v242, v29
  v_mul_f32 v243, v243, v29
  v_accvgpr_write_b32 a56, v240
  v_accvgpr_write_b32 a57, v241
  v_accvgpr_write_b32 a58, v242
  v_accvgpr_write_b32 a59, v243
  v_accvgpr_read_b32 v240, a60
  v_accvgpr_read_b32 v241, a61
  v_accvgpr_read_b32 v242, a62
  v_accvgpr_read_b32 v243, a63
  v_mul_f32 v240, v240, v30
  v_mul_f32 v241, v241, v30
  v_mul_f32 v242, v242, v30
  v_mul_f32 v243, v243, v30
  v_accvgpr_write_b32 a60, v240
  v_accvgpr_write_b32 a61, v241
  v_accvgpr_write_b32 a62, v242
  v_accvgpr_write_b32 a63, v243
  v_accvgpr_read_b32 v240, a64
  v_accvgpr_read_b32 v241, a65
  v_accvgpr_read_b32 v242, a66
  v_accvgpr_read_b32 v243, a67
  v_mul_f32 v240, v240, v27
  v_mul_f32 v241, v241, v27
  v_mul_f32 v242, v242, v27
  v_mul_f32 v243, v243, v27
  v_accvgpr_write_b32 a64, v240
  v_accvgpr_write_b32 a65, v241
  v_accvgpr_write_b32 a66, v242
  v_accvgpr_write_b32 a67, v243
  v_accvgpr_read_b32 v240, a68
  v_accvgpr_read_b32 v241, a69
  v_accvgpr_read_b32 v242, a70
  v_accvgpr_read_b32 v243, a71
  v_mul_f32 v240, v240, v28
  v_mul_f32 v241, v241, v28
  v_mul_f32 v242, v242, v28
  v_mul_f32 v243, v243, v28
  v_accvgpr_write_b32 a68, v240
  v_accvgpr_write_b32 a69, v241
  v_accvgpr_write_b32 a70, v242
  v_accvgpr_write_b32 a71, v243
  v_accvgpr_read_b32 v240, a72
  v_accvgpr_read_b32 v241, a73
  v_accvgpr_read_b32 v242, a74
  v_accvgpr_read_b32 v243, a75
  v_mul_f32 v240, v240, v29
  v_mul_f32 v241, v241, v29
  v_mul_f32 v242, v242, v29
  v_mul_f32 v243, v243, v29
  v_accvgpr_write_b32 a72, v240
  v_accvgpr_write_b32 a73, v241
  v_accvgpr_write_b32 a74, v242
  v_accvgpr_write_b32 a75, v243
  v_accvgpr_read_b32 v240, a76
  v_accvgpr_read_b32 v241, a77
  v_accvgpr_read_b32 v242, a78
  v_accvgpr_read_b32 v243, a79
  v_mul_f32 v240, v240, v30
  v_mul_f32 v241, v241, v30
  v_mul_f32 v242, v242, v30
  v_mul_f32 v243, v243, v30
  v_accvgpr_write_b32 a76, v240
  v_accvgpr_write_b32 a77, v241
  v_accvgpr_write_b32 a78, v242
  v_accvgpr_write_b32 a79, v243
  v_accvgpr_read_b32 v240, a80
  v_accvgpr_read_b32 v241, a81
  v_accvgpr_read_b32 v242, a82
  v_accvgpr_read_b32 v243, a83
  v_mul_f32 v240, v240, v27
  v_mul_f32 v241, v241, v27
  v_mul_f32 v242, v242, v27
  v_mul_f32 v243, v243, v27
  v_accvgpr_write_b32 a80, v240
  v_accvgpr_write_b32 a81, v241
  v_accvgpr_write_b32 a82, v242
  v_accvgpr_write_b32 a83, v243
  v_accvgpr_read_b32 v240, a84
  v_accvgpr_read_b32 v241, a85
  v_accvgpr_read_b32 v242, a86
  v_accvgpr_read_b32 v243, a87
  v_mul_f32 v240, v240, v28
  v_mul_f32 v241, v241, v28
  v_mul_f32 v242, v242, v28
  v_mul_f32 v243, v243, v28
  v_accvgpr_write_b32 a84, v240
  v_accvgpr_write_b32 a85, v241
  v_accvgpr_write_b32 a86, v242
  v_accvgpr_write_b32 a87, v243
  v_accvgpr_read_b32 v240, a88
  v_accvgpr_read_b32 v241, a89
  v_accvgpr_read_b32 v242, a90
  v_accvgpr_read_b32 v243, a91
  v_mul_f32 v240, v240, v29
  v_mul_f32 v241, v241, v29
  v_mul_f32 v242, v242, v29
  v_mul_f32 v243, v243, v29
  v_accvgpr_write_b32 a88, v240
  v_accvgpr_write_b32 a89, v241
  v_accvgpr_write_b32 a90, v242
  v_accvgpr_write_b32 a91, v243
  v_accvgpr_read_b32 v240, a92
  v_accvgpr_read_b32 v241, a93
  v_accvgpr_read_b32 v242, a94
  v_accvgpr_read_b32 v243, a95
  v_mul_f32 v240, v240, v30
  v_mul_f32 v241, v241, v30
  v_mul_f32 v242, v242, v30
  v_mul_f32 v243, v243, v30
  v_accvgpr_write_b32 a92, v240
  v_accvgpr_write_b32 a93, v241
  v_accvgpr_write_b32 a94, v242
  v_accvgpr_write_b32 a95, v243
  v_accvgpr_read_b32 v240, a96
  v_accvgpr_read_b32 v241, a97
  v_accvgpr_read_b32 v242, a98
  v_accvgpr_read_b32 v243, a99
  v_mul_f32 v240, v240, v27
  v_mul_f32 v241, v241, v27
  v_mul_f32 v242, v242, v27
  v_mul_f32 v243, v243, v27
  v_accvgpr_write_b32 a96, v240
  v_accvgpr_write_b32 a97, v241
  v_accvgpr_write_b32 a98, v242
  v_accvgpr_write_b32 a99, v243
  v_accvgpr_read_b32 v240, a100
  v_accvgpr_read_b32 v241, a101
  v_accvgpr_read_b32 v242, a102
  v_accvgpr_read_b32 v243, a103
  v_mul_f32 v240, v240, v28
  v_mul_f32 v241, v241, v28
  v_mul_f32 v242, v242, v28
  v_mul_f32 v243, v243, v28
  v_accvgpr_write_b32 a100, v240
  v_accvgpr_write_b32 a101, v241
  v_accvgpr_write_b32 a102, v242
  v_accvgpr_write_b32 a103, v243
  v_accvgpr_read_b32 v240, a104
  v_accvgpr_read_b32 v241, a105
  v_accvgpr_read_b32 v242, a106
  v_accvgpr_read_b32 v243, a107
  v_mul_f32 v240, v240, v29
  v_mul_f32 v241, v241, v29
  v_mul_f32 v242, v242, v29
  v_mul_f32 v243, v243, v29
  v_accvgpr_write_b32 a104, v240
  v_accvgpr_write_b32 a105, v241
  v_accvgpr_write_b32 a106, v242
  v_accvgpr_write_b32 a107, v243
  v_accvgpr_read_b32 v240, a108
  v_accvgpr_read_b32 v241, a109
  v_accvgpr_read_b32 v242, a110
  v_accvgpr_read_b32 v243, a111
  v_mul_f32 v240, v240, v30
  v_mul_f32 v241, v241, v30
  v_mul_f32 v242, v242, v30
  v_mul_f32 v243, v243, v30
  v_accvgpr_write_b32 a108, v240
  v_accvgpr_write_b32 a109, v241
  v_accvgpr_write_b32 a110, v242
  v_accvgpr_write_b32 a111, v243
  v_accvgpr_read_b32 v240, a112
  v_accvgpr_read_b32 v241, a113
  v_accvgpr_read_b32 v242, a114
  v_accvgpr_read_b32 v243, a115
  v_mul_f32 v240, v240, v27
  v_mul_f32 v241, v241, v27
  v_mul_f32 v242, v242, v27
  v_mul_f32 v243, v243, v27
  v_accvgpr_write_b32 a112, v240
  v_accvgpr_write_b32 a113, v241
  v_accvgpr_write_b32 a114, v242
  v_accvgpr_write_b32 a115, v243
  v_accvgpr_read_b32 v240, a116
  v_accvgpr_read_b32 v241, a117
  v_accvgpr_read_b32 v242, a118
  v_accvgpr_read_b32 v243, a119
  v_mul_f32 v240, v240, v28
  v_mul_f32 v241, v241, v28
  v_mul_f32 v242, v242, v28
  v_mul_f32 v243, v243, v28
  v_accvgpr_write_b32 a116, v240
  v_accvgpr_write_b32 a117, v241
  v_accvgpr_write_b32 a118, v242
  v_accvgpr_write_b32 a119, v243
  v_accvgpr_read_b32 v240, a120
  v_accvgpr_read_b32 v241, a121
  v_accvgpr_read_b32 v242, a122
  v_accvgpr_read_b32 v243, a123
  v_mul_f32 v240, v240, v29
  v_mul_f32 v241, v241, v29
  v_mul_f32 v242, v242, v29
  v_mul_f32 v243, v243, v29
  v_accvgpr_write_b32 a120, v240
  v_accvgpr_write_b32 a121, v241
  v_accvgpr_write_b32 a122, v242
  v_accvgpr_write_b32 a123, v243
  v_accvgpr_read_b32 v240, a124
  v_accvgpr_read_b32 v241, a125
  v_accvgpr_read_b32 v242, a126
  v_accvgpr_read_b32 v243, a127
  v_mul_f32 v240, v240, v30
  v_mul_f32 v241, v241, v30
  v_mul_f32 v242, v242, v30
  v_mul_f32 v243, v243, v30
  v_accvgpr_write_b32 a124, v240
  v_accvgpr_write_b32 a125, v241
  v_accvgpr_write_b32 a126, v242
  v_accvgpr_write_b32 a127, v243
  v_exp_f32 v240, v32
  s_nop 0
  v_exp_f32 v241, v33
  s_nop 0
  v_add_f32 v246, v240, v241
  v_cvt_pk_bf16_f32 v160, v240, v241
  v_exp_f32 v242, v34
  s_nop 0
  v_add_f32 v246, v246, v242
  v_exp_f32 v243, v35
  s_nop 0
  v_add_f32 v246, v246, v243
  v_cvt_pk_bf16_f32 v161, v242, v243
  v_exp_f32 v244, v36
  s_nop 0
  v_exp_f32 v245, v37
  s_nop 0
  v_add_f32 v247, v244, v245
  v_cvt_pk_bf16_f32 v168, v244, v245
  v_exp_f32 v240, v38
  s_nop 0
  v_add_f32 v247, v247, v240
  v_exp_f32 v241, v39
  s_nop 0
  v_add_f32 v247, v247, v241
  v_cvt_pk_bf16_f32 v169, v240, v241
  v_exp_f32 v242, v40
  s_nop 0
  v_exp_f32 v243, v41
  s_nop 0
  v_add_f32 v248, v242, v243
  v_cvt_pk_bf16_f32 v176, v242, v243
  v_exp_f32 v244, v42
  s_nop 0
  v_add_f32 v248, v248, v244
  v_exp_f32 v245, v43
  s_nop 0
  v_add_f32 v248, v248, v245
  v_cvt_pk_bf16_f32 v177, v244, v245
  v_exp_f32 v240, v44
  s_nop 0
  v_exp_f32 v241, v45
  s_nop 0
  v_add_f32 v249, v240, v241
  v_cvt_pk_bf16_f32 v184, v240, v241
  v_exp_f32 v242, v46
  s_nop 0
  v_add_f32 v249, v249, v242
  v_exp_f32 v243, v47
  s_nop 0
  v_add_f32 v249, v249, v243
  v_cvt_pk_bf16_f32 v185, v242, v243
  v_exp_f32 v244, v48
  s_nop 0
  v_add_f32 v246, v246, v244
  v_exp_f32 v245, v49
  s_nop 0
  v_add_f32 v246, v246, v245
  v_cvt_pk_bf16_f32 v162, v244, v245
  v_exp_f32 v240, v50
  s_nop 0
  v_add_f32 v246, v246, v240
  v_exp_f32 v241, v51
  s_nop 0
  v_add_f32 v246, v246, v241
  v_cvt_pk_bf16_f32 v163, v240, v241
  v_exp_f32 v242, v52
  s_nop 0
  v_add_f32 v247, v247, v242
  v_exp_f32 v243, v53
  s_nop 0
  v_add_f32 v247, v247, v243
  v_cvt_pk_bf16_f32 v170, v242, v243
  v_exp_f32 v244, v54
  s_nop 0
  v_add_f32 v247, v247, v244
  v_exp_f32 v245, v55
  s_nop 0
  v_add_f32 v247, v247, v245
  v_cvt_pk_bf16_f32 v171, v244, v245
  v_exp_f32 v240, v56
  s_nop 0
  v_add_f32 v248, v248, v240
  v_exp_f32 v241, v57
  s_nop 0
  v_add_f32 v248, v248, v241
  v_cvt_pk_bf16_f32 v178, v240, v241
  v_exp_f32 v242, v58
  s_nop 0
  v_add_f32 v248, v248, v242
  v_exp_f32 v243, v59
  s_nop 0
  v_add_f32 v248, v248, v243
  v_cvt_pk_bf16_f32 v179, v242, v243
  v_exp_f32 v244, v60
  s_nop 0
  v_add_f32 v249, v249, v244
  v_exp_f32 v245, v61
  s_nop 0
  v_add_f32 v249, v249, v245
  v_cvt_pk_bf16_f32 v186, v244, v245
  v_exp_f32 v240, v62
  s_nop 0
  v_add_f32 v249, v249, v240
  v_exp_f32 v241, v63
  s_nop 0
  v_add_f32 v249, v249, v241
  v_cvt_pk_bf16_f32 v187, v240, v241
  v_exp_f32 v242, v64
  s_nop 0
  v_add_f32 v246, v246, v242
  v_exp_f32 v243, v65
  s_nop 0
  v_add_f32 v246, v246, v243
  v_cvt_pk_bf16_f32 v164, v242, v243
  v_exp_f32 v244, v66
  s_nop 0
  v_add_f32 v246, v246, v244
  v_exp_f32 v245, v67
  s_nop 0
  v_add_f32 v246, v246, v245
  v_cvt_pk_bf16_f32 v165, v244, v245
  v_exp_f32 v240, v68
  s_nop 0
  v_add_f32 v247, v247, v240
  v_exp_f32 v241, v69
  s_nop 0
  v_add_f32 v247, v247, v241
  v_cvt_pk_bf16_f32 v172, v240, v241
  v_exp_f32 v242, v70
  s_nop 0
  v_add_f32 v247, v247, v242
  v_exp_f32 v243, v71
  s_nop 0
  v_add_f32 v247, v247, v243
  v_cvt_pk_bf16_f32 v173, v242, v243
  v_exp_f32 v244, v72
  s_nop 0
  v_add_f32 v248, v248, v244
  v_exp_f32 v245, v73
  s_nop 0
  v_add_f32 v248, v248, v245
  v_cvt_pk_bf16_f32 v180, v244, v245
  v_exp_f32 v240, v74
  s_nop 0
  v_add_f32 v248, v248, v240
  v_exp_f32 v241, v75
  s_nop 0
  v_add_f32 v248, v248, v241
  v_cvt_pk_bf16_f32 v181, v240, v241
  v_exp_f32 v242, v76
  s_nop 0
  v_add_f32 v249, v249, v242
  v_exp_f32 v243, v77
  s_nop 0
  v_add_f32 v249, v249, v243
  v_cvt_pk_bf16_f32 v188, v242, v243
  v_exp_f32 v244, v78
  s_nop 0
  v_add_f32 v249, v249, v244
  v_exp_f32 v245, v79
  s_nop 0
  v_add_f32 v249, v249, v245
  v_cvt_pk_bf16_f32 v189, v244, v245
  v_exp_f32 v240, v80
  s_nop 0
  v_add_f32 v246, v246, v240
  v_exp_f32 v241, v81
  s_nop 0
  v_add_f32 v246, v246, v241
  v_cvt_pk_bf16_f32 v166, v240, v241
  v_exp_f32 v242, v82
  s_nop 0
  v_add_f32 v246, v246, v242
  v_exp_f32 v243, v83
  s_nop 0
  v_add_f32 v246, v246, v243
  v_cvt_pk_bf16_f32 v167, v242, v243
  v_exp_f32 v244, v84
  s_nop 0
  v_add_f32 v247, v247, v244
  v_exp_f32 v245, v85
  s_nop 0
  v_add_f32 v247, v247, v245
  v_cvt_pk_bf16_f32 v174, v244, v245
  v_exp_f32 v240, v86
  s_nop 0
  v_add_f32 v247, v247, v240
  v_exp_f32 v241, v87
  s_nop 0
  v_add_f32 v247, v247, v241
  v_cvt_pk_bf16_f32 v175, v240, v241
  v_exp_f32 v242, v88
  s_nop 0
  v_add_f32 v248, v248, v242
  v_exp_f32 v243, v89
  s_nop 0
  v_add_f32 v248, v248, v243
  v_cvt_pk_bf16_f32 v182, v242, v243
  v_exp_f32 v244, v90
  s_nop 0
  v_add_f32 v248, v248, v244
  v_exp_f32 v245, v91
  s_nop 0
  v_add_f32 v248, v248, v245
  v_cvt_pk_bf16_f32 v183, v244, v245
  v_exp_f32 v240, v92
  s_nop 0
  v_add_f32 v249, v249, v240
  v_exp_f32 v241, v93
  s_nop 0
  v_add_f32 v249, v249, v241
  v_cvt_pk_bf16_f32 v190, v240, v241
  v_exp_f32 v242, v94
  s_nop 0
  v_add_f32 v249, v249, v242
  v_exp_f32 v243, v95
  s_nop 0
  v_add_f32 v249, v249, v243
  v_cvt_pk_bf16_f32 v191, v242, v243
  v_exp_f32 v240, v96
  s_nop 0
  v_exp_f32 v241, v97
  s_nop 0
  v_add_f32 v250, v240, v241
  v_cvt_pk_bf16_f32 v192, v240, v241
  v_exp_f32 v242, v98
  s_nop 0
  v_add_f32 v250, v250, v242
  v_exp_f32 v243, v99
  s_nop 0
  v_add_f32 v250, v250, v243
  v_cvt_pk_bf16_f32 v193, v242, v243
  s_nop 1
  s_cmp_eq_u32 s81, 1
  s_cbranch_scc1 LB_back1
  s_cmp_eq_u32 s81, 3
  s_cbranch_scc1 LB_back3
  s_cmp_eq_u32 s81, 5
  s_cbranch_scc1 LB_back5
  s_branch LB_back7
LB_rare1:
  s_nop 7
  s_nop 7
  v_max3_f32 v27, v96, v97, v98
  v_max3_f32 v27, v27, v99, v112
  v_max3_f32 v27, v27, v113, v114
  v_max3_f32 v27, v27, v115, v128
  v_max3_f32 v27, v27, v129, v130
  v_max3_f32 v27, v27, v131, v144
  v_max3_f32 v27, v27, v145, v146
  v_max_f32 v27, v27, v147
  v_max3_f32 v28, v100, v101, v102
  v_max3_f32 v28, v28, v103, v116
  v_max3_f32 v28, v28, v117, v118
  v_max3_f32 v28, v28, v119, v132
  v_max3_f32 v28, v28, v133, v134
  v_max3_f32 v28, v28, v135, v148
  v_max3_f32 v28, v28, v149, v150
  v_max_f32 v28, v28, v151
  v_max3_f32 v29, v104, v105, v106
  v_max3_f32 v29, v29, v107, v120
  v_max3_f32 v29, v29, v121, v122
  v_max3_f32 v29, v29, v123, v136
  v_max3_f32 v29, v29, v137, v138
  v_max3_f32 v29, v29, v139, v152
  v_max3_f32 v29, v29, v153, v154
  v_max_f32 v29, v29, v155
  v_max3_f32 v30, v108, v109, v110
  v_max3_f32 v30, v30, v111, v124
  v_max3_f32 v30, v30, v125, v126
  v_max3_f32 v30, v30, v127, v140
  v_max3_f32 v30, v30, v141, v142
  v_max3_f32 v30, v30, v143, v156
  v_max3_f32 v30, v30, v157, v158
  v_max_f32 v30, v30, v159
  v_mov_b32 v254, v27
  s_nop 1
  v_permlane32_swap_b32 v27, v254
  v_max_f32 v27, v27, v254
  v_mov_b32 v254, v27
  s_nop 1
  v_permlane16_swap_b32 v27, v254
  v_max_f32 v27, v27, v254
  v_max_f32 v27, 0, v27
  v_mov_b32 v254, v28
  s_nop 1
  v_permlane32_swap_b32 v28, v254
  v_max_f32 v28, v28, v254
  v_mov_b32 v254, v28
  s_nop 1
  v_permlane16_swap_b32 v28, v254
  v_max_f32 v28, v28, v254
  v_max_f32 v28, 0, v28
  v_mov_b32 v254, v29
  s_nop 1
  v_permlane32_swap_b32 v29, v254
  v_max_f32 v29, v29, v254
  v_mov_b32 v254, v29
  s_nop 1
  v_permlane16_swap_b32 v29, v254
  v_max_f32 v29, v29, v254
  v_max_f32 v29, 0, v29
  v_mov_b32 v254, v30
  s_nop 1
  v_permlane32_swap_b32 v30, v254
  v_max_f32 v30, v30, v254
  v_mov_b32 v254, v30
  s_nop 1
  v_permlane16_swap_b32 v30, v254
  v_max_f32 v30, v30, v254
  v_max_f32 v30, 0, v30
  v_sub_f32 v224, v224, v27
  v_sub_f32 v225, v225, v27
  v_sub_f32 v226, v226, v27
  v_sub_f32 v227, v227, v27
  v_sub_f32 v96, v96, v27
  v_sub_f32 v32, v32, v27
  v_sub_f32 v97, v97, v27
  v_sub_f32 v33, v33, v27
  v_sub_f32 v98, v98, v27
  v_sub_f32 v34, v34, v27
  v_sub_f32 v99, v99, v27
  v_sub_f32 v35, v35, v27
  v_sub_f32 v112, v112, v27
  v_sub_f32 v48, v48, v27
  v_sub_f32 v113, v113, v27
  v_sub_f32 v49, v49, v27
  v_sub_f32 v114, v114, v27
  v_sub_f32 v50, v50, v27
  v_sub_f32 v115, v115, v27
  v_sub_f32 v51, v51, v27
  v_sub_f32 v128, v128, v27
  v_sub_f32 v64, v64, v27
  v_sub_f32 v129, v129, v27
  v_sub_f32 v65, v65, v27
  v_sub_f32 v130, v130, v27
  v_sub_f32 v66, v66, v27
  v_sub_f32 v131, v131, v27
  v_sub_f32 v67, v67, v27
  v_sub_f32 v144, v144, v27
  v_sub_f32 v80, v80, v27
  v_sub_f32 v145, v145, v27
  v_sub_f32 v81, v81, v27
  v_sub_f32 v146, v146, v27
  v_sub_f32 v82, v82, v27
  v_sub_f32 v147, v147, v27
  v_sub_f32 v83, v83, v27
  v_sub_f32 v228, v228, v28
  v_sub_f32 v229, v229, v28
  v_sub_f32 v230, v230, v28
  v_sub_f32 v231, v231, v28
  v_sub_f32 v100, v100, v28
  v_sub_f32 v36, v36, v28
  v_sub_f32 v101, v101, v28
  v_sub_f32 v37, v37, v28
  v_sub_f32 v102, v102, v28
  v_sub_f32 v38, v38, v28
  v_sub_f32 v103, v103, v28
  v_sub_f32 v39, v39, v28
  v_sub_f32 v116, v116, v28
  v_sub_f32 v52, v52, v28
  v_sub_f32 v117, v117, v28
  v_sub_f32 v53, v53, v28
  v_sub_f32 v118, v118, v28
  v_sub_f32 v54, v54, v28
  v_sub_f32 v119, v119, v28
  v_sub_f32 v55, v55, v28
  v_sub_f32 v132, v132, v28
  v_sub_f32 v68, v68, v28
  v_sub_f32 v133, v133, v28
  v_sub_f32 v69, v69, v28
  v_sub_f32 v134, v134, v28
  v_sub_f32 v70, v70, v28
  v_sub_f32 v135, v135, v28
  v_sub_f32 v71, v71, v28
  v_sub_f32 v148, v148, v28
  v_sub_f32 v84, v84, v28
  v_sub_f32 v149, v149, v28
  v_sub_f32 v85, v85, v28
  v_sub_f32 v150, v150, v28
  v_sub_f32 v86, v86, v28
  v_sub_f32 v151, v151, v28
  v_sub_f32 v87, v87, v28
  v_sub_f32 v232, v232, v29
  v_sub_f32 v233, v233, v29
  v_sub_f32 v234, v234, v29
  v_sub_f32 v235, v235, v29
  v_sub_f32 v104, v104, v29
  v_sub_f32 v40, v40, v29
  v_sub_f32 v105, v105, v29
  v_sub_f32 v41, v41, v29
  v_sub_f32 v106, v106, v29
  v_sub_f32 v42, v42, v29
  v_sub_f32 v107, v107, v29
  v_sub_f32 v43, v43, v29
  v_sub_f32 v120, v120, v29
  v_sub_f32 v56, v56, v29
  v_sub_f32 v121, v121, v29
  v_sub_f32 v57, v57, v29
  v_sub_f32 v122, v122, v29
  v_sub_f32 v58, v58, v29
  v_sub_f32 v123, v123, v29
  v_sub_f32 v59, v59, v29
  v_sub_f32 v136, v136, v29
  v_sub_f32 v72, v72, v29
  v_sub_f32 v137, v137, v29
  v_sub_f32 v73, v73, v29
  v_sub_f32 v138, v138, v29
  v_sub_f32 v74, v74, v29
  v_sub_f32 v139, v139, v29
  v_sub_f32 v75, v75, v29
  v_sub_f32 v152, v152, v29
  v_sub_f32 v88, v88, v29
  v_sub_f32 v153, v153, v29
  v_sub_f32 v89, v89, v29
  v_sub_f32 v154, v154, v29
  v_sub_f32 v90, v90, v29
  v_sub_f32 v155, v155, v29
  v_sub_f32 v91, v91, v29
  v_sub_f32 v236, v236, v30
  v_sub_f32 v237, v237, v30
  v_sub_f32 v238, v238, v30
  v_sub_f32 v239, v239, v30
  v_sub_f32 v108, v108, v30
  v_sub_f32 v44, v44, v30
  v_sub_f32 v109, v109, v30
  v_sub_f32 v45, v45, v30
  v_sub_f32 v110, v110, v30
  v_sub_f32 v46, v46, v30
  v_sub_f32 v111, v111, v30
  v_sub_f32 v47, v47, v30
  v_sub_f32 v124, v124, v30
  v_sub_f32 v60, v60, v30
  v_sub_f32 v125, v125, v30
  v_sub_f32 v61, v61, v30
  v_sub_f32 v126, v126, v30
  v_sub_f32 v62, v62, v30
  v_sub_f32 v127, v127, v30
  v_sub_f32 v63, v63, v30
  v_sub_f32 v140, v140, v30
  v_sub_f32 v76, v76, v30
  v_sub_f32 v141, v141, v30
  v_sub_f32 v77, v77, v30
  v_sub_f32 v142, v142, v30
  v_sub_f32 v78, v78, v30
  v_sub_f32 v143, v143, v30
  v_sub_f32 v79, v79, v30
  v_sub_f32 v156, v156, v30
  v_sub_f32 v92, v92, v30
  v_sub_f32 v157, v157, v30
  v_sub_f32 v93, v93, v30
  v_sub_f32 v158, v158, v30
  v_sub_f32 v94, v94, v30
  v_sub_f32 v159, v159, v30
  v_sub_f32 v95, v95, v30
  v_sub_f32 v27, 0, v27
  v_sub_f32 v28, 0, v28
  v_sub_f32 v29, 0, v29
  v_sub_f32 v30, 0, v30
  v_exp_f32 v27, v27
  v_exp_f32 v28, v28
  v_exp_f32 v29, v29
  v_exp_f32 v30, v30
  s_nop 0
  v_mul_f32 v23, v23, v27
  v_mul_f32 v24, v24, v28
  v_mul_f32 v25, v25, v29
  v_mul_f32 v26, v26, v30
  v_accvgpr_read_b32 v240, a0
  v_accvgpr_read_b32 v241, a1
  v_accvgpr_read_b32 v242, a2
  v_accvgpr_read_b32 v243, a3
  v_mul_f32 v240, v240, v27
  v_mul_f32 v241, v241, v27
  v_mul_f32 v242, v242, v27
  v_mul_f32 v243, v243, v27
  v_accvgpr_write_b32 a0, v240
  v_accvgpr_write_b32 a1, v241
  v_accvgpr_write_b32 a2, v242
  v_accvgpr_write_b32 a3, v243
  v_accvgpr_read_b32 v240, a4
  v_accvgpr_read_b32 v241, a5
  v_accvgpr_read_b32 v242, a6
  v_accvgpr_read_b32 v243, a7
  v_mul_f32 v240, v240, v28
  v_mul_f32 v241, v241, v28
  v_mul_f32 v242, v242, v28
  v_mul_f32 v243, v243, v28
  v_accvgpr_write_b32 a4, v240
  v_accvgpr_write_b32 a5, v241
  v_accvgpr_write_b32 a6, v242
  v_accvgpr_write_b32 a7, v243
  v_accvgpr_read_b32 v240, a8
  v_accvgpr_read_b32 v241, a9
  v_accvgpr_read_b32 v242, a10
  v_accvgpr_read_b32 v243, a11
  v_mul_f32 v240, v240, v29
  v_mul_f32 v241, v241, v29
  v_mul_f32 v242, v242, v29
  v_mul_f32 v243, v243, v29
  v_accvgpr_write_b32 a8, v240
  v_accvgpr_write_b32 a9, v241
  v_accvgpr_write_b32 a10, v242
  v_accvgpr_write_b32 a11, v243
  v_accvgpr_read_b32 v240, a12
  v_accvgpr_read_b32 v241, a13
  v_accvgpr_read_b32 v242, a14
  v_accvgpr_read_b32 v243, a15
  v_mul_f32 v240, v240, v30
  v_mul_f32 v241, v241, v30
  v_mul_f32 v242, v242, v30
  v_mul_f32 v243, v243, v30
  v_accvgpr_write_b32 a12, v240
  v_accvgpr_write_b32 a13, v241
  v_accvgpr_write_b32 a14, v242
  v_accvgpr_write_b32 a15, v243
  v_accvgpr_read_b32 v240, a16
  v_accvgpr_read_b32 v241, a17
  v_accvgpr_read_b32 v242, a18
  v_accvgpr_read_b32 v243, a19
  v_mul_f32 v240, v240, v27
  v_mul_f32 v241, v241, v27
  v_mul_f32 v242, v242, v27
  v_mul_f32 v243, v243, v27
  v_accvgpr_write_b32 a16, v240
  v_accvgpr_write_b32 a17, v241
  v_accvgpr_write_b32 a18, v242
  v_accvgpr_write_b32 a19, v243
  v_accvgpr_read_b32 v240, a20
  v_accvgpr_read_b32 v241, a21
  v_accvgpr_read_b32 v242, a22
  v_accvgpr_read_b32 v243, a23
  v_mul_f32 v240, v240, v28
  v_mul_f32 v241, v241, v28
  v_mul_f32 v242, v242, v28
  v_mul_f32 v243, v243, v28
  v_accvgpr_write_b32 a20, v240
  v_accvgpr_write_b32 a21, v241
  v_accvgpr_write_b32 a22, v242
  v_accvgpr_write_b32 a23, v243
  v_accvgpr_read_b32 v240, a24
  v_accvgpr_read_b32 v241, a25
  v_accvgpr_read_b32 v242, a26
  v_accvgpr_read_b32 v243, a27
  v_mul_f32 v240, v240, v29
  v_mul_f32 v241, v241, v29
  v_mul_f32 v242, v242, v29
  v_mul_f32 v243, v243, v29
  v_accvgpr_write_b32 a24, v240
  v_accvgpr_write_b32 a25, v241
  v_accvgpr_write_b32 a26, v242
  v_accvgpr_write_b32 a27, v243
  v_accvgpr_read_b32 v240, a28
  v_accvgpr_read_b32 v241, a29
  v_accvgpr_read_b32 v242, a30
  v_accvgpr_read_b32 v243, a31
  v_mul_f32 v240, v240, v30
  v_mul_f32 v241, v241, v30
  v_mul_f32 v242, v242, v30
  v_mul_f32 v243, v243, v30
  v_accvgpr_write_b32 a28, v240
  v_accvgpr_write_b32 a29, v241
  v_accvgpr_write_b32 a30, v242
  v_accvgpr_write_b32 a31, v243
  v_accvgpr_read_b32 v240, a32
  v_accvgpr_read_b32 v241, a33
  v_accvgpr_read_b32 v242, a34
  v_accvgpr_read_b32 v243, a35
  v_mul_f32 v240, v240, v27
  v_mul_f32 v241, v241, v27
  v_mul_f32 v242, v242, v27
  v_mul_f32 v243, v243, v27
  v_accvgpr_write_b32 a32, v240
  v_accvgpr_write_b32 a33, v241
  v_accvgpr_write_b32 a34, v242
  v_accvgpr_write_b32 a35, v243
  v_accvgpr_read_b32 v240, a36
  v_accvgpr_read_b32 v241, a37
  v_accvgpr_read_b32 v242, a38
  v_accvgpr_read_b32 v243, a39
  v_mul_f32 v240, v240, v28
  v_mul_f32 v241, v241, v28
  v_mul_f32 v242, v242, v28
  v_mul_f32 v243, v243, v28
  v_accvgpr_write_b32 a36, v240
  v_accvgpr_write_b32 a37, v241
  v_accvgpr_write_b32 a38, v242
  v_accvgpr_write_b32 a39, v243
  v_accvgpr_read_b32 v240, a40
  v_accvgpr_read_b32 v241, a41
  v_accvgpr_read_b32 v242, a42
  v_accvgpr_read_b32 v243, a43
  v_mul_f32 v240, v240, v29
  v_mul_f32 v241, v241, v29
  v_mul_f32 v242, v242, v29
  v_mul_f32 v243, v243, v29
  v_accvgpr_write_b32 a40, v240
  v_accvgpr_write_b32 a41, v241
  v_accvgpr_write_b32 a42, v242
  v_accvgpr_write_b32 a43, v243
  v_accvgpr_read_b32 v240, a44
  v_accvgpr_read_b32 v241, a45
  v_accvgpr_read_b32 v242, a46
  v_accvgpr_read_b32 v243, a47
  v_mul_f32 v240, v240, v30
  v_mul_f32 v241, v241, v30
  v_mul_f32 v242, v242, v30
  v_mul_f32 v243, v243, v30
  v_accvgpr_write_b32 a44, v240
  v_accvgpr_write_b32 a45, v241
  v_accvgpr_write_b32 a46, v242
  v_accvgpr_write_b32 a47, v243
  v_accvgpr_read_b32 v240, a48
  v_accvgpr_read_b32 v241, a49
  v_accvgpr_read_b32 v242, a50
  v_accvgpr_read_b32 v243, a51
  v_mul_f32 v240, v240, v27
  v_mul_f32 v241, v241, v27
  v_mul_f32 v242, v242, v27
  v_mul_f32 v243, v243, v27
  v_accvgpr_write_b32 a48, v240
  v_accvgpr_write_b32 a49, v241
  v_accvgpr_write_b32 a50, v242
  v_accvgpr_write_b32 a51, v243
  v_accvgpr_read_b32 v240, a52
  v_accvgpr_read_b32 v241, a53
  v_accvgpr_read_b32 v242, a54
  v_accvgpr_read_b32 v243, a55
  v_mul_f32 v240, v240, v28
  v_mul_f32 v241, v241, v28
  v_mul_f32 v242, v242, v28
  v_mul_f32 v243, v243, v28
  v_accvgpr_write_b32 a52, v240
  v_accvgpr_write_b32 a53, v241
  v_accvgpr_write_b32 a54, v242
  v_accvgpr_write_b32 a55, v243
  v_accvgpr_read_b32 v240, a56
  v_accvgpr_read_b32 v241, a57
  v_accvgpr_read_b32 v242, a58
  v_accvgpr_read_b32 v243, a59
  v_mul_f32 v240, v240, v29
  v_mul_f32 v241, v241, v29
  v_mul_f32 v242, v242, v29
  v_mul_f32 v243, v243, v29
  v_accvgpr_write_b32 a56, v240
  v_accvgpr_write_b32 a57, v241
  v_accvgpr_write_b32 a58, v242
  v_accvgpr_write_b32 a59, v243
  v_accvgpr_read_b32 v240, a60
  v_accvgpr_read_b32 v241, a61
  v_accvgpr_read_b32 v242, a62
  v_accvgpr_read_b32 v243, a63
  v_mul_f32 v240, v240, v30
  v_mul_f32 v241, v241, v30
  v_mul_f32 v242, v242, v30
  v_mul_f32 v243, v243, v30
  v_accvgpr_write_b32 a60, v240
  v_accvgpr_write_b32 a61, v241
  v_accvgpr_write_b32 a62, v242
  v_accvgpr_write_b32 a63, v243
  v_accvgpr_read_b32 v240, a64
  v_accvgpr_read_b32 v241, a65
  v_accvgpr_read_b32 v242, a66
  v_accvgpr_read_b32 v243, a67
  v_mul_f32 v240, v240, v27
  v_mul_f32 v241, v241, v27
  v_mul_f32 v242, v242, v27
  v_mul_f32 v243, v243, v27
  v_accvgpr_write_b32 a64, v240
  v_accvgpr_write_b32 a65, v241
  v_accvgpr_write_b32 a66, v242
  v_accvgpr_write_b32 a67, v243
  v_accvgpr_read_b32 v240, a68
  v_accvgpr_read_b32 v241, a69
  v_accvgpr_read_b32 v242, a70
  v_accvgpr_read_b32 v243, a71
  v_mul_f32 v240, v240, v28
  v_mul_f32 v241, v241, v28
  v_mul_f32 v242, v242, v28
  v_mul_f32 v243, v243, v28
  v_accvgpr_write_b32 a68, v240
  v_accvgpr_write_b32 a69, v241
  v_accvgpr_write_b32 a70, v242
  v_accvgpr_write_b32 a71, v243
  v_accvgpr_read_b32 v240, a72
  v_accvgpr_read_b32 v241, a73
  v_accvgpr_read_b32 v242, a74
  v_accvgpr_read_b32 v243, a75
  v_mul_f32 v240, v240, v29
  v_mul_f32 v241, v241, v29
  v_mul_f32 v242, v242, v29
  v_mul_f32 v243, v243, v29
  v_accvgpr_write_b32 a72, v240
  v_accvgpr_write_b32 a73, v241
  v_accvgpr_write_b32 a74, v242
  v_accvgpr_write_b32 a75, v243
  v_accvgpr_read_b32 v240, a76
  v_accvgpr_read_b32 v241, a77
  v_accvgpr_read_b32 v242, a78
  v_accvgpr_read_b32 v243, a79
  v_mul_f32 v240, v240, v30
  v_mul_f32 v241, v241, v30
  v_mul_f32 v242, v242, v30
  v_mul_f32 v243, v243, v30
  v_accvgpr_write_b32 a76, v240
  v_accvgpr_write_b32 a77, v241
  v_accvgpr_write_b32 a78, v242
  v_accvgpr_write_b32 a79, v243
  v_accvgpr_read_b32 v240, a80
  v_accvgpr_read_b32 v241, a81
  v_accvgpr_read_b32 v242, a82
  v_accvgpr_read_b32 v243, a83
  v_mul_f32 v240, v240, v27
  v_mul_f32 v241, v241, v27
  v_mul_f32 v242, v242, v27
  v_mul_f32 v243, v243, v27
  v_accvgpr_write_b32 a80, v240
  v_accvgpr_write_b32 a81, v241
  v_accvgpr_write_b32 a82, v242
  v_accvgpr_write_b32 a83, v243
  v_accvgpr_read_b32 v240, a84
  v_accvgpr_read_b32 v241, a85
  v_accvgpr_read_b32 v242, a86
  v_accvgpr_read_b32 v243, a87
  v_mul_f32 v240, v240, v28
  v_mul_f32 v241, v241, v28
  v_mul_f32 v242, v242, v28
  v_mul_f32 v243, v243, v28
  v_accvgpr_write_b32 a84, v240
  v_accvgpr_write_b32 a85, v241
  v_accvgpr_write_b32 a86, v242
  v_accvgpr_write_b32 a87, v243
  v_accvgpr_read_b32 v240, a88
  v_accvgpr_read_b32 v241, a89
  v_accvgpr_read_b32 v242, a90
  v_accvgpr_read_b32 v243, a91
  v_mul_f32 v240, v240, v29
  v_mul_f32 v241, v241, v29
  v_mul_f32 v242, v242, v29
  v_mul_f32 v243, v243, v29
  v_accvgpr_write_b32 a88, v240
  v_accvgpr_write_b32 a89, v241
  v_accvgpr_write_b32 a90, v242
  v_accvgpr_write_b32 a91, v243
  v_accvgpr_read_b32 v240, a92
  v_accvgpr_read_b32 v241, a93
  v_accvgpr_read_b32 v242, a94
  v_accvgpr_read_b32 v243, a95
  v_mul_f32 v240, v240, v30
  v_mul_f32 v241, v241, v30
  v_mul_f32 v242, v242, v30
  v_mul_f32 v243, v243, v30
  v_accvgpr_write_b32 a92, v240
  v_accvgpr_write_b32 a93, v241
  v_accvgpr_write_b32 a94, v242
  v_accvgpr_write_b32 a95, v243
  v_accvgpr_read_b32 v240, a96
  v_accvgpr_read_b32 v241, a97
  v_accvgpr_read_b32 v242, a98
  v_accvgpr_read_b32 v243, a99
  v_mul_f32 v240, v240, v27
  v_mul_f32 v241, v241, v27
  v_mul_f32 v242, v242, v27
  v_mul_f32 v243, v243, v27
  v_accvgpr_write_b32 a96, v240
  v_accvgpr_write_b32 a97, v241
  v_accvgpr_write_b32 a98, v242
  v_accvgpr_write_b32 a99, v243
  v_accvgpr_read_b32 v240, a100
  v_accvgpr_read_b32 v241, a101
  v_accvgpr_read_b32 v242, a102
  v_accvgpr_read_b32 v243, a103
  v_mul_f32 v240, v240, v28
  v_mul_f32 v241, v241, v28
  v_mul_f32 v242, v242, v28
  v_mul_f32 v243, v243, v28
  v_accvgpr_write_b32 a100, v240
  v_accvgpr_write_b32 a101, v241
  v_accvgpr_write_b32 a102, v242
  v_accvgpr_write_b32 a103, v243
  v_accvgpr_read_b32 v240, a104
  v_accvgpr_read_b32 v241, a105
  v_accvgpr_read_b32 v242, a106
  v_accvgpr_read_b32 v243, a107
  v_mul_f32 v240, v240, v29
  v_mul_f32 v241, v241, v29
  v_mul_f32 v242, v242, v29
  v_mul_f32 v243, v243, v29
  v_accvgpr_write_b32 a104, v240
  v_accvgpr_write_b32 a105, v241
  v_accvgpr_write_b32 a106, v242
  v_accvgpr_write_b32 a107, v243
  v_accvgpr_read_b32 v240, a108
  v_accvgpr_read_b32 v241, a109
  v_accvgpr_read_b32 v242, a110
  v_accvgpr_read_b32 v243, a111
  v_mul_f32 v240, v240, v30
  v_mul_f32 v241, v241, v30
  v_mul_f32 v242, v242, v30
  v_mul_f32 v243, v243, v30
  v_accvgpr_write_b32 a108, v240
  v_accvgpr_write_b32 a109, v241
  v_accvgpr_write_b32 a110, v242
  v_accvgpr_write_b32 a111, v243
  v_accvgpr_read_b32 v240, a112
  v_accvgpr_read_b32 v241, a113
  v_accvgpr_read_b32 v242, a114
  v_accvgpr_read_b32 v243, a115
  v_mul_f32 v240, v240, v27
  v_mul_f32 v241, v241, v27
  v_mul_f32 v242, v242, v27
  v_mul_f32 v243, v243, v27
  v_accvgpr_write_b32 a112, v240
  v_accvgpr_write_b32 a113, v241
  v_accvgpr_write_b32 a114, v242
  v_accvgpr_write_b32 a115, v243
  v_accvgpr_read_b32 v240, a116
  v_accvgpr_read_b32 v241, a117
  v_accvgpr_read_b32 v242, a118
  v_accvgpr_read_b32 v243, a119
  v_mul_f32 v240, v240, v28
  v_mul_f32 v241, v241, v28
  v_mul_f32 v242, v242, v28
  v_mul_f32 v243, v243, v28
  v_accvgpr_write_b32 a116, v240
  v_accvgpr_write_b32 a117, v241
  v_accvgpr_write_b32 a118, v242
  v_accvgpr_write_b32 a119, v243
  v_accvgpr_read_b32 v240, a120
  v_accvgpr_read_b32 v241, a121
  v_accvgpr_read_b32 v242, a122
  v_accvgpr_read_b32 v243, a123
  v_mul_f32 v240, v240, v29
  v_mul_f32 v241, v241, v29
  v_mul_f32 v242, v242, v29
  v_mul_f32 v243, v243, v29
  v_accvgpr_write_b32 a120, v240
  v_accvgpr_write_b32 a121, v241
  v_accvgpr_write_b32 a122, v242
  v_accvgpr_write_b32 a123, v243
  v_accvgpr_read_b32 v240, a124
  v_accvgpr_read_b32 v241, a125
  v_accvgpr_read_b32 v242, a126
  v_accvgpr_read_b32 v243, a127
  v_mul_f32 v240, v240, v30
  v_mul_f32 v241, v241, v30
  v_mul_f32 v242, v242, v30
  v_mul_f32 v243, v243, v30
  v_accvgpr_write_b32 a124, v240
  v_accvgpr_write_b32 a125, v241
  v_accvgpr_write_b32 a126, v242
  v_accvgpr_write_b32 a127, v243
  v_exp_f32 v240, v96
  s_nop 0
  v_exp_f32 v241, v97
  s_nop 0
  v_add_f32 v250, v240, v241
  v_cvt_pk_bf16_f32 v192, v240, v241
  v_exp_f32 v242, v98
  s_nop 0
  v_add_f32 v250, v250, v242
  v_exp_f32 v243, v99
  s_nop 0
  v_add_f32 v250, v250, v243
  v_cvt_pk_bf16_f32 v193, v242, v243
  v_exp_f32 v244, v100
  s_nop 0
  v_exp_f32 v245, v101
  s_nop 0
  v_add_f32 v251, v244, v245
  v_cvt_pk_bf16_f32 v200, v244, v245
  v_exp_f32 v240, v102
  s_nop 0
  v_add_f32 v251, v251, v240
  v_exp_f32 v241, v103
  s_nop 0
  v_add_f32 v251, v251, v241
  v_cvt_pk_bf16_f32 v201, v240, v241
  v_exp_f32 v242, v104
  s_nop 0
  v_exp_f32 v243, v105
  s_nop 0
  v_add_f32 v252, v242, v243
  v_cvt_pk_bf16_f32 v208, v242, v243
  v_exp_f32 v244, v106
  s_nop 0
  v_add_f32 v252, v252, v244
  v_exp_f32 v245, v107
  s_nop 0
  v_add_f32 v252, v252, v245
  v_cvt_pk_bf16_f32 v209, v244, v245
  v_exp_f32 v240, v108
  s_nop 0
  v_exp_f32 v241, v109
  s_nop 0
  v_add_f32 v253, v240, v241
  v_cvt_pk_bf16_f32 v216, v240, v241
  v_exp_f32 v242, v110
  s_nop 0
  v_add_f32 v253, v253, v242
  v_exp_f32 v243, v111
  s_nop 0
  v_add_f32 v253, v253, v243
  v_cvt_pk_bf16_f32 v217, v242, v243
  v_exp_f32 v244, v112
  s_nop 0
  v_add_f32 v250, v250, v244
  v_exp_f32 v245, v113
  s_nop 0
  v_add_f32 v250, v250, v245
  v_cvt_pk_bf16_f32 v194, v244, v245
  v_exp_f32 v240, v114
  s_nop 0
  v_add_f32 v250, v250, v240
  v_exp_f32 v241, v115
  s_nop 0
  v_add_f32 v250, v250, v241
  v_cvt_pk_bf16_f32 v195, v240, v241
  v_exp_f32 v242, v116
  s_nop 0
  v_add_f32 v251, v251, v242
  v_exp_f32 v243, v117
  s_nop 0
  v_add_f32 v251, v251, v243
  v_cvt_pk_bf16_f32 v202, v242, v243
  v_exp_f32 v244, v118
  s_nop 0
  v_add_f32 v251, v251, v244
  v_exp_f32 v245, v119
  s_nop 0
  v_add_f32 v251, v251, v245
  v_cvt_pk_bf16_f32 v203, v244, v245
  v_exp_f32 v240, v120
  s_nop 0
  v_add_f32 v252, v252, v240
  v_exp_f32 v241, v121
  s_nop 0
  v_add_f32 v252, v252, v241
  v_cvt_pk_bf16_f32 v210, v240, v241
  v_exp_f32 v242, v122
  s_nop 0
  v_add_f32 v252, v252, v242
  v_exp_f32 v243, v123
  s_nop 0
  v_add_f32 v252, v252, v243
  v_cvt_pk_bf16_f32 v211, v242, v243
  v_exp_f32 v244, v124
  s_nop 0
  v_add_f32 v253, v253, v244
  v_exp_f32 v245, v125
  s_nop 0
  v_add_f32 v253, v253, v245
  v_cvt_pk_bf16_f32 v218, v244, v245
  v_exp_f32 v240, v126
  s_nop 0
  v_add_f32 v253, v253, v240
  v_exp_f32 v241, v127
  s_nop 0
  v_add_f32 v253, v253, v241
  v_cvt_pk_bf16_f32 v219, v240, v241
  v_exp_f32 v242, v128
  s_nop 0
  v_add_f32 v250, v250, v242
  v_exp_f32 v243, v129
  s_nop 0
  v_add_f32 v250, v250, v243
  v_cvt_pk_bf16_f32 v196, v242, v243
  v_exp_f32 v244, v130
  s_nop 0
  v_add_f32 v250, v250, v244
  v_exp_f32 v245, v131
  s_nop 0
  v_add_f32 v250, v250, v245
  v_cvt_pk_bf16_f32 v197, v244, v245
  v_exp_f32 v240, v132
  s_nop 0
  v_add_f32 v251, v251, v240
  v_exp_f32 v241, v133
  s_nop 0
  v_add_f32 v251, v251, v241
  v_cvt_pk_bf16_f32 v204, v240, v241
  v_exp_f32 v242, v134
  s_nop 0
  v_add_f32 v251, v251, v242
  v_exp_f32 v243, v135
  s_nop 0
  v_add_f32 v251, v251, v243
  v_cvt_pk_bf16_f32 v205, v242, v243
  v_exp_f32 v244, v136
  s_nop 0
  v_add_f32 v252, v252, v244
  v_exp_f32 v245, v137
  s_nop 0
  v_add_f32 v252, v252, v245
  v_cvt_pk_bf16_f32 v212, v244, v245
  v_exp_f32 v240, v138
  s_nop 0
  v_add_f32 v252, v252, v240
  v_exp_f32 v241, v139
  s_nop 0
  v_add_f32 v252, v252, v241
  v_cvt_pk_bf16_f32 v213, v240, v241
  v_exp_f32 v242, v140
  s_nop 0
  v_add_f32 v253, v253, v242
  v_exp_f32 v243, v141
  s_nop 0
  v_add_f32 v253, v253, v243
  v_cvt_pk_bf16_f32 v220, v242, v243
  v_exp_f32 v244, v142
  s_nop 0
  v_add_f32 v253, v253, v244
  v_exp_f32 v245, v143
  s_nop 0
  v_add_f32 v253, v253, v245
  v_cvt_pk_bf16_f32 v221, v244, v245
  v_exp_f32 v240, v144
  s_nop 0
  v_add_f32 v250, v250, v240
  v_exp_f32 v241, v145
  s_nop 0
  v_add_f32 v250, v250, v241
  v_cvt_pk_bf16_f32 v198, v240, v241
  v_exp_f32 v242, v146
  s_nop 0
  v_add_f32 v250, v250, v242
  v_exp_f32 v243, v147
  s_nop 0
  v_add_f32 v250, v250, v243
  v_cvt_pk_bf16_f32 v199, v242, v243
  v_exp_f32 v244, v148
  s_nop 0
  v_add_f32 v251, v251, v244
  v_exp_f32 v245, v149
  s_nop 0
  v_add_f32 v251, v251, v245
  v_cvt_pk_bf16_f32 v206, v244, v245
  v_exp_f32 v240, v150
  s_nop 0
  v_add_f32 v251, v251, v240
  v_exp_f32 v241, v151
  s_nop 0
  v_add_f32 v251, v251, v241
  v_cvt_pk_bf16_f32 v207, v240, v241
  v_exp_f32 v242, v152
  s_nop 0
  v_add_f32 v252, v252, v242
  v_exp_f32 v243, v153
  s_nop 0
  v_add_f32 v252, v252, v243
  v_cvt_pk_bf16_f32 v214, v242, v243
  v_exp_f32 v244, v154
  s_nop 0
  v_add_f32 v252, v252, v244
  v_exp_f32 v245, v155
  s_nop 0
  v_add_f32 v252, v252, v245
  v_cvt_pk_bf16_f32 v215, v244, v245
  v_exp_f32 v240, v156
  s_nop 0
  v_add_f32 v253, v253, v240
  v_exp_f32 v241, v157
  s_nop 0
  v_add_f32 v253, v253, v241
  v_cvt_pk_bf16_f32 v222, v240, v241
  v_exp_f32 v242, v158
  s_nop 0
  v_add_f32 v253, v253, v242
  v_exp_f32 v243, v159
  s_nop 0
  v_add_f32 v253, v253, v243
  v_cvt_pk_bf16_f32 v223, v242, v243
  v_exp_f32 v240, v32
  s_nop 0
  v_exp_f32 v241, v33
  s_nop 0
  v_add_f32 v246, v240, v241
  v_cvt_pk_bf16_f32 v160, v240, v241
  v_exp_f32 v242, v34
  s_nop 0
  v_add_f32 v246, v246, v242
  v_exp_f32 v243, v35
  s_nop 0
  v_add_f32 v246, v246, v243
  v_cvt_pk_bf16_f32 v161, v242, v243
  s_nop 1
  s_cmp_eq_u32 s81, 0
  s_cbranch_scc1 LB_back0
  s_cmp_eq_u32 s81, 2
  s_cbranch_scc1 LB_back2
  s_cmp_eq_u32 s81, 4
  s_cbranch_scc1 LB_back4
  s_cmp_eq_u32 s81, 6
  s_cbranch_scc1 LB_back6
  s_branch LB_back8
LC_slowh:
  s_mov_b32 s75, 0
LC_slh:
  buffer_load_dword v10, v9, s[44:47], 0 offen offset:0 sc1
  s_waitcnt vmcnt(0)
  v_cmp_ne_u32 vcc, 128, v10
  s_add_u32 s75, s75, 1
  s_nop 0
  s_cbranch_vccz LC_backh
  s_cmp_lt_u32 s75, 0x40000
  s_cbranch_scc1 LC_slh
  s_branch LC_backh
LC_slowo:
  s_mov_b32 s75, 0
LC_slo:
  buffer_load_dword v10, v9, s[44:47], 0 offen offset:128 sc1
  s_waitcnt vmcnt(0)
  v_cmp_ne_u32 vcc, 128, v10
  s_add_u32 s75, s75, 1
  s_nop 0
  s_cbranch_vccz LC_backo
  s_cmp_lt_u32 s75, 0x40000
  s_cbranch_scc1 LC_slo
  s_branch LC_backo
LC_slowd:
  s_mov_b32 s75, 0
LC_sld:
  buffer_load_dword v10, v13, s[44:47], 0 offen offset:0 sc1
  s_waitcnt vmcnt(0)
  v_cmp_ne_u32 vcc, 1, v10
  s_add_u32 s75, s75, 1
  s_nop 0
  s_cbranch_vccz LC_backd
  s_cmp_lt_u32 s75, 0x40000
  s_cbranch_scc1 LC_sld
  s_branch LC_backd
LC_sloww:
  s_mov_b32 s75, 0
LC_slw:
  buffer_load_dword v10, v13, s[44:47], 0 offen offset:4 sc1
  s_waitcnt vmcnt(0)
  v_cmp_ne_u32 vcc, 1, v10
  s_add_u32 s75, s75, 1
  s_nop 0
  s_cbranch_vccz LC_backw
  s_cmp_lt_u32 s75, 0x40000
  s_cbranch_scc1 LC_slw
  s_branch LC_backw
LB_drain:
  s_waitcnt vmcnt(0) lgkmcnt(0)
  buffer_load_dword v10, v9, s[44:47], 0 offen offset:0 sc1
  s_barrier
  s_add_u32 s73, s88, 0
  s_add_u32 s73, s73, s86
  s_lshl_b32 s68, s73, 17
  s_lshl_b32 s70, s73, 13
  s_add_u32 s71, s70, 4096
  s_add_u32 m0, s74, 0
  v_exp_f32 v240, v132
  v_exp_f32 v241, v133
  v_add_f32 v251, v251, v240
  buffer_load_dwordx4 v1, s[48:51], s68 offen offset:0 lds
  v_exp_f32 v242, v134
  v_add_f32 v251, v251, v241
  v_cvt_pk_bf16_f32 v204, v240, v241
  v_exp_f32 v243, v135
  v_add_f32 v251, v251, v242
  v_exp_f32 v244, v136
  v_add_f32 v251, v251, v243
  v_cvt_pk_bf16_f32 v205, v242, v243
  v_exp_f32 v245, v137
  v_add_f32 v252, v252, v244
  buffer_load_dwordx4 v2, s[48:51], s68 offen offset:1032 lds
  v_exp_f32 v240, v138
  v_add_f32 v252, v252, v245
  v_cvt_pk_bf16_f32 v212, v244, v245
  v_exp_f32 v241, v139
  v_add_f32 v252, v252, v240
  v_exp_f32 v242, v140
  v_add_f32 v252, v252, v241
  v_cvt_pk_bf16_f32 v213, v240, v241
  v_exp_f32 v243, v141
  v_add_f32 v253, v253, v242
  buffer_load_dwordx4 v3, s[48:51], s68 offen offset:2064 lds
  v_exp_f32 v244, v142
  v_add_f32 v253, v253, v243
  v_cvt_pk_bf16_f32 v220, v242, v243
  v_exp_f32 v245, v143
  v_add_f32 v253, v253, v244
  v_exp_f32 v240, v144
  v_add_f32 v253, v253, v245
  v_cvt_pk_bf16_f32 v221, v244, v245
  v_exp_f32 v241, v145
  v_add_f32 v250, v250, v240
  buffer_load_dwordx4 v4, s[48:51], s68 offen offset:3096 lds
  v_exp_f32 v242, v146
  v_add_f32 v250, v250, v241
  v_cvt_pk_bf16_f32 v198, v240, v241
  v_exp_f32 v243, v147
  v_add_f32 v250, v250, v242
  v_exp_f32 v244, v148
  ds_read_b64_tr_b16 a[192:193], v13 offset:49152
  v_add_f32 v250, v250, v243
  v_cvt_pk_bf16_f32 v199, v242, v243
  v_exp_f32 v245, v149
  ds_read_b64_tr_b16 a[194:195], v13 offset:53248
  v_add_f32 v251, v251, v244
  v_exp_f32 v240, v150
  ds_read_b64_tr_b16 a[196:197], v13 offset:57344
  v_add_f32 v251, v251, v245
  v_cvt_pk_bf16_f32 v206, v244, v245
  v_exp_f32 v241, v151
  ds_read_b64_tr_b16 a[198:199], v13 offset:61440
  v_add_f32 v251, v251, v240
  s_waitcnt vmcnt(4)
  v_cmp_ne_u32 vcc, 128, v10
  s_nop 1
  s_cbranch_vccnz LC_slowh
LC_backh:
  v_exp_f32 v242, v152
  ds_read_b64_tr_b16 a[200:201], v14 offset:49152
  v_add_f32 v251, v251, v241
  v_cvt_pk_bf16_f32 v207, v240, v241
  buffer_load_dwordx4 a[128:131], v5, s[60:63], s70 offen sc1
  v_exp_f32 v243, v153
  ds_read_b64_tr_b16 a[202:203], v14 offset:53248
  v_add_f32 v252, v252, v242
  buffer_load_dwordx4 a[132:135], v6, s[60:63], s70 offen sc1
  v_exp_f32 v244, v154
  ds_read_b64_tr_b16 a[204:205], v14 offset:57344
  v_add_f32 v252, v252, v243
  v_cvt_pk_bf16_f32 v214, v242, v243
  buffer_load_dwordx4 a[136:139], v7, s[60:63], s70 offen sc1
  v_exp_f32 v245, v155
  ds_read_b64_tr_b16 a[206:207], v14 offset:61440
  v_add_f32 v252, v252, v244
  buffer_load_dwordx4 a[140:143], v8, s[60:63], s70 offen sc1
  v_exp_f32 v240, v156
  ds_read_b64_tr_b16 a[208:209], v13 offset:49664
  v_add_f32 v252, v252, v245
  v_cvt_pk_bf16_f32 v215, v244, v245
  buffer_load_dwordx4 a[144:147], v5, s[60:63], s71 offen sc1
  v_exp_f32 v241, v157
  ds_read_b64_tr_b16 a[210:211], v13 offset:53760
  v_add_f32 v253, v253, v240
  buffer_load_dwordx4 a[148:151], v6, s[60:63], s71 offen sc1
  v_exp_f32 v242, v158
  ds_read_b64_tr_b16 a[212:213], v13 offset:57856
  v_add_f32 v253, v253, v241
  v_cvt_pk_bf16_f32 v222, v240, v241
  buffer_load_dwordx4 a[152:155], v7, s[60:63], s71 offen sc1
  v_exp_f32 v243, v159
  ds_read_b64_tr_b16 a[214:215], v13 offset:61952
  v_add_f32 v253, v253, v242
  buffer_load_dwordx4 a[156:159], v8, s[60:63], s71 offen sc1
  ds_read_b64_tr_b16 a[216:217], v14 offset:49664
  v_add_f32 v253, v253, v243
  v_cvt_pk_bf16_f32 v223, v242, v243
  ds_read_b64_tr_b16 a[218:219], v14 offset:53760
  v_max3_f32 v254, v250, v251, v252
  v_max_f32 v254, v254, v253
  ds_read_b64_tr_b16 a[220:221], v14 offset:57856
  v_cmp_nge_f32 vcc, s82, v254
  s_waitcnt lgkmcnt(11)
  ds_read_b64_tr_b16 a[222:223], v14 offset:61952
  s_nop 1
  s_cbranch_vccnz LB_stub4
LB_back4:
  v_mfma_f32_16x16x32_bf16 a[0:3], a[192:195], v[192:195], a[0:3]
  s_add_u32 s73, s89, 0
  s_add_u32 s73, s73, s86
  s_lshl_b32 s68, s73, 17
  s_lshl_b32 s70, s73, 13
  s_add_u32 s71, s70, 4096
  s_add_u32 m0, s74, 16512
  v_mfma_f32_16x16x32_bf16 a[0:3], a[196:199], v[196:199], a[0:3]
  ds_read_b64_tr_b16 a[224:225], v13 offset:50176
  v_mfma_f32_16x16x32_bf16 a[4:7], a[196:199], v[204:207], a[4:7]
  buffer_load_dwordx4 v1, s[48:51], s68 offen offset:0 lds
  v_and_b32 v32, 63, v0
  v_lshrrev_b32 v33, 4, v32
  v_mfma_f32_16x16x32_bf16 a[4:7], a[192:195], v[200:203], a[4:7]
  ds_read_b64_tr_b16 a[226:227], v13 offset:54272
  v_bfe_u32 v34, v32, 2, 2
  v_and_b32 v35, 3, v32
  v_mfma_f32_16x16x32_bf16 a[8:11], a[192:195], v[208:211], a[8:11]
  v_lshlrev_b32 v36, 4, v32
  v_mfma_f32_16x16x32_bf16 a[8:11], a[196:199], v[212:215], a[8:11]
  s_lshl_b32 s72, s15, 2
  s_add_u32 s72, s72, s9
  ds_read_b64_tr_b16 a[228:229], v13 offset:58368
  v_add_f32 v23, v23, v250
  v_mfma_f32_16x16x32_bf16 a[12:15], a[196:199], v[220:223], a[12:15]
  s_lshl_b32 s72, s72, 19
  s_add_u32 s75, s13, 0
  v_mfma_f32_16x16x32_bf16 a[12:15], a[192:195], v[216:219], a[12:15]
  s_waitcnt lgkmcnt(11)
  s_and_b32 s75, s75, 3
  ds_read_b64_tr_b16 a[230:231], v13 offset:62464
  buffer_load_dwordx4 a[160:163], v5, s[60:63], s70 offen sc1
  v_mfma_f32_16x16x32_bf16 a[16:19], a[200:203], v[192:195], a[16:19]
  s_lshl_b32 s75, s75, 10
  s_add_u32 s75, s75, s72
  v_mfma_f32_16x16x32_bf16 a[16:19], a[204:207], v[196:199], a[16:19]
  ds_read_b64_tr_b16 a[232:233], v14 offset:50176
  v_add_f32 v24, v24, v251
  v_add_u32 v15, s75, v36
  v_mfma_f32_16x16x32_bf16 a[20:23], a[204:207], v[204:207], a[20:23]
  s_add_u32 s75, s13, 1
  s_and_b32 s75, s75, 3
  v_mfma_f32_16x16x32_bf16 a[20:23], a[200:203], v[200:203], a[20:23]
  s_lshl_b32 s75, s75, 10
  s_add_u32 s75, s75, s72
  ds_read_b64_tr_b16 a[234:235], v14 offset:54272
  v_mfma_f32_16x16x32_bf16 a[24:27], a[200:203], v[208:211], a[24:27]
  buffer_load_dwordx4 a[164:167], v6, s[60:63], s70 offen sc1
  v_add_u32 v16, s75, v36
  v_mfma_f32_16x16x32_bf16 a[24:27], a[204:207], v[212:215], a[24:27]
  s_add_u32 s75, s13, 2
  s_and_b32 s75, s75, 3
  ds_read_b64_tr_b16 a[236:237], v14 offset:58368
  v_add_f32 v25, v25, v252
  v_mfma_f32_16x16x32_bf16 a[28:31], a[204:207], v[220:223], a[28:31]
  s_lshl_b32 s75, s75, 10
  s_add_u32 s75, s75, s72
  v_mfma_f32_16x16x32_bf16 a[28:31], a[200:203], v[216:219], a[28:31]
  s_waitcnt lgkmcnt(11)
  ds_read_b64_tr_b16 a[238:239], v14 offset:62464
  v_add_u32 v17, s75, v36
  v_mfma_f32_16x16x32_bf16 a[32:35], a[208:211], v[192:195], a[32:35]
  s_add_u32 s75, s13, 3
  s_and_b32 s75, s75, 3
  v_mfma_f32_16x16x32_bf16 a[32:35], a[212:215], v[196:199], a[32:35]
  s_lshl_b32 s75, s75, 10
  ds_read_b64_tr_b16 a[240:241], v13 offset:50688
  buffer_load_dwordx4 v2, s[48:51], s68 offen offset:1032 lds
  v_add_f32 v26, v26, v253
  v_mfma_f32_16x16x32_bf16 a[36:39], a[212:215], v[204:207], a[36:39]
  s_add_u32 s75, s75, s72
  v_add_u32 v18, s75, v36
  v_mfma_f32_16x16x32_bf16 a[36:39], a[208:211], v[200:203], a[36:39]
  ds_read_b64_tr_b16 a[242:243], v13 offset:54784
  v_mul_u32_u24 v37, 1032, v33
  v_lshl_add_u32 v37, v34, 6, v37
  v_mfma_f32_16x16x32_bf16 a[40:43], a[208:211], v[208:211], a[40:43]
  v_lshl_add_u32 v19, v35, 4, v37
  v_mfma_f32_16x16x32_bf16 a[40:43], a[212:215], v[212:215], a[40:43]
  ds_read_b64_tr_b16 a[244:245], v13 offset:58880
  v_add_u32 v20, 66048, v19
  s_lshl_b32 s72, s15, 19
  v_mfma_f32_16x16x32_bf16 a[44:47], a[212:215], v[220:223], a[44:47]
  s_lshl_b32 s75, s9, 17
  buffer_load_dwordx4 a[168:171], v7, s[60:63], s70 offen sc1
  v_mfma_f32_16x16x32_bf16 a[44:47], a[208:211], v[216:219], a[44:47]
  s_waitcnt lgkmcnt(11)
  s_add_u32 s72, s72, s75
  s_lshl_b32 s75, s13, 15
  ds_read_b64_tr_b16 a[246:247], v13 offset:62976
  v_mfma_f32_16x16x32_bf16 a[48:51], a[216:219], v[192:195], a[48:51]
  s_add_u32 s72, s72, s75
  s_lshl_b32 s75, s14, 8
  v_mov_b32 v254, v23
  s_nop 1
  v_permlane32_swap_b32 v23, v254
  v_add_f32 v23, v23, v254
  v_mov_b32 v254, v23
  s_nop 1
  v_permlane16_swap_b32 v23, v254
  v_add_f32 v23, v23, v254
  v_rcp_f32 v23, v23
  v_mfma_f32_16x16x32_bf16 a[48:51], a[220:223], v[196:199], a[48:51]
  s_add_u32 s72, s72, s75
  ds_read_b64_tr_b16 a[248:249], v14 offset:50688
  v_mfma_f32_16x16x32_bf16 a[52:55], a[220:223], v[204:207], a[52:55]
  v_and_b32 v37, 15, v32
  v_lshlrev_b32 v37, 11, v37
  v_mfma_f32_16x16x32_bf16 a[52:55], a[216:219], v[200:203], a[52:55]
  ds_read_b64_tr_b16 a[250:251], v14 offset:54784
  buffer_load_dwordx4 a[172:175], v8, s[60:63], s70 offen sc1
  v_lshl_add_u32 v37, v33, 4, v37
  v_add_u32 v21, s72, v37
  v_mfma_f32_16x16x32_bf16 a[56:59], a[216:219], v[208:211], a[56:59]
  s_lshl_b32 s72, s15, 5
  v_mfma_f32_16x16x32_bf16 a[56:59], a[220:223], v[212:215], a[56:59]
  s_lshl_b32 s75, s14, 2
  s_add_u32 s72, s72, s75
  ds_read_b64_tr_b16 a[252:253], v14 offset:58880
  v_mfma_f32_16x16x32_bf16 a[60:63], a[220:223], v[220:223], a[60:63]
  s_mul_i32 s75, s9, 12288
  v_mfma_f32_16x16x32_bf16 a[60:63], a[216:219], v[216:219], a[60:63]
  s_waitcnt lgkmcnt(11)
  ds_read_b64_tr_b16 a[254:255], v14 offset:62976
  v_add_u32 v37, s75, v36
  s_add_u32 s75, s13, 0
  v_mfma_f32_16x16x32_bf16 a[64:67], a[224:227], v[192:195], a[64:67]
  s_and_b32 s75, s75, 3
  s_add_u32 s75, s75, s72
  buffer_load_dwordx4 v3, s[48:51], s68 offen offset:2064 lds
  v_mov_b32 v254, v24
  s_nop 1
  v_permlane32_swap_b32 v24, v254
  v_add_f32 v24, v24, v254
  v_mov_b32 v254, v24
  s_nop 1
  v_permlane16_swap_b32 v24, v254
  v_add_f32 v24, v24, v254
  v_rcp_f32 v24, v24
  v_mfma_f32_16x16x32_bf16 a[64:67], a[228:231], v[196:199], a[64:67]
  s_mul_i32 s75, s75, 49152
  v_mfma_f32_16x16x32_bf16 a[68:71], a[228:231], v[204:207], a[68:71]
  s_add_u32 s75, s75, 0
  v_add_u32 v12, s75, v37
  v_mfma_f32_16x16x32_bf16 a[68:71], a[224:227], v[200:203], a[68:71]
  s_add_u32 s75, s13, 3
  v_mfma_f32_16x16x32_bf16 a[72:75], a[224:227], v[208:211], a[72:75]
  s_and_b32 s75, s75, 3
  s_add_u32 s75, s75, s72
  v_mfma_f32_16x16x32_bf16 a[72:75], a[228:231], v[212:215], a[72:75]
  s_mul_i32 s75, s75, 49152
  s_add_u32 s75, s75, 0
  buffer_load_dwordx4 a[176:179], v5, s[60:63], s71 offen sc1
  v_mfma_f32_16x16x32_bf16 a[76:79], a[228:231], v[220:223], a[76:79]
  v_add_u32 v27, s75, v37
  v_mfma_f32_16x16x32_bf16 a[76:79], a[224:227], v[216:219], a[76:79]
  s_waitcnt lgkmcnt(8)
  s_add_u32 s75, s13, 2
  s_and_b32 s75, s75, 3
  v_mfma_f32_16x16x32_bf16 a[80:83], a[232:235], v[192:195], a[80:83]
  s_add_u32 s75, s75, s72
  s_mul_i32 s75, s75, 49152
  v_mov_b32 v254, v25
  s_nop 1
  v_permlane32_swap_b32 v25, v254
  v_add_f32 v25, v25, v254
  v_mov_b32 v254, v25
  s_nop 1
  v_permlane16_swap_b32 v25, v254
  v_add_f32 v25, v25, v254
  v_rcp_f32 v25, v25
  v_mfma_f32_16x16x32_bf16 a[80:83], a[236:239], v[196:199], a[80:83]
  s_add_u32 s75, s75, 4096
  v_mfma_f32_16x16x32_bf16 a[84:87], a[236:239], v[204:207], a[84:87]
  buffer_load_dwordx4 a[180:183], v6, s[60:63], s71 offen sc1
  v_add_u32 v28, s75, v37
  s_add_u32 s75, s13, 1
  v_mfma_f32_16x16x32_bf16 a[84:87], a[232:235], v[200:203], a[84:87]
  s_and_b32 s75, s75, 3
  v_mfma_f32_16x16x32_bf16 a[88:91], a[232:235], v[208:211], a[88:91]
  s_add_u32 s75, s75, s72
  s_mul_i32 s75, s75, 49152
  v_mfma_f32_16x16x32_bf16 a[88:91], a[236:239], v[212:215], a[88:91]
  s_add_u32 s75, s75, 8192
  v_add_u32 v29, s75, v37
  v_mfma_f32_16x16x32_bf16 a[92:95], a[236:239], v[220:223], a[92:95]
  s_add_u32 s75, s13, s72
  v_mfma_f32_16x16x32_bf16 a[92:95], a[232:235], v[216:219], a[92:95]
  s_waitcnt lgkmcnt(4)
  buffer_load_dwordx4 v4, s[48:51], s68 offen offset:3096 lds
  s_lshl_b32 s75, s75, 7
  v_mov_b32 v30, s75
  v_mfma_f32_16x16x32_bf16 a[96:99], a[240:243], v[192:195], a[96:99]
  v_min_u32 v38, 2, v35
  v_mov_b32 v254, v26
  s_nop 1
  v_permlane32_swap_b32 v26, v254
  v_add_f32 v26, v26, v254
  v_mov_b32 v254, v26
  s_nop 1
  v_permlane16_swap_b32 v26, v254
  v_add_f32 v26, v26, v254
  v_rcp_f32 v26, v26
  v_mfma_f32_16x16x32_bf16 a[96:99], a[244:247], v[196:199], a[96:99]
  s_add_u32 s75, s13, 1
  v_add_u32 v38, s75, v38
  v_mfma_f32_16x16x32_bf16 a[100:103], a[244:247], v[204:207], a[100:103]
  v_and_b32 v38, 3, v38
  v_add_u32 v38, s72, v38
  v_mfma_f32_16x16x32_bf16 a[100:103], a[240:243], v[200:203], a[100:103]
  v_lshlrev_b32 v38, 7, v38
  v_mfma_f32_16x16x32_bf16 a[104:107], a[240:243], v[208:211], a[104:107]
  s_mov_b32 s52, s20
  s_and_b32 s53, s21, 0xffff
  buffer_load_dwordx4 a[184:187], v7, s[60:63], s71 offen sc1
  v_mfma_f32_16x16x32_bf16 a[104:107], a[244:247], v[212:215], a[104:107]
  s_mov_b32 s54, 8388608
  s_mov_b32 s55, 0x20000
  v_mfma_f32_16x16x32_bf16 a[108:111], a[244:247], v[220:223], a[108:111]
  s_mov_b32 s56, s30
  v_mfma_f32_16x16x32_bf16 a[108:111], a[240:243], v[216:219], a[108:111]
  s_waitcnt lgkmcnt(0)
  s_and_b32 s57, s31, 0xffff
  s_mov_b32 s58, 12582912
  v_mfma_f32_16x16x32_bf16 a[112:115], a[248:251], v[192:195], a[112:115]
  s_mov_b32 s59, 0x20000
  v_mfma_f32_16x16x32_bf16 a[112:115], a[252:255], v[196:199], a[112:115]
  s_mov_b32 s36, s26
  s_and_b32 s37, s27, 0xffff
  buffer_load_dwordx4 a[188:191], v8, s[60:63], s71 offen sc1
  v_mfma_f32_16x16x32_bf16 a[116:119], a[252:255], v[204:207], a[116:119]
  s_mov_b32 s38, 4194304
  s_mov_b32 s39, 0x20000
  v_mfma_f32_16x16x32_bf16 a[116:119], a[248:251], v[200:203], a[116:119]
  s_mov_b32 s40, s28
  v_mfma_f32_16x16x32_bf16 a[120:123], a[248:251], v[208:211], a[120:123]
  s_and_b32 s41, s29, 0xffff
  s_mov_b32 s42, 4194304
  v_mfma_f32_16x16x32_bf16 a[120:123], a[252:255], v[212:215], a[120:123]
  s_mov_b32 s43, 0x20000
  v_mfma_f32_16x16x32_bf16 a[124:127], a[252:255], v[220:223], a[124:127]
  v_mfma_f32_16x16x32_bf16 a[124:127], a[248:251], v[216:219], a[124:127]
  v_mov_b32 v13, v38
  s_add_u32 s73, s90, 0
  s_add_u32 s73, s73, s86
  s_lshl_b32 s68, s73, 17
  s_lshl_b32 s70, s73, 13
  s_add_u32 s71, s70, 4096
  s_add_u32 m0, s74, 33024
  s_nop 0
  buffer_load_dwordx4 v1, s[48:51], s68 offen offset:0 lds
  buffer_load_dwordx4 a[192:195], v5, s[60:63], s70 offen sc1
  buffer_load_dwordx4 a[196:199], v6, s[60:63], s70 offen sc1
  buffer_load_dwordx4 v2, s[48:51], s68 offen offset:1032 lds
  buffer_load_dwordx4 a[200:203], v7, s[60:63], s70 offen sc1
  buffer_load_dwordx4 a[204:207], v8, s[60:63], s70 offen sc1
  buffer_load_dwordx4 v3, s[48:51], s68 offen offset:2064 lds
  buffer_load_dwordx4 a[208:211], v5, s[60:63], s71 offen sc1
  buffer_load_dwordx4 a[212:215], v6, s[60:63], s71 offen sc1
  buffer_load_dwordx4 v4, s[48:51], s68 offen offset:3096 lds
  buffer_load_dwordx4 a[216:219], v7, s[60:63], s71 offen sc1
  buffer_load_dwordx4 a[220:223], v8, s[60:63], s71 offen sc1
  v_and_b32 v96, 15, v0
  v_bfe_u32 v97, v0, 4, 2
  v_lshlrev_b32 v97, 2, v97
  v_sub_u32 v98, v96, v97
  v_mov_b32 v99, 0x3f80
  v_mov_b32 v240, 0
  v_mov_b32 v241, 0
  v_mov_b32 v242, 0
  v_mov_b32 v243, 0
  v_mov_b32 v244, 0
  v_mov_b32 v245, 0
  v_cmp_eq_u32 vcc, 0, v98
  s_nop 1
  v_cndmask_b32 v242, v242, v99, vcc
  v_cmp_eq_u32 vcc, 1, v98
  s_nop 1
  v_cndmask_b32 v242, v242, 1.0, vcc
  v_cmp_eq_u32 vcc, 2, v98
  s_nop 1
  v_cndmask_b32 v243, v243, v99, vcc
  v_cmp_eq_u32 vcc, 3, v98
  s_nop 1
  v_cndmask_b32 v243, v243, 1.0, vcc
  s_add_u32 s76, s30, 12582912
  s_addc_u32 s77, s31, 0
  s_and_b32 s77, s77, 0xffff
  s_mov_b32 s78, 12582912
  s_mov_b32 s79, 0x20000
  s_waitcnt vmcnt(24)
  s_barrier
  ds_read_b64_tr_b16 a[224:225], v19 offset:0
  ds_read_b64_tr_b16 a[226:227], v19 offset:4128
  ds_read_b64_tr_b16 a[228:229], v19 offset:8256
  ds_read_b64_tr_b16 a[230:231], v19 offset:12384
  ds_read_b64_tr_b16 a[232:233], v19 offset:8
  ds_read_b64_tr_b16 a[234:235], v19 offset:4136
  ds_read_b64_tr_b16 a[236:237], v19 offset:8264
  ds_read_b64_tr_b16 a[238:239], v19 offset:12392
  s_waitcnt lgkmcnt(4)
  v_mfma_f32_16x16x32_bf16 v[64:67], a[224:227], a[132:135], 0
  s_add_u32 s73, s91, 0
  s_add_u32 s73, s73, s86
  s_lshl_b32 s68, s73, 17
  ds_read_b64_tr_b16 a[240:241], v19 offset:256
  v_mfma_f32_16x16x32_bf16 v[64:67], a[228:231], a[148:151], v[64:67]
  s_lshl_b32 s70, s73, 13
  s_add_u32 s71, s70, 4096
  s_add_u32 m0, s74, 49536
  v_accvgpr_read_b32 v224, a0
  v_accvgpr_read_b32 v225, a1
  v_mfma_f32_16x16x32_bf16 v[96:99], a[224:227], a[136:139], 0
  ds_read_b64_tr_b16 a[242:243], v19 offset:4384
  buffer_load_dwordx4 v1, s[48:51], s68 offen offset:0 lds
  v_accvgpr_read_b32 v226, a2
  v_mfma_f32_16x16x32_bf16 v[96:99], a[228:231], a[152:155], v[96:99]
  v_accvgpr_read_b32 v227, a3
  v_accvgpr_read_b32 v228, a16
  v_mfma_f32_16x16x32_bf16 v[128:131], a[224:227], a[140:143], 0
  ds_read_b64_tr_b16 a[244:245], v19 offset:8512
  v_accvgpr_read_b32 v229, a17
  v_mfma_f32_16x16x32_bf16 v[128:131], a[228:231], a[156:159], v[128:131]
  v_accvgpr_read_b32 v230, a18
  v_accvgpr_read_b32 v231, a19
  v_mfma_f32_16x16x32_bf16 v[32:35], a[224:227], a[128:131], 0
  ds_read_b64_tr_b16 a[246:247], v19 offset:12640
  v_mul_f32 v224, v224, v23
  v_mfma_f32_16x16x32_bf16 v[32:35], a[228:231], a[144:147], v[32:35]
  s_waitcnt lgkmcnt(4)
  v_mul_f32 v225, v225, v23
  v_mfma_f32_16x16x32_bf16 v[68:71], a[232:235], a[132:135], 0
  ds_read_b64_tr_b16 a[248:249], v19 offset:264
  v_mul_f32 v226, v226, v23
  v_mul_f32 v227, v227, v23
  v_mfma_f32_16x16x32_bf16 v[68:71], a[236:239], a[148:151], v[68:71]
  buffer_load_dwordx4 v2, s[48:51], s68 offen offset:1032 lds
  v_mul_f32 v228, v228, v23
  v_mfma_f32_16x16x32_bf16 v[100:103], a[232:235], a[136:139], 0
  ds_read_b64_tr_b16 a[250:251], v19 offset:4392
  v_mul_f32 v229, v229, v23
  v_mul_f32 v230, v230, v23
  v_mfma_f32_16x16x32_bf16 v[100:103], a[236:239], a[152:155], v[100:103]
  v_mul_f32 v231, v231, v23
  v_mfma_f32_16x16x32_bf16 v[132:135], a[232:235], a[140:143], 0
  ds_read_b64_tr_b16 a[252:253], v19 offset:8520
  v_cvt_pk_bf16_f32 v160, v224, v225
  v_cvt_pk_bf16_f32 v161, v226, v227
  v_mfma_f32_16x16x32_bf16 v[132:135], a[236:239], a[156:159], v[132:135]
  v_cvt_pk_bf16_f32 v162, v228, v229
  v_mfma_f32_16x16x32_bf16 v[36:39], a[232:235], a[128:131], 0
  ds_read_b64_tr_b16 a[254:255], v19 offset:12648
  v_cvt_pk_bf16_f32 v163, v230, v231
  v_mfma_f32_16x16x32_bf16 v[36:39], a[236:239], a[144:147], v[36:39]
  s_waitcnt lgkmcnt(4)
  v_mfma_f32_16x16x32_bf16 v[72:75], a[240:243], a[132:135], 0
  ds_read_b64_tr_b16 a[224:225], v19 offset:512
  buffer_store_dwordx4 v[160:163], v22, s[64:67], 0 offen offset:0 sc1
  v_accvgpr_read_b32 v232, a4
  v_accvgpr_read_b32 v233, a5
  v_mfma_f32_16x16x32_bf16 v[72:75], a[244:247], a[148:151], v[72:75]
  buffer_load_dwordx4 v3, s[48:51], s68 offen offset:2064 lds
  v_accvgpr_read_b32 v234, a6
  v_mfma_f32_16x16x32_bf16 v[104:107], a[240:243], a[136:139], 0
  ds_read_b64_tr_b16 a[226:227], v19 offset:4640
  v_accvgpr_read_b32 v235, a7
  v_accvgpr_read_b32 v236, a20
  v_mfma_f32_16x16x32_bf16 v[104:107], a[244:247], a[152:155], v[104:107]
  v_accvgpr_read_b32 v237, a21
  v_mfma_f32_16x16x32_bf16 v[136:139], a[240:243], a[140:143], 0
  ds_read_b64_tr_b16 a[228:229], v19 offset:8768
  v_accvgpr_read_b32 v238, a22
  v_accvgpr_read_b32 v239, a23
  v_mfma_f32_16x16x32_bf16 v[136:139], a[244:247], a[156:159], v[136:139]
  v_mul_f32 v232, v232, v24
  v_mfma_f32_16x16x32_bf16 v[40:43], a[240:243], a[128:131], 0
  ds_read_b64_tr_b16 a[230:231], v19 offset:12896
  v_mul_f32 v233, v233, v24
  v_mfma_f32_16x16x32_bf16 v[40:43], a[244:247], a[144:147], v[40:43]
  s_waitcnt lgkmcnt(4)
  v_mul_f32 v234, v234, v24
  v_mul_f32 v235, v235, v24
  v_mfma_f32_16x16x32_bf16 v[76:79], a[248:251], a[132:135], 0
  ds_read_b64_tr_b16 a[232:233], v19 offset:520
  buffer_load_dwordx4 v4, s[48:51], s68 offen offset:3096 lds
  v_mul_f32 v236, v236, v24
  v_mfma_f32_16x16x32_bf16 v[76:79], a[252:255], a[148:151], v[76:79]
  v_mul_f32 v237, v237, v24
  v_mul_f32 v238, v238, v24
  v_mfma_f32_16x16x32_bf16 v[108:111], a[248:251], a[136:139], 0
  ds_read_b64_tr_b16 a[234:235], v19 offset:4648
  v_mul_f32 v239, v239, v24
  v_mfma_f32_16x16x32_bf16 v[108:111], a[252:255], a[152:155], v[108:111]
  v_cvt_pk_bf16_f32 v164, v232, v233
  v_cvt_pk_bf16_f32 v165, v234, v235
  v_mfma_f32_16x16x32_bf16 v[140:143], a[248:251], a[140:143], 0
  ds_read_b64_tr_b16 a[236:237], v19 offset:8776
  v_cvt_pk_bf16_f32 v166, v236, v237
  v_mfma_f32_16x16x32_bf16 v[140:143], a[252:255], a[156:159], v[140:143]
  v_cvt_pk_bf16_f32 v167, v238, v239
  v_mfma_f32_16x16x32_bf16 v[44:47], a[248:251], a[128:131], 0
  s_add_u32 s73, s88, 4
  s_add_u32 s73, s73, s86
  s_lshl_b32 s68, s73, 17
  ds_read_b64_tr_b16 a[238:239], v19 offset:12904
  v_mfma_f32_16x16x32_bf16 v[44:47], a[252:255], a[144:147], v[44:47]
  s_waitcnt lgkmcnt(4)
  s_lshl_b32 s70, s73, 13
  s_add_u32 s71, s70, 4096
  s_add_u32 m0, s74, 66048
  buffer_store_dwordx4 v[164:167], v22, s[64:67], 0 offen offset:1024 sc1
  v_accvgpr_read_b32 v224, a8
  v_accvgpr_read_b32 v225, a9
  v_mfma_f32_16x16x32_bf16 v[80:83], a[224:227], a[132:135], 0
  ds_read_b64_tr_b16 a[240:241], v19 offset:768
  buffer_load_dwordx4 v1, s[48:51], s68 offen offset:0 lds
  v_accvgpr_read_b32 v226, a10
  v_mfma_f32_16x16x32_bf16 v[80:83], a[228:231], a[148:151], v[80:83]
  v_accvgpr_read_b32 v227, a11
  v_accvgpr_read_b32 v228, a24
  v_mfma_f32_16x16x32_bf16 v[112:115], a[224:227], a[136:139], 0
  ds_read_b64_tr_b16 a[242:243], v19 offset:4896
  v_accvgpr_read_b32 v229, a25
  v_mfma_f32_16x16x32_bf16 v[112:115], a[228:231], a[152:155], v[112:115]
  v_accvgpr_read_b32 v230, a26
  v_accvgpr_read_b32 v231, a27
  v_mfma_f32_16x16x32_bf16 v[144:147], a[224:227], a[140:143], 0
  ds_read_b64_tr_b16 a[244:245], v19 offset:9024
  v_mul_f32 v224, v224, v25
  v_mfma_f32_16x16x32_bf16 v[144:147], a[228:231], a[156:159], v[144:147]
  v_mul_f32 v225, v225, v25
  v_mfma_f32_16x16x32_bf16 v[48:51], a[224:227], a[128:131], 0
  ds_read_b64_tr_b16 a[246:247], v19 offset:13152
  v_mul_f32 v226, v226, v25
  v_mul_f32 v227, v227, v25
  v_mfma_f32_16x16x32_bf16 v[48:51], a[228:231], a[144:147], v[48:51]
  s_waitcnt lgkmcnt(4)
  v_mul_f32 v228, v228, v25
  v_mfma_f32_16x16x32_bf16 v[84:87], a[232:235], a[132:135], 0
  ds_read_b64_tr_b16 a[248:249], v19 offset:776
  buffer_load_dwordx4 v2, s[48:51], s68 offen offset:1032 lds
  v_mul_f32 v229, v229, v25
  v_mul_f32 v230, v230, v25
  v_mfma_f32_16x16x32_bf16 v[84:87], a[236:239], a[148:151], v[84:87]
  v_mul_f32 v231, v231, v25
  v_mfma_f32_16x16x32_bf16 v[116:119], a[232:235], a[136:139], 0
  ds_read_b64_tr_b16 a[250:251], v19 offset:4904
  v_cvt_pk_bf16_f32 v168, v224, v225
  v_cvt_pk_bf16_f32 v169, v226, v227
  v_mfma_f32_16x16x32_bf16 v[116:119], a[236:239], a[152:155], v[116:119]
  v_cvt_pk_bf16_f32 v170, v228, v229
  v_mfma_f32_16x16x32_bf16 v[148:151], a[232:235], a[140:143], 0
  ds_read_b64_tr_b16 a[252:253], v19 offset:9032
  v_cvt_pk_bf16_f32 v171, v230, v231
  v_mfma_f32_16x16x32_bf16 v[148:151], a[236:239], a[156:159], v[148:151]
  v_mfma_f32_16x16x32_bf16 v[52:55], a[232:235], a[128:131], 0
  ds_read_b64_tr_b16 a[254:255], v19 offset:13160
  buffer_store_dwordx4 v[168:171], v22, s[64:67], 0 offen offset:2048 sc1
  v_accvgpr_read_b32 v232, a12
  v_accvgpr_read_b32 v233, a13
  v_mfma_f32_16x16x32_bf16 v[52:55], a[236:239], a[144:147], v[52:55]
  s_waitcnt lgkmcnt(4)
  s_waitcnt vmcnt(21)
  s_barrier
  buffer_load_dwordx4 v3, s[48:51], s68 offen offset:2064 lds
  v_accvgpr_read_b32 v234, a14
  v_mfma_f32_16x16x32_bf16 v[88:91], a[240:243], a[132:135], 0
  ds_read_b64_tr_b16 a[224:225], v19 offset:16512
  v_accvgpr_read_b32 v235, a15
  v_accvgpr_read_b32 v236, a28
  v_mfma_f32_16x16x32_bf16 v[88:91], a[244:247], a[148:151], v[88:91]
  v_accvgpr_read_b32 v237, a29
  v_mfma_f32_16x16x32_bf16 v[120:123], a[240:243], a[136:139], 0
  ds_read_b64_tr_b16 a[226:227], v19 offset:20640
  v_accvgpr_read_b32 v238, a30
  v_accvgpr_read_b32 v239, a31
  v_mfma_f32_16x16x32_bf16 v[120:123], a[244:247], a[152:155], v[120:123]
  v_mul_f32 v232, v232, v26
  v_mfma_f32_16x16x32_bf16 v[152:155], a[240:243], a[140:143], 0
  ds_read_b64_tr_b16 a[228:229], v19 offset:24768
  v_mul_f32 v233, v233, v26
  v_mfma_f32_16x16x32_bf16 v[152:155], a[244:247], a[156:159], v[152:155]
  v_mul_f32 v234, v234, v26
  v_mul_f32 v235, v235, v26
  v_mfma_f32_16x16x32_bf16 v[56:59], a[240:243], a[128:131], 0
  ds_read_b64_tr_b16 a[230:231], v19 offset:28896
  v_mul_f32 v236, v236, v26
  v_mfma_f32_16x16x32_bf16 v[56:59], a[244:247], a[144:147], v[56:59]
  s_waitcnt lgkmcnt(4)
  buffer_load_dwordx4 v4, s[48:51], s68 offen offset:3096 lds
  v_mul_f32 v237, v237, v26
  v_mul_f32 v238, v238, v26
  v_mfma_f32_16x16x32_bf16 v[92:95], a[248:251], a[132:135], 0
  ds_read_b64_tr_b16 a[232:233], v19 offset:16520
  v_mul_f32 v239, v239, v26
  v_mfma_f32_16x16x32_bf16 v[92:95], a[252:255], a[148:151], v[92:95]
  v_cvt_pk_bf16_f32 v172, v232, v233
  v_cvt_pk_bf16_f32 v173, v234, v235
  v_mfma_f32_16x16x32_bf16 v[124:127], a[248:251], a[136:139], 0
  ds_read_b64_tr_b16 a[234:235], v19 offset:20648
  v_cvt_pk_bf16_f32 v174, v236, v237
  v_mfma_f32_16x16x32_bf16 v[124:127], a[252:255], a[152:155], v[124:127]
  v_cvt_pk_bf16_f32 v175, v238, v239
  v_mfma_f32_16x16x32_bf16 v[156:159], a[248:251], a[140:143], 0
  ds_read_b64_tr_b16 a[236:237], v19 offset:24776
  v_mfma_f32_16x16x32_bf16 v[156:159], a[252:255], a[156:159], v[156:159]
  buffer_store_dwordx4 v[172:175], v22, s[64:67], 0 offen offset:3072 sc1
  v_mfma_f32_16x16x32_bf16 v[60:63], a[248:251], a[128:131], 0
  ds_read_b64_tr_b16 a[238:239], v19 offset:28904
  v_mfma_f32_16x16x32_bf16 v[60:63], a[252:255], a[144:147], v[60:63]
  s_waitcnt lgkmcnt(4)
  v_mfma_f32_16x16x32_bf16 v[64:67], a[224:227], a[164:167], v[64:67]
  s_add_u32 s73, s91, 0
  s_add_u32 s73, s73, s86
  s_lshl_b32 s68, s73, 17
  ds_read_b64_tr_b16 a[240:241], v19 offset:16768
  v_mfma_f32_16x16x32_bf16 v[64:67], a[228:231], a[180:183], v[64:67]
  s_lshl_b32 s70, s73, 13
  s_add_u32 s71, s70, 4096
  v_accvgpr_read_b32 v224, a32
  v_accvgpr_read_b32 v225, a33
  v_mfma_f32_16x16x32_bf16 v[96:99], a[224:227], a[168:171], v[96:99]
  ds_read_b64_tr_b16 a[242:243], v19 offset:20896
  buffer_load_dwordx4 a[0:3], v5, s[60:63], s70 offen sc1
  v_accvgpr_read_b32 v226, a34
  v_mfma_f32_16x16x32_bf16 v[96:99], a[228:231], a[184:187], v[96:99]
  v_accvgpr_read_b32 v227, a35
  v_accvgpr_read_b32 v228, a48
  v_mfma_f32_16x16x32_bf16 v[128:131], a[224:227], a[172:175], v[128:131]
  ds_read_b64_tr_b16 a[244:245], v19 offset:25024
  v_accvgpr_read_b32 v229, a49
  v_mfma_f32_16x16x32_bf16 v[128:131], a[228:231], a[188:191], v[128:131]
  v_accvgpr_read_b32 v230, a50
  v_accvgpr_read_b32 v231, a51
  v_mfma_f32_16x16x32_bf16 v[32:35], a[224:227], a[160:163], v[32:35]
  ds_read_b64_tr_b16 a[246:247], v19 offset:29152
  v_mul_f32 v224, v224, v23
  v_mfma_f32_16x16x32_bf16 v[32:35], a[228:231], a[176:179], v[32:35]
  s_waitcnt lgkmcnt(4)
  buffer_load_dwordx4 a[4:7], v6, s[60:63], s70 offen sc1
  v_mul_f32 v225, v225, v23
  v_mfma_f32_16x16x32_bf16 v[68:71], a[232:235], a[164:167], v[68:71]
  ds_read_b64_tr_b16 a[248:249], v19 offset:16776
  v_mul_f32 v226, v226, v23
  v_mul_f32 v227, v227, v23
  v_mfma_f32_16x16x32_bf16 v[68:71], a[236:239], a[180:183], v[68:71]
  v_mul_f32 v228, v228, v23
  v_mfma_f32_16x16x32_bf16 v[100:103], a[232:235], a[168:171], v[100:103]
  ds_read_b64_tr_b16 a[250:251], v19 offset:20904
  v_mul_f32 v229, v229, v23
  v_mul_f32 v230, v230, v23
  v_mfma_f32_16x16x32_bf16 v[100:103], a[236:239], a[184:187], v[100:103]
  v_mul_f32 v231, v231, v23
  v_mfma_f32_16x16x32_bf16 v[132:135], a[232:235], a[172:175], v[132:135]
  ds_read_b64_tr_b16 a[252:253], v19 offset:25032
  buffer_load_dwordx4 a[8:11], v7, s[60:63], s70 offen sc1
  v_cvt_pk_bf16_f32 v176, v224, v225
  v_cvt_pk_bf16_f32 v177, v226, v227
  v_mfma_f32_16x16x32_bf16 v[132:135], a[236:239], a[188:191], v[132:135]
  v_cvt_pk_bf16_f32 v178, v228, v229
  v_mfma_f32_16x16x32_bf16 v[36:39], a[232:235], a[160:163], v[36:39]
  ds_read_b64_tr_b16 a[254:255], v19 offset:29160
  v_cvt_pk_bf16_f32 v179, v230, v231
  v_mfma_f32_16x16x32_bf16 v[36:39], a[236:239], a[176:179], v[36:39]
  s_waitcnt lgkmcnt(4)
  v_mfma_f32_16x16x32_bf16 v[72:75], a[240:243], a[164:167], v[72:75]
  ds_read_b64_tr_b16 a[224:225], v19 offset:17024
  buffer_store_dwordx4 v[176:179], v22, s[64:67], s83 offen offset:0 sc1
  v_accvgpr_read_b32 v232, a36
  v_accvgpr_read_b32 v233, a37
  v_mfma_f32_16x16x32_bf16 v[72:75], a[244:247], a[180:183], v[72:75]
  buffer_load_dwordx4 a[12:15], v8, s[60:63], s70 offen sc1
  v_accvgpr_read_b32 v234, a38
  v_mfma_f32_16x16x32_bf16 v[104:107], a[240:243], a[168:171], v[104:107]
  ds_read_b64_tr_b16 a[226:227], v19 offset:21152
  v_accvgpr_read_b32 v235, a39
  v_accvgpr_read_b32 v236, a52
  v_mfma_f32_16x16x32_bf16 v[104:107], a[244:247], a[184:187], v[104:107]
  v_accvgpr_read_b32 v237, a53
  v_mfma_f32_16x16x32_bf16 v[136:139], a[240:243], a[172:175], v[136:139]
  ds_read_b64_tr_b16 a[228:229], v19 offset:25280
  v_accvgpr_read_b32 v238, a54
  v_accvgpr_read_b32 v239, a55
  v_mfma_f32_16x16x32_bf16 v[136:139], a[244:247], a[188:191], v[136:139]
  v_mul_f32 v232, v232, v24
  v_mfma_f32_16x16x32_bf16 v[40:43], a[240:243], a[160:163], v[40:43]
  ds_read_b64_tr_b16 a[230:231], v19 offset:29408
  buffer_load_dwordx4 a[16:19], v5, s[60:63], s71 offen sc1
  v_mul_f32 v233, v233, v24
  v_mfma_f32_16x16x32_bf16 v[40:43], a[244:247], a[176:179], v[40:43]
  s_waitcnt lgkmcnt(4)
  v_mul_f32 v234, v234, v24
  v_mul_f32 v235, v235, v24
  v_mfma_f32_16x16x32_bf16 v[76:79], a[248:251], a[164:167], v[76:79]
  ds_read_b64_tr_b16 a[232:233], v19 offset:17032
  v_mul_f32 v236, v236, v24
  v_mfma_f32_16x16x32_bf16 v[76:79], a[252:255], a[180:183], v[76:79]
  v_mul_f32 v237, v237, v24
  v_mul_f32 v238, v238, v24
  v_mfma_f32_16x16x32_bf16 v[108:111], a[248:251], a[168:171], v[108:111]
  ds_read_b64_tr_b16 a[234:235], v19 offset:21160
  v_mul_f32 v239, v239, v24
  v_mfma_f32_16x16x32_bf16 v[108:111], a[252:255], a[184:187], v[108:111]
  buffer_load_dwordx4 a[20:23], v6, s[60:63], s71 offen sc1
  v_cvt_pk_bf16_f32 v180, v232, v233
  v_cvt_pk_bf16_f32 v181, v234, v235
  v_mfma_f32_16x16x32_bf16 v[140:143], a[248:251], a[172:175], v[140:143]
  ds_read_b64_tr_b16 a[236:237], v19 offset:25288
  v_cvt_pk_bf16_f32 v182, v236, v237
  v_mfma_f32_16x16x32_bf16 v[140:143], a[252:255], a[188:191], v[140:143]
  v_cvt_pk_bf16_f32 v183, v238, v239
  v_mfma_f32_16x16x32_bf16 v[44:47], a[248:251], a[160:163], v[44:47]
  ds_read_b64_tr_b16 a[238:239], v19 offset:29416
  v_mfma_f32_16x16x32_bf16 v[44:47], a[252:255], a[176:179], v[44:47]
  s_waitcnt lgkmcnt(4)
  buffer_store_dwordx4 v[180:183], v22, s[64:67], s83 offen offset:1024 sc1
  v_accvgpr_read_b32 v224, a40
  v_accvgpr_read_b32 v225, a41
  v_mfma_f32_16x16x32_bf16 v[80:83], a[224:227], a[164:167], v[80:83]
  ds_read_b64_tr_b16 a[240:241], v19 offset:17280
  buffer_load_dwordx4 a[24:27], v7, s[60:63], s71 offen sc1
  v_accvgpr_read_b32 v226, a42
  v_mfma_f32_16x16x32_bf16 v[80:83], a[228:231], a[180:183], v[80:83]
  v_accvgpr_read_b32 v227, a43
  v_accvgpr_read_b32 v228, a56
  v_mfma_f32_16x16x32_bf16 v[112:115], a[224:227], a[168:171], v[112:115]
  ds_read_b64_tr_b16 a[242:243], v19 offset:21408
  v_accvgpr_read_b32 v229, a57
  v_mfma_f32_16x16x32_bf16 v[112:115], a[228:231], a[184:187], v[112:115]
  v_accvgpr_read_b32 v230, a58
  v_accvgpr_read_b32 v231, a59
  v_mfma_f32_16x16x32_bf16 v[144:147], a[224:227], a[172:175], v[144:147]
  ds_read_b64_tr_b16 a[244:245], v19 offset:25536
  v_mul_f32 v224, v224, v25
  v_mfma_f32_16x16x32_bf16 v[144:147], a[228:231], a[188:191], v[144:147]
  buffer_load_dwordx4 a[28:31], v8, s[60:63], s71 offen sc1
  v_mul_f32 v225, v225, v25
  v_mfma_f32_16x16x32_bf16 v[48:51], a[224:227], a[160:163], v[48:51]
  ds_read_b64_tr_b16 a[246:247], v19 offset:29664
  v_mul_f32 v226, v226, v25
  v_mul_f32 v227, v227, v25
  v_mfma_f32_16x16x32_bf16 v[48:51], a[228:231], a[176:179], v[48:51]
  s_waitcnt lgkmcnt(4)
  v_mul_f32 v228, v228, v25
  v_mfma_f32_16x16x32_bf16 v[84:87], a[232:235], a[164:167], v[84:87]
  s_add_u32 s73, s89, 4
  s_add_u32 s73, s73, s86
  s_lshl_b32 s68, s73, 17
  ds_read_b64_tr_b16 a[248:249], v19 offset:17288
  v_mul_f32 v229, v229, v25
  v_mul_f32 v230, v230, v25
  v_mfma_f32_16x16x32_bf16 v[84:87], a[236:239], a[180:183], v[84:87]
  s_lshl_b32 s70, s73, 13
  s_add_u32 s71, s70, 4096
  s_add_u32 m0, s74, 82560
  v_mul_f32 v231, v231, v25
  v_mfma_f32_16x16x32_bf16 v[116:119], a[232:235], a[168:171], v[116:119]
  ds_read_b64_tr_b16 a[250:251], v19 offset:21416
  buffer_load_dwordx4 v1, s[48:51], s68 offen offset:0 lds
  v_cvt_pk_bf16_f32 v184, v224, v225
  v_cvt_pk_bf16_f32 v185, v226, v227
  v_mfma_f32_16x16x32_bf16 v[116:119], a[236:239], a[184:187], v[116:119]
  v_cvt_pk_bf16_f32 v186, v228, v229
  v_mfma_f32_16x16x32_bf16 v[148:151], a[232:235], a[172:175], v[148:151]
  ds_read_b64_tr_b16 a[252:253], v19 offset:25544
  v_cvt_pk_bf16_f32 v187, v230, v231
  v_mfma_f32_16x16x32_bf16 v[148:151], a[236:239], a[188:191], v[148:151]
  v_mfma_f32_16x16x32_bf16 v[52:55], a[232:235], a[160:163], v[52:55]
  ds_read_b64_tr_b16 a[254:255], v19 offset:29672
  buffer_store_dwordx4 v[184:187], v22, s[64:67], s83 offen offset:2048 sc1
  v_accvgpr_read_b32 v232, a44
  v_accvgpr_read_b32 v233, a45
  v_mfma_f32_16x16x32_bf16 v[52:55], a[236:239], a[176:179], v[52:55]
  s_waitcnt lgkmcnt(4)
  s_waitcnt vmcnt(24)
  s_barrier
  buffer_load_dwordx4 v2, s[48:51], s68 offen offset:1032 lds
  v_accvgpr_read_b32 v234, a46
  v_mfma_f32_16x16x32_bf16 v[88:91], a[240:243], a[164:167], v[88:91]
  ds_read_b64_tr_b16 a[224:225], v19 offset:33024
  v_accvgpr_read_b32 v235, a47
  v_accvgpr_read_b32 v236, a60
  v_mfma_f32_16x16x32_bf16 v[88:91], a[244:247], a[180:183], v[88:91]
  v_accvgpr_read_b32 v237, a61
  v_mfma_f32_16x16x32_bf16 v[120:123], a[240:243], a[168:171], v[120:123]
  ds_read_b64_tr_b16 a[226:227], v19 offset:37152
  v_accvgpr_read_b32 v238, a62
  v_accvgpr_read_b32 v239, a63
  v_mfma_f32_16x16x32_bf16 v[120:123], a[244:247], a[184:187], v[120:123]
  v_mul_f32 v232, v232, v26
  v_mfma_f32_16x16x32_bf16 v[152:155], a[240:243], a[172:175], v[152:155]
  ds_read_b64_tr_b16 a[228:229], v19 offset:41280
  buffer_load_dwordx4 v3, s[48:51], s68 offen offset:2064 lds
  v_mul_f32 v233, v233, v26
  v_mfma_f32_16x16x32_bf16 v[152:155], a[244:247], a[188:191], v[152:155]
  v_mul_f32 v234, v234, v26
  v_mul_f32 v235, v235, v26
  v_mfma_f32_16x16x32_bf16 v[56:59], a[240:243], a[160:163], v[56:59]
  ds_read_b64_tr_b16 a[230:231], v19 offset:45408
  v_mul_f32 v236, v236, v26
  v_mfma_f32_16x16x32_bf16 v[56:59], a[244:247], a[176:179], v[56:59]
  s_waitcnt lgkmcnt(4)
  v_mul_f32 v237, v237, v26
  v_mul_f32 v238, v238, v26
  v_mfma_f32_16x16x32_bf16 v[92:95], a[248:251], a[164:167], v[92:95]
  ds_read_b64_tr_b16 a[232:233], v19 offset:33032
  v_mul_f32 v239, v239, v26
  v_mfma_f32_16x16x32_bf16 v[92:95], a[252:255], a[180:183], v[92:95]
  buffer_load_dwordx4 v4, s[48:51], s68 offen offset:3096 lds
  v_cvt_pk_bf16_f32 v188, v232, v233
  v_cvt_pk_bf16_f32 v189, v234, v235
  v_mfma_f32_16x16x32_bf16 v[124:127], a[248:251], a[168:171], v[124:127]
  ds_read_b64_tr_b16 a[234:235], v19 offset:37160
  v_cvt_pk_bf16_f32 v190, v236, v237
  v_mfma_f32_16x16x32_bf16 v[124:127], a[252:255], a[184:187], v[124:127]
  v_cvt_pk_bf16_f32 v191, v238, v239
  v_mfma_f32_16x16x32_bf16 v[156:159], a[248:251], a[172:175], v[156:159]
  ds_read_b64_tr_b16 a[236:237], v19 offset:41288
  v_mfma_f32_16x16x32_bf16 v[156:159], a[252:255], a[188:191], v[156:159]
  buffer_store_dwordx4 v[188:191], v22, s[64:67], s83 offen offset:3072 sc1
  v_mfma_f32_16x16x32_bf16 v[60:63], a[248:251], a[160:163], v[60:63]
  ds_read_b64_tr_b16 a[238:239], v19 offset:45416
  v_mfma_f32_16x16x32_bf16 v[60:63], a[252:255], a[176:179], v[60:63]
  s_waitcnt lgkmcnt(4)
  v_mfma_f32_16x16x32_bf16 v[64:67], a[224:227], a[196:199], v[64:67]
  s_add_u32 s73, s88, 4
  s_add_u32 s73, s73, s86
  s_lshl_b32 s68, s73, 17
  ds_read_b64_tr_b16 a[240:241], v19 offset:33280
  v_mfma_f32_16x16x32_bf16 v[64:67], a[228:231], a[212:215], v[64:67]
  s_lshl_b32 s70, s73, 13
  s_add_u32 s71, s70, 4096
  v_accvgpr_read_b32 v224, a64
  v_accvgpr_read_b32 v225, a65
  v_mfma_f32_16x16x32_bf16 v[96:99], a[224:227], a[200:203], v[96:99]
  ds_read_b64_tr_b16 a[242:243], v19 offset:37408
  buffer_load_dwordx4 a[32:35], v5, s[60:63], s70 offen sc1
  v_accvgpr_read_b32 v226, a66
  v_mfma_f32_16x16x32_bf16 v[96:99], a[228:231], a[216:219], v[96:99]
  v_accvgpr_read_b32 v227, a67
  v_accvgpr_read_b32 v228, a80
  v_mfma_f32_16x16x32_bf16 v[128:131], a[224:227], a[204:207], v[128:131]
  ds_read_b64_tr_b16 a[244:245], v19 offset:41536
  v_accvgpr_read_b32 v229, a81
  v_mfma_f32_16x16x32_bf16 v[128:131], a[228:231], a[220:223], v[128:131]
  v_accvgpr_read_b32 v230, a82
  v_accvgpr_read_b32 v231, a83
  v_mfma_f32_16x16x32_bf16 v[32:35], a[224:227], a[192:195], v[32:35]
  ds_read_b64_tr_b16 a[246:247], v19 offset:45664
  v_mul_f32 v224, v224, v23
  v_mfma_f32_16x16x32_bf16 v[32:35], a[228:231], a[208:211], v[32:35]
  s_waitcnt lgkmcnt(4)
  buffer_load_dwordx4 a[36:39], v6, s[60:63], s70 offen sc1
  v_mul_f32 v225, v225, v23
  v_mfma_f32_16x16x32_bf16 v[68:71], a[232:235], a[196:199], v[68:71]
  ds_read_b64_tr_b16 a[248:249], v19 offset:33288
  v_mul_f32 v226, v226, v23
  v_mul_f32 v227, v227, v23
  v_mfma_f32_16x16x32_bf16 v[68:71], a[236:239], a[212:215], v[68:71]
  v_mul_f32 v228, v228, v23
  v_mfma_f32_16x16x32_bf16 v[100:103], a[232:235], a[200:203], v[100:103]
  ds_read_b64_tr_b16 a[250:251], v19 offset:37416
  v_mul_f32 v229, v229, v23
  v_mul_f32 v230, v230, v23
  v_mfma_f32_16x16x32_bf16 v[100:103], a[236:239], a[216:219], v[100:103]
  v_mul_f32 v231, v231, v23
  v_mfma_f32_16x16x32_bf16 v[132:135], a[232:235], a[204:207], v[132:135]
  ds_read_b64_tr_b16 a[252:253], v19 offset:41544
  buffer_load_dwordx4 a[40:43], v7, s[60:63], s70 offen sc1
  v_cvt_pk_bf16_f32 v192, v224, v225
  v_cvt_pk_bf16_f32 v193, v226, v227
  v_mfma_f32_16x16x32_bf16 v[132:135], a[236:239], a[220:223], v[132:135]
  v_cvt_pk_bf16_f32 v194, v228, v229
  v_mfma_f32_16x16x32_bf16 v[36:39], a[232:235], a[192:195], v[36:39]
  ds_read_b64_tr_b16 a[254:255], v19 offset:45672
  v_cvt_pk_bf16_f32 v195, v230, v231
  v_mfma_f32_16x16x32_bf16 v[36:39], a[236:239], a[208:211], v[36:39]
  s_waitcnt lgkmcnt(4)
  v_mfma_f32_16x16x32_bf16 v[72:75], a[240:243], a[196:199], v[72:75]
  ds_read_b64_tr_b16 a[224:225], v19 offset:33536
  buffer_store_dwordx4 v[192:195], v22, s[64:67], s84 offen offset:0 sc1
  v_accvgpr_read_b32 v232, a68
  v_accvgpr_read_b32 v233, a69
  v_mfma_f32_16x16x32_bf16 v[72:75], a[244:247], a[212:215], v[72:75]
  buffer_load_dwordx4 a[44:47], v8, s[60:63], s70 offen sc1
  v_accvgpr_read_b32 v234, a70
  v_mfma_f32_16x16x32_bf16 v[104:107], a[240:243], a[200:203], v[104:107]
  ds_read_b64_tr_b16 a[226:227], v19 offset:37664
  v_accvgpr_read_b32 v235, a71
  v_accvgpr_read_b32 v236, a84
  v_mfma_f32_16x16x32_bf16 v[104:107], a[244:247], a[216:219], v[104:107]
  v_accvgpr_read_b32 v237, a85
  v_mfma_f32_16x16x32_bf16 v[136:139], a[240:243], a[204:207], v[136:139]
  ds_read_b64_tr_b16 a[228:229], v19 offset:41792
  v_accvgpr_read_b32 v238, a86
  v_accvgpr_read_b32 v239, a87
  v_mfma_f32_16x16x32_bf16 v[136:139], a[244:247], a[220:223], v[136:139]
  v_mul_f32 v232, v232, v24
  v_mfma_f32_16x16x32_bf16 v[40:43], a[240:243], a[192:195], v[40:43]
  ds_read_b64_tr_b16 a[230:231], v19 offset:45920
  buffer_load_dwordx4 a[48:51], v5, s[60:63], s71 offen sc1
  v_mul_f32 v233, v233, v24
  v_mfma_f32_16x16x32_bf16 v[40:43], a[244:247], a[208:211], v[40:43]
  s_waitcnt lgkmcnt(4)
  v_mul_f32 v234, v234, v24
  v_mul_f32 v235, v235, v24
  v_mfma_f32_16x16x32_bf16 v[76:79], a[248:251], a[196:199], v[76:79]
  ds_read_b64_tr_b16 a[232:233], v19 offset:33544
  v_mul_f32 v236, v236, v24
  v_mfma_f32_16x16x32_bf16 v[76:79], a[252:255], a[212:215], v[76:79]
  v_mul_f32 v237, v237, v24
  v_mul_f32 v238, v238, v24
  v_mfma_f32_16x16x32_bf16 v[108:111], a[248:251], a[200:203], v[108:111]
  ds_read_b64_tr_b16 a[234:235], v19 offset:37672
  v_mul_f32 v239, v239, v24
  v_mfma_f32_16x16x32_bf16 v[108:111], a[252:255], a[216:219], v[108:111]
  buffer_load_dwordx4 a[52:55], v6, s[60:63], s71 offen sc1
  v_cvt_pk_bf16_f32 v196, v232, v233
  v_cvt_pk_bf16_f32 v197, v234, v235
  v_mfma_f32_16x16x32_bf16 v[140:143], a[248:251], a[204:207], v[140:143]
  ds_read_b64_tr_b16 a[236:237], v19 offset:41800
  v_cvt_pk_bf16_f32 v198, v236, v237
  v_mfma_f32_16x16x32_bf16 v[140:143], a[252:255], a[220:223], v[140:143]
  v_cvt_pk_bf16_f32 v199, v238, v239
  v_mfma_f32_16x16x32_bf16 v[44:47], a[248:251], a[192:195], v[44:47]
  ds_read_b64_tr_b16 a[238:239], v19 offset:45928
  v_mfma_f32_16x16x32_bf16 v[44:47], a[252:255], a[208:211], v[44:47]
  s_waitcnt lgkmcnt(4)
  buffer_store_dwordx4 v[196:199], v22, s[64:67], s84 offen offset:1024 sc1
  v_accvgpr_read_b32 v224, a72
  v_accvgpr_read_b32 v225, a73
  v_mfma_f32_16x16x32_bf16 v[80:83], a[224:227], a[196:199], v[80:83]
  ds_read_b64_tr_b16 a[240:241], v19 offset:33792
  buffer_load_dwordx4 a[56:59], v7, s[60:63], s71 offen sc1
  v_accvgpr_read_b32 v226, a74
  v_mfma_f32_16x16x32_bf16 v[80:83], a[228:231], a[212:215], v[80:83]
  v_accvgpr_read_b32 v227, a75
  v_accvgpr_read_b32 v228, a88
  v_mfma_f32_16x16x32_bf16 v[112:115], a[224:227], a[200:203], v[112:115]
  ds_read_b64_tr_b16 a[242:243], v19 offset:37920
  v_accvgpr_read_b32 v229, a89
  v_mfma_f32_16x16x32_bf16 v[112:115], a[228:231], a[216:219], v[112:115]
  v_accvgpr_read_b32 v230, a90
  v_accvgpr_read_b32 v231, a91
  v_mfma_f32_16x16x32_bf16 v[144:147], a[224:227], a[204:207], v[144:147]
  ds_read_b64_tr_b16 a[244:245], v19 offset:42048
  v_mul_f32 v224, v224, v25
  v_mfma_f32_16x16x32_bf16 v[144:147], a[228:231], a[220:223], v[144:147]
  buffer_load_dwordx4 a[60:63], v8, s[60:63], s71 offen sc1
  v_mul_f32 v225, v225, v25
  v_mfma_f32_16x16x32_bf16 v[48:51], a[224:227], a[192:195], v[48:51]
  ds_read_b64_tr_b16 a[246:247], v19 offset:46176
  v_mul_f32 v226, v226, v25
  v_mul_f32 v227, v227, v25
  v_mfma_f32_16x16x32_bf16 v[48:51], a[228:231], a[208:211], v[48:51]
  s_waitcnt lgkmcnt(4)
  v_mul_f32 v228, v228, v25
  v_mfma_f32_16x16x32_bf16 v[84:87], a[232:235], a[196:199], v[84:87]
  s_add_u32 s73, s90, 4
  s_add_u32 s73, s73, s86
  s_lshl_b32 s68, s73, 17
  ds_read_b64_tr_b16 a[248:249], v19 offset:33800
  v_mul_f32 v229, v229, v25
  v_mul_f32 v230, v230, v25
  v_mfma_f32_16x16x32_bf16 v[84:87], a[236:239], a[212:215], v[84:87]
  s_lshl_b32 s70, s73, 13
  s_add_u32 s71, s70, 4096
  s_add_u32 m0, s74, 99072
  v_mul_f32 v231, v231, v25
  v_mfma_f32_16x16x32_bf16 v[116:119], a[232:235], a[200:203], v[116:119]
  ds_read_b64_tr_b16 a[250:251], v19 offset:37928
  buffer_load_dwordx4 v1, s[48:51], s68 offen offset:0 lds
  v_cvt_pk_bf16_f32 v200, v224, v225
  v_cvt_pk_bf16_f32 v201, v226, v227
  v_mfma_f32_16x16x32_bf16 v[116:119], a[236:239], a[216:219], v[116:119]
  v_cvt_pk_bf16_f32 v202, v228, v229
  v_mfma_f32_16x16x32_bf16 v[148:151], a[232:235], a[204:207], v[148:151]
  ds_read_b64_tr_b16 a[252:253], v19 offset:42056
  v_cvt_pk_bf16_f32 v203, v230, v231
  v_mfma_f32_16x16x32_bf16 v[148:151], a[236:239], a[220:223], v[148:151]
  v_mfma_f32_16x16x32_bf16 v[52:55], a[232:235], a[192:195], v[52:55]
  ds_read_b64_tr_b16 a[254:255], v19 offset:46184
  buffer_store_dwordx4 v[200:203], v22, s[64:67], s84 offen offset:2048 sc1
  v_accvgpr_read_b32 v232, a76
  v_accvgpr_read_b32 v233, a77
  v_mfma_f32_16x16x32_bf16 v[52:55], a[236:239], a[208:211], v[52:55]
  s_waitcnt lgkmcnt(4)
  s_waitcnt vmcnt(18)
  s_barrier
  buffer_load_dwordx4 v2, s[48:51], s68 offen offset:1032 lds
  v_accvgpr_read_b32 v234, a78
  v_mfma_f32_16x16x32_bf16 v[88:91], a[240:243], a[196:199], v[88:91]
  ds_read_b64_tr_b16 a[224:225], v19 offset:49536
  v_accvgpr_read_b32 v235, a79
  v_accvgpr_read_b32 v236, a92
  v_mfma_f32_16x16x32_bf16 v[88:91], a[244:247], a[212:215], v[88:91]
  v_accvgpr_read_b32 v237, a93
  v_mfma_f32_16x16x32_bf16 v[120:123], a[240:243], a[200:203], v[120:123]
  ds_read_b64_tr_b16 a[226:227], v19 offset:53664
  v_accvgpr_read_b32 v238, a94
  v_accvgpr_read_b32 v239, a95
  v_mfma_f32_16x16x32_bf16 v[120:123], a[244:247], a[216:219], v[120:123]
  v_mul_f32 v232, v232, v26
  v_mfma_f32_16x16x32_bf16 v[152:155], a[240:243], a[204:207], v[152:155]
  ds_read_b64_tr_b16 a[228:229], v19 offset:57792
  buffer_load_dwordx4 v3, s[48:51], s68 offen offset:2064 lds
  v_mul_f32 v233, v233, v26
  v_mfma_f32_16x16x32_bf16 v[152:155], a[244:247], a[220:223], v[152:155]
  v_mul_f32 v234, v234, v26
  v_mul_f32 v235, v235, v26
  v_mfma_f32_16x16x32_bf16 v[56:59], a[240:243], a[192:195], v[56:59]
  ds_read_b64_tr_b16 a[230:231], v19 offset:61920
  v_mul_f32 v236, v236, v26
  v_mfma_f32_16x16x32_bf16 v[56:59], a[244:247], a[208:211], v[56:59]
  s_waitcnt lgkmcnt(4)
  v_mul_f32 v237, v237, v26
  v_mul_f32 v238, v238, v26
  v_mfma_f32_16x16x32_bf16 v[92:95], a[248:251], a[196:199], v[92:95]
  ds_read_b64_tr_b16 a[232:233], v19 offset:49544
  v_mul_f32 v239, v239, v26
  v_mfma_f32_16x16x32_bf16 v[92:95], a[252:255], a[212:215], v[92:95]
  buffer_load_dwordx4 v4, s[48:51], s68 offen offset:3096 lds
  v_cvt_pk_bf16_f32 v204, v232, v233
  v_cvt_pk_bf16_f32 v205, v234, v235
  v_mfma_f32_16x16x32_bf16 v[124:127], a[248:251], a[200:203], v[124:127]
  ds_read_b64_tr_b16 a[234:235], v19 offset:53672
  v_cvt_pk_bf16_f32 v206, v236, v237
  v_mfma_f32_16x16x32_bf16 v[124:127], a[252:255], a[216:219], v[124:127]
  v_cvt_pk_bf16_f32 v207, v238, v239
  v_mfma_f32_16x16x32_bf16 v[156:159], a[248:251], a[204:207], v[156:159]
  ds_read_b64_tr_b16 a[236:237], v19 offset:57800
  v_mfma_f32_16x16x32_bf16 v[156:159], a[252:255], a[220:223], v[156:159]
  buffer_store_dwordx4 v[204:207], v22, s[64:67], s84 offen offset:3072 sc1
  v_mfma_f32_16x16x32_bf16 v[60:63], a[248:251], a[192:195], v[60:63]
  ds_read_b64_tr_b16 a[238:239], v19 offset:61928
  v_mfma_f32_16x16x32_bf16 v[60:63], a[252:255], a[208:211], v[60:63]
  s_waitcnt lgkmcnt(4)
  v_mfma_f32_16x16x32_bf16 v[64:67], a[224:227], a[4:7], v[64:67]
  s_add_u32 s73, s89, 4
  s_add_u32 s73, s73, s86
  s_lshl_b32 s68, s73, 17
  ds_read_b64_tr_b16 a[240:241], v19 offset:49792
  v_mfma_f32_16x16x32_bf16 v[64:67], a[228:231], a[20:23], v[64:67]
  s_lshl_b32 s70, s73, 13
  s_add_u32 s71, s70, 4096
  v_accvgpr_read_b32 v224, a96
  v_accvgpr_read_b32 v225, a97
  v_mfma_f32_16x16x32_bf16 v[96:99], a[224:227], a[8:11], v[96:99]
  ds_read_b64_tr_b16 a[242:243], v19 offset:53920
  buffer_load_dwordx4 a[64:67], v5, s[60:63], s70 offen sc1
  v_accvgpr_read_b32 v226, a98
  v_mfma_f32_16x16x32_bf16 v[96:99], a[228:231], a[24:27], v[96:99]
  v_accvgpr_read_b32 v227, a99
  v_accvgpr_read_b32 v228, a112
  v_mfma_f32_16x16x32_bf16 v[128:131], a[224:227], a[12:15], v[128:131]
  ds_read_b64_tr_b16 a[244:245], v19 offset:58048
  v_accvgpr_read_b32 v229, a113
  v_mfma_f32_16x16x32_bf16 v[128:131], a[228:231], a[28:31], v[128:131]
  v_accvgpr_read_b32 v230, a114
  v_accvgpr_read_b32 v231, a115
  v_mfma_f32_16x16x32_bf16 v[32:35], a[224:227], a[0:3], v[32:35]
  ds_read_b64_tr_b16 a[246:247], v19 offset:62176
  v_mul_f32 v224, v224, v23
  v_mfma_f32_16x16x32_bf16 v[32:35], a[228:231], a[16:19], v[32:35]
  s_waitcnt lgkmcnt(4)
  buffer_load_dwordx4 a[68:71], v6, s[60:63], s70 offen sc1
  v_mul_f32 v225, v225, v23
  v_mfma_f32_16x16x32_bf16 v[68:71], a[232:235], a[4:7], v[68:71]
  ds_read_b64_tr_b16 a[248:249], v19 offset:49800
  v_mul_f32 v226, v226, v23
  v_mul_f32 v227, v227, v23
  v_mfma_f32_16x16x32_bf16 v[68:71], a[236:239], a[20:23], v[68:71]
  v_mul_f32 v228, v228, v23
  v_mfma_f32_16x16x32_bf16 v[100:103], a[232:235], a[8:11], v[100:103]
  ds_read_b64_tr_b16 a[250:251], v19 offset:53928
  v_mul_f32 v229, v229, v23
  v_mul_f32 v230, v230, v23
  v_mfma_f32_16x16x32_bf16 v[100:103], a[236:239], a[24:27], v[100:103]
  v_mul_f32 v231, v231, v23
  v_mfma_f32_16x16x32_bf16 v[132:135], a[232:235], a[12:15], v[132:135]
  ds_read_b64_tr_b16 a[252:253], v19 offset:58056
  buffer_load_dwordx4 a[72:75], v7, s[60:63], s70 offen sc1
  v_cvt_pk_bf16_f32 v208, v224, v225
  v_cvt_pk_bf16_f32 v209, v226, v227
  v_mfma_f32_16x16x32_bf16 v[132:135], a[236:239], a[28:31], v[132:135]
  v_cvt_pk_bf16_f32 v210, v228, v229
  v_mfma_f32_16x16x32_bf16 v[36:39], a[232:235], a[0:3], v[36:39]
  ds_read_b64_tr_b16 a[254:255], v19 offset:62184
  v_cvt_pk_bf16_f32 v211, v230, v231
  v_mfma_f32_16x16x32_bf16 v[36:39], a[236:239], a[16:19], v[36:39]
  s_waitcnt lgkmcnt(4)
  v_mfma_f32_16x16x32_bf16 v[72:75], a[240:243], a[4:7], v[72:75]
  ds_read_b64_tr_b16 a[224:225], v19 offset:50048
  buffer_store_dwordx4 v[208:211], v22, s[64:67], s85 offen offset:0 sc1
  v_accvgpr_read_b32 v232, a100
  v_accvgpr_read_b32 v233, a101
  v_mfma_f32_16x16x32_bf16 v[72:75], a[244:247], a[20:23], v[72:75]
  buffer_load_dwordx4 a[76:79], v8, s[60:63], s70 offen sc1
  v_accvgpr_read_b32 v234, a102
  v_mfma_f32_16x16x32_bf16 v[104:107], a[240:243], a[8:11], v[104:107]
  ds_read_b64_tr_b16 a[226:227], v19 offset:54176
  v_accvgpr_read_b32 v235, a103
  v_accvgpr_read_b32 v236, a116
  v_mfma_f32_16x16x32_bf16 v[104:107], a[244:247], a[24:27], v[104:107]
  v_accvgpr_read_b32 v237, a117
  v_mfma_f32_16x16x32_bf16 v[136:139], a[240:243], a[12:15], v[136:139]
  ds_read_b64_tr_b16 a[228:229], v19 offset:58304
  v_accvgpr_read_b32 v238, a118
  v_accvgpr_read_b32 v239, a119
  v_mfma_f32_16x16x32_bf16 v[136:139], a[244:247], a[28:31], v[136:139]
  v_mul_f32 v232, v232, v24
  v_mfma_f32_16x16x32_bf16 v[40:43], a[240:243], a[0:3], v[40:43]
  ds_read_b64_tr_b16 a[230:231], v19 offset:62432
  buffer_load_dwordx4 a[80:83], v5, s[60:63], s71 offen sc1
  v_mul_f32 v233, v233, v24
  v_mfma_f32_16x16x32_bf16 v[40:43], a[244:247], a[16:19], v[40:43]
  s_waitcnt lgkmcnt(4)
  v_mul_f32 v234, v234, v24
  v_mul_f32 v235, v235, v24
  v_mfma_f32_16x16x32_bf16 v[76:79], a[248:251], a[4:7], v[76:79]
  ds_read_b64_tr_b16 a[232:233], v19 offset:50056
  v_mul_f32 v236, v236, v24
  v_mfma_f32_16x16x32_bf16 v[76:79], a[252:255], a[20:23], v[76:79]
  v_mul_f32 v237, v237, v24
  v_mul_f32 v238, v238, v24
  v_mfma_f32_16x16x32_bf16 v[108:111], a[248:251], a[8:11], v[108:111]
  ds_read_b64_tr_b16 a[234:235], v19 offset:54184
  v_mul_f32 v239, v239, v24
  v_mfma_f32_16x16x32_bf16 v[108:111], a[252:255], a[24:27], v[108:111]
  buffer_load_dwordx4 a[84:87], v6, s[60:63], s71 offen sc1
  v_cvt_pk_bf16_f32 v212, v232, v233
  v_cvt_pk_bf16_f32 v213, v234, v235
  v_mfma_f32_16x16x32_bf16 v[140:143], a[248:251], a[12:15], v[140:143]
  ds_read_b64_tr_b16 a[236:237], v19 offset:58312
  v_cvt_pk_bf16_f32 v214, v236, v237
  v_mfma_f32_16x16x32_bf16 v[140:143], a[252:255], a[28:31], v[140:143]
  v_cvt_pk_bf16_f32 v215, v238, v239
  v_mfma_f32_16x16x32_bf16 v[44:47], a[248:251], a[0:3], v[44:47]
  ds_read_b64_tr_b16 a[238:239], v19 offset:62440
  v_mfma_f32_16x16x32_bf16 v[44:47], a[252:255], a[16:19], v[44:47]
  s_waitcnt lgkmcnt(4)
  buffer_store_dwordx4 v[212:215], v22, s[64:67], s85 offen offset:1024 sc1
  v_accvgpr_read_b32 v224, a104
  v_accvgpr_read_b32 v225, a105
  v_mfma_f32_16x16x32_bf16 v[80:83], a[224:227], a[4:7], v[80:83]
  ds_read_b64_tr_b16 a[240:241], v19 offset:50304
  buffer_load_dwordx4 a[88:91], v7, s[60:63], s71 offen sc1
  v_accvgpr_read_b32 v226, a106
  v_mfma_f32_16x16x32_bf16 v[80:83], a[228:231], a[20:23], v[80:83]
  v_accvgpr_read_b32 v227, a107
  v_accvgpr_read_b32 v228, a120
  v_mfma_f32_16x16x32_bf16 v[112:115], a[224:227], a[8:11], v[112:115]
  ds_read_b64_tr_b16 a[242:243], v19 offset:54432
  v_accvgpr_read_b32 v229, a121
  v_mfma_f32_16x16x32_bf16 v[112:115], a[228:231], a[24:27], v[112:115]
  v_accvgpr_read_b32 v230, a122
  v_accvgpr_read_b32 v231, a123
  v_mfma_f32_16x16x32_bf16 v[144:147], a[224:227], a[12:15], v[144:147]
  ds_read_b64_tr_b16 a[244:245], v19 offset:58560
  v_mul_f32 v224, v224, v25
  v_mfma_f32_16x16x32_bf16 v[144:147], a[228:231], a[28:31], v[144:147]
  buffer_load_dwordx4 a[92:95], v8, s[60:63], s71 offen sc1
  v_mul_f32 v225, v225, v25
  v_mfma_f32_16x16x32_bf16 v[48:51], a[224:227], a[0:3], v[48:51]
  ds_read_b64_tr_b16 a[246:247], v19 offset:62688
  v_mul_f32 v226, v226, v25
  v_mul_f32 v227, v227, v25
  v_mfma_f32_16x16x32_bf16 v[48:51], a[228:231], a[16:19], v[48:51]
  s_waitcnt lgkmcnt(4)
  v_mul_f32 v228, v228, v25
  v_mfma_f32_16x16x32_bf16 v[84:87], a[232:235], a[4:7], v[84:87]
  s_add_u32 s73, s91, 4
  s_add_u32 s73, s73, s86
  s_lshl_b32 s68, s73, 17
  ds_read_b64_tr_b16 a[248:249], v19 offset:50312
  v_mul_f32 v229, v229, v25
  v_mul_f32 v230, v230, v25
  v_mfma_f32_16x16x32_bf16 v[84:87], a[236:239], a[20:23], v[84:87]
  s_lshl_b32 s70, s73, 13
  s_add_u32 s71, s70, 4096
  s_add_u32 m0, s74, 0
  v_mul_f32 v231, v231, v25
  v_mfma_f32_16x16x32_bf16 v[116:119], a[232:235], a[8:11], v[116:119]
  ds_read_b64_tr_b16 a[250:251], v19 offset:54440
  buffer_load_dwordx4 v1, s[48:51], s68 offen offset:0 lds
  v_cvt_pk_bf16_f32 v216, v224, v225
  v_cvt_pk_bf16_f32 v217, v226, v227
  v_mfma_f32_16x16x32_bf16 v[116:119], a[236:239], a[24:27], v[116:119]
  v_cvt_pk_bf16_f32 v218, v228, v229
  v_mfma_f32_16x16x32_bf16 v[148:151], a[232:235], a[12:15], v[148:151]
  ds_read_b64_tr_b16 a[252:253], v19 offset:58568
  v_cvt_pk_bf16_f32 v219, v230, v231
  v_mfma_f32_16x16x32_bf16 v[148:151], a[236:239], a[28:31], v[148:151]
  v_mfma_f32_16x16x32_bf16 v[52:55], a[232:235], a[0:3], v[52:55]
  ds_read_b64_tr_b16 a[254:255], v19 offset:62696
  buffer_store_dwordx4 v[216:219], v22, s[64:67], s85 offen offset:2048 sc1
  v_accvgpr_read_b32 v232, a108
  v_accvgpr_read_b32 v233, a109
  v_mfma_f32_16x16x32_bf16 v[52:55], a[236:239], a[16:19], v[52:55]
  s_waitcnt lgkmcnt(4)
  s_waitcnt vmcnt(18)
  s_barrier
  buffer_load_dwordx4 v2, s[48:51], s68 offen offset:1032 lds
  v_accvgpr_read_b32 v234, a110
  v_mfma_f32_16x16x32_bf16 v[88:91], a[240:243], a[4:7], v[88:91]
  ds_read_b64_tr_b16 a[224:225], v20 offset:0
  v_accvgpr_read_b32 v235, a111
  v_accvgpr_read_b32 v236, a124
  v_mfma_f32_16x16x32_bf16 v[88:91], a[244:247], a[20:23], v[88:91]
  v_accvgpr_read_b32 v237, a125
  v_mfma_f32_16x16x32_bf16 v[120:123], a[240:243], a[8:11], v[120:123]
  ds_read_b64_tr_b16 a[226:227], v20 offset:4128
  v_accvgpr_read_b32 v238, a126
  v_accvgpr_read_b32 v239, a127
  v_mfma_f32_16x16x32_bf16 v[120:123], a[244:247], a[24:27], v[120:123]
  v_mul_f32 v232, v232, v26
  v_mfma_f32_16x16x32_bf16 v[152:155], a[240:243], a[12:15], v[152:155]
  ds_read_b64_tr_b16 a[228:229], v20 offset:8256
  buffer_load_dwordx4 v3, s[48:51], s68 offen offset:2064 lds
  v_mul_f32 v233, v233, v26
  v_mfma_f32_16x16x32_bf16 v[152:155], a[244:247], a[28:31], v[152:155]
  v_mul_f32 v234, v234, v26
  v_mul_f32 v235, v235, v26
  v_mfma_f32_16x16x32_bf16 v[56:59], a[240:243], a[0:3], v[56:59]
  ds_read_b64_tr_b16 a[230:231], v20 offset:12384
  v_mul_f32 v236, v236, v26
  v_mfma_f32_16x16x32_bf16 v[56:59], a[244:247], a[16:19], v[56:59]
  s_waitcnt lgkmcnt(4)
  v_mul_f32 v237, v237, v26
  v_mul_f32 v238, v238, v26
  v_mfma_f32_16x16x32_bf16 v[92:95], a[248:251], a[4:7], v[92:95]
  ds_read_b64_tr_b16 a[232:233], v20 offset:8
  v_mul_f32 v239, v239, v26
  v_mfma_f32_16x16x32_bf16 v[92:95], a[252:255], a[20:23], v[92:95]
  buffer_load_dwordx4 v4, s[48:51], s68 offen offset:3096 lds
  v_cvt_pk_bf16_f32 v220, v232, v233
  v_cvt_pk_bf16_f32 v221, v234, v235
  v_mfma_f32_16x16x32_bf16 v[124:127], a[248:251], a[8:11], v[124:127]
  ds_read_b64_tr_b16 a[234:235], v20 offset:4136
  v_cvt_pk_bf16_f32 v222, v236, v237
  v_mfma_f32_16x16x32_bf16 v[124:127], a[252:255], a[24:27], v[124:127]
  v_cvt_pk_bf16_f32 v223, v238, v239
  v_mfma_f32_16x16x32_bf16 v[156:159], a[248:251], a[12:15], v[156:159]
  ds_read_b64_tr_b16 a[236:237], v20 offset:8264
  v_mfma_f32_16x16x32_bf16 v[156:159], a[252:255], a[28:31], v[156:159]
  buffer_store_dwordx4 v[220:223], v22, s[64:67], s85 offen offset:3072 sc1
  v_mfma_f32_16x16x32_bf16 v[60:63], a[248:251], a[0:3], v[60:63]
  ds_read_b64_tr_b16 a[238:239], v20 offset:12392
  v_mfma_f32_16x16x32_bf16 v[60:63], a[252:255], a[16:19], v[60:63]
  s_waitcnt lgkmcnt(4)
  v_mfma_f32_16x16x32_bf16 v[64:67], a[224:227], a[36:39], v[64:67]
  s_add_u32 s73, s90, 4
  s_add_u32 s73, s73, s86
  s_lshl_b32 s68, s73, 17
  ds_read_b64_tr_b16 a[240:241], v20 offset:256
  v_mfma_f32_16x16x32_bf16 v[64:67], a[228:231], a[52:55], v[64:67]
  s_lshl_b32 s70, s73, 13
  s_add_u32 s71, s70, 4096
  v_mfma_f32_16x16x32_bf16 v[96:99], a[224:227], a[40:43], v[96:99]
  ds_read_b64_tr_b16 a[242:243], v20 offset:4384
  buffer_load_dwordx4 a[96:99], v5, s[60:63], s70 offen sc1
  v_mfma_f32_16x16x32_bf16 v[96:99], a[228:231], a[56:59], v[96:99]
  v_mfma_f32_16x16x32_bf16 v[128:131], a[224:227], a[44:47], v[128:131]
  ds_read_b64_tr_b16 a[244:245], v20 offset:8512
  v_mfma_f32_16x16x32_bf16 v[128:131], a[228:231], a[60:63], v[128:131]
  buffer_load_dwordx4 a[100:103], v6, s[60:63], s70 offen sc1
  v_mfma_f32_16x16x32_bf16 v[32:35], a[224:227], a[32:35], v[32:35]
  ds_read_b64_tr_b16 a[246:247], v20 offset:12640
  v_mfma_f32_16x16x32_bf16 v[32:35], a[228:231], a[48:51], v[32:35]
  s_waitcnt lgkmcnt(4)
  v_mfma_f32_16x16x32_bf16 v[68:71], a[232:235], a[36:39], v[68:71]
  ds_read_b64_tr_b16 a[248:249], v20 offset:264
  buffer_load_dwordx4 a[104:107], v7, s[60:63], s70 offen sc1
  v_mfma_f32_16x16x32_bf16 v[68:71], a[236:239], a[52:55], v[68:71]
  v_mfma_f32_16x16x32_bf16 v[100:103], a[232:235], a[40:43], v[100:103]
  ds_read_b64_tr_b16 a[250:251], v20 offset:4392
  v_mfma_f32_16x16x32_bf16 v[100:103], a[236:239], a[56:59], v[100:103]
  buffer_load_dwordx4 a[108:111], v8, s[60:63], s70 offen sc1
  v_mfma_f32_16x16x32_bf16 v[132:135], a[232:235], a[44:47], v[132:135]
  ds_read_b64_tr_b16 a[252:253], v20 offset:8520
  v_mfma_f32_16x16x32_bf16 v[132:135], a[236:239], a[60:63], v[132:135]
  v_mfma_f32_16x16x32_bf16 v[36:39], a[232:235], a[32:35], v[36:39]
  ds_read_b64_tr_b16 a[254:255], v20 offset:12648
  buffer_load_dwordx4 a[112:115], v5, s[60:63], s71 offen sc1
  v_mfma_f32_16x16x32_bf16 v[36:39], a[236:239], a[48:51], v[36:39]
  s_waitcnt lgkmcnt(4)
  v_mfma_f32_16x16x32_bf16 v[72:75], a[240:243], a[36:39], v[72:75]
  ds_read_b64_tr_b16 a[224:225], v20 offset:512
  v_mfma_f32_16x16x32_bf16 v[72:75], a[244:247], a[52:55], v[72:75]
  buffer_load_dwordx4 a[116:119], v6, s[60:63], s71 offen sc1
  v_mfma_f32_16x16x32_bf16 v[104:107], a[240:243], a[40:43], v[104:107]
  ds_read_b64_tr_b16 a[226:227], v20 offset:4640
  v_mfma_f32_16x16x32_bf16 v[104:107], a[244:247], a[56:59], v[104:107]
  v_mfma_f32_16x16x32_bf16 v[136:139], a[240:243], a[44:47], v[136:139]
  ds_read_b64_tr_b16 a[228:229], v20 offset:8768
  buffer_load_dwordx4 a[120:123], v7, s[60:63], s71 offen sc1
  v_mfma_f32_16x16x32_bf16 v[136:139], a[244:247], a[60:63], v[136:139]
  v_mfma_f32_16x16x32_bf16 v[40:43], a[240:243], a[32:35], v[40:43]
  ds_read_b64_tr_b16 a[230:231], v20 offset:12896
  v_mfma_f32_16x16x32_bf16 v[40:43], a[244:247], a[48:51], v[40:43]
  s_waitcnt lgkmcnt(4)
  buffer_load_dwordx4 a[124:127], v8, s[60:63], s71 offen sc1
  v_mfma_f32_16x16x32_bf16 v[76:79], a[248:251], a[36:39], v[76:79]
  s_add_u32 s73, s91, 4
  s_add_u32 s73, s73, s86
  s_lshl_b32 s68, s73, 17
  ds_read_b64_tr_b16 a[232:233], v20 offset:520
  v_mfma_f32_16x16x32_bf16 v[76:79], a[252:255], a[52:55], v[76:79]
  s_lshl_b32 s70, s73, 13
  s_add_u32 s71, s70, 4096
  v_mfma_f32_16x16x32_bf16 v[108:111], a[248:251], a[40:43], v[108:111]
  ds_read_b64_tr_b16 a[234:235], v20 offset:4648
  buffer_load_dwordx4 a[128:131], v5, s[60:63], s70 offen sc1
  v_mfma_f32_16x16x32_bf16 v[108:111], a[252:255], a[56:59], v[108:111]
  v_mfma_f32_16x16x32_bf16 v[140:143], a[248:251], a[44:47], v[140:143]
  ds_read_b64_tr_b16 a[236:237], v20 offset:8776
  v_mfma_f32_16x16x32_bf16 v[140:143], a[252:255], a[60:63], v[140:143]
  buffer_load_dwordx4 a[132:135], v6, s[60:63], s70 offen sc1
  v_mfma_f32_16x16x32_bf16 v[44:47], a[248:251], a[32:35], v[44:47]
  ds_read_b64_tr_b16 a[238:239], v20 offset:12904
  v_mfma_f32_16x16x32_bf16 v[44:47], a[252:255], a[48:51], v[44:47]
  s_waitcnt lgkmcnt(4)
  v_mfma_f32_16x16x32_bf16 v[80:83], a[224:227], a[36:39], v[80:83]
  ds_read_b64_tr_b16 a[240:241], v20 offset:768
  buffer_load_dwordx4 a[136:139], v7, s[60:63], s70 offen sc1
  v_mfma_f32_16x16x32_bf16 v[80:83], a[228:231], a[52:55], v[80:83]
  v_mfma_f32_16x16x32_bf16 v[112:115], a[224:227], a[40:43], v[112:115]
  ds_read_b64_tr_b16 a[242:243], v20 offset:4896
  v_mfma_f32_16x16x32_bf16 v[112:115], a[228:231], a[56:59], v[112:115]
  buffer_load_dwordx4 a[140:143], v8, s[60:63], s70 offen sc1
  v_mfma_f32_16x16x32_bf16 v[144:147], a[224:227], a[44:47], v[144:147]
  ds_read_b64_tr_b16 a[244:245], v20 offset:9024
  v_mfma_f32_16x16x32_bf16 v[144:147], a[228:231], a[60:63], v[144:147]
  v_mfma_f32_16x16x32_bf16 v[48:51], a[224:227], a[32:35], v[48:51]
  ds_read_b64_tr_b16 a[246:247], v20 offset:13152
  buffer_load_dwordx4 a[144:147], v5, s[60:63], s71 offen sc1
  v_mfma_f32_16x16x32_bf16 v[48:51], a[228:231], a[48:51], v[48:51]
  s_waitcnt lgkmcnt(4)
  v_mfma_f32_16x16x32_bf16 v[84:87], a[232:235], a[36:39], v[84:87]
  ds_read_b64_tr_b16 a[248:249], v20 offset:776
  v_mfma_f32_16x16x32_bf16 v[84:87], a[236:239], a[52:55], v[84:87]
  buffer_load_dwordx4 a[148:151], v6, s[60:63], s71 offen sc1
  v_mfma_f32_16x16x32_bf16 v[116:119], a[232:235], a[40:43], v[116:119]
  ds_read_b64_tr_b16 a[250:251], v20 offset:4904
  v_mfma_f32_16x16x32_bf16 v[116:119], a[236:239], a[56:59], v[116:119]
  v_mfma_f32_16x16x32_bf16 v[148:151], a[232:235], a[44:47], v[148:151]
  ds_read_b64_tr_b16 a[252:253], v20 offset:9032
  buffer_load_dwordx4 a[152:155], v7, s[60:63], s71 offen sc1
  v_mfma_f32_16x16x32_bf16 v[148:151], a[236:239], a[60:63], v[148:151]
  v_mfma_f32_16x16x32_bf16 v[52:55], a[232:235], a[32:35], v[52:55]
  ds_read_b64_tr_b16 a[254:255], v20 offset:13160
  v_mfma_f32_16x16x32_bf16 v[52:55], a[236:239], a[48:51], v[52:55]
  s_waitcnt lgkmcnt(4)
  s_waitcnt vmcnt(21)
  s_barrier
  buffer_load_dwordx4 a[156:159], v8, s[60:63], s71 offen sc1
  v_mfma_f32_16x16x32_bf16 v[88:91], a[240:243], a[36:39], v[88:91]
  s_add_u32 s73, s88, 8
  s_add_u32 s73, s73, s86
  s_lshl_b32 s68, s73, 17
  ds_read_b64_tr_b16 a[224:225], v20 offset:16512
  v_mfma_f32_16x16x32_bf16 v[88:91], a[244:247], a[52:55], v[88:91]
  s_lshl_b32 s70, s73, 13
  s_add_u32 s71, s70, 4096
  s_add_u32 m0, s74, 16512
  v_mfma_f32_16x16x32_bf16 v[120:123], a[240:243], a[40:43], v[120:123]
  ds_read_b64_tr_b16 a[226:227], v20 offset:20640
  buffer_load_dwordx4 v1, s[48:51], s68 offen offset:0 lds
  v_mfma_f32_16x16x32_bf16 v[120:123], a[244:247], a[56:59], v[120:123]
  v_mfma_f32_16x16x32_bf16 v[152:155], a[240:243], a[44:47], v[152:155]
  ds_read_b64_tr_b16 a[228:229], v20 offset:24768
  v_mfma_f32_16x16x32_bf16 v[152:155], a[244:247], a[60:63], v[152:155]
  buffer_load_dwordx4 v2, s[48:51], s68 offen offset:1032 lds
  v_mfma_f32_16x16x32_bf16 v[56:59], a[240:243], a[32:35], v[56:59]
  ds_read_b64_tr_b16 a[230:231], v20 offset:28896
  v_mfma_f32_16x16x32_bf16 v[56:59], a[244:247], a[48:51], v[56:59]
  s_waitcnt lgkmcnt(4)
  v_mfma_f32_16x16x32_bf16 v[92:95], a[248:251], a[36:39], v[92:95]
  ds_read_b64_tr_b16 a[232:233], v20 offset:16520
  buffer_load_dwordx4 v3, s[48:51], s68 offen offset:2064 lds
  v_mfma_f32_16x16x32_bf16 v[92:95], a[252:255], a[52:55], v[92:95]
  v_mfma_f32_16x16x32_bf16 v[124:127], a[248:251], a[40:43], v[124:127]
  ds_read_b64_tr_b16 a[234:235], v20 offset:20648
  v_mfma_f32_16x16x32_bf16 v[124:127], a[252:255], a[56:59], v[124:127]
  buffer_load_dwordx4 v4, s[48:51], s68 offen offset:3096 lds
  v_mfma_f32_16x16x32_bf16 v[156:159], a[248:251], a[44:47], v[156:159]
  ds_read_b64_tr_b16 a[236:237], v20 offset:24776
  v_mfma_f32_16x16x32_bf16 v[156:159], a[252:255], a[60:63], v[156:159]
  v_mfma_f32_16x16x32_bf16 v[60:63], a[248:251], a[32:35], v[60:63]
  ds_read_b64_tr_b16 a[238:239], v20 offset:28904
  v_mfma_f32_16x16x32_bf16 v[60:63], a[252:255], a[48:51], v[60:63]
  s_waitcnt lgkmcnt(4)
  v_mfma_f32_16x16x32_bf16 v[64:67], a[224:227], a[68:71], v[64:67]
  s_add_u32 s73, s88, 8
  s_add_u32 s73, s73, s86
  s_lshl_b32 s68, s73, 17
  ds_read_b64_tr_b16 a[240:241], v20 offset:16768
  v_mfma_f32_16x16x32_bf16 v[64:67], a[228:231], a[84:87], v[64:67]
  s_lshl_b32 s70, s73, 13
  s_add_u32 s71, s70, 4096
  v_mfma_f32_16x16x32_bf16 v[96:99], a[224:227], a[72:75], v[96:99]
  ds_read_b64_tr_b16 a[242:243], v20 offset:20896
  buffer_load_dwordx4 a[160:163], v5, s[60:63], s70 offen sc1
  v_mfma_f32_16x16x32_bf16 v[96:99], a[228:231], a[88:91], v[96:99]
  v_mfma_f32_16x16x32_bf16 v[128:131], a[224:227], a[76:79], v[128:131]
  ds_read_b64_tr_b16 a[244:245], v20 offset:25024
  v_mfma_f32_16x16x32_bf16 v[128:131], a[228:231], a[92:95], v[128:131]
  buffer_load_dwordx4 a[164:167], v6, s[60:63], s70 offen sc1
  v_mfma_f32_16x16x32_bf16 v[32:35], a[224:227], a[64:67], v[32:35]
  ds_read_b64_tr_b16 a[246:247], v20 offset:29152
  v_mfma_f32_16x16x32_bf16 v[32:35], a[228:231], a[80:83], v[32:35]
  s_waitcnt lgkmcnt(4)
  v_mfma_f32_16x16x32_bf16 v[68:71], a[232:235], a[68:71], v[68:71]
  ds_read_b64_tr_b16 a[248:249], v20 offset:16776
  buffer_load_dwordx4 a[168:171], v7, s[60:63], s70 offen sc1
  v_mfma_f32_16x16x32_bf16 v[68:71], a[236:239], a[84:87], v[68:71]
  v_mfma_f32_16x16x32_bf16 v[100:103], a[232:235], a[72:75], v[100:103]
  ds_read_b64_tr_b16 a[250:251], v20 offset:20904
  v_mfma_f32_16x16x32_bf16 v[100:103], a[236:239], a[88:91], v[100:103]
  buffer_load_dwordx4 a[172:175], v8, s[60:63], s70 offen sc1
  v_mfma_f32_16x16x32_bf16 v[132:135], a[232:235], a[76:79], v[132:135]
  ds_read_b64_tr_b16 a[252:253], v20 offset:25032
  v_mfma_f32_16x16x32_bf16 v[132:135], a[236:239], a[92:95], v[132:135]
  v_mfma_f32_16x16x32_bf16 v[36:39], a[232:235], a[64:67], v[36:39]
  ds_read_b64_tr_b16 a[254:255], v20 offset:29160
  buffer_load_dwordx4 a[176:179], v5, s[60:63], s71 offen sc1
  v_mfma_f32_16x16x32_bf16 v[36:39], a[236:239], a[80:83], v[36:39]
  s_waitcnt lgkmcnt(4)
  v_mfma_f32_16x16x32_bf16 v[72:75], a[240:243], a[68:71], v[72:75]
  ds_read_b64_tr_b16 a[224:225], v20 offset:17024
  v_mfma_f32_16x16x32_bf16 v[72:75], a[244:247], a[84:87], v[72:75]
  buffer_load_dwordx4 a[180:183], v6, s[60:63], s71 offen sc1
  v_mfma_f32_16x16x32_bf16 v[104:107], a[240:243], a[72:75], v[104:107]
  ds_read_b64_tr_b16 a[226:227], v20 offset:21152
  v_mfma_f32_16x16x32_bf16 v[104:107], a[244:247], a[88:91], v[104:107]
  v_mfma_f32_16x16x32_bf16 v[136:139], a[240:243], a[76:79], v[136:139]
  ds_read_b64_tr_b16 a[228:229], v20 offset:25280
  buffer_load_dwordx4 a[184:187], v7, s[60:63], s71 offen sc1
  v_mfma_f32_16x16x32_bf16 v[136:139], a[244:247], a[92:95], v[136:139]
  v_mfma_f32_16x16x32_bf16 v[40:43], a[240:243], a[64:67], v[40:43]
  ds_read_b64_tr_b16 a[230:231], v20 offset:29408
  v_mfma_f32_16x16x32_bf16 v[40:43], a[244:247], a[80:83], v[40:43]
  s_waitcnt lgkmcnt(4)
  buffer_load_dwordx4 a[188:191], v8, s[60:63], s71 offen sc1
  v_mfma_f32_16x16x32_bf16 v[76:79], a[248:251], a[68:71], v[76:79]
  s_add_u32 s73, s89, 8
  s_add_u32 s73, s73, s86
  s_lshl_b32 s68, s73, 17
  ds_read_b64_tr_b16 a[232:233], v20 offset:17032
  v_mfma_f32_16x16x32_bf16 v[76:79], a[252:255], a[84:87], v[76:79]
  s_lshl_b32 s70, s73, 13
  s_add_u32 s71, s70, 4096
  s_add_u32 m0, s74, 33024
  v_mfma_f32_16x16x32_bf16 v[108:111], a[248:251], a[72:75], v[108:111]
  ds_read_b64_tr_b16 a[234:235], v20 offset:21160
  buffer_load_dwordx4 v1, s[48:51], s68 offen offset:0 lds
  v_mfma_f32_16x16x32_bf16 v[108:111], a[252:255], a[88:91], v[108:111]
  v_mfma_f32_16x16x32_bf16 v[140:143], a[248:251], a[76:79], v[140:143]
  ds_read_b64_tr_b16 a[236:237], v20 offset:25288
  v_mfma_f32_16x16x32_bf16 v[140:143], a[252:255], a[92:95], v[140:143]
  buffer_load_dwordx4 a[192:195], v5, s[60:63], s70 offen sc1
  v_mfma_f32_16x16x32_bf16 v[44:47], a[248:251], a[64:67], v[44:47]
  ds_read_b64_tr_b16 a[238:239], v20 offset:29416
  v_mfma_f32_16x16x32_bf16 v[44:47], a[252:255], a[80:83], v[44:47]
  s_waitcnt lgkmcnt(4)
  v_mfma_f32_16x16x32_bf16 v[80:83], a[224:227], a[68:71], v[80:83]
  ds_read_b64_tr_b16 a[240:241], v20 offset:17280
  buffer_load_dwordx4 a[196:199], v6, s[60:63], s70 offen sc1
  v_mfma_f32_16x16x32_bf16 v[80:83], a[228:231], a[84:87], v[80:83]
  v_mfma_f32_16x16x32_bf16 v[112:115], a[224:227], a[72:75], v[112:115]
  ds_read_b64_tr_b16 a[242:243], v20 offset:21408
  v_mfma_f32_16x16x32_bf16 v[112:115], a[228:231], a[88:91], v[112:115]
  buffer_load_dwordx4 v2, s[48:51], s68 offen offset:1032 lds
  v_mfma_f32_16x16x32_bf16 v[144:147], a[224:227], a[76:79], v[144:147]
  ds_read_b64_tr_b16 a[244:245], v20 offset:25536
  v_mfma_f32_16x16x32_bf16 v[144:147], a[228:231], a[92:95], v[144:147]
  v_mfma_f32_16x16x32_bf16 v[48:51], a[224:227], a[64:67], v[48:51]
  ds_read_b64_tr_b16 a[246:247], v20 offset:29664
  buffer_load_dwordx4 a[200:203], v7, s[60:63], s70 offen sc1
  v_mfma_f32_16x16x32_bf16 v[48:51], a[228:231], a[80:83], v[48:51]
  s_waitcnt lgkmcnt(4)
  v_mfma_f32_16x16x32_bf16 v[84:87], a[232:235], a[68:71], v[84:87]
  ds_read_b64_tr_b16 a[248:249], v20 offset:17288
  v_mfma_f32_16x16x32_bf16 v[84:87], a[236:239], a[84:87], v[84:87]
  buffer_load_dwordx4 a[204:207], v8, s[60:63], s70 offen sc1
  v_mfma_f32_16x16x32_bf16 v[116:119], a[232:235], a[72:75], v[116:119]
  ds_read_b64_tr_b16 a[250:251], v20 offset:21416
  v_mfma_f32_16x16x32_bf16 v[116:119], a[236:239], a[88:91], v[116:119]
  v_mfma_f32_16x16x32_bf16 v[148:151], a[232:235], a[76:79], v[148:151]
  ds_read_b64_tr_b16 a[252:253], v20 offset:25544
  buffer_load_dwordx4 v3, s[48:51], s68 offen offset:2064 lds
  v_mfma_f32_16x16x32_bf16 v[148:151], a[236:239], a[92:95], v[148:151]
  v_mfma_f32_16x16x32_bf16 v[52:55], a[232:235], a[64:67], v[52:55]
  ds_read_b64_tr_b16 a[254:255], v20 offset:29672
  v_mfma_f32_16x16x32_bf16 v[52:55], a[236:239], a[80:83], v[52:55]
  s_waitcnt lgkmcnt(4)
  s_waitcnt vmcnt(27)
  s_barrier
  buffer_load_dwordx4 a[208:211], v5, s[60:63], s71 offen sc1
  v_mfma_f32_16x16x32_bf16 v[88:91], a[240:243], a[68:71], v[88:91]
  ds_read_b64_tr_b16 a[224:225], v20 offset:33024
  s_lshl_b32 s72, s11, 8
  s_add_u32 s72, s72, 65664
  v_mov_b32 v246, s72
  s_mov_b64 exec, 1
  buffer_atomic_add v11, v246, s[44:47], 0 offen sc1
  s_mov_b64 exec, -1
  v_mfma_f32_16x16x32_bf16 v[88:91], a[244:247], a[84:87], v[88:91]
  v_mfma_f32_16x16x32_bf16 v[120:123], a[240:243], a[72:75], v[120:123]
  ds_read_b64_tr_b16 a[226:227], v20 offset:37152
  buffer_load_dwordx4 a[212:215], v6, s[60:63], s71 offen sc1
  v_mfma_f32_16x16x32_bf16 v[120:123], a[244:247], a[88:91], v[120:123]
  v_mfma_f32_16x16x32_bf16 v[152:155], a[240:243], a[76:79], v[152:155]
  ds_read_b64_tr_b16 a[228:229], v20 offset:41280
  v_mfma_f32_16x16x32_bf16 v[152:155], a[244:247], a[92:95], v[152:155]
  buffer_load_dwordx4 v4, s[48:51], s68 offen offset:3096 lds
  v_mfma_f32_16x16x32_bf16 v[56:59], a[240:243], a[64:67], v[56:59]
  ds_read_b64_tr_b16 a[230:231], v20 offset:45408
  v_mfma_f32_16x16x32_bf16 v[56:59], a[244:247], a[80:83], v[56:59]
  s_waitcnt lgkmcnt(4)
  v_mfma_f32_16x16x32_bf16 v[92:95], a[248:251], a[68:71], v[92:95]
  ds_read_b64_tr_b16 a[232:233], v20 offset:33032
  buffer_load_dwordx4 a[216:219], v7, s[60:63], s71 offen sc1
  v_mfma_f32_16x16x32_bf16 v[92:95], a[252:255], a[84:87], v[92:95]
  v_mfma_f32_16x16x32_bf16 v[124:127], a[248:251], a[72:75], v[124:127]
  ds_read_b64_tr_b16 a[234:235], v20 offset:37160
  v_mfma_f32_16x16x32_bf16 v[124:127], a[252:255], a[88:91], v[124:127]
  buffer_load_dwordx4 a[220:223], v8, s[60:63], s71 offen sc1
  v_mfma_f32_16x16x32_bf16 v[156:159], a[248:251], a[76:79], v[156:159]
  ds_read_b64_tr_b16 a[236:237], v20 offset:41288
  v_mfma_f32_16x16x32_bf16 v[156:159], a[252:255], a[92:95], v[156:159]
  v_mfma_f32_16x16x32_bf16 v[60:63], a[248:251], a[64:67], v[60:63]
  ds_read_b64_tr_b16 a[238:239], v20 offset:45416
  v_mfma_f32_16x16x32_bf16 v[60:63], a[252:255], a[80:83], v[60:63]
  s_waitcnt lgkmcnt(4)
  v_mfma_f32_16x16x32_bf16 v[64:67], a[224:227], a[100:103], v[64:67]
  s_add_u32 s73, s90, 8
  s_add_u32 s73, s73, s86
  s_lshl_b32 s68, s73, 17
  ds_read_b64_tr_b16 a[240:241], v20 offset:33280
  v_mfma_f32_16x16x32_bf16 v[64:67], a[228:231], a[116:119], v[64:67]
  s_lshl_b32 s70, s73, 13
  s_add_u32 s71, s70, 4096
  s_add_u32 m0, s74, 49536
  v_mfma_f32_16x16x32_bf16 v[96:99], a[224:227], a[104:107], v[96:99]
  ds_read_b64_tr_b16 a[242:243], v20 offset:37408
  buffer_load_dwordx4 v1, s[48:51], s68 offen offset:0 lds
  v_mfma_f32_16x16x32_bf16 v[96:99], a[228:231], a[120:123], v[96:99]
  v_lshlrev_b32 v252, 4, v0
  v_mfma_f32_16x16x32_bf16 v[128:131], a[224:227], a[108:111], v[128:131]
  ds_read_b64_tr_b16 a[244:245], v20 offset:41536
  v_mfma_f32_16x16x32_bf16 v[128:131], a[228:231], a[124:127], v[128:131]
  buffer_load_dwordx4 v[248:251], v252, s[4:7], s3 offen
  v_mfma_f32_16x16x32_bf16 v[32:35], a[224:227], a[96:99], v[32:35]
  ds_read_b64_tr_b16 a[246:247], v20 offset:45664
  v_mfma_f32_16x16x32_bf16 v[32:35], a[228:231], a[112:115], v[32:35]
  s_waitcnt lgkmcnt(4)
  buffer_load_dwordx4 a[0:3], v5, s[60:63], s70 offen sc1
  v_mfma_f32_16x16x32_bf16 v[68:71], a[232:235], a[100:103], v[68:71]
  ds_read_b64_tr_b16 a[248:249], v20 offset:33288
  v_mfma_f32_16x16x32_bf16 v[68:71], a[236:239], a[116:119], v[68:71]
  v_mfma_f32_16x16x32_bf16 v[100:103], a[232:235], a[104:107], v[100:103]
  ds_read_b64_tr_b16 a[250:251], v20 offset:37416
  v_mfma_f32_16x16x32_bf16 v[100:103], a[236:239], a[120:123], v[100:103]
  v_mfma_f32_16x16x32_bf16 v[132:135], a[232:235], a[108:111], v[132:135]
  ds_read_b64_tr_b16 a[252:253], v20 offset:41544
  buffer_load_dwordx4 a[4:7], v6, s[60:63], s70 offen sc1
  v_mfma_f32_16x16x32_bf16 v[132:135], a[236:239], a[124:127], v[132:135]
  v_mfma_f32_16x16x32_bf16 v[36:39], a[232:235], a[96:99], v[36:39]
  ds_read_b64_tr_b16 a[254:255], v20 offset:45672
  v_mfma_f32_16x16x32_bf16 v[36:39], a[236:239], a[112:115], v[36:39]
  s_waitcnt lgkmcnt(4)
  v_mfma_f32_16x16x32_bf16 v[72:75], a[240:243], a[100:103], v[72:75]
  ds_read_b64_tr_b16 a[224:225], v20 offset:33536
  v_mfma_f32_16x16x32_bf16 v[72:75], a[244:247], a[116:119], v[72:75]
  buffer_load_dwordx4 v2, s[48:51], s68 offen offset:1032 lds
  v_mfma_f32_16x16x32_bf16 v[104:107], a[240:243], a[104:107], v[104:107]
  ds_read_b64_tr_b16 a[226:227], v20 offset:37664
  v_mfma_f32_16x16x32_bf16 v[104:107], a[244:247], a[120:123], v[104:107]
  v_mfma_f32_16x16x32_bf16 v[136:139], a[240:243], a[108:111], v[136:139]
  ds_read_b64_tr_b16 a[228:229], v20 offset:41792
  v_mfma_f32_16x16x32_bf16 v[136:139], a[244:247], a[124:127], v[136:139]
  v_mfma_f32_16x16x32_bf16 v[40:43], a[240:243], a[96:99], v[40:43]
  ds_read_b64_tr_b16 a[230:231], v20 offset:45920
  buffer_load_dwordx4 a[8:11], v7, s[60:63], s70 offen sc1
  v_mfma_f32_16x16x32_bf16 v[40:43], a[244:247], a[112:115], v[40:43]
  s_waitcnt lgkmcnt(4)
  v_mfma_f32_16x16x32_bf16 v[76:79], a[248:251], a[100:103], v[76:79]
  ds_read_b64_tr_b16 a[232:233], v20 offset:33544
  v_mfma_f32_16x16x32_bf16 v[76:79], a[252:255], a[116:119], v[76:79]
  v_mfma_f32_16x16x32_bf16 v[108:111], a[248:251], a[104:107], v[108:111]
  ds_read_b64_tr_b16 a[234:235], v20 offset:37672
  v_mfma_f32_16x16x32_bf16 v[108:111], a[252:255], a[120:123], v[108:111]
  buffer_load_dwordx4 a[12:15], v8, s[60:63], s70 offen sc1
  v_mfma_f32_16x16x32_bf16 v[140:143], a[248:251], a[108:111], v[140:143]
  ds_read_b64_tr_b16 a[236:237], v20 offset:41800
  v_mfma_f32_16x16x32_bf16 v[140:143], a[252:255], a[124:127], v[140:143]
  v_mfma_f32_16x16x32_bf16 v[44:47], a[248:251], a[96:99], v[44:47]
  ds_read_b64_tr_b16 a[238:239], v20 offset:45928
  v_mfma_f32_16x16x32_bf16 v[44:47], a[252:255], a[112:115], v[44:47]
  s_waitcnt lgkmcnt(4)
  v_mfma_f32_16x16x32_bf16 v[80:83], a[224:227], a[100:103], v[80:83]
  ds_read_b64_tr_b16 a[240:241], v20 offset:33792
  buffer_load_dwordx4 v3, s[48:51], s68 offen offset:2064 lds
  v_mfma_f32_16x16x32_bf16 v[80:83], a[228:231], a[116:119], v[80:83]
  v_mfma_f32_16x16x32_bf16 v[112:115], a[224:227], a[104:107], v[112:115]
  ds_read_b64_tr_b16 a[242:243], v20 offset:37920
  v_mfma_f32_16x16x32_bf16 v[112:115], a[228:231], a[120:123], v[112:115]
  v_mfma_f32_16x16x32_bf16 v[144:147], a[224:227], a[108:111], v[144:147]
  ds_read_b64_tr_b16 a[244:245], v20 offset:42048
  v_mfma_f32_16x16x32_bf16 v[144:147], a[228:231], a[124:127], v[144:147]
  buffer_load_dwordx4 a[16:19], v5, s[60:63], s71 offen sc1
  v_mfma_f32_16x16x32_bf16 v[48:51], a[224:227], a[96:99], v[48:51]
  ds_read_b64_tr_b16 a[246:247], v20 offset:46176
  v_mfma_f32_16x16x32_bf16 v[48:51], a[228:231], a[112:115], v[48:51]
  s_waitcnt lgkmcnt(4)
  v_mfma_f32_16x16x32_bf16 v[84:87], a[232:235], a[100:103], v[84:87]
  ds_read_b64_tr_b16 a[248:249], v20 offset:33800
  v_mfma_f32_16x16x32_bf16 v[84:87], a[236:239], a[116:119], v[84:87]
  v_mfma_f32_16x16x32_bf16 v[116:119], a[232:235], a[104:107], v[116:119]
  ds_read_b64_tr_b16 a[250:251], v20 offset:37928
  buffer_load_dwordx4 a[20:23], v6, s[60:63], s71 offen sc1
  v_mfma_f32_16x16x32_bf16 v[116:119], a[236:239], a[120:123], v[116:119]
  v_mfma_f32_16x16x32_bf16 v[148:151], a[232:235], a[108:111], v[148:151]
  ds_read_b64_tr_b16 a[252:253], v20 offset:42056
  v_mfma_f32_16x16x32_bf16 v[148:151], a[236:239], a[124:127], v[148:151]
  v_mfma_f32_16x16x32_bf16 v[52:55], a[232:235], a[96:99], v[52:55]
  ds_read_b64_tr_b16 a[254:255], v20 offset:46184
  v_mfma_f32_16x16x32_bf16 v[52:55], a[236:239], a[112:115], v[52:55]
  s_waitcnt lgkmcnt(4)
  s_waitcnt vmcnt(35)
  s_barrier
  buffer_load_dwordx4 v4, s[48:51], s68 offen offset:3096 lds
  v_mfma_f32_16x16x32_bf16 v[88:91], a[240:243], a[100:103], v[88:91]
  ds_read_b64_tr_b16 a[224:225], v19 offset:0
  v_mfma_f32_16x16x32_bf16 v[88:91], a[244:247], a[116:119], v[88:91]
  v_mfma_f32_16x16x32_bf16 v[120:123], a[240:243], a[104:107], v[120:123]
  ds_read_b64_tr_b16 a[226:227], v19 offset:4128
  v_mfma_f32_16x16x32_bf16 v[120:123], a[244:247], a[120:123], v[120:123]
  v_mfma_f32_16x16x32_bf16 v[152:155], a[240:243], a[108:111], v[152:155]
  ds_read_b64_tr_b16 a[228:229], v19 offset:8256
  buffer_load_dwordx4 a[24:27], v7, s[60:63], s71 offen sc1
  v_mfma_f32_16x16x32_bf16 v[152:155], a[244:247], a[124:127], v[152:155]
  v_mfma_f32_16x16x32_bf16 v[56:59], a[240:243], a[96:99], v[56:59]
  ds_read_b64_tr_b16 a[230:231], v19 offset:12384
  v_mfma_f32_16x16x32_bf16 v[56:59], a[244:247], a[112:115], v[56:59]
  s_waitcnt lgkmcnt(4)
  v_mfma_f32_16x16x32_bf16 v[92:95], a[248:251], a[100:103], v[92:95]
  ds_read_b64_tr_b16 a[232:233], v19 offset:8
  v_mfma_f32_16x16x32_bf16 v[92:95], a[252:255], a[116:119], v[92:95]
  buffer_load_dwordx4 a[28:31], v8, s[60:63], s71 offen sc1
  v_mfma_f32_16x16x32_bf16 v[124:127], a[248:251], a[104:107], v[124:127]
  ds_read_b64_tr_b16 a[234:235], v19 offset:4136
  v_mfma_f32_16x16x32_bf16 v[124:127], a[252:255], a[120:123], v[124:127]
  v_mfma_f32_16x16x32_bf16 v[156:159], a[248:251], a[108:111], v[156:159]
  ds_read_b64_tr_b16 a[236:237], v19 offset:8264
  v_mfma_f32_16x16x32_bf16 v[156:159], a[252:255], a[124:127], v[156:159]
  v_mfma_f32_16x16x32_bf16 v[60:63], a[248:251], a[96:99], v[60:63]
  ds_read_b64_tr_b16 a[238:239], v19 offset:12392
  v_mfma_f32_16x16x32_bf16 v[60:63], a[252:255], a[112:115], v[60:63]
  s_waitcnt lgkmcnt(4)
  v_mfma_f32_16x16x32_bf16 v[64:67], a[224:227], a[132:135], v[64:67]
  s_add_u32 s73, s91, 8
  s_add_u32 s73, s73, s86
  s_lshl_b32 s68, s73, 17
  ds_read_b64_tr_b16 a[240:241], v19 offset:256
  v_mfma_f32_16x16x32_bf16 v[64:67], a[228:231], a[148:151], v[64:67]
  s_lshl_b32 s70, s73, 13
  s_add_u32 s71, s70, 4096
  s_add_u32 m0, s74, 66048
  v_mfma_f32_16x16x32_bf16 v[96:99], a[224:227], a[136:139], v[96:99]
  ds_read_b64_tr_b16 a[242:243], v19 offset:4384
  buffer_load_dwordx4 v1, s[48:51], s68 offen offset:0 lds
  v_mfma_f32_16x16x32_bf16 v[96:99], a[228:231], a[152:155], v[96:99]
  v_mfma_f32_16x16x32_bf16 v[128:131], a[224:227], a[140:143], v[128:131]
  ds_read_b64_tr_b16 a[244:245], v19 offset:8512
  v_mfma_f32_16x16x32_bf16 v[128:131], a[228:231], a[156:159], v[128:131]
  v_mfma_f32_16x16x32_bf16 v[32:35], a[224:227], a[128:131], v[32:35]
  ds_read_b64_tr_b16 a[246:247], v19 offset:12640
  buffer_load_dwordx4 a[32:35], v5, s[60:63], s70 offen sc1
  v_mfma_f32_16x16x32_bf16 v[32:35], a[228:231], a[144:147], v[32:35]
  s_waitcnt lgkmcnt(4)
  v_mfma_f32_16x16x32_bf16 v[68:71], a[232:235], a[132:135], v[68:71]
  ds_read_b64_tr_b16 a[248:249], v19 offset:264
  v_mfma_f32_16x16x32_bf16 v[68:71], a[236:239], a[148:151], v[68:71]
  v_mfma_f32_16x16x32_bf16 v[100:103], a[232:235], a[136:139], v[100:103]
  ds_read_b64_tr_b16 a[250:251], v19 offset:4392
  v_mfma_f32_16x16x32_bf16 v[100:103], a[236:239], a[152:155], v[100:103]
  buffer_load_dwordx4 a[36:39], v6, s[60:63], s70 offen sc1
  v_mfma_f32_16x16x32_bf16 v[132:135], a[232:235], a[140:143], v[132:135]
  ds_read_b64_tr_b16 a[252:253], v19 offset:8520
  v_mfma_f32_16x16x32_bf16 v[132:135], a[236:239], a[156:159], v[132:135]
  v_mfma_f32_16x16x32_bf16 v[36:39], a[232:235], a[128:131], v[36:39]
  ds_read_b64_tr_b16 a[254:255], v19 offset:12648
  v_mfma_f32_16x16x32_bf16 v[36:39], a[236:239], a[144:147], v[36:39]
  s_waitcnt lgkmcnt(4)
  v_mfma_f32_16x16x32_bf16 v[72:75], a[240:243], a[132:135], v[72:75]
  ds_read_b64_tr_b16 a[224:225], v19 offset:512
  buffer_load_dwordx4 v2, s[48:51], s68 offen offset:1032 lds
  v_mfma_f32_16x16x32_bf16 v[72:75], a[244:247], a[148:151], v[72:75]
  v_mfma_f32_16x16x32_bf16 v[104:107], a[240:243], a[136:139], v[104:107]
  ds_read_b64_tr_b16 a[226:227], v19 offset:4640
  v_mfma_f32_16x16x32_bf16 v[104:107], a[244:247], a[152:155], v[104:107]
  v_mfma_f32_16x16x32_bf16 v[136:139], a[240:243], a[140:143], v[136:139]
  ds_read_b64_tr_b16 a[228:229], v19 offset:8768
  buffer_load_dwordx4 a[40:43], v7, s[60:63], s70 offen sc1
  v_mfma_f32_16x16x32_bf16 v[136:139], a[244:247], a[156:159], v[136:139]
  v_mfma_f32_16x16x32_bf16 v[40:43], a[240:243], a[128:131], v[40:43]
  ds_read_b64_tr_b16 a[230:231], v19 offset:12896
  v_mfma_f32_16x16x32_bf16 v[40:43], a[244:247], a[144:147], v[40:43]
  s_waitcnt lgkmcnt(4)
  v_mfma_f32_16x16x32_bf16 v[76:79], a[248:251], a[132:135], v[76:79]
  ds_read_b64_tr_b16 a[232:233], v19 offset:520
  v_mfma_f32_16x16x32_bf16 v[76:79], a[252:255], a[148:151], v[76:79]
  buffer_load_dwordx4 a[44:47], v8, s[60:63], s70 offen sc1
  v_mfma_f32_16x16x32_bf16 v[108:111], a[248:251], a[136:139], v[108:111]
  ds_read_b64_tr_b16 a[234:235], v19 offset:4648
  v_mfma_f32_16x16x32_bf16 v[108:111], a[252:255], a[152:155], v[108:111]
  v_mfma_f32_16x16x32_bf16 v[140:143], a[248:251], a[140:143], v[140:143]
  ds_read_b64_tr_b16 a[236:237], v19 offset:8776
  v_mfma_f32_16x16x32_bf16 v[140:143], a[252:255], a[156:159], v[140:143]
  v_mfma_f32_16x16x32_bf16 v[44:47], a[248:251], a[128:131], v[44:47]
  ds_read_b64_tr_b16 a[238:239], v19 offset:12904
  buffer_load_dwordx4 v3, s[48:51], s68 offen offset:2064 lds
  v_mfma_f32_16x16x32_bf16 v[44:47], a[252:255], a[144:147], v[44:47]
  s_waitcnt lgkmcnt(4)
  v_mfma_f32_16x16x32_bf16 v[80:83], a[224:227], a[132:135], v[80:83]
  ds_read_b64_tr_b16 a[240:241], v19 offset:768
  v_mfma_f32_16x16x32_bf16 v[80:83], a[228:231], a[148:151], v[80:83]
  v_mfma_f32_16x16x32_bf16 v[112:115], a[224:227], a[136:139], v[112:115]
  ds_read_b64_tr_b16 a[242:243], v19 offset:4896
  buffer_load_dwordx4 a[48:51], v5, s[60:63], s71 offen sc1
  v_mfma_f32_16x16x32_bf16 v[112:115], a[228:231], a[152:155], v[112:115]
  v_mfma_f32_16x16x32_bf16 v[144:147], a[224:227], a[140:143], v[144:147]
  ds_read_b64_tr_b16 a[244:245], v19 offset:9024
  v_mfma_f32_16x16x32_bf16 v[144:147], a[228:231], a[156:159], v[144:147]
  v_mfma_f32_16x16x32_bf16 v[48:51], a[224:227], a[128:131], v[48:51]
  ds_read_b64_tr_b16 a[246:247], v19 offset:13152
  v_mfma_f32_16x16x32_bf16 v[48:51], a[228:231], a[144:147], v[48:51]
  s_waitcnt lgkmcnt(4)
  buffer_load_dwordx4 a[52:55], v6, s[60:63], s71 offen sc1
  v_mfma_f32_16x16x32_bf16 v[84:87], a[232:235], a[132:135], v[84:87]
  ds_read_b64_tr_b16 a[248:249], v19 offset:776
  v_mfma_f32_16x16x32_bf16 v[84:87], a[236:239], a[148:151], v[84:87]
  v_mfma_f32_16x16x32_bf16 v[116:119], a[232:235], a[136:139], v[116:119]
  ds_read_b64_tr_b16 a[250:251], v19 offset:4904
  v_mfma_f32_16x16x32_bf16 v[116:119], a[236:239], a[152:155], v[116:119]
  v_mfma_f32_16x16x32_bf16 v[148:151], a[232:235], a[140:143], v[148:151]
  ds_read_b64_tr_b16 a[252:253], v19 offset:9032
  buffer_load_dwordx4 v4, s[48:51], s68 offen offset:3096 lds
  v_mfma_f32_16x16x32_bf16 v[148:151], a[236:239], a[156:159], v[148:151]
  v_mfma_f32_16x16x32_bf16 v[52:55], a[232:235], a[128:131], v[52:55]
  ds_read_b64_tr_b16 a[254:255], v19 offset:13160
  v_mfma_f32_16x16x32_bf16 v[52:55], a[236:239], a[144:147], v[52:55]
  s_waitcnt lgkmcnt(4)
  s_waitcnt vmcnt(36)
  s_barrier
  v_mfma_f32_16x16x32_bf16 v[88:91], a[240:243], a[132:135], v[88:91]
  ds_read_b64_tr_b16 a[224:225], v19 offset:16512
  buffer_load_dwordx4 a[56:59], v7, s[60:63], s71 offen sc1
  v_mfma_f32_16x16x32_bf16 v[88:91], a[244:247], a[148:151], v[88:91]
  v_mfma_f32_16x16x32_bf16 v[120:123], a[240:243], a[136:139], v[120:123]
  ds_read_b64_tr_b16 a[226:227], v19 offset:20640
  v_mfma_f32_16x16x32_bf16 v[120:123], a[244:247], a[152:155], v[120:123]
  v_mfma_f32_16x16x32_bf16 v[152:155], a[240:243], a[140:143], v[152:155]
  ds_read_b64_tr_b16 a[228:229], v19 offset:24768
  v_mfma_f32_16x16x32_bf16 v[152:155], a[244:247], a[156:159], v[152:155]
  buffer_load_dwordx4 a[60:63], v8, s[60:63], s71 offen sc1
  v_mfma_f32_16x16x32_bf16 v[56:59], a[240:243], a[128:131], v[56:59]
  ds_read_b64_tr_b16 a[230:231], v19 offset:28896
  v_mfma_f32_16x16x32_bf16 v[56:59], a[244:247], a[144:147], v[56:59]
  s_waitcnt lgkmcnt(4)
  v_mfma_f32_16x16x32_bf16 v[92:95], a[248:251], a[132:135], v[92:95]
  ds_read_b64_tr_b16 a[232:233], v19 offset:16520
  v_mfma_f32_16x16x32_bf16 v[92:95], a[252:255], a[148:151], v[92:95]
  v_mfma_f32_16x16x32_bf16 v[124:127], a[248:251], a[136:139], v[124:127]
  ds_read_b64_tr_b16 a[234:235], v19 offset:20648
  buffer_load_dword v10, v9, s[44:47], 0 offen offset:128 sc1
  v_mfma_f32_16x16x32_bf16 v[124:127], a[252:255], a[152:155], v[124:127]
  v_mfma_f32_16x16x32_bf16 v[156:159], a[248:251], a[140:143], v[156:159]
  ds_read_b64_tr_b16 a[236:237], v19 offset:24776
  v_mfma_f32_16x16x32_bf16 v[156:159], a[252:255], a[156:159], v[156:159]
  v_mfma_f32_16x16x32_bf16 v[60:63], a[248:251], a[128:131], v[60:63]
  ds_read_b64_tr_b16 a[238:239], v19 offset:28904
  v_mfma_f32_16x16x32_bf16 v[60:63], a[252:255], a[144:147], v[60:63]
  s_waitcnt lgkmcnt(4)
  v_mfma_f32_16x16x32_bf16 v[64:67], a[224:227], a[164:167], v[64:67]
  s_add_u32 s73, s92, 12
  s_add_u32 s73, s73, s86
  s_lshl_b32 s68, s73, 17
  ds_read_b64_tr_b16 a[240:241], v19 offset:16768
  v_mfma_f32_16x16x32_bf16 v[64:67], a[228:231], a[180:183], v[64:67]
  s_lshl_b32 s70, s73, 13
  s_add_u32 s71, s70, 4096
  s_add_u32 m0, s74, 82560
  v_mfma_f32_16x16x32_bf16 v[96:99], a[224:227], a[168:171], v[96:99]
  ds_read_b64_tr_b16 a[242:243], v19 offset:20896
  buffer_load_dwordx4 v1, s[48:51], s68 offen offset:0 lds
  v_mfma_f32_16x16x32_bf16 v[96:99], a[228:231], a[184:187], v[96:99]
  v_mfma_f32_16x16x32_bf16 v[128:131], a[224:227], a[172:175], v[128:131]
  ds_read_b64_tr_b16 a[244:245], v19 offset:25024
  v_mfma_f32_16x16x32_bf16 v[128:131], a[228:231], a[188:191], v[128:131]
  v_mfma_f32_16x16x32_bf16 v[32:35], a[224:227], a[160:163], v[32:35]
  ds_read_b64_tr_b16 a[246:247], v19 offset:29152
  v_mfma_f32_16x16x32_bf16 v[32:35], a[228:231], a[176:179], v[32:35]
  s_waitcnt lgkmcnt(4)
  buffer_load_dwordx4 a[64:67], v5, s[60:63], s70 offen sc1
  v_mfma_f32_16x16x32_bf16 v[68:71], a[232:235], a[164:167], v[68:71]
  ds_read_b64_tr_b16 a[248:249], v19 offset:16776
  v_mfma_f32_16x16x32_bf16 v[68:71], a[236:239], a[180:183], v[68:71]
  v_mfma_f32_16x16x32_bf16 v[100:103], a[232:235], a[168:171], v[100:103]
  ds_read_b64_tr_b16 a[250:251], v19 offset:20904
  v_mfma_f32_16x16x32_bf16 v[100:103], a[236:239], a[184:187], v[100:103]
  v_mfma_f32_16x16x32_bf16 v[132:135], a[232:235], a[172:175], v[132:135]
  ds_read_b64_tr_b16 a[252:253], v19 offset:25032
  buffer_load_dwordx4 a[68:71], v6, s[60:63], s70 offen sc1
  v_mfma_f32_16x16x32_bf16 v[132:135], a[236:239], a[188:191], v[132:135]
  v_mfma_f32_16x16x32_bf16 v[36:39], a[232:235], a[160:163], v[36:39]
  ds_read_b64_tr_b16 a[254:255], v19 offset:29160
  v_mfma_f32_16x16x32_bf16 v[36:39], a[236:239], a[176:179], v[36:39]
  s_waitcnt lgkmcnt(4)
  v_mfma_f32_16x16x32_bf16 v[72:75], a[240:243], a[164:167], v[72:75]
  ds_read_b64_tr_b16 a[224:225], v19 offset:17024
  v_mfma_f32_16x16x32_bf16 v[72:75], a[244:247], a[180:183], v[72:75]
  buffer_load_dwordx4 v2, s[48:51], s68 offen offset:1032 lds
  v_mfma_f32_16x16x32_bf16 v[104:107], a[240:243], a[168:171], v[104:107]
  ds_read_b64_tr_b16 a[226:227], v19 offset:21152
  v_mfma_f32_16x16x32_bf16 v[104:107], a[244:247], a[184:187], v[104:107]
  v_mfma_f32_16x16x32_bf16 v[136:139], a[240:243], a[172:175], v[136:139]
  ds_read_b64_tr_b16 a[228:229], v19 offset:25280
  v_mfma_f32_16x16x32_bf16 v[136:139], a[244:247], a[188:191], v[136:139]
  v_mfma_f32_16x16x32_bf16 v[40:43], a[240:243], a[160:163], v[40:43]
  ds_read_b64_tr_b16 a[230:231], v19 offset:29408
  buffer_load_dwordx4 a[72:75], v7, s[60:63], s70 offen sc1
  v_mfma_f32_16x16x32_bf16 v[40:43], a[244:247], a[176:179], v[40:43]
  s_waitcnt lgkmcnt(4)
  v_mfma_f32_16x16x32_bf16 v[76:79], a[248:251], a[164:167], v[76:79]
  ds_read_b64_tr_b16 a[232:233], v19 offset:17032
  v_mfma_f32_16x16x32_bf16 v[76:79], a[252:255], a[180:183], v[76:79]
  v_mfma_f32_16x16x32_bf16 v[108:111], a[248:251], a[168:171], v[108:111]
  ds_read_b64_tr_b16 a[234:235], v19 offset:21160
  v_mfma_f32_16x16x32_bf16 v[108:111], a[252:255], a[184:187], v[108:111]
  buffer_load_dwordx4 a[76:79], v8, s[60:63], s70 offen sc1
  v_mfma_f32_16x16x32_bf16 v[140:143], a[248:251], a[172:175], v[140:143]
  ds_read_b64_tr_b16 a[236:237], v19 offset:25288
  v_mfma_f32_16x16x32_bf16 v[140:143], a[252:255], a[188:191], v[140:143]
  v_mfma_f32_16x16x32_bf16 v[44:47], a[248:251], a[160:163], v[44:47]
  ds_read_b64_tr_b16 a[238:239], v19 offset:29416
  v_mfma_f32_16x16x32_bf16 v[44:47], a[252:255], a[176:179], v[44:47]
  s_waitcnt lgkmcnt(4)
  v_mfma_f32_16x16x32_bf16 v[80:83], a[224:227], a[164:167], v[80:83]
  ds_read_b64_tr_b16 a[240:241], v19 offset:17280
  buffer_load_dwordx4 v3, s[48:51], s68 offen offset:2064 lds
  v_mfma_f32_16x16x32_bf16 v[80:83], a[228:231], a[180:183], v[80:83]
  v_mfma_f32_16x16x32_bf16 v[112:115], a[224:227], a[168:171], v[112:115]
  ds_read_b64_tr_b16 a[242:243], v19 offset:21408
  v_mfma_f32_16x16x32_bf16 v[112:115], a[228:231], a[184:187], v[112:115]
  v_mfma_f32_16x16x32_bf16 v[144:147], a[224:227], a[172:175], v[144:147]
  ds_read_b64_tr_b16 a[244:245], v19 offset:25536
  v_mfma_f32_16x16x32_bf16 v[144:147], a[228:231], a[188:191], v[144:147]
  buffer_load_dwordx4 a[80:83], v5, s[60:63], s71 offen sc1
  v_mfma_f32_16x16x32_bf16 v[48:51], a[224:227], a[160:163], v[48:51]
  ds_read_b64_tr_b16 a[246:247], v19 offset:29664
  v_mfma_f32_16x16x32_bf16 v[48:51], a[228:231], a[176:179], v[48:51]
  s_waitcnt lgkmcnt(4)
  v_mfma_f32_16x16x32_bf16 v[84:87], a[232:235], a[164:167], v[84:87]
  ds_read_b64_tr_b16 a[248:249], v19 offset:17288
  v_mfma_f32_16x16x32_bf16 v[84:87], a[236:239], a[180:183], v[84:87]
  v_mfma_f32_16x16x32_bf16 v[116:119], a[232:235], a[168:171], v[116:119]
  ds_read_b64_tr_b16 a[250:251], v19 offset:21416
  buffer_load_dwordx4 a[84:87], v6, s[60:63], s71 offen sc1
  v_mfma_f32_16x16x32_bf16 v[116:119], a[236:239], a[184:187], v[116:119]
  v_mfma_f32_16x16x32_bf16 v[148:151], a[232:235], a[172:175], v[148:151]
  ds_read_b64_tr_b16 a[252:253], v19 offset:25544
  v_mfma_f32_16x16x32_bf16 v[148:151], a[236:239], a[188:191], v[148:151]
  v_mfma_f32_16x16x32_bf16 v[52:55], a[232:235], a[160:163], v[52:55]
  ds_read_b64_tr_b16 a[254:255], v19 offset:29672
  v_mfma_f32_16x16x32_bf16 v[52:55], a[236:239], a[176:179], v[52:55]
  s_waitcnt lgkmcnt(4)
  s_waitcnt vmcnt(35)
  s_barrier
  buffer_load_dwordx4 v4, s[48:51], s68 offen offset:3096 lds
  v_mfma_f32_16x16x32_bf16 v[88:91], a[240:243], a[164:167], v[88:91]
  ds_read_b64_tr_b16 a[224:225], v19 offset:33024
  v_mfma_f32_16x16x32_bf16 v[88:91], a[244:247], a[180:183], v[88:91]
  v_mfma_f32_16x16x32_bf16 v[120:123], a[240:243], a[168:171], v[120:123]
  ds_read_b64_tr_b16 a[226:227], v19 offset:37152
  v_mfma_f32_16x16x32_bf16 v[120:123], a[244:247], a[184:187], v[120:123]
  v_mfma_f32_16x16x32_bf16 v[152:155], a[240:243], a[172:175], v[152:155]
  ds_read_b64_tr_b16 a[228:229], v19 offset:41280
  buffer_load_dwordx4 a[88:91], v7, s[60:63], s71 offen sc1
  v_mfma_f32_16x16x32_bf16 v[152:155], a[244:247], a[188:191], v[152:155]
  v_mfma_f32_16x16x32_bf16 v[56:59], a[240:243], a[160:163], v[56:59]
  ds_read_b64_tr_b16 a[230:231], v19 offset:45408
  v_mfma_f32_16x16x32_bf16 v[56:59], a[244:247], a[176:179], v[56:59]
  s_waitcnt lgkmcnt(4)
  v_mfma_f32_16x16x32_bf16 v[92:95], a[248:251], a[164:167], v[92:95]
  ds_read_b64_tr_b16 a[232:233], v19 offset:33032
  v_mfma_f32_16x16x32_bf16 v[92:95], a[252:255], a[180:183], v[92:95]
  buffer_load_dwordx4 a[92:95], v8, s[60:63], s71 offen sc1
  v_mfma_f32_16x16x32_bf16 v[124:127], a[248:251], a[168:171], v[124:127]
  ds_read_b64_tr_b16 a[234:235], v19 offset:37160
  v_mfma_f32_16x16x32_bf16 v[124:127], a[252:255], a[184:187], v[124:127]
  v_mfma_f32_16x16x32_bf16 v[156:159], a[248:251], a[172:175], v[156:159]
  ds_read_b64_tr_b16 a[236:237], v19 offset:41288
  v_mfma_f32_16x16x32_bf16 v[156:159], a[252:255], a[188:191], v[156:159]
  v_mfma_f32_16x16x32_bf16 v[60:63], a[248:251], a[160:163], v[60:63]
  ds_read_b64_tr_b16 a[238:239], v19 offset:45416
  v_mfma_f32_16x16x32_bf16 v[60:63], a[252:255], a[176:179], v[60:63]
  s_waitcnt lgkmcnt(4)
  v_mfma_f32_16x16x32_bf16 v[64:67], a[224:227], a[196:199], v[64:67]
  s_add_u32 s73, s93, 12
  s_add_u32 s73, s73, s86
  s_lshl_b32 s68, s73, 17
  ds_read_b64_tr_b16 a[240:241], v19 offset:33280
  v_mfma_f32_16x16x32_bf16 v[64:67], a[228:231], a[212:215], v[64:67]
  s_lshl_b32 s70, s73, 13
  s_add_u32 s71, s70, 4096
  s_add_u32 m0, s74, 99072
  v_mfma_f32_16x16x32_bf16 v[96:99], a[224:227], a[200:203], v[96:99]
  ds_read_b64_tr_b16 a[242:243], v19 offset:37408
  buffer_load_dwordx4 v1, s[48:51], s68 offen offset:0 lds
  v_mfma_f32_16x16x32_bf16 v[96:99], a[228:231], a[216:219], v[96:99]
  v_mfma_f32_16x16x32_bf16 v[128:131], a[224:227], a[204:207], v[128:131]
  ds_read_b64_tr_b16 a[244:245], v19 offset:41536
  v_mfma_f32_16x16x32_bf16 v[128:131], a[228:231], a[220:223], v[128:131]
  v_mfma_f32_16x16x32_bf16 v[32:35], a[224:227], a[192:195], v[32:35]
  ds_read_b64_tr_b16 a[246:247], v19 offset:45664
  v_mfma_f32_16x16x32_bf16 v[32:35], a[228:231], a[208:211], v[32:35]
  s_waitcnt lgkmcnt(4)
  buffer_load_dwordx4 a[96:99], v5, s[60:63], s70 offen sc1
  v_mfma_f32_16x16x32_bf16 v[68:71], a[232:235], a[196:199], v[68:71]
  ds_read_b64_tr_b16 a[248:249], v19 offset:33288
  v_mfma_f32_16x16x32_bf16 v[68:71], a[236:239], a[212:215], v[68:71]
  v_mfma_f32_16x16x32_bf16 v[100:103], a[232:235], a[200:203], v[100:103]
  ds_read_b64_tr_b16 a[250:251], v19 offset:37416
  v_mfma_f32_16x16x32_bf16 v[100:103], a[236:239], a[216:219], v[100:103]
  v_mfma_f32_16x16x32_bf16 v[132:135], a[232:235], a[204:207], v[132:135]
  ds_read_b64_tr_b16 a[252:253], v19 offset:41544
  buffer_load_dwordx4 a[100:103], v6, s[60:63], s70 offen sc1
  v_mfma_f32_16x16x32_bf16 v[132:135], a[236:239], a[220:223], v[132:135]
  v_mfma_f32_16x16x32_bf16 v[36:39], a[232:235], a[192:195], v[36:39]
  ds_read_b64_tr_b16 a[254:255], v19 offset:45672
  v_mfma_f32_16x16x32_bf16 v[36:39], a[236:239], a[208:211], v[36:39]
  s_waitcnt lgkmcnt(4)
  v_mfma_f32_16x16x32_bf16 v[72:75], a[240:243], a[196:199], v[72:75]
  ds_read_b64_tr_b16 a[224:225], v19 offset:33536
  v_mfma_f32_16x16x32_bf16 v[72:75], a[244:247], a[212:215], v[72:75]
  buffer_load_dwordx4 v2, s[48:51], s68 offen offset:1032 lds
  v_mfma_f32_16x16x32_bf16 v[104:107], a[240:243], a[200:203], v[104:107]
  ds_read_b64_tr_b16 a[226:227], v19 offset:37664
  v_mfma_f32_16x16x32_bf16 v[104:107], a[244:247], a[216:219], v[104:107]
  v_mfma_f32_16x16x32_bf16 v[136:139], a[240:243], a[204:207], v[136:139]
  ds_read_b64_tr_b16 a[228:229], v19 offset:41792
  v_mfma_f32_16x16x32_bf16 v[136:139], a[244:247], a[220:223], v[136:139]
  v_mfma_f32_16x16x32_bf16 v[40:43], a[240:243], a[192:195], v[40:43]
  ds_read_b64_tr_b16 a[230:231], v19 offset:45920
  buffer_load_dwordx4 a[104:107], v7, s[60:63], s70 offen sc1
  v_mfma_f32_16x16x32_bf16 v[40:43], a[244:247], a[208:211], v[40:43]
  s_waitcnt lgkmcnt(4)
  v_mfma_f32_16x16x32_bf16 v[76:79], a[248:251], a[196:199], v[76:79]
  ds_read_b64_tr_b16 a[232:233], v19 offset:33544
  v_mfma_f32_16x16x32_bf16 v[76:79], a[252:255], a[212:215], v[76:79]
  v_mfma_f32_16x16x32_bf16 v[108:111], a[248:251], a[200:203], v[108:111]
  ds_read_b64_tr_b16 a[234:235], v19 offset:37672
  v_mfma_f32_16x16x32_bf16 v[108:111], a[252:255], a[216:219], v[108:111]
  buffer_load_dwordx4 a[108:111], v8, s[60:63], s70 offen sc1
  v_mfma_f32_16x16x32_bf16 v[140:143], a[248:251], a[204:207], v[140:143]
  ds_read_b64_tr_b16 a[236:237], v19 offset:41800
  v_mfma_f32_16x16x32_bf16 v[140:143], a[252:255], a[220:223], v[140:143]
  v_mfma_f32_16x16x32_bf16 v[44:47], a[248:251], a[192:195], v[44:47]
  ds_read_b64_tr_b16 a[238:239], v19 offset:45928
  v_mfma_f32_16x16x32_bf16 v[44:47], a[252:255], a[208:211], v[44:47]
  s_waitcnt lgkmcnt(4)
  v_mfma_f32_16x16x32_bf16 v[80:83], a[224:227], a[196:199], v[80:83]
  ds_read_b64_tr_b16 a[240:241], v19 offset:33792
  buffer_load_dwordx4 v3, s[48:51], s68 offen offset:2064 lds
  v_mfma_f32_16x16x32_bf16 v[80:83], a[228:231], a[212:215], v[80:83]
  v_mfma_f32_16x16x32_bf16 v[112:115], a[224:227], a[200:203], v[112:115]
  ds_read_b64_tr_b16 a[242:243], v19 offset:37920
  v_mfma_f32_16x16x32_bf16 v[112:115], a[228:231], a[216:219], v[112:115]
  v_mfma_f32_16x16x32_bf16 v[144:147], a[224:227], a[204:207], v[144:147]
  ds_read_b64_tr_b16 a[244:245], v19 offset:42048
  v_mfma_f32_16x16x32_bf16 v[144:147], a[228:231], a[220:223], v[144:147]
  buffer_load_dwordx4 a[112:115], v5, s[60:63], s71 offen sc1
  v_mfma_f32_16x16x32_bf16 v[48:51], a[224:227], a[192:195], v[48:51]
  ds_read_b64_tr_b16 a[246:247], v19 offset:46176
  v_mfma_f32_16x16x32_bf16 v[48:51], a[228:231], a[208:211], v[48:51]
  s_waitcnt lgkmcnt(4)
  v_mfma_f32_16x16x32_bf16 v[84:87], a[232:235], a[196:199], v[84:87]
  ds_read_b64_tr_b16 a[248:249], v19 offset:33800
  v_mfma_f32_16x16x32_bf16 v[84:87], a[236:239], a[212:215], v[84:87]
  v_mfma_f32_16x16x32_bf16 v[116:119], a[232:235], a[200:203], v[116:119]
  ds_read_b64_tr_b16 a[250:251], v19 offset:37928
  buffer_load_dwordx4 a[116:119], v6, s[60:63], s71 offen sc1
  v_mfma_f32_16x16x32_bf16 v[116:119], a[236:239], a[216:219], v[116:119]
  v_mfma_f32_16x16x32_bf16 v[148:151], a[232:235], a[204:207], v[148:151]
  ds_read_b64_tr_b16 a[252:253], v19 offset:42056
  v_mfma_f32_16x16x32_bf16 v[148:151], a[236:239], a[220:223], v[148:151]
  v_mfma_f32_16x16x32_bf16 v[52:55], a[232:235], a[192:195], v[52:55]
  ds_read_b64_tr_b16 a[254:255], v19 offset:46184
  v_mfma_f32_16x16x32_bf16 v[52:55], a[236:239], a[208:211], v[52:55]
  s_waitcnt lgkmcnt(4)
  s_waitcnt vmcnt(21)
  s_barrier
  buffer_load_dwordx4 v4, s[48:51], s68 offen offset:3096 lds
  v_mfma_f32_16x16x32_bf16 v[88:91], a[240:243], a[196:199], v[88:91]
  ds_read_b64_tr_b16 a[224:225], v19 offset:49536
  v_cmp_ne_u32 vcc, 128, v10
  s_nop 1
  s_cbranch_vccnz LC_slowo
LC_backo:
  v_mfma_f32_16x16x32_bf16 v[88:91], a[244:247], a[212:215], v[88:91]
  v_mfma_f32_16x16x32_bf16 v[120:123], a[240:243], a[200:203], v[120:123]
  ds_read_b64_tr_b16 a[226:227], v19 offset:53664
  v_mfma_f32_16x16x32_bf16 v[120:123], a[244:247], a[216:219], v[120:123]
  v_mfma_f32_16x16x32_bf16 v[152:155], a[240:243], a[204:207], v[152:155]
  ds_read_b64_tr_b16 a[228:229], v19 offset:57792
  buffer_load_dwordx4 a[120:123], v7, s[60:63], s71 offen sc1
  v_mfma_f32_16x16x32_bf16 v[152:155], a[244:247], a[220:223], v[152:155]
  v_mfma_f32_16x16x32_bf16 v[56:59], a[240:243], a[192:195], v[56:59]
  ds_read_b64_tr_b16 a[230:231], v19 offset:61920
  v_mfma_f32_16x16x32_bf16 v[56:59], a[244:247], a[208:211], v[56:59]
  s_waitcnt lgkmcnt(4)
  v_mfma_f32_16x16x32_bf16 v[92:95], a[248:251], a[196:199], v[92:95]
  ds_read_b64_tr_b16 a[232:233], v19 offset:49544
  v_mfma_f32_16x16x32_bf16 v[92:95], a[252:255], a[212:215], v[92:95]
  buffer_load_dwordx4 a[124:127], v8, s[60:63], s71 offen sc1
  v_mfma_f32_16x16x32_bf16 v[124:127], a[248:251], a[200:203], v[124:127]
  ds_read_b64_tr_b16 a[234:235], v19 offset:53672
  v_mfma_f32_16x16x32_bf16 v[124:127], a[252:255], a[216:219], v[124:127]
  v_mfma_f32_16x16x32_bf16 v[156:159], a[248:251], a[204:207], v[156:159]
  ds_read_b64_tr_b16 a[236:237], v19 offset:57800
  v_mfma_f32_16x16x32_bf16 v[156:159], a[252:255], a[220:223], v[156:159]
  v_mfma_f32_16x16x32_bf16 v[60:63], a[248:251], a[192:195], v[60:63]
  ds_read_b64_tr_b16 a[238:239], v19 offset:61928
  v_mfma_f32_16x16x32_bf16 v[60:63], a[252:255], a[208:211], v[60:63]
  s_waitcnt lgkmcnt(4)
  v_mfma_f32_16x16x32_bf16 v[64:67], a[224:227], a[4:7], v[64:67]
  s_add_u32 s73, s88, 0
  s_add_u32 s73, s73, s87
  s_lshl_b32 s68, s73, 17
  ds_read_b64_tr_b16 a[240:241], v19 offset:49792
  v_mfma_f32_16x16x32_bf16 v[64:67], a[228:231], a[20:23], v[64:67]
  s_lshl_b32 s70, s73, 13
  s_add_u32 s71, s70, 4096
  s_add_u32 m0, s74, 0
  v_mfma_f32_16x16x32_bf16 v[96:99], a[224:227], a[8:11], v[96:99]
  ds_read_b64_tr_b16 a[242:243], v19 offset:53920
  buffer_load_dwordx4 v1, s[52:55], s68 offen offset:0 lds
  v_mfma_f32_16x16x32_bf16 v[96:99], a[228:231], a[24:27], v[96:99]
  v_mfma_f32_16x16x32_bf16 v[128:131], a[224:227], a[12:15], v[128:131]
  ds_read_b64_tr_b16 a[244:245], v19 offset:58048
  v_mfma_f32_16x16x32_bf16 v[128:131], a[228:231], a[28:31], v[128:131]
  v_mfma_f32_16x16x32_bf16 v[32:35], a[224:227], a[0:3], v[32:35]
  ds_read_b64_tr_b16 a[246:247], v19 offset:62176
  v_mfma_f32_16x16x32_bf16 v[32:35], a[228:231], a[16:19], v[32:35]
  s_waitcnt lgkmcnt(4)
  buffer_load_dwordx4 a[128:131], v15, s[64:67], s70 offen sc1
  v_mfma_f32_16x16x32_bf16 v[68:71], a[232:235], a[4:7], v[68:71]
  ds_read_b64_tr_b16 a[248:249], v19 offset:49800
  v_mfma_f32_16x16x32_bf16 v[68:71], a[236:239], a[20:23], v[68:71]
  v_mfma_f32_16x16x32_bf16 v[100:103], a[232:235], a[8:11], v[100:103]
  ds_read_b64_tr_b16 a[250:251], v19 offset:53928
  v_mfma_f32_16x16x32_bf16 v[100:103], a[236:239], a[24:27], v[100:103]
  v_mfma_f32_16x16x32_bf16 v[132:135], a[232:235], a[12:15], v[132:135]
  ds_read_b64_tr_b16 a[252:253], v19 offset:58056
  buffer_load_dwordx4 a[132:135], v16, s[64:67], s70 offen sc1
  v_mfma_f32_16x16x32_bf16 v[132:135], a[236:239], a[28:31], v[132:135]
  v_mfma_f32_16x16x32_bf16 v[36:39], a[232:235], a[0:3], v[36:39]
  ds_read_b64_tr_b16 a[254:255], v19 offset:62184
  v_mfma_f32_16x16x32_bf16 v[36:39], a[236:239], a[16:19], v[36:39]
  s_waitcnt lgkmcnt(4)
  v_mfma_f32_16x16x32_bf16 v[72:75], a[240:243], a[4:7], v[72:75]
  ds_read_b64_tr_b16 a[224:225], v19 offset:50048
  v_mfma_f32_16x16x32_bf16 v[72:75], a[244:247], a[20:23], v[72:75]
  buffer_load_dwordx4 v2, s[52:55], s68 offen offset:1032 lds
  v_mfma_f32_16x16x32_bf16 v[104:107], a[240:243], a[8:11], v[104:107]
  ds_read_b64_tr_b16 a[226:227], v19 offset:54176
  v_mfma_f32_16x16x32_bf16 v[104:107], a[244:247], a[24:27], v[104:107]
  v_mfma_f32_16x16x32_bf16 v[136:139], a[240:243], a[12:15], v[136:139]
  ds_read_b64_tr_b16 a[228:229], v19 offset:58304
  v_mfma_f32_16x16x32_bf16 v[136:139], a[244:247], a[28:31], v[136:139]
  v_mfma_f32_16x16x32_bf16 v[40:43], a[240:243], a[0:3], v[40:43]
  ds_read_b64_tr_b16 a[230:231], v19 offset:62432
  buffer_load_dwordx4 a[136:139], v17, s[64:67], s70 offen sc1
  v_mfma_f32_16x16x32_bf16 v[40:43], a[244:247], a[16:19], v[40:43]
  s_waitcnt lgkmcnt(4)
  v_mfma_f32_16x16x32_bf16 v[76:79], a[248:251], a[4:7], v[76:79]
  ds_read_b64_tr_b16 a[232:233], v19 offset:50056
  v_mfma_f32_16x16x32_bf16 v[76:79], a[252:255], a[20:23], v[76:79]
  v_mfma_f32_16x16x32_bf16 v[108:111], a[248:251], a[8:11], v[108:111]
  ds_read_b64_tr_b16 a[234:235], v19 offset:54184
  v_mfma_f32_16x16x32_bf16 v[108:111], a[252:255], a[24:27], v[108:111]
  buffer_load_dwordx4 a[140:143], v18, s[64:67], s70 offen sc1
  v_mfma_f32_16x16x32_bf16 v[140:143], a[248:251], a[12:15], v[140:143]
  ds_read_b64_tr_b16 a[236:237], v19 offset:58312
  v_mfma_f32_16x16x32_bf16 v[140:143], a[252:255], a[28:31], v[140:143]
  v_mfma_f32_16x16x32_bf16 v[44:47], a[248:251], a[0:3], v[44:47]
  ds_read_b64_tr_b16 a[238:239], v19 offset:62440
  v_mfma_f32_16x16x32_bf16 v[44:47], a[252:255], a[16:19], v[44:47]
  s_waitcnt lgkmcnt(4)
  v_mfma_f32_16x16x32_bf16 v[80:83], a[224:227], a[4:7], v[80:83]
  ds_read_b64_tr_b16 a[240:241], v19 offset:50304
  buffer_load_dwordx4 v3, s[52:55], s68 offen offset:2064 lds
  v_mfma_f32_16x16x32_bf16 v[80:83], a[228:231], a[20:23], v[80:83]
  v_mfma_f32_16x16x32_bf16 v[112:115], a[224:227], a[8:11], v[112:115]
  ds_read_b64_tr_b16 a[242:243], v19 offset:54432
  v_mfma_f32_16x16x32_bf16 v[112:115], a[228:231], a[24:27], v[112:115]
  v_mfma_f32_16x16x32_bf16 v[144:147], a[224:227], a[12:15], v[144:147]
  ds_read_b64_tr_b16 a[244:245], v19 offset:58560
  v_mfma_f32_16x16x32_bf16 v[144:147], a[228:231], a[28:31], v[144:147]
  buffer_load_dwordx4 a[144:147], v15, s[64:67], s71 offen sc1
  v_mfma_f32_16x16x32_bf16 v[48:51], a[224:227], a[0:3], v[48:51]
  ds_read_b64_tr_b16 a[246:247], v19 offset:62688
  v_mfma_f32_16x16x32_bf16 v[48:51], a[228:231], a[16:19], v[48:51]
  s_waitcnt lgkmcnt(4)
  v_mfma_f32_16x16x32_bf16 v[84:87], a[232:235], a[4:7], v[84:87]
  ds_read_b64_tr_b16 a[248:249], v19 offset:50312
  v_mfma_f32_16x16x32_bf16 v[84:87], a[236:239], a[20:23], v[84:87]
  v_mfma_f32_16x16x32_bf16 v[116:119], a[232:235], a[8:11], v[116:119]
  ds_read_b64_tr_b16 a[250:251], v19 offset:54440
  buffer_load_dwordx4 a[148:151], v16, s[64:67], s71 offen sc1
  v_mfma_f32_16x16x32_bf16 v[116:119], a[236:239], a[24:27], v[116:119]
  v_mfma_f32_16x16x32_bf16 v[148:151], a[232:235], a[12:15], v[148:151]
  ds_read_b64_tr_b16 a[252:253], v19 offset:58568
  v_mfma_f32_16x16x32_bf16 v[148:151], a[236:239], a[28:31], v[148:151]
  v_mfma_f32_16x16x32_bf16 v[52:55], a[232:235], a[0:3], v[52:55]
  ds_read_b64_tr_b16 a[254:255], v19 offset:62696
  v_mfma_f32_16x16x32_bf16 v[52:55], a[236:239], a[16:19], v[52:55]
  s_waitcnt lgkmcnt(4)
  s_waitcnt vmcnt(34)
  s_barrier
  buffer_load_dwordx4 v4, s[52:55], s68 offen offset:3096 lds
  v_mfma_f32_16x16x32_bf16 v[88:91], a[240:243], a[4:7], v[88:91]
  ds_read_b64_tr_b16 a[224:225], v20 offset:0
  v_mfma_f32_16x16x32_bf16 v[88:91], a[244:247], a[20:23], v[88:91]
  v_mfma_f32_16x16x32_bf16 v[120:123], a[240:243], a[8:11], v[120:123]
  ds_read_b64_tr_b16 a[226:227], v20 offset:4128
  v_mfma_f32_16x16x32_bf16 v[120:123], a[244:247], a[24:27], v[120:123]
  v_mfma_f32_16x16x32_bf16 v[152:155], a[240:243], a[12:15], v[152:155]
  ds_read_b64_tr_b16 a[228:229], v20 offset:8256
  buffer_load_dwordx4 a[152:155], v17, s[64:67], s71 offen sc1
  v_mfma_f32_16x16x32_bf16 v[152:155], a[244:247], a[28:31], v[152:155]
  v_mfma_f32_16x16x32_bf16 v[56:59], a[240:243], a[0:3], v[56:59]
  ds_read_b64_tr_b16 a[230:231], v20 offset:12384
  v_mfma_f32_16x16x32_bf16 v[56:59], a[244:247], a[16:19], v[56:59]
  s_waitcnt lgkmcnt(4)
  v_mfma_f32_16x16x32_bf16 v[92:95], a[248:251], a[4:7], v[92:95]
  ds_read_b64_tr_b16 a[232:233], v20 offset:8
  v_mfma_f32_16x16x32_bf16 v[92:95], a[252:255], a[20:23], v[92:95]
  buffer_load_dwordx4 a[156:159], v18, s[64:67], s71 offen sc1
  v_mfma_f32_16x16x32_bf16 v[124:127], a[248:251], a[8:11], v[124:127]
  ds_read_b64_tr_b16 a[234:235], v20 offset:4136
  v_mfma_f32_16x16x32_bf16 v[124:127], a[252:255], a[24:27], v[124:127]
  v_mfma_f32_16x16x32_bf16 v[156:159], a[248:251], a[12:15], v[156:159]
  ds_read_b64_tr_b16 a[236:237], v20 offset:8264
  v_mfma_f32_16x16x32_bf16 v[156:159], a[252:255], a[28:31], v[156:159]
  v_mfma_f32_16x16x32_bf16 v[60:63], a[248:251], a[0:3], v[60:63]
  ds_read_b64_tr_b16 a[238:239], v20 offset:12392
  v_mfma_f32_16x16x32_bf16 v[60:63], a[252:255], a[16:19], v[60:63]
  s_waitcnt lgkmcnt(4)
  v_mfma_f32_16x16x32_bf16 v[64:67], a[224:227], a[36:39], v[64:67]
  s_add_u32 s73, s89, 0
  s_add_u32 s73, s73, s87
  s_lshl_b32 s68, s73, 17
  ds_read_b64_tr_b16 a[240:241], v20 offset:256
  v_mfma_f32_16x16x32_bf16 v[64:67], a[228:231], a[52:55], v[64:67]
  s_lshl_b32 s70, s73, 13
  s_add_u32 s71, s70, 4096
  s_add_u32 m0, s74, 16512
  v_mfma_f32_16x16x32_bf16 v[96:99], a[224:227], a[40:43], v[96:99]
  ds_read_b64_tr_b16 a[242:243], v20 offset:4384
  buffer_load_dwordx4 v1, s[52:55], s68 offen offset:0 lds
  v_mfma_f32_16x16x32_bf16 v[96:99], a[228:231], a[56:59], v[96:99]
  v_mfma_f32_16x16x32_bf16 v[128:131], a[224:227], a[44:47], v[128:131]
  ds_read_b64_tr_b16 a[244:245], v20 offset:8512
  v_mfma_f32_16x16x32_bf16 v[128:131], a[228:231], a[60:63], v[128:131]
  v_mfma_f32_16x16x32_bf16 v[32:35], a[224:227], a[32:35], v[32:35]
  ds_read_b64_tr_b16 a[246:247], v20 offset:12640
  v_mfma_f32_16x16x32_bf16 v[32:35], a[228:231], a[48:51], v[32:35]
  s_waitcnt lgkmcnt(4)
  buffer_load_dwordx4 a[160:163], v15, s[64:67], s70 offen sc1
  v_mfma_f32_16x16x32_bf16 v[68:71], a[232:235], a[36:39], v[68:71]
  ds_read_b64_tr_b16 a[248:249], v20 offset:264
  v_mfma_f32_16x16x32_bf16 v[68:71], a[236:239], a[52:55], v[68:71]
  v_mfma_f32_16x16x32_bf16 v[100:103], a[232:235], a[40:43], v[100:103]
  ds_read_b64_tr_b16 a[250:251], v20 offset:4392
  v_mfma_f32_16x16x32_bf16 v[100:103], a[236:239], a[56:59], v[100:103]
  v_mfma_f32_16x16x32_bf16 v[132:135], a[232:235], a[44:47], v[132:135]
  ds_read_b64_tr_b16 a[252:253], v20 offset:8520
  buffer_load_dwordx4 a[164:167], v16, s[64:67], s70 offen sc1
  v_mfma_f32_16x16x32_bf16 v[132:135], a[236:239], a[60:63], v[132:135]
  v_mfma_f32_16x16x32_bf16 v[36:39], a[232:235], a[32:35], v[36:39]
  ds_read_b64_tr_b16 a[254:255], v20 offset:12648
  v_mfma_f32_16x16x32_bf16 v[36:39], a[236:239], a[48:51], v[36:39]
  s_waitcnt lgkmcnt(4)
  v_mfma_f32_16x16x32_bf16 v[72:75], a[240:243], a[36:39], v[72:75]
  ds_read_b64_tr_b16 a[224:225], v20 offset:512
  v_mfma_f32_16x16x32_bf16 v[72:75], a[244:247], a[52:55], v[72:75]
  buffer_load_dwordx4 v2, s[52:55], s68 offen offset:1032 lds
  v_mfma_f32_16x16x32_bf16 v[104:107], a[240:243], a[40:43], v[104:107]
  ds_read_b64_tr_b16 a[226:227], v20 offset:4640
  v_mfma_f32_16x16x32_bf16 v[104:107], a[244:247], a[56:59], v[104:107]
  v_mfma_f32_16x16x32_bf16 v[136:139], a[240:243], a[44:47], v[136:139]
  ds_read_b64_tr_b16 a[228:229], v20 offset:8768
  v_mfma_f32_16x16x32_bf16 v[136:139], a[244:247], a[60:63], v[136:139]
  v_mfma_f32_16x16x32_bf16 v[40:43], a[240:243], a[32:35], v[40:43]
  ds_read_b64_tr_b16 a[230:231], v20 offset:12896
  buffer_load_dwordx4 a[168:171], v17, s[64:67], s70 offen sc1
  v_mfma_f32_16x16x32_bf16 v[40:43], a[244:247], a[48:51], v[40:43]
  s_waitcnt lgkmcnt(4)
  v_mfma_f32_16x16x32_bf16 v[76:79], a[248:251], a[36:39], v[76:79]
  ds_read_b64_tr_b16 a[232:233], v20 offset:520
  v_mfma_f32_16x16x32_bf16 v[76:79], a[252:255], a[52:55], v[76:79]
  v_mfma_f32_16x16x32_bf16 v[108:111], a[248:251], a[40:43], v[108:111]
  ds_read_b64_tr_b16 a[234:235], v20 offset:4648
  v_mfma_f32_16x16x32_bf16 v[108:111], a[252:255], a[56:59], v[108:111]
  buffer_load_dwordx4 a[172:175], v18, s[64:67], s70 offen sc1
  v_mfma_f32_16x16x32_bf16 v[140:143], a[248:251], a[44:47], v[140:143]
  ds_read_b64_tr_b16 a[236:237], v20 offset:8776
  v_mfma_f32_16x16x32_bf16 v[140:143], a[252:255], a[60:63], v[140:143]
  v_mfma_f32_16x16x32_bf16 v[44:47], a[248:251], a[32:35], v[44:47]
  ds_read_b64_tr_b16 a[238:239], v20 offset:12904
  v_mfma_f32_16x16x32_bf16 v[44:47], a[252:255], a[48:51], v[44:47]
  s_waitcnt lgkmcnt(4)
  v_mfma_f32_16x16x32_bf16 v[80:83], a[224:227], a[36:39], v[80:83]
  ds_read_b64_tr_b16 a[240:241], v20 offset:768
  buffer_load_dwordx4 v3, s[52:55], s68 offen offset:2064 lds
  v_mfma_f32_16x16x32_bf16 v[80:83], a[228:231], a[52:55], v[80:83]
  v_mfma_f32_16x16x32_bf16 v[112:115], a[224:227], a[40:43], v[112:115]
  ds_read_b64_tr_b16 a[242:243], v20 offset:4896
  v_mfma_f32_16x16x32_bf16 v[112:115], a[228:231], a[56:59], v[112:115]
  v_mfma_f32_16x16x32_bf16 v[144:147], a[224:227], a[44:47], v[144:147]
  ds_read_b64_tr_b16 a[244:245], v20 offset:9024
  v_mfma_f32_16x16x32_bf16 v[144:147], a[228:231], a[60:63], v[144:147]
  buffer_load_dwordx4 a[176:179], v15, s[64:67], s71 offen sc1
  v_mfma_f32_16x16x32_bf16 v[48:51], a[224:227], a[32:35], v[48:51]
  ds_read_b64_tr_b16 a[246:247], v20 offset:13152
  v_mfma_f32_16x16x32_bf16 v[48:51], a[228:231], a[48:51], v[48:51]
  s_waitcnt lgkmcnt(4)
  v_mfma_f32_16x16x32_bf16 v[84:87], a[232:235], a[36:39], v[84:87]
  ds_read_b64_tr_b16 a[248:249], v20 offset:776
  v_mfma_f32_16x16x32_bf16 v[84:87], a[236:239], a[52:55], v[84:87]
  v_mfma_f32_16x16x32_bf16 v[116:119], a[232:235], a[40:43], v[116:119]
  ds_read_b64_tr_b16 a[250:251], v20 offset:4904
  buffer_load_dwordx4 a[180:183], v16, s[64:67], s71 offen sc1
  v_mfma_f32_16x16x32_bf16 v[116:119], a[236:239], a[56:59], v[116:119]
  v_mfma_f32_16x16x32_bf16 v[148:151], a[232:235], a[44:47], v[148:151]
  ds_read_b64_tr_b16 a[252:253], v20 offset:9032
  v_mfma_f32_16x16x32_bf16 v[148:151], a[236:239], a[60:63], v[148:151]
  v_mfma_f32_16x16x32_bf16 v[52:55], a[232:235], a[32:35], v[52:55]
  ds_read_b64_tr_b16 a[254:255], v20 offset:13160
  v_mfma_f32_16x16x32_bf16 v[52:55], a[236:239], a[48:51], v[52:55]
  s_waitcnt lgkmcnt(4)
  s_waitcnt vmcnt(33)
  s_barrier
  buffer_load_dwordx4 v4, s[52:55], s68 offen offset:3096 lds
  v_mfma_f32_16x16x32_bf16 v[88:91], a[240:243], a[36:39], v[88:91]
  ds_read_b64_tr_b16 a[224:225], v20 offset:16512
  v_mfma_f32_16x16x32_bf16 v[88:91], a[244:247], a[52:55], v[88:91]
  v_mfma_f32_16x16x32_bf16 v[120:123], a[240:243], a[40:43], v[120:123]
  ds_read_b64_tr_b16 a[226:227], v20 offset:20640
  v_mfma_f32_16x16x32_bf16 v[120:123], a[244:247], a[56:59], v[120:123]
  v_mfma_f32_16x16x32_bf16 v[152:155], a[240:243], a[44:47], v[152:155]
  ds_read_b64_tr_b16 a[228:229], v20 offset:24768
  buffer_load_dwordx4 a[184:187], v17, s[64:67], s71 offen sc1
  v_mfma_f32_16x16x32_bf16 v[152:155], a[244:247], a[60:63], v[152:155]
  v_mfma_f32_16x16x32_bf16 v[56:59], a[240:243], a[32:35], v[56:59]
  ds_read_b64_tr_b16 a[230:231], v20 offset:28896
  v_mfma_f32_16x16x32_bf16 v[56:59], a[244:247], a[48:51], v[56:59]
  s_waitcnt lgkmcnt(4)
  v_mfma_f32_16x16x32_bf16 v[92:95], a[248:251], a[36:39], v[92:95]
  ds_read_b64_tr_b16 a[232:233], v20 offset:16520
  v_mfma_f32_16x16x32_bf16 v[92:95], a[252:255], a[52:55], v[92:95]
  buffer_load_dwordx4 a[188:191], v18, s[64:67], s71 offen sc1
  v_mfma_f32_16x16x32_bf16 v[124:127], a[248:251], a[40:43], v[124:127]
  ds_read_b64_tr_b16 a[234:235], v20 offset:20648
  v_mfma_f32_16x16x32_bf16 v[124:127], a[252:255], a[56:59], v[124:127]
  v_mfma_f32_16x16x32_bf16 v[156:159], a[248:251], a[44:47], v[156:159]
  ds_read_b64_tr_b16 a[236:237], v20 offset:24776
  v_mfma_f32_16x16x32_bf16 v[156:159], a[252:255], a[60:63], v[156:159]
  v_mfma_f32_16x16x32_bf16 v[60:63], a[248:251], a[32:35], v[60:63]
  ds_read_b64_tr_b16 a[238:239], v20 offset:28904
  v_mfma_f32_16x16x32_bf16 v[60:63], a[252:255], a[48:51], v[60:63]
  s_waitcnt lgkmcnt(4)
  v_mfma_f32_16x16x32_bf16 v[64:67], a[224:227], a[68:71], v[64:67]
  s_add_u32 s73, s90, 0
  s_add_u32 s73, s73, s87
  s_lshl_b32 s68, s73, 17
  ds_read_b64_tr_b16 a[240:241], v20 offset:16768
  v_mfma_f32_16x16x32_bf16 v[64:67], a[228:231], a[84:87], v[64:67]
  s_lshl_b32 s70, s73, 13
  s_add_u32 s71, s70, 4096
  s_add_u32 m0, s74, 33024
  v_mfma_f32_16x16x32_bf16 v[96:99], a[224:227], a[72:75], v[96:99]
  ds_read_b64_tr_b16 a[242:243], v20 offset:20896
  buffer_load_dwordx4 v1, s[52:55], s68 offen offset:0 lds
  v_mfma_f32_16x16x32_bf16 v[96:99], a[228:231], a[88:91], v[96:99]
  v_lshlrev_b32 v252, 4, v0
  v_mfma_f32_16x16x32_bf16 v[128:131], a[224:227], a[76:79], v[128:131]
  ds_read_b64_tr_b16 a[244:245], v20 offset:25024
  v_mfma_f32_16x16x32_bf16 v[128:131], a[228:231], a[92:95], v[128:131]
  buffer_load_dwordx4 v[248:251], v252, s[4:7], s3 offen
  v_mfma_f32_16x16x32_bf16 v[32:35], a[224:227], a[64:67], v[32:35]
  ds_read_b64_tr_b16 a[246:247], v20 offset:29152
  v_mfma_f32_16x16x32_bf16 v[32:35], a[228:231], a[80:83], v[32:35]
  s_waitcnt lgkmcnt(4)
  buffer_load_dwordx4 a[192:195], v15, s[64:67], s70 offen sc1
  v_mfma_f32_16x16x32_bf16 v[68:71], a[232:235], a[68:71], v[68:71]
  ds_read_b64_tr_b16 a[248:249], v20 offset:16776
  v_mfma_f32_16x16x32_bf16 v[68:71], a[236:239], a[84:87], v[68:71]
  v_mfma_f32_16x16x32_bf16 v[100:103], a[232:235], a[72:75], v[100:103]
  ds_read_b64_tr_b16 a[250:251], v20 offset:20904
  v_mfma_f32_16x16x32_bf16 v[100:103], a[236:239], a[88:91], v[100:103]
  v_mfma_f32_16x16x32_bf16 v[132:135], a[232:235], a[76:79], v[132:135]
  ds_read_b64_tr_b16 a[252:253], v20 offset:25032
  buffer_load_dwordx4 a[196:199], v16, s[64:67], s70 offen sc1
  v_mfma_f32_16x16x32_bf16 v[132:135], a[236:239], a[92:95], v[132:135]
  v_mfma_f32_16x16x32_bf16 v[36:39], a[232:235], a[64:67], v[36:39]
  ds_read_b64_tr_b16 a[254:255], v20 offset:29160
  v_mfma_f32_16x16x32_bf16 v[36:39], a[236:239], a[80:83], v[36:39]
  s_waitcnt lgkmcnt(4)
  v_mfma_f32_16x16x32_bf16 v[72:75], a[240:243], a[68:71], v[72:75]
  ds_read_b64_tr_b16 a[224:225], v20 offset:17024
  v_mfma_f32_16x16x32_bf16 v[72:75], a[244:247], a[84:87], v[72:75]
  buffer_load_dwordx4 v2, s[52:55], s68 offen offset:1032 lds
  v_mfma_f32_16x16x32_bf16 v[104:107], a[240:243], a[72:75], v[104:107]
  ds_read_b64_tr_b16 a[226:227], v20 offset:21152
  v_mfma_f32_16x16x32_bf16 v[104:107], a[244:247], a[88:91], v[104:107]
  v_mfma_f32_16x16x32_bf16 v[136:139], a[240:243], a[76:79], v[136:139]
  ds_read_b64_tr_b16 a[228:229], v20 offset:25280
  v_mfma_f32_16x16x32_bf16 v[136:139], a[244:247], a[92:95], v[136:139]
  v_mfma_f32_16x16x32_bf16 v[40:43], a[240:243], a[64:67], v[40:43]
  ds_read_b64_tr_b16 a[230:231], v20 offset:29408
  buffer_load_dwordx4 a[200:203], v17, s[64:67], s70 offen sc1
  v_mfma_f32_16x16x32_bf16 v[40:43], a[244:247], a[80:83], v[40:43]
  s_waitcnt lgkmcnt(4)
  v_mfma_f32_16x16x32_bf16 v[76:79], a[248:251], a[68:71], v[76:79]
  ds_read_b64_tr_b16 a[232:233], v20 offset:17032
  v_mfma_f32_16x16x32_bf16 v[76:79], a[252:255], a[84:87], v[76:79]
  v_mfma_f32_16x16x32_bf16 v[108:111], a[248:251], a[72:75], v[108:111]
  ds_read_b64_tr_b16 a[234:235], v20 offset:21160
  v_mfma_f32_16x16x32_bf16 v[108:111], a[252:255], a[88:91], v[108:111]
  buffer_load_dwordx4 a[204:207], v18, s[64:67], s70 offen sc1
  v_mfma_f32_16x16x32_bf16 v[140:143], a[248:251], a[76:79], v[140:143]
  ds_read_b64_tr_b16 a[236:237], v20 offset:25288
  v_mfma_f32_16x16x32_bf16 v[140:143], a[252:255], a[92:95], v[140:143]
  v_mfma_f32_16x16x32_bf16 v[44:47], a[248:251], a[64:67], v[44:47]
  ds_read_b64_tr_b16 a[238:239], v20 offset:29416
  v_mfma_f32_16x16x32_bf16 v[44:47], a[252:255], a[80:83], v[44:47]
  s_waitcnt lgkmcnt(4)
  v_mfma_f32_16x16x32_bf16 v[80:83], a[224:227], a[68:71], v[80:83]
  ds_read_b64_tr_b16 a[240:241], v20 offset:17280
  buffer_load_dwordx4 v3, s[52:55], s68 offen offset:2064 lds
  v_mfma_f32_16x16x32_bf16 v[80:83], a[228:231], a[84:87], v[80:83]
  v_mfma_f32_16x16x32_bf16 v[112:115], a[224:227], a[72:75], v[112:115]
  ds_read_b64_tr_b16 a[242:243], v20 offset:21408
  v_mfma_f32_16x16x32_bf16 v[112:115], a[228:231], a[88:91], v[112:115]
  v_mfma_f32_16x16x32_bf16 v[144:147], a[224:227], a[76:79], v[144:147]
  ds_read_b64_tr_b16 a[244:245], v20 offset:25536
  v_mfma_f32_16x16x32_bf16 v[144:147], a[228:231], a[92:95], v[144:147]
  buffer_load_dwordx4 a[208:211], v15, s[64:67], s71 offen sc1
  v_mfma_f32_16x16x32_bf16 v[48:51], a[224:227], a[64:67], v[48:51]
  ds_read_b64_tr_b16 a[246:247], v20 offset:29664
  v_mfma_f32_16x16x32_bf16 v[48:51], a[228:231], a[80:83], v[48:51]
  s_waitcnt lgkmcnt(4)
  v_mfma_f32_16x16x32_bf16 v[84:87], a[232:235], a[68:71], v[84:87]
  ds_read_b64_tr_b16 a[248:249], v20 offset:17288
  v_mfma_f32_16x16x32_bf16 v[84:87], a[236:239], a[84:87], v[84:87]
  v_mfma_f32_16x16x32_bf16 v[116:119], a[232:235], a[72:75], v[116:119]
  ds_read_b64_tr_b16 a[250:251], v20 offset:21416
  buffer_load_dwordx4 a[212:215], v16, s[64:67], s71 offen sc1
  v_mfma_f32_16x16x32_bf16 v[116:119], a[236:239], a[88:91], v[116:119]
  v_mfma_f32_16x16x32_bf16 v[148:151], a[232:235], a[76:79], v[148:151]
  ds_read_b64_tr_b16 a[252:253], v20 offset:25544
  v_mfma_f32_16x16x32_bf16 v[148:151], a[236:239], a[92:95], v[148:151]
  v_mfma_f32_16x16x32_bf16 v[52:55], a[232:235], a[64:67], v[52:55]
  ds_read_b64_tr_b16 a[254:255], v20 offset:29672
  v_mfma_f32_16x16x32_bf16 v[52:55], a[236:239], a[80:83], v[52:55]
  s_waitcnt lgkmcnt(4)
  s_waitcnt vmcnt(34)
  s_barrier
  buffer_load_dwordx4 v4, s[52:55], s68 offen offset:3096 lds
  v_mfma_f32_16x16x32_bf16 v[88:91], a[240:243], a[68:71], v[88:91]
  ds_read_b64_tr_b16 a[224:225], v20 offset:33024
  v_mfma_f32_16x16x32_bf16 v[88:91], a[244:247], a[84:87], v[88:91]
  v_mfma_f32_16x16x32_bf16 v[120:123], a[240:243], a[72:75], v[120:123]
  ds_read_b64_tr_b16 a[226:227], v20 offset:37152
  v_mfma_f32_16x16x32_bf16 v[120:123], a[244:247], a[88:91], v[120:123]
  v_mfma_f32_16x16x32_bf16 v[152:155], a[240:243], a[76:79], v[152:155]
  ds_read_b64_tr_b16 a[228:229], v20 offset:41280
  buffer_load_dwordx4 a[216:219], v17, s[64:67], s71 offen sc1
  v_mfma_f32_16x16x32_bf16 v[152:155], a[244:247], a[92:95], v[152:155]
  v_mfma_f32_16x16x32_bf16 v[56:59], a[240:243], a[64:67], v[56:59]
  ds_read_b64_tr_b16 a[230:231], v20 offset:45408
  v_mfma_f32_16x16x32_bf16 v[56:59], a[244:247], a[80:83], v[56:59]
  s_waitcnt lgkmcnt(4)
  v_mfma_f32_16x16x32_bf16 v[92:95], a[248:251], a[68:71], v[92:95]
  ds_read_b64_tr_b16 a[232:233], v20 offset:33032
  v_mfma_f32_16x16x32_bf16 v[92:95], a[252:255], a[84:87], v[92:95]
  buffer_load_dwordx4 a[220:223], v18, s[64:67], s71 offen sc1
  v_mfma_f32_16x16x32_bf16 v[124:127], a[248:251], a[72:75], v[124:127]
  ds_read_b64_tr_b16 a[234:235], v20 offset:37160
  v_mfma_f32_16x16x32_bf16 v[124:127], a[252:255], a[88:91], v[124:127]
  v_mfma_f32_16x16x32_bf16 v[156:159], a[248:251], a[76:79], v[156:159]
  ds_read_b64_tr_b16 a[236:237], v20 offset:41288
  v_mfma_f32_16x16x32_bf16 v[156:159], a[252:255], a[92:95], v[156:159]
  v_mfma_f32_16x16x32_bf16 v[60:63], a[248:251], a[64:67], v[60:63]
  ds_read_b64_tr_b16 a[238:239], v20 offset:45416
  v_mfma_f32_16x16x32_bf16 v[60:63], a[252:255], a[80:83], v[60:63]
  s_waitcnt lgkmcnt(4)
  v_mfma_f32_16x16x32_bf16 v[64:67], a[224:227], a[100:103], v[64:67]
  s_add_u32 s73, s91, 0
  s_add_u32 s73, s73, s87
  s_lshl_b32 s68, s73, 17
  ds_read_b64_tr_b16 a[240:241], v20 offset:33280
  v_mfma_f32_16x16x32_bf16 v[64:67], a[228:231], a[116:119], v[64:67]
  s_lshl_b32 s70, s73, 13
  s_add_u32 s71, s70, 4096
  s_add_u32 m0, s74, 49536
  v_mfma_f32_16x16x32_bf16 v[96:99], a[224:227], a[104:107], v[96:99]
  ds_read_b64_tr_b16 a[242:243], v20 offset:37408
  buffer_load_dwordx4 v1, s[52:55], s68 offen offset:0 lds
  v_mfma_f32_16x16x32_bf16 v[96:99], a[228:231], a[120:123], v[96:99]
  v_mfma_f32_16x16x32_bf16 v[128:131], a[224:227], a[108:111], v[128:131]
  ds_read_b64_tr_b16 a[244:245], v20 offset:41536
  v_mfma_f32_16x16x32_bf16 v[128:131], a[228:231], a[124:127], v[128:131]
  v_mfma_f32_16x16x32_bf16 v[32:35], a[224:227], a[96:99], v[32:35]
  ds_read_b64_tr_b16 a[246:247], v20 offset:45664
  v_mfma_f32_16x16x32_bf16 v[32:35], a[228:231], a[112:115], v[32:35]
  s_waitcnt lgkmcnt(4)
  buffer_load_dwordx4 a[0:3], v15, s[64:67], s70 offen sc1
  v_mfma_f32_16x16x32_bf16 v[68:71], a[232:235], a[100:103], v[68:71]
  ds_read_b64_tr_b16 a[248:249], v20 offset:33288
  v_cvt_pk_bf16_f32 v192, v64, v65
  v_mfma_f32_16x16x32_bf16 v[68:71], a[236:239], a[116:119], v[68:71]
  v_mfma_f32_16x16x32_bf16 v[100:103], a[232:235], a[104:107], v[100:103]
  ds_read_b64_tr_b16 a[250:251], v20 offset:37416
  v_cvt_pk_bf16_f32 v208, v96, v97
  v_mfma_f32_16x16x32_bf16 v[100:103], a[236:239], a[120:123], v[100:103]
  v_cvt_pk_bf16_f32 v193, v66, v67
  v_mfma_f32_16x16x32_bf16 v[132:135], a[232:235], a[108:111], v[132:135]
  ds_read_b64_tr_b16 a[252:253], v20 offset:41544
  buffer_load_dwordx4 a[4:7], v16, s[64:67], s70 offen sc1
  v_cvt_pk_bf16_f32 v224, v128, v129
  v_mfma_f32_16x16x32_bf16 v[132:135], a[236:239], a[124:127], v[132:135]
  v_cvt_pk_bf16_f32 v209, v98, v99
  v_mfma_f32_16x16x32_bf16 v[36:39], a[232:235], a[96:99], v[36:39]
  ds_read_b64_tr_b16 a[254:255], v20 offset:45672
  v_mfma_f32_16x16x32_bf16 v[36:39], a[236:239], a[112:115], v[36:39]
  s_waitcnt lgkmcnt(4)
  v_cvt_pk_bf16_f32 v225, v130, v131
  v_mfma_f32_16x16x32_bf16 v[72:75], a[240:243], a[100:103], v[72:75]
  ds_read_b64_tr_b16 a[224:225], v20 offset:33536
  v_cvt_pk_bf16_f32 v194, v68, v69
  v_mfma_f32_16x16x32_bf16 v[72:75], a[244:247], a[116:119], v[72:75]
  buffer_load_dwordx4 v2, s[52:55], s68 offen offset:1032 lds
  v_mfma_f32_16x16x32_bf16 v[104:107], a[240:243], a[104:107], v[104:107]
  ds_read_b64_tr_b16 a[226:227], v20 offset:37664
  v_cvt_pk_bf16_f32 v210, v100, v101
  v_mfma_f32_16x16x32_bf16 v[104:107], a[244:247], a[120:123], v[104:107]
  v_cvt_pk_bf16_f32 v195, v70, v71
  v_mfma_f32_16x16x32_bf16 v[136:139], a[240:243], a[108:111], v[136:139]
  ds_read_b64_tr_b16 a[228:229], v20 offset:41792
  v_cvt_pk_bf16_f32 v226, v132, v133
  v_mfma_f32_16x16x32_bf16 v[136:139], a[244:247], a[124:127], v[136:139]
  v_cvt_pk_bf16_f32 v211, v102, v103
  v_mfma_f32_16x16x32_bf16 v[40:43], a[240:243], a[96:99], v[40:43]
  ds_read_b64_tr_b16 a[230:231], v20 offset:45920
  buffer_load_dwordx4 a[8:11], v17, s[64:67], s70 offen sc1
  v_mfma_f32_16x16x32_bf16 v[40:43], a[244:247], a[112:115], v[40:43]
  s_waitcnt lgkmcnt(4)
  v_cvt_pk_bf16_f32 v227, v134, v135
  v_mfma_f32_16x16x32_bf16 v[76:79], a[248:251], a[100:103], v[76:79]
  ds_read_b64_tr_b16 a[232:233], v20 offset:33544
  v_cvt_pk_bf16_f32 v196, v72, v73
  v_mfma_f32_16x16x32_bf16 v[76:79], a[252:255], a[116:119], v[76:79]
  v_mfma_f32_16x16x32_bf16 v[108:111], a[248:251], a[104:107], v[108:111]
  ds_read_b64_tr_b16 a[234:235], v20 offset:37672
  v_cvt_pk_bf16_f32 v212, v104, v105
  v_mfma_f32_16x16x32_bf16 v[108:111], a[252:255], a[120:123], v[108:111]
  buffer_load_dwordx4 a[12:15], v18, s[64:67], s70 offen sc1
  v_cvt_pk_bf16_f32 v197, v74, v75
  v_mfma_f32_16x16x32_bf16 v[140:143], a[248:251], a[108:111], v[140:143]
  ds_read_b64_tr_b16 a[236:237], v20 offset:41800
  v_cvt_pk_bf16_f32 v228, v136, v137
  v_mfma_f32_16x16x32_bf16 v[140:143], a[252:255], a[124:127], v[140:143]
  v_cvt_pk_bf16_f32 v213, v106, v107
  v_mfma_f32_16x16x32_bf16 v[44:47], a[248:251], a[96:99], v[44:47]
  ds_read_b64_tr_b16 a[238:239], v20 offset:45928
  v_mfma_f32_16x16x32_bf16 v[44:47], a[252:255], a[112:115], v[44:47]
  s_waitcnt lgkmcnt(4)
  v_cvt_pk_bf16_f32 v229, v138, v139
  v_mfma_f32_16x16x32_bf16 v[80:83], a[224:227], a[100:103], v[80:83]
  ds_read_b64_tr_b16 a[240:241], v20 offset:33792
  buffer_load_dwordx4 v3, s[52:55], s68 offen offset:2064 lds
  v_cvt_pk_bf16_f32 v198, v76, v77
  v_mfma_f32_16x16x32_bf16 v[80:83], a[228:231], a[116:119], v[80:83]
  v_mfma_f32_16x16x32_bf16 v[112:115], a[224:227], a[104:107], v[112:115]
  ds_read_b64_tr_b16 a[242:243], v20 offset:37920
  v_cvt_pk_bf16_f32 v214, v108, v109
  v_mfma_f32_16x16x32_bf16 v[112:115], a[228:231], a[120:123], v[112:115]
  v_cvt_pk_bf16_f32 v199, v78, v79
  v_mfma_f32_16x16x32_bf16 v[144:147], a[224:227], a[108:111], v[144:147]
  ds_read_b64_tr_b16 a[244:245], v20 offset:42048
  v_cvt_pk_bf16_f32 v230, v140, v141
  v_mfma_f32_16x16x32_bf16 v[144:147], a[228:231], a[124:127], v[144:147]
  buffer_load_dwordx4 a[16:19], v15, s[64:67], s71 offen sc1
  v_cvt_pk_bf16_f32 v215, v110, v111
  v_mfma_f32_16x16x32_bf16 v[48:51], a[224:227], a[96:99], v[48:51]
  ds_read_b64_tr_b16 a[246:247], v20 offset:46176
  v_mfma_f32_16x16x32_bf16 v[48:51], a[228:231], a[112:115], v[48:51]
  s_waitcnt lgkmcnt(4)
  v_cvt_pk_bf16_f32 v231, v142, v143
  v_mfma_f32_16x16x32_bf16 v[84:87], a[232:235], a[100:103], v[84:87]
  ds_read_b64_tr_b16 a[248:249], v20 offset:33800
  v_cvt_pk_bf16_f32 v200, v80, v81
  v_mfma_f32_16x16x32_bf16 v[84:87], a[236:239], a[116:119], v[84:87]
  v_mfma_f32_16x16x32_bf16 v[116:119], a[232:235], a[104:107], v[116:119]
  ds_read_b64_tr_b16 a[250:251], v20 offset:37928
  buffer_load_dwordx4 a[20:23], v16, s[64:67], s71 offen sc1
  v_cvt_pk_bf16_f32 v216, v112, v113
  v_mfma_f32_16x16x32_bf16 v[116:119], a[236:239], a[120:123], v[116:119]
  v_cvt_pk_bf16_f32 v201, v82, v83
  v_mfma_f32_16x16x32_bf16 v[148:151], a[232:235], a[108:111], v[148:151]
  ds_read_b64_tr_b16 a[252:253], v20 offset:42056
  v_cvt_pk_bf16_f32 v232, v144, v145
  v_mfma_f32_16x16x32_bf16 v[148:151], a[236:239], a[124:127], v[148:151]
  v_cvt_pk_bf16_f32 v217, v114, v115
  v_mfma_f32_16x16x32_bf16 v[52:55], a[232:235], a[96:99], v[52:55]
  ds_read_b64_tr_b16 a[254:255], v20 offset:46184
  v_mfma_f32_16x16x32_bf16 v[52:55], a[236:239], a[112:115], v[52:55]
  s_waitcnt lgkmcnt(4)
  s_waitcnt vmcnt(34)
  s_barrier
  buffer_load_dwordx4 v4, s[52:55], s68 offen offset:3096 lds
  v_cvt_pk_bf16_f32 v233, v146, v147
  v_mfma_f32_16x16x32_bf16 v[88:91], a[240:243], a[100:103], v[88:91]
  ds_read_b64_tr_b16 a[224:225], v19 offset:0
  v_cvt_pk_bf16_f32 v202, v84, v85
  v_mfma_f32_16x16x32_bf16 v[88:91], a[244:247], a[116:119], v[88:91]
  v_mfma_f32_16x16x32_bf16 v[120:123], a[240:243], a[104:107], v[120:123]
  ds_read_b64_tr_b16 a[226:227], v19 offset:4128
  v_cvt_pk_bf16_f32 v218, v116, v117
  v_mfma_f32_16x16x32_bf16 v[120:123], a[244:247], a[120:123], v[120:123]
  v_cvt_pk_bf16_f32 v203, v86, v87
  v_mfma_f32_16x16x32_bf16 v[152:155], a[240:243], a[108:111], v[152:155]
  ds_read_b64_tr_b16 a[228:229], v19 offset:8256
  buffer_load_dwordx4 a[24:27], v17, s[64:67], s71 offen sc1
  v_cvt_pk_bf16_f32 v234, v148, v149
  v_mfma_f32_16x16x32_bf16 v[152:155], a[244:247], a[124:127], v[152:155]
  v_cvt_pk_bf16_f32 v219, v118, v119
  v_mfma_f32_16x16x32_bf16 v[56:59], a[240:243], a[96:99], v[56:59]
  ds_read_b64_tr_b16 a[230:231], v19 offset:12384
  v_mfma_f32_16x16x32_bf16 v[56:59], a[244:247], a[112:115], v[56:59]
  s_waitcnt lgkmcnt(4)
  v_cvt_pk_bf16_f32 v235, v150, v151
  v_mfma_f32_16x16x32_bf16 v[92:95], a[248:251], a[100:103], v[92:95]
  ds_read_b64_tr_b16 a[232:233], v19 offset:8
  v_cvt_pk_bf16_f32 v204, v88, v89
  v_mfma_f32_16x16x32_bf16 v[92:95], a[252:255], a[116:119], v[92:95]
  buffer_load_dwordx4 a[28:31], v18, s[64:67], s71 offen sc1
  v_mfma_f32_16x16x32_bf16 v[124:127], a[248:251], a[104:107], v[124:127]
  ds_read_b64_tr_b16 a[234:235], v19 offset:4136
  v_cvt_pk_bf16_f32 v220, v120, v121
  v_mfma_f32_16x16x32_bf16 v[124:127], a[252:255], a[120:123], v[124:127]
  v_cvt_pk_bf16_f32 v205, v90, v91
  v_mfma_f32_16x16x32_bf16 v[156:159], a[248:251], a[108:111], v[156:159]
  ds_read_b64_tr_b16 a[236:237], v19 offset:8264
  v_cvt_pk_bf16_f32 v236, v152, v153
  v_mfma_f32_16x16x32_bf16 v[156:159], a[252:255], a[124:127], v[156:159]
  v_cvt_pk_bf16_f32 v221, v122, v123
  v_mfma_f32_16x16x32_bf16 v[60:63], a[248:251], a[96:99], v[60:63]
  ds_read_b64_tr_b16 a[238:239], v19 offset:12392
  v_mfma_f32_16x16x32_bf16 v[60:63], a[252:255], a[112:115], v[60:63]
  s_waitcnt lgkmcnt(4)
  v_cvt_pk_bf16_f32 v237, v154, v155
  v_mfma_f32_16x16x32_bf16 v[64:67], a[224:227], a[132:135], 0
  s_add_u32 s73, s88, 4
  s_add_u32 s73, s73, s87
  s_lshl_b32 s68, s73, 17
  ds_read_b64_tr_b16 a[240:241], v19 offset:256
  v_cvt_pk_bf16_f32 v206, v92, v93
  v_mfma_f32_16x16x32_bf16 v[64:67], a[228:231], a[148:151], v[64:67]
  s_lshl_b32 s70, s73, 13
  s_add_u32 s71, s70, 4096
  s_add_u32 m0, s74, 66048
  v_mfma_f32_16x16x32_bf16 v[96:99], a[224:227], a[136:139], 0
  ds_read_b64_tr_b16 a[242:243], v19 offset:4384
  buffer_load_dwordx4 v1, s[52:55], s68 offen offset:0 lds
  v_cvt_pk_bf16_f32 v222, v124, v125
  v_mfma_f32_16x16x32_bf16 v[96:99], a[228:231], a[152:155], v[96:99]
  v_cvt_pk_bf16_f32 v207, v94, v95
  v_mfma_f32_16x16x32_bf16 v[128:131], a[224:227], a[140:143], 0
  ds_read_b64_tr_b16 a[244:245], v19 offset:8512
  v_cvt_pk_bf16_f32 v238, v156, v157
  v_mfma_f32_16x16x32_bf16 v[128:131], a[228:231], a[156:159], v[128:131]
  v_cvt_pk_bf16_f32 v223, v126, v127
  v_mfma_f32_16x16x32_bf16 v[160:163], a[224:227], a[128:131], 0
  ds_read_b64_tr_b16 a[246:247], v19 offset:12640
  v_mfma_f32_16x16x32_bf16 v[160:163], a[228:231], a[144:147], v[160:163]
  s_waitcnt lgkmcnt(4)
  buffer_load_dwordx4 a[32:35], v15, s[64:67], s70 offen sc1
  v_cvt_pk_bf16_f32 v239, v158, v159
  v_mfma_f32_16x16x32_bf16 v[68:71], a[232:235], a[132:135], 0
  ds_read_b64_tr_b16 a[248:249], v19 offset:264
  v_mfma_f32_16x16x32_bf16 v[68:71], a[236:239], a[148:151], v[68:71]
  v_mfma_f32_16x16x32_bf16 v[100:103], a[232:235], a[136:139], 0
  ds_read_b64_tr_b16 a[250:251], v19 offset:4392
  v_mfma_f32_16x16x32_bf16 v[100:103], a[236:239], a[152:155], v[100:103]
  v_mfma_f32_16x16x32_bf16 v[132:135], a[232:235], a[140:143], 0
  ds_read_b64_tr_b16 a[252:253], v19 offset:8520
  buffer_load_dwordx4 a[36:39], v16, s[64:67], s70 offen sc1
  v_mfma_f32_16x16x32_bf16 v[132:135], a[236:239], a[156:159], v[132:135]
  v_mfma_f32_16x16x32_bf16 v[164:167], a[232:235], a[128:131], 0
  ds_read_b64_tr_b16 a[254:255], v19 offset:12648
  v_mfma_f32_16x16x32_bf16 v[164:167], a[236:239], a[144:147], v[164:167]
  s_waitcnt lgkmcnt(4)
  v_mfma_f32_16x16x32_bf16 v[72:75], a[240:243], a[132:135], 0
  ds_read_b64_tr_b16 a[224:225], v19 offset:512
  v_mfma_f32_16x16x32_bf16 v[72:75], a[244:247], a[148:151], v[72:75]
  buffer_load_dwordx4 v2, s[52:55], s68 offen offset:1032 lds
  v_mfma_f32_16x16x32_bf16 v[104:107], a[240:243], a[136:139], 0
  ds_read_b64_tr_b16 a[226:227], v19 offset:4640
  v_mfma_f32_16x16x32_bf16 v[104:107], a[244:247], a[152:155], v[104:107]
  v_mfma_f32_16x16x32_bf16 v[136:139], a[240:243], a[140:143], 0
  ds_read_b64_tr_b16 a[228:229], v19 offset:8768
  v_mfma_f32_16x16x32_bf16 v[136:139], a[244:247], a[156:159], v[136:139]
  v_mfma_f32_16x16x32_bf16 v[168:171], a[240:243], a[128:131], 0
  ds_read_b64_tr_b16 a[230:231], v19 offset:12896
  buffer_load_dwordx4 a[40:43], v17, s[64:67], s70 offen sc1
  v_mfma_f32_16x16x32_bf16 v[168:171], a[244:247], a[144:147], v[168:171]
  s_waitcnt lgkmcnt(4)
  v_mfma_f32_16x16x32_bf16 v[76:79], a[248:251], a[132:135], 0
  ds_read_b64_tr_b16 a[232:233], v19 offset:520
  v_mfma_f32_16x16x32_bf16 v[76:79], a[252:255], a[148:151], v[76:79]
  v_mfma_f32_16x16x32_bf16 v[108:111], a[248:251], a[136:139], 0
  ds_read_b64_tr_b16 a[234:235], v19 offset:4648
  v_mfma_f32_16x16x32_bf16 v[108:111], a[252:255], a[152:155], v[108:111]
  buffer_load_dwordx4 a[44:47], v18, s[64:67], s70 offen sc1
  v_mfma_f32_16x16x32_bf16 v[140:143], a[248:251], a[140:143], 0
  ds_read_b64_tr_b16 a[236:237], v19 offset:8776
  v_mfma_f32_16x16x32_bf16 v[140:143], a[252:255], a[156:159], v[140:143]
  v_mfma_f32_16x16x32_bf16 v[172:175], a[248:251], a[128:131], 0
  ds_read_b64_tr_b16 a[238:239], v19 offset:12904
  v_mfma_f32_16x16x32_bf16 v[172:175], a[252:255], a[144:147], v[172:175]
  s_waitcnt lgkmcnt(4)
  v_mfma_f32_16x16x32_bf16 v[80:83], a[224:227], a[132:135], 0
  ds_read_b64_tr_b16 a[240:241], v19 offset:768
  buffer_load_dwordx4 v3, s[52:55], s68 offen offset:2064 lds
  v_mfma_f32_16x16x32_bf16 v[80:83], a[228:231], a[148:151], v[80:83]
  v_mfma_f32_16x16x32_bf16 v[112:115], a[224:227], a[136:139], 0
  ds_read_b64_tr_b16 a[242:243], v19 offset:4896
  v_mfma_f32_16x16x32_bf16 v[112:115], a[228:231], a[152:155], v[112:115]
  v_mfma_f32_16x16x32_bf16 v[144:147], a[224:227], a[140:143], 0
  ds_read_b64_tr_b16 a[244:245], v19 offset:9024
  v_mfma_f32_16x16x32_bf16 v[144:147], a[228:231], a[156:159], v[144:147]
  buffer_load_dwordx4 a[48:51], v15, s[64:67], s71 offen sc1
  v_mfma_f32_16x16x32_bf16 v[176:179], a[224:227], a[128:131], 0
  ds_read_b64_tr_b16 a[246:247], v19 offset:13152
  v_mfma_f32_16x16x32_bf16 v[176:179], a[228:231], a[144:147], v[176:179]
  s_waitcnt lgkmcnt(4)
  v_mfma_f32_16x16x32_bf16 v[84:87], a[232:235], a[132:135], 0
  ds_read_b64_tr_b16 a[248:249], v19 offset:776
  v_mfma_f32_16x16x32_bf16 v[84:87], a[236:239], a[148:151], v[84:87]
  v_mfma_f32_16x16x32_bf16 v[116:119], a[232:235], a[136:139], 0
  ds_read_b64_tr_b16 a[250:251], v19 offset:4904
  buffer_load_dwordx4 a[52:55], v16, s[64:67], s71 offen sc1
  v_mfma_f32_16x16x32_bf16 v[116:119], a[236:239], a[152:155], v[116:119]
  v_mfma_f32_16x16x32_bf16 v[148:151], a[232:235], a[140:143], 0
  ds_read_b64_tr_b16 a[252:253], v19 offset:9032
  v_mfma_f32_16x16x32_bf16 v[148:151], a[236:239], a[156:159], v[148:151]
  v_mfma_f32_16x16x32_bf16 v[180:183], a[232:235], a[128:131], 0
  ds_read_b64_tr_b16 a[254:255], v19 offset:13160
  v_mfma_f32_16x16x32_bf16 v[180:183], a[236:239], a[144:147], v[180:183]
  s_waitcnt lgkmcnt(4)
  s_waitcnt vmcnt(34)
  s_barrier
  buffer_load_dwordx4 v4, s[52:55], s68 offen offset:3096 lds
  v_mfma_f32_16x16x32_bf16 v[88:91], a[240:243], a[132:135], 0
  ds_read_b64_tr_b16 a[224:225], v19 offset:16512
  v_mfma_f32_16x16x32_bf16 v[88:91], a[244:247], a[148:151], v[88:91]
  v_mfma_f32_16x16x32_bf16 v[120:123], a[240:243], a[136:139], 0
  ds_read_b64_tr_b16 a[226:227], v19 offset:20640
  v_mfma_f32_16x16x32_bf16 v[120:123], a[244:247], a[152:155], v[120:123]
  v_mfma_f32_16x16x32_bf16 v[152:155], a[240:243], a[140:143], 0
  ds_read_b64_tr_b16 a[228:229], v19 offset:24768
  buffer_load_dwordx4 a[56:59], v17, s[64:67], s71 offen sc1
  v_mfma_f32_16x16x32_bf16 v[152:155], a[244:247], a[156:159], v[152:155]
  v_mfma_f32_16x16x32_bf16 v[184:187], a[240:243], a[128:131], 0
  ds_read_b64_tr_b16 a[230:231], v19 offset:28896
  v_mfma_f32_16x16x32_bf16 v[184:187], a[244:247], a[144:147], v[184:187]
  s_waitcnt lgkmcnt(4)
  v_mfma_f32_16x16x32_bf16 v[92:95], a[248:251], a[132:135], 0
  ds_read_b64_tr_b16 a[232:233], v19 offset:16520
  v_mfma_f32_16x16x32_bf16 v[92:95], a[252:255], a[148:151], v[92:95]
  buffer_load_dwordx4 a[60:63], v18, s[64:67], s71 offen sc1
  v_mfma_f32_16x16x32_bf16 v[124:127], a[248:251], a[136:139], 0
  ds_read_b64_tr_b16 a[234:235], v19 offset:20648
  v_mfma_f32_16x16x32_bf16 v[124:127], a[252:255], a[152:155], v[124:127]
  v_mfma_f32_16x16x32_bf16 v[156:159], a[248:251], a[140:143], 0
  ds_read_b64_tr_b16 a[236:237], v19 offset:24776
  v_mfma_f32_16x16x32_bf16 v[156:159], a[252:255], a[156:159], v[156:159]
  v_mfma_f32_16x16x32_bf16 v[188:191], a[248:251], a[128:131], 0
  ds_read_b64_tr_b16 a[238:239], v19 offset:28904
  v_mfma_f32_16x16x32_bf16 v[188:191], a[252:255], a[144:147], v[188:191]
  s_waitcnt lgkmcnt(4)
  v_mfma_f32_16x16x32_bf16 v[64:67], a[224:227], a[164:167], v[64:67]
  s_add_u32 s73, s89, 4
  s_add_u32 s73, s73, s87
  s_lshl_b32 s68, s73, 17
  ds_read_b64_tr_b16 a[240:241], v19 offset:16768
  v_mfma_f32_16x16x32_bf16 v[64:67], a[228:231], a[180:183], v[64:67]
  s_lshl_b32 s70, s73, 13
  s_add_u32 s71, s70, 4096
  s_add_u32 m0, s74, 82560
  v_mfma_f32_16x16x32_bf16 v[96:99], a[224:227], a[168:171], v[96:99]
  ds_read_b64_tr_b16 a[242:243], v19 offset:20896
  buffer_load_dwordx4 v1, s[52:55], s68 offen offset:0 lds
  v_mfma_f32_16x16x32_bf16 v[96:99], a[228:231], a[184:187], v[96:99]
  v_mfma_f32_16x16x32_bf16 v[128:131], a[224:227], a[172:175], v[128:131]
  ds_read_b64_tr_b16 a[244:245], v19 offset:25024
  v_mfma_f32_16x16x32_bf16 v[128:131], a[228:231], a[188:191], v[128:131]
  buffer_load_dwordx4 a[64:67], v15, s[64:67], s70 offen sc1
  v_mfma_f32_16x16x32_bf16 v[160:163], a[224:227], a[160:163], v[160:163]
  ds_read_b64_tr_b16 a[246:247], v19 offset:29152
  v_mfma_f32_16x16x32_bf16 v[160:163], a[228:231], a[176:179], v[160:163]
  s_waitcnt lgkmcnt(4)
  v_mfma_f32_16x16x32_bf16 v[68:71], a[232:235], a[164:167], v[68:71]
  ds_read_b64_tr_b16 a[248:249], v19 offset:16776
  v_mfma_f32_16x16x32_bf16 v[68:71], a[236:239], a[180:183], v[68:71]
  buffer_load_dwordx4 a[68:71], v16, s[64:67], s70 offen sc1
  v_mfma_f32_16x16x32_bf16 v[100:103], a[232:235], a[168:171], v[100:103]
  ds_read_b64_tr_b16 a[250:251], v19 offset:20904
  v_mfma_f32_16x16x32_bf16 v[100:103], a[236:239], a[184:187], v[100:103]
  v_mfma_f32_16x16x32_bf16 v[132:135], a[232:235], a[172:175], v[132:135]
  ds_read_b64_tr_b16 a[252:253], v19 offset:25032
  v_mfma_f32_16x16x32_bf16 v[132:135], a[236:239], a[188:191], v[132:135]
  buffer_store_dwordx4 v[192:195], v12, s[56:59], 0 offen offset:0 sc1
  v_mfma_f32_16x16x32_bf16 v[164:167], a[232:235], a[160:163], v[164:167]
  ds_read_b64_tr_b16 a[254:255], v19 offset:29160
  v_mfma_f32_16x16x32_bf16 v[164:167], a[236:239], a[176:179], v[164:167]
  s_waitcnt lgkmcnt(4)
  v_mfma_f32_16x16x32_bf16 v[72:75], a[240:243], a[164:167], v[72:75]
  ds_read_b64_tr_b16 a[224:225], v19 offset:17024
  v_mfma_f32_16x16x32_bf16 v[72:75], a[244:247], a[180:183], v[72:75]
  buffer_load_dwordx4 v2, s[52:55], s68 offen offset:1032 lds
  v_mfma_f32_16x16x32_bf16 v[104:107], a[240:243], a[168:171], v[104:107]
  ds_read_b64_tr_b16 a[226:227], v19 offset:21152
  v_mfma_f32_16x16x32_bf16 v[104:107], a[244:247], a[184:187], v[104:107]
  v_mfma_f32_16x16x32_bf16 v[136:139], a[240:243], a[172:175], v[136:139]
  ds_read_b64_tr_b16 a[228:229], v19 offset:25280
  v_mfma_f32_16x16x32_bf16 v[136:139], a[244:247], a[188:191], v[136:139]
  buffer_load_dwordx4 a[72:75], v17, s[64:67], s70 offen sc1
  v_mfma_f32_16x16x32_bf16 v[168:171], a[240:243], a[160:163], v[168:171]
  ds_read_b64_tr_b16 a[230:231], v19 offset:29408
  v_mfma_f32_16x16x32_bf16 v[168:171], a[244:247], a[176:179], v[168:171]
  s_waitcnt lgkmcnt(4)
  v_mfma_f32_16x16x32_bf16 v[76:79], a[248:251], a[164:167], v[76:79]
  ds_read_b64_tr_b16 a[232:233], v19 offset:17032
  buffer_load_dwordx4 a[76:79], v18, s[64:67], s70 offen sc1
  v_mfma_f32_16x16x32_bf16 v[76:79], a[252:255], a[180:183], v[76:79]
  v_mfma_f32_16x16x32_bf16 v[108:111], a[248:251], a[168:171], v[108:111]
  ds_read_b64_tr_b16 a[234:235], v19 offset:21160
  v_mfma_f32_16x16x32_bf16 v[108:111], a[252:255], a[184:187], v[108:111]
  v_mfma_f32_16x16x32_bf16 v[140:143], a[248:251], a[172:175], v[140:143]
  ds_read_b64_tr_b16 a[236:237], v19 offset:25288
  buffer_store_dwordx4 v[196:199], v12, s[56:59], 0 offen offset:1024 sc1
  v_mfma_f32_16x16x32_bf16 v[140:143], a[252:255], a[188:191], v[140:143]
  v_mfma_f32_16x16x32_bf16 v[172:175], a[248:251], a[160:163], v[172:175]
  ds_read_b64_tr_b16 a[238:239], v19 offset:29416
  v_mfma_f32_16x16x32_bf16 v[172:175], a[252:255], a[176:179], v[172:175]
  s_waitcnt lgkmcnt(4)
  v_mfma_f32_16x16x32_bf16 v[80:83], a[224:227], a[164:167], v[80:83]
  ds_read_b64_tr_b16 a[240:241], v19 offset:17280
  buffer_load_dwordx4 v3, s[52:55], s68 offen offset:2064 lds
  v_mfma_f32_16x16x32_bf16 v[80:83], a[228:231], a[180:183], v[80:83]
  v_mfma_f32_16x16x32_bf16 v[112:115], a[224:227], a[168:171], v[112:115]
  ds_read_b64_tr_b16 a[242:243], v19 offset:21408
  v_mfma_f32_16x16x32_bf16 v[112:115], a[228:231], a[184:187], v[112:115]
  v_mfma_f32_16x16x32_bf16 v[144:147], a[224:227], a[172:175], v[144:147]
  ds_read_b64_tr_b16 a[244:245], v19 offset:25536
  buffer_load_dwordx4 a[80:83], v15, s[64:67], s71 offen sc1
  v_mfma_f32_16x16x32_bf16 v[144:147], a[228:231], a[188:191], v[144:147]
  v_mfma_f32_16x16x32_bf16 v[176:179], a[224:227], a[160:163], v[176:179]
  ds_read_b64_tr_b16 a[246:247], v19 offset:29664
  v_mfma_f32_16x16x32_bf16 v[176:179], a[228:231], a[176:179], v[176:179]
  s_waitcnt lgkmcnt(4)
  v_mfma_f32_16x16x32_bf16 v[84:87], a[232:235], a[164:167], v[84:87]
  ds_read_b64_tr_b16 a[248:249], v19 offset:17288
  buffer_load_dwordx4 a[84:87], v16, s[64:67], s71 offen sc1
  v_mfma_f32_16x16x32_bf16 v[84:87], a[236:239], a[180:183], v[84:87]
  v_mfma_f32_16x16x32_bf16 v[116:119], a[232:235], a[168:171], v[116:119]
  ds_read_b64_tr_b16 a[250:251], v19 offset:21416
  v_mfma_f32_16x16x32_bf16 v[116:119], a[236:239], a[184:187], v[116:119]
  buffer_store_dwordx4 v[200:203], v12, s[56:59], 0 offen offset:2048 sc1
  v_mfma_f32_16x16x32_bf16 v[148:151], a[232:235], a[172:175], v[148:151]
  ds_read_b64_tr_b16 a[252:253], v19 offset:25544
  v_mfma_f32_16x16x32_bf16 v[148:151], a[236:239], a[188:191], v[148:151]
  v_mfma_f32_16x16x32_bf16 v[180:183], a[232:235], a[160:163], v[180:183]
  ds_read_b64_tr_b16 a[254:255], v19 offset:29672
  v_mfma_f32_16x16x32_bf16 v[180:183], a[236:239], a[176:179], v[180:183]
  s_waitcnt lgkmcnt(4)
  s_waitcnt vmcnt(36)
  s_barrier
  buffer_load_dwordx4 v4, s[52:55], s68 offen offset:3096 lds
  v_mfma_f32_16x16x32_bf16 v[88:91], a[240:243], a[164:167], v[88:91]
  ds_read_b64_tr_b16 a[224:225], v19 offset:33024
  v_mfma_f32_16x16x32_bf16 v[88:91], a[244:247], a[180:183], v[88:91]
  v_mfma_f32_16x16x32_bf16 v[120:123], a[240:243], a[168:171], v[120:123]
  ds_read_b64_tr_b16 a[226:227], v19 offset:37152
  v_mfma_f32_16x16x32_bf16 v[120:123], a[244:247], a[184:187], v[120:123]
  buffer_load_dwordx4 a[88:91], v17, s[64:67], s71 offen sc1
  v_mfma_f32_16x16x32_bf16 v[152:155], a[240:243], a[172:175], v[152:155]
  ds_read_b64_tr_b16 a[228:229], v19 offset:41280
  v_mfma_f32_16x16x32_bf16 v[152:155], a[244:247], a[188:191], v[152:155]
  v_mfma_f32_16x16x32_bf16 v[184:187], a[240:243], a[160:163], v[184:187]
  ds_read_b64_tr_b16 a[230:231], v19 offset:45408
  v_mfma_f32_16x16x32_bf16 v[184:187], a[244:247], a[176:179], v[184:187]
  s_waitcnt lgkmcnt(4)
  buffer_load_dwordx4 a[92:95], v18, s[64:67], s71 offen sc1
  v_mfma_f32_16x16x32_bf16 v[92:95], a[248:251], a[164:167], v[92:95]
  ds_read_b64_tr_b16 a[232:233], v19 offset:33032
  v_mfma_f32_16x16x32_bf16 v[92:95], a[252:255], a[180:183], v[92:95]
  v_mfma_f32_16x16x32_bf16 v[124:127], a[248:251], a[168:171], v[124:127]
  ds_read_b64_tr_b16 a[234:235], v19 offset:37160
  v_mfma_f32_16x16x32_bf16 v[124:127], a[252:255], a[184:187], v[124:127]
  buffer_store_dwordx4 v[204:207], v12, s[56:59], 0 offen offset:3072 sc1
  v_mfma_f32_16x16x32_bf16 v[156:159], a[248:251], a[172:175], v[156:159]
  ds_read_b64_tr_b16 a[236:237], v19 offset:41288
  v_mfma_f32_16x16x32_bf16 v[156:159], a[252:255], a[188:191], v[156:159]
  v_mfma_f32_16x16x32_bf16 v[188:191], a[248:251], a[160:163], v[188:191]
  ds_read_b64_tr_b16 a[238:239], v19 offset:45416
  v_mfma_f32_16x16x32_bf16 v[188:191], a[252:255], a[176:179], v[188:191]
  s_waitcnt lgkmcnt(4)
  v_mfma_f32_16x16x32_bf16 v[64:67], a[224:227], a[196:199], v[64:67]
  s_add_u32 s73, s90, 4
  s_add_u32 s73, s73, s87
  s_lshl_b32 s68, s73, 17
  ds_read_b64_tr_b16 a[240:241], v19 offset:33280
  v_mfma_f32_16x16x32_bf16 v[64:67], a[228:231], a[212:215], v[64:67]
  s_lshl_b32 s70, s73, 13
  s_add_u32 s71, s70, 4096
  s_add_u32 m0, s74, 99072
  v_mfma_f32_16x16x32_bf16 v[96:99], a[224:227], a[200:203], v[96:99]
  ds_read_b64_tr_b16 a[242:243], v19 offset:37408
  buffer_load_dwordx4 v1, s[52:55], s68 offen offset:0 lds
  v_mfma_f32_16x16x32_bf16 v[96:99], a[228:231], a[216:219], v[96:99]
  v_mfma_f32_16x16x32_bf16 v[128:131], a[224:227], a[204:207], v[128:131]
  ds_read_b64_tr_b16 a[244:245], v19 offset:41536
  v_mfma_f32_16x16x32_bf16 v[128:131], a[228:231], a[220:223], v[128:131]
  buffer_load_dwordx4 a[96:99], v15, s[64:67], s70 offen sc1
  v_mfma_f32_16x16x32_bf16 v[160:163], a[224:227], a[192:195], v[160:163]
  ds_read_b64_tr_b16 a[246:247], v19 offset:45664
  v_mfma_f32_16x16x32_bf16 v[160:163], a[228:231], a[208:211], v[160:163]
  s_waitcnt lgkmcnt(4)
  v_mfma_f32_16x16x32_bf16 v[68:71], a[232:235], a[196:199], v[68:71]
  ds_read_b64_tr_b16 a[248:249], v19 offset:33288
  v_mfma_f32_16x16x32_bf16 v[68:71], a[236:239], a[212:215], v[68:71]
  buffer_load_dwordx4 a[100:103], v16, s[64:67], s70 offen sc1
  v_mfma_f32_16x16x32_bf16 v[100:103], a[232:235], a[200:203], v[100:103]
  ds_read_b64_tr_b16 a[250:251], v19 offset:37416
  v_mfma_f32_16x16x32_bf16 v[100:103], a[236:239], a[216:219], v[100:103]
  v_mfma_f32_16x16x32_bf16 v[132:135], a[232:235], a[204:207], v[132:135]
  ds_read_b64_tr_b16 a[252:253], v19 offset:41544
  v_mfma_f32_16x16x32_bf16 v[132:135], a[236:239], a[220:223], v[132:135]
  buffer_store_dwordx4 v[208:211], v12, s[56:59], s83 offen offset:0 sc1
  v_mfma_f32_16x16x32_bf16 v[164:167], a[232:235], a[192:195], v[164:167]
  ds_read_b64_tr_b16 a[254:255], v19 offset:45672
  v_mfma_f32_16x16x32_bf16 v[164:167], a[236:239], a[208:211], v[164:167]
  s_waitcnt lgkmcnt(4)
  v_mfma_f32_16x16x32_bf16 v[72:75], a[240:243], a[196:199], v[72:75]
  ds_read_b64_tr_b16 a[224:225], v19 offset:33536
  v_mfma_f32_16x16x32_bf16 v[72:75], a[244:247], a[212:215], v[72:75]
  buffer_load_dwordx4 v2, s[52:55], s68 offen offset:1032 lds
  v_mfma_f32_16x16x32_bf16 v[104:107], a[240:243], a[200:203], v[104:107]
  ds_read_b64_tr_b16 a[226:227], v19 offset:37664
  v_mfma_f32_16x16x32_bf16 v[104:107], a[244:247], a[216:219], v[104:107]
  v_mfma_f32_16x16x32_bf16 v[136:139], a[240:243], a[204:207], v[136:139]
  ds_read_b64_tr_b16 a[228:229], v19 offset:41792
  v_mfma_f32_16x16x32_bf16 v[136:139], a[244:247], a[220:223], v[136:139]
  buffer_load_dwordx4 a[104:107], v17, s[64:67], s70 offen sc1
  v_mfma_f32_16x16x32_bf16 v[168:171], a[240:243], a[192:195], v[168:171]
  ds_read_b64_tr_b16 a[230:231], v19 offset:45920
  v_mfma_f32_16x16x32_bf16 v[168:171], a[244:247], a[208:211], v[168:171]
  s_waitcnt lgkmcnt(4)
  v_mfma_f32_16x16x32_bf16 v[76:79], a[248:251], a[196:199], v[76:79]
  ds_read_b64_tr_b16 a[232:233], v19 offset:33544
  buffer_load_dwordx4 a[108:111], v18, s[64:67], s70 offen sc1
  v_mfma_f32_16x16x32_bf16 v[76:79], a[252:255], a[212:215], v[76:79]
  v_mfma_f32_16x16x32_bf16 v[108:111], a[248:251], a[200:203], v[108:111]
  ds_read_b64_tr_b16 a[234:235], v19 offset:37672
  v_mfma_f32_16x16x32_bf16 v[108:111], a[252:255], a[216:219], v[108:111]
  v_mfma_f32_16x16x32_bf16 v[140:143], a[248:251], a[204:207], v[140:143]
  ds_read_b64_tr_b16 a[236:237], v19 offset:41800
  buffer_store_dwordx4 v[212:215], v12, s[56:59], s83 offen offset:1024 sc1
  v_mfma_f32_16x16x32_bf16 v[140:143], a[252:255], a[220:223], v[140:143]
  v_mfma_f32_16x16x32_bf16 v[172:175], a[248:251], a[192:195], v[172:175]
  ds_read_b64_tr_b16 a[238:239], v19 offset:45928
  v_mfma_f32_16x16x32_bf16 v[172:175], a[252:255], a[208:211], v[172:175]
  s_waitcnt lgkmcnt(4)
  v_mfma_f32_16x16x32_bf16 v[80:83], a[224:227], a[196:199], v[80:83]
  ds_read_b64_tr_b16 a[240:241], v19 offset:33792
  buffer_load_dwordx4 v3, s[52:55], s68 offen offset:2064 lds
  v_mfma_f32_16x16x32_bf16 v[80:83], a[228:231], a[212:215], v[80:83]
  v_mfma_f32_16x16x32_bf16 v[112:115], a[224:227], a[200:203], v[112:115]
  ds_read_b64_tr_b16 a[242:243], v19 offset:37920
  v_mfma_f32_16x16x32_bf16 v[112:115], a[228:231], a[216:219], v[112:115]
  v_mfma_f32_16x16x32_bf16 v[144:147], a[224:227], a[204:207], v[144:147]
  ds_read_b64_tr_b16 a[244:245], v19 offset:42048
  buffer_load_dwordx4 a[112:115], v15, s[64:67], s71 offen sc1
  v_mfma_f32_16x16x32_bf16 v[144:147], a[228:231], a[220:223], v[144:147]
  v_mfma_f32_16x16x32_bf16 v[176:179], a[224:227], a[192:195], v[176:179]
  ds_read_b64_tr_b16 a[246:247], v19 offset:46176
  v_mfma_f32_16x16x32_bf16 v[176:179], a[228:231], a[208:211], v[176:179]
  s_waitcnt lgkmcnt(4)
  v_mfma_f32_16x16x32_bf16 v[84:87], a[232:235], a[196:199], v[84:87]
  ds_read_b64_tr_b16 a[248:249], v19 offset:33800
  buffer_load_dwordx4 a[116:119], v16, s[64:67], s71 offen sc1
  v_mfma_f32_16x16x32_bf16 v[84:87], a[236:239], a[212:215], v[84:87]
  v_mfma_f32_16x16x32_bf16 v[116:119], a[232:235], a[200:203], v[116:119]
  ds_read_b64_tr_b16 a[250:251], v19 offset:37928
  v_mfma_f32_16x16x32_bf16 v[116:119], a[236:239], a[216:219], v[116:119]
  buffer_store_dwordx4 v[216:219], v12, s[56:59], s83 offen offset:2048 sc1
  v_mfma_f32_16x16x32_bf16 v[148:151], a[232:235], a[204:207], v[148:151]
  ds_read_b64_tr_b16 a[252:253], v19 offset:42056
  v_mfma_f32_16x16x32_bf16 v[148:151], a[236:239], a[220:223], v[148:151]
  v_mfma_f32_16x16x32_bf16 v[180:183], a[232:235], a[192:195], v[180:183]
  ds_read_b64_tr_b16 a[254:255], v19 offset:46184
  v_mfma_f32_16x16x32_bf16 v[180:183], a[236:239], a[208:211], v[180:183]
  s_waitcnt lgkmcnt(4)
  s_waitcnt vmcnt(40)
  s_barrier
  buffer_load_dwordx4 v4, s[52:55], s68 offen offset:3096 lds
  v_mfma_f32_16x16x32_bf16 v[88:91], a[240:243], a[196:199], v[88:91]
  ds_read_b64_tr_b16 a[224:225], v19 offset:49536
  v_mfma_f32_16x16x32_bf16 v[88:91], a[244:247], a[212:215], v[88:91]
  v_mfma_f32_16x16x32_bf16 v[120:123], a[240:243], a[200:203], v[120:123]
  ds_read_b64_tr_b16 a[226:227], v19 offset:53664
  v_mfma_f32_16x16x32_bf16 v[120:123], a[244:247], a[216:219], v[120:123]
  buffer_load_dwordx4 a[120:123], v17, s[64:67], s71 offen sc1
  v_mfma_f32_16x16x32_bf16 v[152:155], a[240:243], a[204:207], v[152:155]
  ds_read_b64_tr_b16 a[228:229], v19 offset:57792
  v_mfma_f32_16x16x32_bf16 v[152:155], a[244:247], a[220:223], v[152:155]
  v_mfma_f32_16x16x32_bf16 v[184:187], a[240:243], a[192:195], v[184:187]
  ds_read_b64_tr_b16 a[230:231], v19 offset:61920
  v_mfma_f32_16x16x32_bf16 v[184:187], a[244:247], a[208:211], v[184:187]
  s_waitcnt lgkmcnt(4)
  buffer_load_dwordx4 a[124:127], v18, s[64:67], s71 offen sc1
  v_mfma_f32_16x16x32_bf16 v[92:95], a[248:251], a[196:199], v[92:95]
  ds_read_b64_tr_b16 a[232:233], v19 offset:49544
  v_mfma_f32_16x16x32_bf16 v[92:95], a[252:255], a[212:215], v[92:95]
  v_mfma_f32_16x16x32_bf16 v[124:127], a[248:251], a[200:203], v[124:127]
  ds_read_b64_tr_b16 a[234:235], v19 offset:53672
  v_mfma_f32_16x16x32_bf16 v[124:127], a[252:255], a[216:219], v[124:127]
  buffer_store_dwordx4 v[220:223], v12, s[56:59], s83 offen offset:3072 sc1
  v_mfma_f32_16x16x32_bf16 v[156:159], a[248:251], a[204:207], v[156:159]
  ds_read_b64_tr_b16 a[236:237], v19 offset:57800
  v_mfma_f32_16x16x32_bf16 v[156:159], a[252:255], a[220:223], v[156:159]
  v_mfma_f32_16x16x32_bf16 v[188:191], a[248:251], a[192:195], v[188:191]
  ds_read_b64_tr_b16 a[238:239], v19 offset:61928
  v_mfma_f32_16x16x32_bf16 v[188:191], a[252:255], a[208:211], v[188:191]
  s_waitcnt lgkmcnt(4)
  v_mfma_f32_16x16x32_bf16 v[64:67], a[224:227], a[4:7], v[64:67]
  s_add_u32 s73, s91, 4
  s_add_u32 s73, s73, s87
  s_lshl_b32 s68, s73, 17
  ds_read_b64_tr_b16 a[240:241], v19 offset:49792
  v_mfma_f32_16x16x32_bf16 v[64:67], a[228:231], a[20:23], v[64:67]
  s_lshl_b32 s70, s73, 13
  s_add_u32 s71, s70, 4096
  s_add_u32 m0, s74, 0
  v_mfma_f32_16x16x32_bf16 v[96:99], a[224:227], a[8:11], v[96:99]
  ds_read_b64_tr_b16 a[242:243], v19 offset:53920
  buffer_load_dwordx4 v1, s[52:55], s68 offen offset:0 lds
  v_mfma_f32_16x16x32_bf16 v[96:99], a[228:231], a[24:27], v[96:99]
  v_mfma_f32_16x16x32_bf16 v[128:131], a[224:227], a[12:15], v[128:131]
  ds_read_b64_tr_b16 a[244:245], v19 offset:58048
  v_mfma_f32_16x16x32_bf16 v[128:131], a[228:231], a[28:31], v[128:131]
  buffer_load_dwordx4 a[128:131], v15, s[64:67], s70 offen sc1
  v_mfma_f32_16x16x32_bf16 v[160:163], a[224:227], a[0:3], v[160:163]
  ds_read_b64_tr_b16 a[246:247], v19 offset:62176
  v_mfma_f32_16x16x32_bf16 v[160:163], a[228:231], a[16:19], v[160:163]
  s_waitcnt lgkmcnt(4)
  v_mfma_f32_16x16x32_bf16 v[68:71], a[232:235], a[4:7], v[68:71]
  ds_read_b64_tr_b16 a[248:249], v19 offset:49800
  v_mfma_f32_16x16x32_bf16 v[68:71], a[236:239], a[20:23], v[68:71]
  buffer_load_dwordx4 a[132:135], v16, s[64:67], s70 offen sc1
  v_mfma_f32_16x16x32_bf16 v[100:103], a[232:235], a[8:11], v[100:103]
  ds_read_b64_tr_b16 a[250:251], v19 offset:53928
  v_mfma_f32_16x16x32_bf16 v[100:103], a[236:239], a[24:27], v[100:103]
  v_mfma_f32_16x16x32_bf16 v[132:135], a[232:235], a[12:15], v[132:135]
  ds_read_b64_tr_b16 a[252:253], v19 offset:58056
  v_mfma_f32_16x16x32_bf16 v[132:135], a[236:239], a[28:31], v[132:135]
  buffer_store_dwordx4 v[224:227], v12, s[56:59], s84 offen offset:0 sc1
  v_mfma_f32_16x16x32_bf16 v[164:167], a[232:235], a[0:3], v[164:167]
  ds_read_b64_tr_b16 a[254:255], v19 offset:62184
  v_mfma_f32_16x16x32_bf16 v[164:167], a[236:239], a[16:19], v[164:167]
  s_waitcnt lgkmcnt(4)
  v_mfma_f32_16x16x32_bf16 v[72:75], a[240:243], a[4:7], v[72:75]
  ds_read_b64_tr_b16 a[224:225], v19 offset:50048
  v_mfma_f32_16x16x32_bf16 v[72:75], a[244:247], a[20:23], v[72:75]
  buffer_load_dwordx4 v2, s[52:55], s68 offen offset:1032 lds
  v_mfma_f32_16x16x32_bf16 v[104:107], a[240:243], a[8:11], v[104:107]
  ds_read_b64_tr_b16 a[226:227], v19 offset:54176
  v_mfma_f32_16x16x32_bf16 v[104:107], a[244:247], a[24:27], v[104:107]
  v_mfma_f32_16x16x32_bf16 v[136:139], a[240:243], a[12:15], v[136:139]
  ds_read_b64_tr_b16 a[228:229], v19 offset:58304
  v_mfma_f32_16x16x32_bf16 v[136:139], a[244:247], a[28:31], v[136:139]
  buffer_load_dwordx4 a[136:139], v17, s[64:67], s70 offen sc1
  v_mfma_f32_16x16x32_bf16 v[168:171], a[240:243], a[0:3], v[168:171]
  ds_read_b64_tr_b16 a[230:231], v19 offset:62432
  v_mfma_f32_16x16x32_bf16 v[168:171], a[244:247], a[16:19], v[168:171]
  s_waitcnt lgkmcnt(4)
  v_mfma_f32_16x16x32_bf16 v[76:79], a[248:251], a[4:7], v[76:79]
  ds_read_b64_tr_b16 a[232:233], v19 offset:50056
  buffer_load_dwordx4 a[140:143], v18, s[64:67], s70 offen sc1
  v_mfma_f32_16x16x32_bf16 v[76:79], a[252:255], a[20:23], v[76:79]
  v_mfma_f32_16x16x32_bf16 v[108:111], a[248:251], a[8:11], v[108:111]
  ds_read_b64_tr_b16 a[234:235], v19 offset:54184
  v_mfma_f32_16x16x32_bf16 v[108:111], a[252:255], a[24:27], v[108:111]
  v_mfma_f32_16x16x32_bf16 v[140:143], a[248:251], a[12:15], v[140:143]
  ds_read_b64_tr_b16 a[236:237], v19 offset:58312
  buffer_store_dwordx4 v[228:231], v12, s[56:59], s84 offen offset:1024 sc1
  v_mfma_f32_16x16x32_bf16 v[140:143], a[252:255], a[28:31], v[140:143]
  v_mfma_f32_16x16x32_bf16 v[172:175], a[248:251], a[0:3], v[172:175]
  ds_read_b64_tr_b16 a[238:239], v19 offset:62440
  v_mfma_f32_16x16x32_bf16 v[172:175], a[252:255], a[16:19], v[172:175]
  s_waitcnt lgkmcnt(4)
  v_mfma_f32_16x16x32_bf16 v[80:83], a[224:227], a[4:7], v[80:83]
  ds_read_b64_tr_b16 a[240:241], v19 offset:50304
  buffer_load_dwordx4 v3, s[52:55], s68 offen offset:2064 lds
  v_mfma_f32_16x16x32_bf16 v[80:83], a[228:231], a[20:23], v[80:83]
  v_mfma_f32_16x16x32_bf16 v[112:115], a[224:227], a[8:11], v[112:115]
  ds_read_b64_tr_b16 a[242:243], v19 offset:54432
  v_mfma_f32_16x16x32_bf16 v[112:115], a[228:231], a[24:27], v[112:115]
  v_mfma_f32_16x16x32_bf16 v[144:147], a[224:227], a[12:15], v[144:147]
  ds_read_b64_tr_b16 a[244:245], v19 offset:58560
  buffer_load_dwordx4 a[144:147], v15, s[64:67], s71 offen sc1
  v_mfma_f32_16x16x32_bf16 v[144:147], a[228:231], a[28:31], v[144:147]
  v_mfma_f32_16x16x32_bf16 v[176:179], a[224:227], a[0:3], v[176:179]
  ds_read_b64_tr_b16 a[246:247], v19 offset:62688
  v_mfma_f32_16x16x32_bf16 v[176:179], a[228:231], a[16:19], v[176:179]
  s_waitcnt lgkmcnt(4)
  v_mfma_f32_16x16x32_bf16 v[84:87], a[232:235], a[4:7], v[84:87]
  ds_read_b64_tr_b16 a[248:249], v19 offset:50312
  buffer_load_dwordx4 a[148:151], v16, s[64:67], s71 offen sc1
  v_mfma_f32_16x16x32_bf16 v[84:87], a[236:239], a[20:23], v[84:87]
  v_mfma_f32_16x16x32_bf16 v[116:119], a[232:235], a[8:11], v[116:119]
  ds_read_b64_tr_b16 a[250:251], v19 offset:54440
  v_mfma_f32_16x16x32_bf16 v[116:119], a[236:239], a[24:27], v[116:119]
  buffer_store_dwordx4 v[232:235], v12, s[56:59], s84 offen offset:2048 sc1
  v_mfma_f32_16x16x32_bf16 v[148:151], a[232:235], a[12:15], v[148:151]
  ds_read_b64_tr_b16 a[252:253], v19 offset:58568
  v_mfma_f32_16x16x32_bf16 v[148:151], a[236:239], a[28:31], v[148:151]
  v_mfma_f32_16x16x32_bf16 v[180:183], a[232:235], a[0:3], v[180:183]
  ds_read_b64_tr_b16 a[254:255], v19 offset:62696
  v_mfma_f32_16x16x32_bf16 v[180:183], a[236:239], a[16:19], v[180:183]
  s_waitcnt lgkmcnt(4)
  s_waitcnt vmcnt(44)
  s_barrier
  buffer_load_dwordx4 v4, s[52:55], s68 offen offset:3096 lds
  v_mfma_f32_16x16x32_bf16 v[88:91], a[240:243], a[4:7], v[88:91]
  ds_read_b64_tr_b16 a[224:225], v20 offset:0
  v_mfma_f32_16x16x32_bf16 v[88:91], a[244:247], a[20:23], v[88:91]
  v_mfma_f32_16x16x32_bf16 v[120:123], a[240:243], a[8:11], v[120:123]
  ds_read_b64_tr_b16 a[226:227], v20 offset:4128
  v_mfma_f32_16x16x32_bf16 v[120:123], a[244:247], a[24:27], v[120:123]
  buffer_load_dwordx4 a[152:155], v17, s[64:67], s71 offen sc1
  v_mfma_f32_16x16x32_bf16 v[152:155], a[240:243], a[12:15], v[152:155]
  ds_read_b64_tr_b16 a[228:229], v20 offset:8256
  v_mfma_f32_16x16x32_bf16 v[152:155], a[244:247], a[28:31], v[152:155]
  v_mfma_f32_16x16x32_bf16 v[184:187], a[240:243], a[0:3], v[184:187]
  ds_read_b64_tr_b16 a[230:231], v20 offset:12384
  v_mfma_f32_16x16x32_bf16 v[184:187], a[244:247], a[16:19], v[184:187]
  s_waitcnt lgkmcnt(4)
  buffer_load_dwordx4 a[156:159], v18, s[64:67], s71 offen sc1
  v_mfma_f32_16x16x32_bf16 v[92:95], a[248:251], a[4:7], v[92:95]
  ds_read_b64_tr_b16 a[232:233], v20 offset:8
  v_mfma_f32_16x16x32_bf16 v[92:95], a[252:255], a[20:23], v[92:95]
  v_mfma_f32_16x16x32_bf16 v[124:127], a[248:251], a[8:11], v[124:127]
  ds_read_b64_tr_b16 a[234:235], v20 offset:4136
  v_mfma_f32_16x16x32_bf16 v[124:127], a[252:255], a[24:27], v[124:127]
  buffer_store_dwordx4 v[236:239], v12, s[56:59], s84 offen offset:3072 sc1
  v_mfma_f32_16x16x32_bf16 v[156:159], a[248:251], a[12:15], v[156:159]
  ds_read_b64_tr_b16 a[236:237], v20 offset:8264
  v_mfma_f32_16x16x32_bf16 v[156:159], a[252:255], a[28:31], v[156:159]
  v_mfma_f32_16x16x32_bf16 v[188:191], a[248:251], a[0:3], v[188:191]
  ds_read_b64_tr_b16 a[238:239], v20 offset:12392
  v_mfma_f32_16x16x32_bf16 v[188:191], a[252:255], a[16:19], v[188:191]
  s_waitcnt lgkmcnt(4)
  v_mfma_f32_16x16x32_bf16 v[64:67], a[224:227], a[36:39], v[64:67]
  s_add_u32 s73, s88, 8
  s_add_u32 s73, s73, s87
  s_lshl_b32 s68, s73, 17
  ds_read_b64_tr_b16 a[240:241], v20 offset:256
  v_mfma_f32_16x16x32_bf16 v[64:67], a[228:231], a[52:55], v[64:67]
  s_lshl_b32 s70, s73, 13
  s_add_u32 s71, s70, 4096
  s_add_u32 m0, s74, 16512
  v_mfma_f32_16x16x32_bf16 v[96:99], a[224:227], a[40:43], v[96:99]
  ds_read_b64_tr_b16 a[242:243], v20 offset:4384
  buffer_load_dwordx4 v1, s[52:55], s68 offen offset:0 lds
  v_mfma_f32_16x16x32_bf16 v[96:99], a[228:231], a[56:59], v[96:99]
  v_mfma_f32_16x16x32_bf16 v[128:131], a[224:227], a[44:47], v[128:131]
  ds_read_b64_tr_b16 a[244:245], v20 offset:8512
  v_mfma_f32_16x16x32_bf16 v[128:131], a[228:231], a[60:63], v[128:131]
  v_mfma_f32_16x16x32_bf16 v[160:163], a[224:227], a[32:35], v[160:163]
  ds_read_b64_tr_b16 a[246:247], v20 offset:12640
  v_mfma_f32_16x16x32_bf16 v[160:163], a[228:231], a[48:51], v[160:163]
  s_waitcnt lgkmcnt(4)
  buffer_load_dwordx4 a[160:163], v15, s[64:67], s70 offen sc1
  v_mfma_f32_16x16x32_bf16 v[68:71], a[232:235], a[36:39], v[68:71]
  ds_read_b64_tr_b16 a[248:249], v20 offset:264
  v_mfma_f32_16x16x32_bf16 v[68:71], a[236:239], a[52:55], v[68:71]
  v_mfma_f32_16x16x32_bf16 v[100:103], a[232:235], a[40:43], v[100:103]
  ds_read_b64_tr_b16 a[250:251], v20 offset:4392
  v_mfma_f32_16x16x32_bf16 v[100:103], a[236:239], a[56:59], v[100:103]
  v_mfma_f32_16x16x32_bf16 v[132:135], a[232:235], a[44:47], v[132:135]
  ds_read_b64_tr_b16 a[252:253], v20 offset:8520
  buffer_load_dwordx4 a[164:167], v16, s[64:67], s70 offen sc1
  v_mfma_f32_16x16x32_bf16 v[132:135], a[236:239], a[60:63], v[132:135]
  v_mfma_f32_16x16x32_bf16 v[164:167], a[232:235], a[32:35], v[164:167]
  ds_read_b64_tr_b16 a[254:255], v20 offset:12648
  v_mfma_f32_16x16x32_bf16 v[164:167], a[236:239], a[48:51], v[164:167]
  s_waitcnt lgkmcnt(4)
  v_mfma_f32_16x16x32_bf16 v[72:75], a[240:243], a[36:39], v[72:75]
  ds_read_b64_tr_b16 a[224:225], v20 offset:512
  v_mfma_f32_16x16x32_bf16 v[72:75], a[244:247], a[52:55], v[72:75]
  buffer_load_dwordx4 v2, s[52:55], s68 offen offset:1032 lds
  v_mfma_f32_16x16x32_bf16 v[104:107], a[240:243], a[40:43], v[104:107]
  ds_read_b64_tr_b16 a[226:227], v20 offset:4640
  v_mfma_f32_16x16x32_bf16 v[104:107], a[244:247], a[56:59], v[104:107]
  v_mfma_f32_16x16x32_bf16 v[136:139], a[240:243], a[44:47], v[136:139]
  ds_read_b64_tr_b16 a[228:229], v20 offset:8768
  v_mfma_f32_16x16x32_bf16 v[136:139], a[244:247], a[60:63], v[136:139]
  v_mfma_f32_16x16x32_bf16 v[168:171], a[240:243], a[32:35], v[168:171]
  ds_read_b64_tr_b16 a[230:231], v20 offset:12896
  buffer_load_dwordx4 a[168:171], v17, s[64:67], s70 offen sc1
  v_mfma_f32_16x16x32_bf16 v[168:171], a[244:247], a[48:51], v[168:171]
  s_waitcnt lgkmcnt(4)
  v_mfma_f32_16x16x32_bf16 v[76:79], a[248:251], a[36:39], v[76:79]
  ds_read_b64_tr_b16 a[232:233], v20 offset:520
  v_mfma_f32_16x16x32_bf16 v[76:79], a[252:255], a[52:55], v[76:79]
  v_mfma_f32_16x16x32_bf16 v[108:111], a[248:251], a[40:43], v[108:111]
  ds_read_b64_tr_b16 a[234:235], v20 offset:4648
  v_mfma_f32_16x16x32_bf16 v[108:111], a[252:255], a[56:59], v[108:111]
  buffer_load_dwordx4 a[172:175], v18, s[64:67], s70 offen sc1
  v_mfma_f32_16x16x32_bf16 v[140:143], a[248:251], a[44:47], v[140:143]
  ds_read_b64_tr_b16 a[236:237], v20 offset:8776
  v_mfma_f32_16x16x32_bf16 v[140:143], a[252:255], a[60:63], v[140:143]
  v_mfma_f32_16x16x32_bf16 v[172:175], a[248:251], a[32:35], v[172:175]
  ds_read_b64_tr_b16 a[238:239], v20 offset:12904
  v_mfma_f32_16x16x32_bf16 v[172:175], a[252:255], a[48:51], v[172:175]
  s_waitcnt lgkmcnt(4)
  v_mfma_f32_16x16x32_bf16 v[80:83], a[224:227], a[36:39], v[80:83]
  ds_read_b64_tr_b16 a[240:241], v20 offset:768
  buffer_load_dwordx4 v3, s[52:55], s68 offen offset:2064 lds
  v_mfma_f32_16x16x32_bf16 v[80:83], a[228:231], a[52:55], v[80:83]
  v_mfma_f32_16x16x32_bf16 v[112:115], a[224:227], a[40:43], v[112:115]
  ds_read_b64_tr_b16 a[242:243], v20 offset:4896
  v_mfma_f32_16x16x32_bf16 v[112:115], a[228:231], a[56:59], v[112:115]
  v_mfma_f32_16x16x32_bf16 v[144:147], a[224:227], a[44:47], v[144:147]
  ds_read_b64_tr_b16 a[244:245], v20 offset:9024
  v_mfma_f32_16x16x32_bf16 v[144:147], a[228:231], a[60:63], v[144:147]
  buffer_load_dwordx4 a[176:179], v15, s[64:67], s71 offen sc1
  v_mfma_f32_16x16x32_bf16 v[176:179], a[224:227], a[32:35], v[176:179]
  ds_read_b64_tr_b16 a[246:247], v20 offset:13152
  v_mfma_f32_16x16x32_bf16 v[176:179], a[228:231], a[48:51], v[176:179]
  s_waitcnt lgkmcnt(4)
  v_mfma_f32_16x16x32_bf16 v[84:87], a[232:235], a[36:39], v[84:87]
  ds_read_b64_tr_b16 a[248:249], v20 offset:776
  v_mfma_f32_16x16x32_bf16 v[84:87], a[236:239], a[52:55], v[84:87]
  v_mfma_f32_16x16x32_bf16 v[116:119], a[232:235], a[40:43], v[116:119]
  ds_read_b64_tr_b16 a[250:251], v20 offset:4904
  buffer_load_dwordx4 a[180:183], v16, s[64:67], s71 offen sc1
  v_mfma_f32_16x16x32_bf16 v[116:119], a[236:239], a[56:59], v[116:119]
  v_mfma_f32_16x16x32_bf16 v[148:151], a[232:235], a[44:47], v[148:151]
  ds_read_b64_tr_b16 a[252:253], v20 offset:9032
  v_mfma_f32_16x16x32_bf16 v[148:151], a[236:239], a[60:63], v[148:151]
  v_mfma_f32_16x16x32_bf16 v[180:183], a[232:235], a[32:35], v[180:183]
  ds_read_b64_tr_b16 a[254:255], v20 offset:13160
  v_mfma_f32_16x16x32_bf16 v[180:183], a[236:239], a[48:51], v[180:183]
  s_waitcnt lgkmcnt(4)
  s_waitcnt vmcnt(42)
  s_barrier
  buffer_load_dwordx4 v4, s[52:55], s68 offen offset:3096 lds
  v_mfma_f32_16x16x32_bf16 v[88:91], a[240:243], a[36:39], v[88:91]
  ds_read_b64_tr_b16 a[224:225], v20 offset:16512
  v_mfma_f32_16x16x32_bf16 v[88:91], a[244:247], a[52:55], v[88:91]
  v_mfma_f32_16x16x32_bf16 v[120:123], a[240:243], a[40:43], v[120:123]
  ds_read_b64_tr_b16 a[226:227], v20 offset:20640
  v_mfma_f32_16x16x32_bf16 v[120:123], a[244:247], a[56:59], v[120:123]
  v_mfma_f32_16x16x32_bf16 v[152:155], a[240:243], a[44:47], v[152:155]
  ds_read_b64_tr_b16 a[228:229], v20 offset:24768
  buffer_load_dwordx4 a[184:187], v17, s[64:67], s71 offen sc1
  v_mfma_f32_16x16x32_bf16 v[152:155], a[244:247], a[60:63], v[152:155]
  v_mfma_f32_16x16x32_bf16 v[184:187], a[240:243], a[32:35], v[184:187]
  ds_read_b64_tr_b16 a[230:231], v20 offset:28896
  v_mfma_f32_16x16x32_bf16 v[184:187], a[244:247], a[48:51], v[184:187]
  s_waitcnt lgkmcnt(4)
  v_mfma_f32_16x16x32_bf16 v[92:95], a[248:251], a[36:39], v[92:95]
  ds_read_b64_tr_b16 a[232:233], v20 offset:16520
  v_mfma_f32_16x16x32_bf16 v[92:95], a[252:255], a[52:55], v[92:95]
  buffer_load_dwordx4 a[188:191], v18, s[64:67], s71 offen sc1
  v_mfma_f32_16x16x32_bf16 v[124:127], a[248:251], a[40:43], v[124:127]
  ds_read_b64_tr_b16 a[234:235], v20 offset:20648
  v_mfma_f32_16x16x32_bf16 v[124:127], a[252:255], a[56:59], v[124:127]
  v_mfma_f32_16x16x32_bf16 v[156:159], a[248:251], a[44:47], v[156:159]
  ds_read_b64_tr_b16 a[236:237], v20 offset:24776
  v_mfma_f32_16x16x32_bf16 v[156:159], a[252:255], a[60:63], v[156:159]
  v_mfma_f32_16x16x32_bf16 v[188:191], a[248:251], a[32:35], v[188:191]
  ds_read_b64_tr_b16 a[238:239], v20 offset:28904
  v_mfma_f32_16x16x32_bf16 v[188:191], a[252:255], a[48:51], v[188:191]
  s_waitcnt lgkmcnt(4)
  v_mfma_f32_16x16x32_bf16 v[64:67], a[224:227], a[68:71], v[64:67]
  s_add_u32 s73, s89, 8
  s_add_u32 s73, s73, s87
  s_lshl_b32 s68, s73, 17
  ds_read_b64_tr_b16 a[240:241], v20 offset:16768
  v_mfma_f32_16x16x32_bf16 v[64:67], a[228:231], a[84:87], v[64:67]
  s_lshl_b32 s70, s73, 13
  s_add_u32 s71, s70, 4096
  s_add_u32 m0, s74, 33024
  v_mfma_f32_16x16x32_bf16 v[96:99], a[224:227], a[72:75], v[96:99]
  ds_read_b64_tr_b16 a[242:243], v20 offset:20896
  buffer_load_dwordx4 v1, s[52:55], s68 offen offset:0 lds
  v_mfma_f32_16x16x32_bf16 v[96:99], a[228:231], a[88:91], v[96:99]
  v_lshlrev_b32 v252, 4, v0
  v_mfma_f32_16x16x32_bf16 v[128:131], a[224:227], a[76:79], v[128:131]
  ds_read_b64_tr_b16 a[244:245], v20 offset:25024
  v_mfma_f32_16x16x32_bf16 v[128:131], a[228:231], a[92:95], v[128:131]
  buffer_load_dwordx4 v[248:251], v252, s[4:7], s3 offen
  v_mfma_f32_16x16x32_bf16 v[160:163], a[224:227], a[64:67], v[160:163]
  ds_read_b64_tr_b16 a[246:247], v20 offset:29152
  v_mfma_f32_16x16x32_bf16 v[160:163], a[228:231], a[80:83], v[160:163]
  s_waitcnt lgkmcnt(4)
  buffer_load_dwordx4 a[192:195], v15, s[64:67], s70 offen sc1
  v_mfma_f32_16x16x32_bf16 v[68:71], a[232:235], a[68:71], v[68:71]
  ds_read_b64_tr_b16 a[248:249], v20 offset:16776
  v_mfma_f32_16x16x32_bf16 v[68:71], a[236:239], a[84:87], v[68:71]
  v_mfma_f32_16x16x32_bf16 v[100:103], a[232:235], a[72:75], v[100:103]
  ds_read_b64_tr_b16 a[250:251], v20 offset:20904
  v_mfma_f32_16x16x32_bf16 v[100:103], a[236:239], a[88:91], v[100:103]
  v_mfma_f32_16x16x32_bf16 v[132:135], a[232:235], a[76:79], v[132:135]
  ds_read_b64_tr_b16 a[252:253], v20 offset:25032
  buffer_load_dwordx4 a[196:199], v16, s[64:67], s70 offen sc1
  v_mfma_f32_16x16x32_bf16 v[132:135], a[236:239], a[92:95], v[132:135]
  v_mfma_f32_16x16x32_bf16 v[164:167], a[232:235], a[64:67], v[164:167]
  ds_read_b64_tr_b16 a[254:255], v20 offset:29160
  v_mfma_f32_16x16x32_bf16 v[164:167], a[236:239], a[80:83], v[164:167]
  s_waitcnt lgkmcnt(4)
  v_mfma_f32_16x16x32_bf16 v[72:75], a[240:243], a[68:71], v[72:75]
  ds_read_b64_tr_b16 a[224:225], v20 offset:17024
  v_mfma_f32_16x16x32_bf16 v[72:75], a[244:247], a[84:87], v[72:75]
  buffer_load_dwordx4 v2, s[52:55], s68 offen offset:1032 lds
  v_mfma_f32_16x16x32_bf16 v[104:107], a[240:243], a[72:75], v[104:107]
  ds_read_b64_tr_b16 a[226:227], v20 offset:21152
  v_mfma_f32_16x16x32_bf16 v[104:107], a[244:247], a[88:91], v[104:107]
  v_mfma_f32_16x16x32_bf16 v[136:139], a[240:243], a[76:79], v[136:139]
  ds_read_b64_tr_b16 a[228:229], v20 offset:25280
  v_mfma_f32_16x16x32_bf16 v[136:139], a[244:247], a[92:95], v[136:139]
  v_mfma_f32_16x16x32_bf16 v[168:171], a[240:243], a[64:67], v[168:171]
  ds_read_b64_tr_b16 a[230:231], v20 offset:29408
  buffer_load_dwordx4 a[200:203], v17, s[64:67], s70 offen sc1
  v_mfma_f32_16x16x32_bf16 v[168:171], a[244:247], a[80:83], v[168:171]
  s_waitcnt lgkmcnt(4)
  v_mfma_f32_16x16x32_bf16 v[76:79], a[248:251], a[68:71], v[76:79]
  ds_read_b64_tr_b16 a[232:233], v20 offset:17032
  v_mfma_f32_16x16x32_bf16 v[76:79], a[252:255], a[84:87], v[76:79]
  v_mfma_f32_16x16x32_bf16 v[108:111], a[248:251], a[72:75], v[108:111]
  ds_read_b64_tr_b16 a[234:235], v20 offset:21160
  v_mfma_f32_16x16x32_bf16 v[108:111], a[252:255], a[88:91], v[108:111]
  buffer_load_dwordx4 a[204:207], v18, s[64:67], s70 offen sc1
  v_mfma_f32_16x16x32_bf16 v[140:143], a[248:251], a[76:79], v[140:143]
  ds_read_b64_tr_b16 a[236:237], v20 offset:25288
  v_mfma_f32_16x16x32_bf16 v[140:143], a[252:255], a[92:95], v[140:143]
  v_mfma_f32_16x16x32_bf16 v[172:175], a[248:251], a[64:67], v[172:175]
  ds_read_b64_tr_b16 a[238:239], v20 offset:29416
  v_mfma_f32_16x16x32_bf16 v[172:175], a[252:255], a[80:83], v[172:175]
  s_waitcnt lgkmcnt(4)
  v_mfma_f32_16x16x32_bf16 v[80:83], a[224:227], a[68:71], v[80:83]
  ds_read_b64_tr_b16 a[240:241], v20 offset:17280
  buffer_load_dwordx4 v3, s[52:55], s68 offen offset:2064 lds
  v_mfma_f32_16x16x32_bf16 v[80:83], a[228:231], a[84:87], v[80:83]
  v_mfma_f32_16x16x32_bf16 v[112:115], a[224:227], a[72:75], v[112:115]
  ds_read_b64_tr_b16 a[242:243], v20 offset:21408
  v_mfma_f32_16x16x32_bf16 v[112:115], a[228:231], a[88:91], v[112:115]
  v_mfma_f32_16x16x32_bf16 v[144:147], a[224:227], a[76:79], v[144:147]
  ds_read_b64_tr_b16 a[244:245], v20 offset:25536
  v_mfma_f32_16x16x32_bf16 v[144:147], a[228:231], a[92:95], v[144:147]
  buffer_load_dwordx4 a[208:211], v15, s[64:67], s71 offen sc1
  v_mfma_f32_16x16x32_bf16 v[176:179], a[224:227], a[64:67], v[176:179]
  ds_read_b64_tr_b16 a[246:247], v20 offset:29664
  v_mfma_f32_16x16x32_bf16 v[176:179], a[228:231], a[80:83], v[176:179]
  s_waitcnt lgkmcnt(4)
  v_mfma_f32_16x16x32_bf16 v[84:87], a[232:235], a[68:71], v[84:87]
  ds_read_b64_tr_b16 a[248:249], v20 offset:17288
  v_mfma_f32_16x16x32_bf16 v[84:87], a[236:239], a[84:87], v[84:87]
  v_mfma_f32_16x16x32_bf16 v[116:119], a[232:235], a[72:75], v[116:119]
  ds_read_b64_tr_b16 a[250:251], v20 offset:21416
  buffer_load_dwordx4 a[212:215], v16, s[64:67], s71 offen sc1
  v_mfma_f32_16x16x32_bf16 v[116:119], a[236:239], a[88:91], v[116:119]
  v_mfma_f32_16x16x32_bf16 v[148:151], a[232:235], a[76:79], v[148:151]
  ds_read_b64_tr_b16 a[252:253], v20 offset:25544
  v_mfma_f32_16x16x32_bf16 v[148:151], a[236:239], a[92:95], v[148:151]
  v_mfma_f32_16x16x32_bf16 v[180:183], a[232:235], a[64:67], v[180:183]
  ds_read_b64_tr_b16 a[254:255], v20 offset:29672
  v_mfma_f32_16x16x32_bf16 v[180:183], a[236:239], a[80:83], v[180:183]
  s_waitcnt lgkmcnt(4)
  s_waitcnt vmcnt(22)
  s_barrier
  buffer_load_dwordx4 v4, s[52:55], s68 offen offset:3096 lds
  v_mfma_f32_16x16x32_bf16 v[88:91], a[240:243], a[68:71], v[88:91]
  ds_read_b64_tr_b16 a[224:225], v20 offset:33024
  s_mov_b64 exec, 1
  buffer_store_dword v11, v30, s[44:47], 0 offen offset:0 sc1
  s_mov_b64 exec, -1
  v_mfma_f32_16x16x32_bf16 v[88:91], a[244:247], a[84:87], v[88:91]
  v_mfma_f32_16x16x32_bf16 v[120:123], a[240:243], a[72:75], v[120:123]
  ds_read_b64_tr_b16 a[226:227], v20 offset:37152
  v_mfma_f32_16x16x32_bf16 v[120:123], a[244:247], a[88:91], v[120:123]
  v_mfma_f32_16x16x32_bf16 v[152:155], a[240:243], a[76:79], v[152:155]
  ds_read_b64_tr_b16 a[228:229], v20 offset:41280
  buffer_load_dwordx4 a[216:219], v17, s[64:67], s71 offen sc1
  v_mfma_f32_16x16x32_bf16 v[152:155], a[244:247], a[92:95], v[152:155]
  v_mfma_f32_16x16x32_bf16 v[184:187], a[240:243], a[64:67], v[184:187]
  ds_read_b64_tr_b16 a[230:231], v20 offset:45408
  v_mfma_f32_16x16x32_bf16 v[184:187], a[244:247], a[80:83], v[184:187]
  s_waitcnt lgkmcnt(4)
  v_mfma_f32_16x16x32_bf16 v[92:95], a[248:251], a[68:71], v[92:95]
  ds_read_b64_tr_b16 a[232:233], v20 offset:33032
  v_mfma_f32_16x16x32_bf16 v[92:95], a[252:255], a[84:87], v[92:95]
  buffer_load_dwordx4 a[220:223], v18, s[64:67], s71 offen sc1
  v_mfma_f32_16x16x32_bf16 v[124:127], a[248:251], a[72:75], v[124:127]
  ds_read_b64_tr_b16 a[234:235], v20 offset:37160
  v_mfma_f32_16x16x32_bf16 v[124:127], a[252:255], a[88:91], v[124:127]
  v_mfma_f32_16x16x32_bf16 v[156:159], a[248:251], a[76:79], v[156:159]
  ds_read_b64_tr_b16 a[236:237], v20 offset:41288
  v_mfma_f32_16x16x32_bf16 v[156:159], a[252:255], a[92:95], v[156:159]
  v_mfma_f32_16x16x32_bf16 v[188:191], a[248:251], a[64:67], v[188:191]
  ds_read_b64_tr_b16 a[238:239], v20 offset:45416
  v_mfma_f32_16x16x32_bf16 v[188:191], a[252:255], a[80:83], v[188:191]
  s_waitcnt lgkmcnt(4)
  v_mfma_f32_16x16x32_bf16 v[64:67], a[224:227], a[100:103], v[64:67]
  s_add_u32 s73, s90, 8
  s_add_u32 s73, s73, s87
  s_lshl_b32 s68, s73, 17
  ds_read_b64_tr_b16 a[240:241], v20 offset:33280
  v_mfma_f32_16x16x32_bf16 v[64:67], a[228:231], a[116:119], v[64:67]
  s_lshl_b32 s70, s73, 13
  s_add_u32 s71, s70, 4096
  s_add_u32 m0, s74, 49536
  v_mfma_f32_16x16x32_bf16 v[96:99], a[224:227], a[104:107], v[96:99]
  ds_read_b64_tr_b16 a[242:243], v20 offset:37408
  buffer_load_dwordx4 v1, s[52:55], s68 offen offset:0 lds
  v_mfma_f32_16x16x32_bf16 v[96:99], a[228:231], a[120:123], v[96:99]
  v_mfma_f32_16x16x32_bf16 v[128:131], a[224:227], a[108:111], v[128:131]
  ds_read_b64_tr_b16 a[244:245], v20 offset:41536
  v_mfma_f32_16x16x32_bf16 v[128:131], a[228:231], a[124:127], v[128:131]
  v_mfma_f32_16x16x32_bf16 v[160:163], a[224:227], a[96:99], v[160:163]
  ds_read_b64_tr_b16 a[246:247], v20 offset:45664
  v_mfma_f32_16x16x32_bf16 v[160:163], a[228:231], a[112:115], v[160:163]
  s_waitcnt lgkmcnt(4)
  buffer_load_dwordx4 a[0:3], v15, s[64:67], s70 offen sc1
  v_mfma_f32_16x16x32_bf16 v[68:71], a[232:235], a[100:103], v[68:71]
  ds_read_b64_tr_b16 a[248:249], v20 offset:33288
  v_mfma_f32_16x16x32_bf16 v[68:71], a[236:239], a[116:119], v[68:71]
  v_mfma_f32_16x16x32_bf16 v[100:103], a[232:235], a[104:107], v[100:103]
  ds_read_b64_tr_b16 a[250:251], v20 offset:37416
  v_mfma_f32_16x16x32_bf16 v[100:103], a[236:239], a[120:123], v[100:103]
  v_mfma_f32_16x16x32_bf16 v[132:135], a[232:235], a[108:111], v[132:135]
  ds_read_b64_tr_b16 a[252:253], v20 offset:41544
  buffer_load_dwordx4 a[4:7], v16, s[64:67], s70 offen sc1
  v_mfma_f32_16x16x32_bf16 v[132:135], a[236:239], a[124:127], v[132:135]
  v_mfma_f32_16x16x32_bf16 v[164:167], a[232:235], a[96:99], v[164:167]
  ds_read_b64_tr_b16 a[254:255], v20 offset:45672
  v_mfma_f32_16x16x32_bf16 v[164:167], a[236:239], a[112:115], v[164:167]
  s_waitcnt lgkmcnt(4)
  v_mfma_f32_16x16x32_bf16 v[72:75], a[240:243], a[100:103], v[72:75]
  ds_read_b64_tr_b16 a[224:225], v20 offset:33536
  v_mfma_f32_16x16x32_bf16 v[72:75], a[244:247], a[116:119], v[72:75]
  buffer_load_dwordx4 v2, s[52:55], s68 offen offset:1032 lds
  v_mfma_f32_16x16x32_bf16 v[104:107], a[240:243], a[104:107], v[104:107]
  ds_read_b64_tr_b16 a[226:227], v20 offset:37664
  v_mfma_f32_16x16x32_bf16 v[104:107], a[244:247], a[120:123], v[104:107]
  v_mfma_f32_16x16x32_bf16 v[136:139], a[240:243], a[108:111], v[136:139]
  ds_read_b64_tr_b16 a[228:229], v20 offset:41792
  v_mfma_f32_16x16x32_bf16 v[136:139], a[244:247], a[124:127], v[136:139]
  v_mfma_f32_16x16x32_bf16 v[168:171], a[240:243], a[96:99], v[168:171]
  ds_read_b64_tr_b16 a[230:231], v20 offset:45920
  buffer_load_dwordx4 a[8:11], v17, s[64:67], s70 offen sc1
  v_mfma_f32_16x16x32_bf16 v[168:171], a[244:247], a[112:115], v[168:171]
  s_waitcnt lgkmcnt(4)
  v_mfma_f32_16x16x32_bf16 v[76:79], a[248:251], a[100:103], v[76:79]
  ds_read_b64_tr_b16 a[232:233], v20 offset:33544
  v_mfma_f32_16x16x32_bf16 v[76:79], a[252:255], a[116:119], v[76:79]
  v_mfma_f32_16x16x32_bf16 v[108:111], a[248:251], a[104:107], v[108:111]
  ds_read_b64_tr_b16 a[234:235], v20 offset:37672
  v_mfma_f32_16x16x32_bf16 v[108:111], a[252:255], a[120:123], v[108:111]
  buffer_load_dwordx4 a[12:15], v18, s[64:67], s70 offen sc1
  v_mfma_f32_16x16x32_bf16 v[140:143], a[248:251], a[108:111], v[140:143]
  ds_read_b64_tr_b16 a[236:237], v20 offset:41800
  v_mfma_f32_16x16x32_bf16 v[140:143], a[252:255], a[124:127], v[140:143]
  v_mfma_f32_16x16x32_bf16 v[172:175], a[248:251], a[96:99], v[172:175]
  ds_read_b64_tr_b16 a[238:239], v20 offset:45928
  v_mfma_f32_16x16x32_bf16 v[172:175], a[252:255], a[112:115], v[172:175]
  s_waitcnt lgkmcnt(4)
  v_mfma_f32_16x16x32_bf16 v[80:83], a[224:227], a[100:103], v[80:83]
  ds_read_b64_tr_b16 a[240:241], v20 offset:33792
  buffer_load_dwordx4 v3, s[52:55], s68 offen offset:2064 lds
  v_mfma_f32_16x16x32_bf16 v[80:83], a[228:231], a[116:119], v[80:83]
  v_mfma_f32_16x16x32_bf16 v[112:115], a[224:227], a[104:107], v[112:115]
  ds_read_b64_tr_b16 a[242:243], v20 offset:37920
  v_mfma_f32_16x16x32_bf16 v[112:115], a[228:231], a[120:123], v[112:115]
  v_mfma_f32_16x16x32_bf16 v[144:147], a[224:227], a[108:111], v[144:147]
  ds_read_b64_tr_b16 a[244:245], v20 offset:42048
  v_mfma_f32_16x16x32_bf16 v[144:147], a[228:231], a[124:127], v[144:147]
  buffer_load_dwordx4 a[16:19], v15, s[64:67], s71 offen sc1
  v_mfma_f32_16x16x32_bf16 v[176:179], a[224:227], a[96:99], v[176:179]
  ds_read_b64_tr_b16 a[246:247], v20 offset:46176
  v_mfma_f32_16x16x32_bf16 v[176:179], a[228:231], a[112:115], v[176:179]
  s_waitcnt lgkmcnt(4)
  v_mfma_f32_16x16x32_bf16 v[84:87], a[232:235], a[100:103], v[84:87]
  ds_read_b64_tr_b16 a[248:249], v20 offset:33800
  v_mfma_f32_16x16x32_bf16 v[84:87], a[236:239], a[116:119], v[84:87]
  v_mfma_f32_16x16x32_bf16 v[116:119], a[232:235], a[104:107], v[116:119]
  ds_read_b64_tr_b16 a[250:251], v20 offset:37928
  buffer_load_dwordx4 a[20:23], v16, s[64:67], s71 offen sc1
  v_mfma_f32_16x16x32_bf16 v[116:119], a[236:239], a[120:123], v[116:119]
  v_mfma_f32_16x16x32_bf16 v[148:151], a[232:235], a[108:111], v[148:151]
  ds_read_b64_tr_b16 a[252:253], v20 offset:42056
  v_mfma_f32_16x16x32_bf16 v[148:151], a[236:239], a[124:127], v[148:151]
  v_mfma_f32_16x16x32_bf16 v[180:183], a[232:235], a[96:99], v[180:183]
  ds_read_b64_tr_b16 a[254:255], v20 offset:46184
  v_mfma_f32_16x16x32_bf16 v[180:183], a[236:239], a[112:115], v[180:183]
  s_waitcnt lgkmcnt(4)
  s_waitcnt vmcnt(36)
  s_barrier
  buffer_load_dwordx4 v4, s[52:55], s68 offen offset:3096 lds
  v_mfma_f32_16x16x32_bf16 v[88:91], a[240:243], a[100:103], v[88:91]
  ds_read_b64_tr_b16 a[224:225], v19 offset:0
  v_mfma_f32_16x16x32_bf16 v[88:91], a[244:247], a[116:119], v[88:91]
  v_mfma_f32_16x16x32_bf16 v[120:123], a[240:243], a[104:107], v[120:123]
  ds_read_b64_tr_b16 a[226:227], v19 offset:4128
  v_mfma_f32_16x16x32_bf16 v[120:123], a[244:247], a[120:123], v[120:123]
  v_mfma_f32_16x16x32_bf16 v[152:155], a[240:243], a[108:111], v[152:155]
  ds_read_b64_tr_b16 a[228:229], v19 offset:8256
  buffer_load_dwordx4 a[24:27], v17, s[64:67], s71 offen sc1
  v_mfma_f32_16x16x32_bf16 v[152:155], a[244:247], a[124:127], v[152:155]
  v_mfma_f32_16x16x32_bf16 v[184:187], a[240:243], a[96:99], v[184:187]
  ds_read_b64_tr_b16 a[230:231], v19 offset:12384
  v_mfma_f32_16x16x32_bf16 v[184:187], a[244:247], a[112:115], v[184:187]
  s_waitcnt lgkmcnt(4)
  v_mfma_f32_16x16x32_bf16 v[92:95], a[248:251], a[100:103], v[92:95]
  ds_read_b64_tr_b16 a[232:233], v19 offset:8
  v_mfma_f32_16x16x32_bf16 v[92:95], a[252:255], a[116:119], v[92:95]
  buffer_load_dwordx4 a[28:31], v18, s[64:67], s71 offen sc1
  v_mfma_f32_16x16x32_bf16 v[124:127], a[248:251], a[104:107], v[124:127]
  ds_read_b64_tr_b16 a[234:235], v19 offset:4136
  v_mfma_f32_16x16x32_bf16 v[124:127], a[252:255], a[120:123], v[124:127]
  v_mfma_f32_16x16x32_bf16 v[156:159], a[248:251], a[108:111], v[156:159]
  ds_read_b64_tr_b16 a[236:237], v19 offset:8264
  v_mfma_f32_16x16x32_bf16 v[156:159], a[252:255], a[124:127], v[156:159]
  v_mfma_f32_16x16x32_bf16 v[188:191], a[248:251], a[96:99], v[188:191]
  ds_read_b64_tr_b16 a[238:239], v19 offset:12392
  v_mfma_f32_16x16x32_bf16 v[188:191], a[252:255], a[112:115], v[188:191]
  s_waitcnt lgkmcnt(4)
  v_mfma_f32_16x16x32_bf16 v[64:67], a[224:227], a[132:135], v[64:67]
  s_add_u32 s73, s91, 8
  s_add_u32 s73, s73, s87
  s_lshl_b32 s68, s73, 17
  ds_read_b64_tr_b16 a[240:241], v19 offset:256
  v_mfma_f32_16x16x32_bf16 v[64:67], a[228:231], a[148:151], v[64:67]
  s_lshl_b32 s70, s73, 13
  s_add_u32 s71, s70, 4096
  s_add_u32 m0, s74, 66048
  v_mfma_f32_16x16x32_bf16 v[96:99], a[224:227], a[136:139], v[96:99]
  ds_read_b64_tr_b16 a[242:243], v19 offset:4384
  buffer_load_dwordx4 v1, s[52:55], s68 offen offset:0 lds
  v_mfma_f32_16x16x32_bf16 v[96:99], a[228:231], a[152:155], v[96:99]
  v_mfma_f32_16x16x32_bf16 v[128:131], a[224:227], a[140:143], v[128:131]
  ds_read_b64_tr_b16 a[244:245], v19 offset:8512
  v_mfma_f32_16x16x32_bf16 v[128:131], a[228:231], a[156:159], v[128:131]
  v_mfma_f32_16x16x32_bf16 v[160:163], a[224:227], a[128:131], v[160:163]
  ds_read_b64_tr_b16 a[246:247], v19 offset:12640
  buffer_load_dwordx4 a[32:35], v15, s[64:67], s70 offen sc1
  v_mfma_f32_16x16x32_bf16 v[160:163], a[228:231], a[144:147], v[160:163]
  s_waitcnt lgkmcnt(4)
  v_mfma_f32_16x16x32_bf16 v[68:71], a[232:235], a[132:135], v[68:71]
  ds_read_b64_tr_b16 a[248:249], v19 offset:264
  v_mfma_f32_16x16x32_bf16 v[68:71], a[236:239], a[148:151], v[68:71]
  v_mfma_f32_16x16x32_bf16 v[100:103], a[232:235], a[136:139], v[100:103]
  ds_read_b64_tr_b16 a[250:251], v19 offset:4392
  v_mfma_f32_16x16x32_bf16 v[100:103], a[236:239], a[152:155], v[100:103]
  buffer_load_dwordx4 a[36:39], v16, s[64:67], s70 offen sc1
  v_mfma_f32_16x16x32_bf16 v[132:135], a[232:235], a[140:143], v[132:135]
  ds_read_b64_tr_b16 a[252:253], v19 offset:8520
  v_mfma_f32_16x16x32_bf16 v[132:135], a[236:239], a[156:159], v[132:135]
  v_mfma_f32_16x16x32_bf16 v[164:167], a[232:235], a[128:131], v[164:167]
  ds_read_b64_tr_b16 a[254:255], v19 offset:12648
  v_mfma_f32_16x16x32_bf16 v[164:167], a[236:239], a[144:147], v[164:167]
  s_waitcnt lgkmcnt(4)
  v_mfma_f32_16x16x32_bf16 v[72:75], a[240:243], a[132:135], v[72:75]
  ds_read_b64_tr_b16 a[224:225], v19 offset:512
  buffer_load_dwordx4 v2, s[52:55], s68 offen offset:1032 lds
  v_mfma_f32_16x16x32_bf16 v[72:75], a[244:247], a[148:151], v[72:75]
  v_mfma_f32_16x16x32_bf16 v[104:107], a[240:243], a[136:139], v[104:107]
  ds_read_b64_tr_b16 a[226:227], v19 offset:4640
  v_mfma_f32_16x16x32_bf16 v[104:107], a[244:247], a[152:155], v[104:107]
  v_mfma_f32_16x16x32_bf16 v[136:139], a[240:243], a[140:143], v[136:139]
  ds_read_b64_tr_b16 a[228:229], v19 offset:8768
  buffer_load_dwordx4 a[40:43], v17, s[64:67], s70 offen sc1
  v_mfma_f32_16x16x32_bf16 v[136:139], a[244:247], a[156:159], v[136:139]
  v_mfma_f32_16x16x32_bf16 v[168:171], a[240:243], a[128:131], v[168:171]
  ds_read_b64_tr_b16 a[230:231], v19 offset:12896
  v_mfma_f32_16x16x32_bf16 v[168:171], a[244:247], a[144:147], v[168:171]
  s_waitcnt lgkmcnt(4)
  v_mfma_f32_16x16x32_bf16 v[76:79], a[248:251], a[132:135], v[76:79]
  ds_read_b64_tr_b16 a[232:233], v19 offset:520
  v_mfma_f32_16x16x32_bf16 v[76:79], a[252:255], a[148:151], v[76:79]
  buffer_load_dwordx4 a[44:47], v18, s[64:67], s70 offen sc1
  v_mfma_f32_16x16x32_bf16 v[108:111], a[248:251], a[136:139], v[108:111]
  ds_read_b64_tr_b16 a[234:235], v19 offset:4648
  v_mfma_f32_16x16x32_bf16 v[108:111], a[252:255], a[152:155], v[108:111]
  v_mfma_f32_16x16x32_bf16 v[140:143], a[248:251], a[140:143], v[140:143]
  ds_read_b64_tr_b16 a[236:237], v19 offset:8776
  v_mfma_f32_16x16x32_bf16 v[140:143], a[252:255], a[156:159], v[140:143]
  v_mfma_f32_16x16x32_bf16 v[172:175], a[248:251], a[128:131], v[172:175]
  ds_read_b64_tr_b16 a[238:239], v19 offset:12904
  buffer_load_dwordx4 v3, s[52:55], s68 offen offset:2064 lds
  v_mfma_f32_16x16x32_bf16 v[172:175], a[252:255], a[144:147], v[172:175]
  s_waitcnt lgkmcnt(4)
  v_mfma_f32_16x16x32_bf16 v[80:83], a[224:227], a[132:135], v[80:83]
  ds_read_b64_tr_b16 a[240:241], v19 offset:768
  v_mfma_f32_16x16x32_bf16 v[80:83], a[228:231], a[148:151], v[80:83]
  v_mfma_f32_16x16x32_bf16 v[112:115], a[224:227], a[136:139], v[112:115]
  ds_read_b64_tr_b16 a[242:243], v19 offset:4896
  buffer_load_dwordx4 a[48:51], v15, s[64:67], s71 offen sc1
  v_mfma_f32_16x16x32_bf16 v[112:115], a[228:231], a[152:155], v[112:115]
  v_mfma_f32_16x16x32_bf16 v[144:147], a[224:227], a[140:143], v[144:147]
  ds_read_b64_tr_b16 a[244:245], v19 offset:9024
  v_mfma_f32_16x16x32_bf16 v[144:147], a[228:231], a[156:159], v[144:147]
  v_mfma_f32_16x16x32_bf16 v[176:179], a[224:227], a[128:131], v[176:179]
  ds_read_b64_tr_b16 a[246:247], v19 offset:13152
  v_mfma_f32_16x16x32_bf16 v[176:179], a[228:231], a[144:147], v[176:179]
  s_waitcnt lgkmcnt(4)
  buffer_load_dwordx4 a[52:55], v16, s[64:67], s71 offen sc1
  v_mfma_f32_16x16x32_bf16 v[84:87], a[232:235], a[132:135], v[84:87]
  ds_read_b64_tr_b16 a[248:249], v19 offset:776
  v_mfma_f32_16x16x32_bf16 v[84:87], a[236:239], a[148:151], v[84:87]
  v_mfma_f32_16x16x32_bf16 v[116:119], a[232:235], a[136:139], v[116:119]
  ds_read_b64_tr_b16 a[250:251], v19 offset:4904
  v_mfma_f32_16x16x32_bf16 v[116:119], a[236:239], a[152:155], v[116:119]
  v_mfma_f32_16x16x32_bf16 v[148:151], a[232:235], a[140:143], v[148:151]
  ds_read_b64_tr_b16 a[252:253], v19 offset:9032
  buffer_load_dwordx4 v4, s[52:55], s68 offen offset:3096 lds
  v_mfma_f32_16x16x32_bf16 v[148:151], a[236:239], a[156:159], v[148:151]
  v_mfma_f32_16x16x32_bf16 v[180:183], a[232:235], a[128:131], v[180:183]
  ds_read_b64_tr_b16 a[254:255], v19 offset:13160
  v_mfma_f32_16x16x32_bf16 v[180:183], a[236:239], a[144:147], v[180:183]
  s_waitcnt lgkmcnt(4)
  s_waitcnt vmcnt(36)
  s_barrier
  v_mfma_f32_16x16x32_bf16 v[88:91], a[240:243], a[132:135], v[88:91]
  ds_read_b64_tr_b16 a[224:225], v19 offset:16512
  buffer_load_dwordx4 a[56:59], v17, s[64:67], s71 offen sc1
  v_mfma_f32_16x16x32_bf16 v[88:91], a[244:247], a[148:151], v[88:91]
  v_mfma_f32_16x16x32_bf16 v[120:123], a[240:243], a[136:139], v[120:123]
  ds_read_b64_tr_b16 a[226:227], v19 offset:20640
  v_mfma_f32_16x16x32_bf16 v[120:123], a[244:247], a[152:155], v[120:123]
  v_mfma_f32_16x16x32_bf16 v[152:155], a[240:243], a[140:143], v[152:155]
  ds_read_b64_tr_b16 a[228:229], v19 offset:24768
  v_mfma_f32_16x16x32_bf16 v[152:155], a[244:247], a[156:159], v[152:155]
  buffer_load_dwordx4 a[60:63], v18, s[64:67], s71 offen sc1
  v_mfma_f32_16x16x32_bf16 v[184:187], a[240:243], a[128:131], v[184:187]
  ds_read_b64_tr_b16 a[230:231], v19 offset:28896
  v_mfma_f32_16x16x32_bf16 v[184:187], a[244:247], a[144:147], v[184:187]
  s_waitcnt lgkmcnt(4)
  v_mfma_f32_16x16x32_bf16 v[92:95], a[248:251], a[132:135], v[92:95]
  ds_read_b64_tr_b16 a[232:233], v19 offset:16520
  v_mfma_f32_16x16x32_bf16 v[92:95], a[252:255], a[148:151], v[92:95]
  v_mfma_f32_16x16x32_bf16 v[124:127], a[248:251], a[136:139], v[124:127]
  ds_read_b64_tr_b16 a[234:235], v19 offset:20648
  buffer_load_dword v10, v13, s[44:47], 0 offen offset:0 sc1
  v_mfma_f32_16x16x32_bf16 v[124:127], a[252:255], a[152:155], v[124:127]
  v_mfma_f32_16x16x32_bf16 v[156:159], a[248:251], a[140:143], v[156:159]
  ds_read_b64_tr_b16 a[236:237], v19 offset:24776
  v_mfma_f32_16x16x32_bf16 v[156:159], a[252:255], a[156:159], v[156:159]
  v_mfma_f32_16x16x32_bf16 v[188:191], a[248:251], a[128:131], v[188:191]
  ds_read_b64_tr_b16 a[238:239], v19 offset:28904
  v_mfma_f32_16x16x32_bf16 v[188:191], a[252:255], a[144:147], v[188:191]
  s_waitcnt lgkmcnt(4)
  v_mfma_f32_16x16x32_bf16 v[64:67], a[224:227], a[164:167], v[64:67]
  s_add_u32 s73, s92, 12
  s_add_u32 s73, s73, s87
  s_lshl_b32 s68, s73, 17
  ds_read_b64_tr_b16 a[240:241], v19 offset:16768
  v_mfma_f32_16x16x32_bf16 v[64:67], a[228:231], a[180:183], v[64:67]
  s_lshl_b32 s70, s73, 13
  s_add_u32 s71, s70, 4096
  s_add_u32 m0, s74, 82560
  v_mfma_f32_16x16x32_bf16 v[96:99], a[224:227], a[168:171], v[96:99]
  ds_read_b64_tr_b16 a[242:243], v19 offset:20896
  buffer_load_dwordx4 v1, s[52:55], s68 offen offset:0 lds
  v_mfma_f32_16x16x32_bf16 v[96:99], a[228:231], a[184:187], v[96:99]
  v_mfma_f32_16x16x32_bf16 v[128:131], a[224:227], a[172:175], v[128:131]
  ds_read_b64_tr_b16 a[244:245], v19 offset:25024
  v_mfma_f32_16x16x32_bf16 v[128:131], a[228:231], a[188:191], v[128:131]
  v_mfma_f32_16x16x32_bf16 v[160:163], a[224:227], a[160:163], v[160:163]
  ds_read_b64_tr_b16 a[246:247], v19 offset:29152
  v_mfma_f32_16x16x32_bf16 v[160:163], a[228:231], a[176:179], v[160:163]
  s_waitcnt lgkmcnt(4)
  buffer_load_dwordx4 a[64:67], v15, s[64:67], s70 offen sc1
  v_mfma_f32_16x16x32_bf16 v[68:71], a[232:235], a[164:167], v[68:71]
  ds_read_b64_tr_b16 a[248:249], v19 offset:16776
  v_mfma_f32_16x16x32_bf16 v[68:71], a[236:239], a[180:183], v[68:71]
  v_mfma_f32_16x16x32_bf16 v[100:103], a[232:235], a[168:171], v[100:103]
  ds_read_b64_tr_b16 a[250:251], v19 offset:20904
  v_mfma_f32_16x16x32_bf16 v[100:103], a[236:239], a[184:187], v[100:103]
  v_mfma_f32_16x16x32_bf16 v[132:135], a[232:235], a[172:175], v[132:135]
  ds_read_b64_tr_b16 a[252:253], v19 offset:25032
  buffer_load_dwordx4 a[68:71], v16, s[64:67], s70 offen sc1
  v_mfma_f32_16x16x32_bf16 v[132:135], a[236:239], a[188:191], v[132:135]
  v_mfma_f32_16x16x32_bf16 v[164:167], a[232:235], a[160:163], v[164:167]
  ds_read_b64_tr_b16 a[254:255], v19 offset:29160
  v_mfma_f32_16x16x32_bf16 v[164:167], a[236:239], a[176:179], v[164:167]
  s_waitcnt lgkmcnt(4)
  v_mfma_f32_16x16x32_bf16 v[72:75], a[240:243], a[164:167], v[72:75]
  ds_read_b64_tr_b16 a[224:225], v19 offset:17024
  v_mfma_f32_16x16x32_bf16 v[72:75], a[244:247], a[180:183], v[72:75]
  buffer_load_dwordx4 v2, s[52:55], s68 offen offset:1032 lds
  v_mfma_f32_16x16x32_bf16 v[104:107], a[240:243], a[168:171], v[104:107]
  ds_read_b64_tr_b16 a[226:227], v19 offset:21152
  v_mfma_f32_16x16x32_bf16 v[104:107], a[244:247], a[184:187], v[104:107]
  v_mfma_f32_16x16x32_bf16 v[136:139], a[240:243], a[172:175], v[136:139]
  ds_read_b64_tr_b16 a[228:229], v19 offset:25280
  v_mfma_f32_16x16x32_bf16 v[136:139], a[244:247], a[188:191], v[136:139]
  v_mfma_f32_16x16x32_bf16 v[168:171], a[240:243], a[160:163], v[168:171]
  ds_read_b64_tr_b16 a[230:231], v19 offset:29408
  buffer_load_dwordx4 a[72:75], v17, s[64:67], s70 offen sc1
  v_mfma_f32_16x16x32_bf16 v[168:171], a[244:247], a[176:179], v[168:171]
  s_waitcnt lgkmcnt(4)
  v_mfma_f32_16x16x32_bf16 v[76:79], a[248:251], a[164:167], v[76:79]
  ds_read_b64_tr_b16 a[232:233], v19 offset:17032
  v_mfma_f32_16x16x32_bf16 v[76:79], a[252:255], a[180:183], v[76:79]
  v_mfma_f32_16x16x32_bf16 v[108:111], a[248:251], a[168:171], v[108:111]
  ds_read_b64_tr_b16 a[234:235], v19 offset:21160
  v_mfma_f32_16x16x32_bf16 v[108:111], a[252:255], a[184:187], v[108:111]
  buffer_load_dwordx4 a[76:79], v18, s[64:67], s70 offen sc1
  v_mfma_f32_16x16x32_bf16 v[140:143], a[248:251], a[172:175], v[140:143]
  ds_read_b64_tr_b16 a[236:237], v19 offset:25288
  v_mfma_f32_16x16x32_bf16 v[140:143], a[252:255], a[188:191], v[140:143]
  v_mfma_f32_16x16x32_bf16 v[172:175], a[248:251], a[160:163], v[172:175]
  ds_read_b64_tr_b16 a[238:239], v19 offset:29416
  v_mfma_f32_16x16x32_bf16 v[172:175], a[252:255], a[176:179], v[172:175]
  s_waitcnt lgkmcnt(4)
  v_mfma_f32_16x16x32_bf16 v[80:83], a[224:227], a[164:167], v[80:83]
  ds_read_b64_tr_b16 a[240:241], v19 offset:17280
  buffer_load_dwordx4 v3, s[52:55], s68 offen offset:2064 lds
  v_mfma_f32_16x16x32_bf16 v[80:83], a[228:231], a[180:183], v[80:83]
  v_mfma_f32_16x16x32_bf16 v[112:115], a[224:227], a[168:171], v[112:115]
  ds_read_b64_tr_b16 a[242:243], v19 offset:21408
  v_mfma_f32_16x16x32_bf16 v[112:115], a[228:231], a[184:187], v[112:115]
  v_mfma_f32_16x16x32_bf16 v[144:147], a[224:227], a[172:175], v[144:147]
  ds_read_b64_tr_b16 a[244:245], v19 offset:25536
  v_mfma_f32_16x16x32_bf16 v[144:147], a[228:231], a[188:191], v[144:147]
  buffer_load_dwordx4 a[80:83], v15, s[64:67], s71 offen sc1
  v_mfma_f32_16x16x32_bf16 v[176:179], a[224:227], a[160:163], v[176:179]
  ds_read_b64_tr_b16 a[246:247], v19 offset:29664
  v_mfma_f32_16x16x32_bf16 v[176:179], a[228:231], a[176:179], v[176:179]
  s_waitcnt lgkmcnt(4)
  v_mfma_f32_16x16x32_bf16 v[84:87], a[232:235], a[164:167], v[84:87]
  ds_read_b64_tr_b16 a[248:249], v19 offset:17288
  v_mfma_f32_16x16x32_bf16 v[84:87], a[236:239], a[180:183], v[84:87]
  v_mfma_f32_16x16x32_bf16 v[116:119], a[232:235], a[168:171], v[116:119]
  ds_read_b64_tr_b16 a[250:251], v19 offset:21416
  buffer_load_dwordx4 a[84:87], v16, s[64:67], s71 offen sc1
  v_mfma_f32_16x16x32_bf16 v[116:119], a[236:239], a[184:187], v[116:119]
  v_mfma_f32_16x16x32_bf16 v[148:151], a[232:235], a[172:175], v[148:151]
  ds_read_b64_tr_b16 a[252:253], v19 offset:25544
  v_mfma_f32_16x16x32_bf16 v[148:151], a[236:239], a[188:191], v[148:151]
  v_mfma_f32_16x16x32_bf16 v[180:183], a[232:235], a[160:163], v[180:183]
  ds_read_b64_tr_b16 a[254:255], v19 offset:29672
  v_mfma_f32_16x16x32_bf16 v[180:183], a[236:239], a[176:179], v[180:183]
  s_waitcnt lgkmcnt(4)
  s_waitcnt vmcnt(9)
  s_barrier
  buffer_load_dwordx4 v4, s[52:55], s68 offen offset:3096 lds
  v_mfma_f32_16x16x32_bf16 v[88:91], a[240:243], a[164:167], v[88:91]
  ds_read_b64_tr_b16 a[224:225], v19 offset:33024
  v_cmp_ne_u32 vcc, 1, v10
  s_nop 1
  s_cbranch_vccnz LC_slowd
LC_backd:
  v_mfma_f32_16x16x32_bf16 v[88:91], a[244:247], a[180:183], v[88:91]
  v_mfma_f32_16x16x32_bf16 v[120:123], a[240:243], a[168:171], v[120:123]
  ds_read_b64_tr_b16 a[226:227], v19 offset:37152
  v_mfma_f32_16x16x32_bf16 v[120:123], a[244:247], a[184:187], v[120:123]
  v_mfma_f32_16x16x32_bf16 v[152:155], a[240:243], a[172:175], v[152:155]
  ds_read_b64_tr_b16 a[228:229], v19 offset:41280
  buffer_load_dwordx4 a[88:91], v17, s[64:67], s71 offen sc1
  v_mfma_f32_16x16x32_bf16 v[152:155], a[244:247], a[188:191], v[152:155]
  v_mfma_f32_16x16x32_bf16 v[184:187], a[240:243], a[160:163], v[184:187]
  ds_read_b64_tr_b16 a[230:231], v19 offset:45408
  v_mfma_f32_16x16x32_bf16 v[184:187], a[244:247], a[176:179], v[184:187]
  s_waitcnt lgkmcnt(4)
  v_mfma_f32_16x16x32_bf16 v[92:95], a[248:251], a[164:167], v[92:95]
  ds_read_b64_tr_b16 a[232:233], v19 offset:33032
  v_mfma_f32_16x16x32_bf16 v[92:95], a[252:255], a[180:183], v[92:95]
  buffer_load_dwordx4 a[92:95], v18, s[64:67], s71 offen sc1
  v_mfma_f32_16x16x32_bf16 v[124:127], a[248:251], a[168:171], v[124:127]
  ds_read_b64_tr_b16 a[234:235], v19 offset:37160
  v_mfma_f32_16x16x32_bf16 v[124:127], a[252:255], a[184:187], v[124:127]
  v_mfma_f32_16x16x32_bf16 v[156:159], a[248:251], a[172:175], v[156:159]
  ds_read_b64_tr_b16 a[236:237], v19 offset:41288
  v_mfma_f32_16x16x32_bf16 v[156:159], a[252:255], a[188:191], v[156:159]
  v_mfma_f32_16x16x32_bf16 v[188:191], a[248:251], a[160:163], v[188:191]
  ds_read_b64_tr_b16 a[238:239], v19 offset:45416
  v_mfma_f32_16x16x32_bf16 v[188:191], a[252:255], a[176:179], v[188:191]
  s_waitcnt lgkmcnt(4)
  v_mfma_f32_16x16x32_bf16 v[64:67], a[224:227], a[196:199], v[64:67]
  s_add_u32 s73, s93, 12
  s_add_u32 s73, s73, s87
  s_lshl_b32 s68, s73, 17
  ds_read_b64_tr_b16 a[240:241], v19 offset:33280
  v_mfma_f32_16x16x32_bf16 v[64:67], a[228:231], a[212:215], v[64:67]
  s_lshl_b32 s70, s73, 13
  s_add_u32 s71, s70, 4096
  s_add_u32 m0, s74, 99072
  v_mfma_f32_16x16x32_bf16 v[96:99], a[224:227], a[200:203], v[96:99]
  ds_read_b64_tr_b16 a[242:243], v19 offset:37408
  buffer_load_dwordx4 v1, s[52:55], s68 offen offset:0 lds
  v_mfma_f32_16x16x32_bf16 v[96:99], a[228:231], a[216:219], v[96:99]
  v_mfma_f32_16x16x32_bf16 v[128:131], a[224:227], a[204:207], v[128:131]
  ds_read_b64_tr_b16 a[244:245], v19 offset:41536
  buffer_load_dwordx4 v[192:195], v27, s[56:59], 0 offen offset:0 sc1
  v_mfma_f32_16x16x32_bf16 v[128:131], a[228:231], a[220:223], v[128:131]
  v_mfma_f32_16x16x32_bf16 v[160:163], a[224:227], a[192:195], v[160:163]
  ds_read_b64_tr_b16 a[246:247], v19 offset:45664
  v_mfma_f32_16x16x32_bf16 v[160:163], a[228:231], a[208:211], v[160:163]
  s_waitcnt lgkmcnt(4)
  buffer_load_dwordx4 a[96:99], v15, s[64:67], s70 offen sc1
  v_mfma_f32_16x16x32_bf16 v[68:71], a[232:235], a[196:199], v[68:71]
  ds_read_b64_tr_b16 a[248:249], v19 offset:33288
  v_mfma_f32_16x16x32_bf16 v[68:71], a[236:239], a[212:215], v[68:71]
  buffer_load_dwordx4 v[196:199], v27, s[56:59], 0 offen offset:1024 sc1
  v_mfma_f32_16x16x32_bf16 v[100:103], a[232:235], a[200:203], v[100:103]
  ds_read_b64_tr_b16 a[250:251], v19 offset:37416
  v_mfma_f32_16x16x32_bf16 v[100:103], a[236:239], a[216:219], v[100:103]
  v_mfma_f32_16x16x32_bf16 v[132:135], a[232:235], a[204:207], v[132:135]
  ds_read_b64_tr_b16 a[252:253], v19 offset:41544
  buffer_load_dwordx4 a[100:103], v16, s[64:67], s70 offen sc1
  v_mfma_f32_16x16x32_bf16 v[132:135], a[236:239], a[220:223], v[132:135]
  v_mfma_f32_16x16x32_bf16 v[164:167], a[232:235], a[192:195], v[164:167]
  ds_read_b64_tr_b16 a[254:255], v19 offset:45672
  buffer_load_dwordx4 v[200:203], v27, s[56:59], 0 offen offset:2048 sc1
  v_mfma_f32_16x16x32_bf16 v[164:167], a[236:239], a[208:211], v[164:167]
  s_waitcnt lgkmcnt(4)
  v_mfma_f32_16x16x32_bf16 v[72:75], a[240:243], a[196:199], v[72:75]
  ds_read_b64_tr_b16 a[224:225], v19 offset:33536
  v_mfma_f32_16x16x32_bf16 v[72:75], a[244:247], a[212:215], v[72:75]
  buffer_load_dwordx4 v2, s[52:55], s68 offen offset:1032 lds
  v_mfma_f32_16x16x32_bf16 v[104:107], a[240:243], a[200:203], v[104:107]
  ds_read_b64_tr_b16 a[226:227], v19 offset:37664
  v_mfma_f32_16x16x32_bf16 v[104:107], a[244:247], a[216:219], v[104:107]
  buffer_load_dwordx4 v[204:207], v27, s[56:59], 0 offen offset:3072 sc1
  v_mfma_f32_16x16x32_bf16 v[136:139], a[240:243], a[204:207], v[136:139]
  ds_read_b64_tr_b16 a[228:229], v19 offset:41792
  v_mfma_f32_16x16x32_bf16 v[136:139], a[244:247], a[220:223], v[136:139]
  v_mfma_f32_16x16x32_bf16 v[168:171], a[240:243], a[192:195], v[168:171]
  ds_read_b64_tr_b16 a[230:231], v19 offset:45920
  buffer_load_dwordx4 a[104:107], v17, s[64:67], s70 offen sc1
  v_mfma_f32_16x16x32_bf16 v[168:171], a[244:247], a[208:211], v[168:171]
  s_waitcnt lgkmcnt(4)
  v_mfma_f32_16x16x32_bf16 v[76:79], a[248:251], a[196:199], v[76:79]
  ds_read_b64_tr_b16 a[232:233], v19 offset:33544
  buffer_load_dwordx4 v[208:211], v28, s[56:59], 0 offen offset:0 sc1
  v_mfma_f32_16x16x32_bf16 v[76:79], a[252:255], a[212:215], v[76:79]
  v_mfma_f32_16x16x32_bf16 v[108:111], a[248:251], a[200:203], v[108:111]
  ds_read_b64_tr_b16 a[234:235], v19 offset:37672
  v_mfma_f32_16x16x32_bf16 v[108:111], a[252:255], a[216:219], v[108:111]
  buffer_load_dwordx4 a[108:111], v18, s[64:67], s70 offen sc1
  v_mfma_f32_16x16x32_bf16 v[140:143], a[248:251], a[204:207], v[140:143]
  ds_read_b64_tr_b16 a[236:237], v19 offset:41800
  v_mfma_f32_16x16x32_bf16 v[140:143], a[252:255], a[220:223], v[140:143]
  buffer_load_dwordx4 v[212:215], v28, s[56:59], 0 offen offset:1024 sc1
  v_mfma_f32_16x16x32_bf16 v[172:175], a[248:251], a[192:195], v[172:175]
  ds_read_b64_tr_b16 a[238:239], v19 offset:45928
  v_mfma_f32_16x16x32_bf16 v[172:175], a[252:255], a[208:211], v[172:175]
  s_waitcnt lgkmcnt(4)
  v_mfma_f32_16x16x32_bf16 v[80:83], a[224:227], a[196:199], v[80:83]
  ds_read_b64_tr_b16 a[240:241], v19 offset:33792
  buffer_load_dwordx4 v3, s[52:55], s68 offen offset:2064 lds
  v_mfma_f32_16x16x32_bf16 v[80:83], a[228:231], a[212:215], v[80:83]
  v_mfma_f32_16x16x32_bf16 v[112:115], a[224:227], a[200:203], v[112:115]
  ds_read_b64_tr_b16 a[242:243], v19 offset:37920
  v_mfma_f32_16x16x32_bf16 v[112:115], a[228:231], a[216:219], v[112:115]
  buffer_load_dwordx4 v[216:219], v28, s[56:59], 0 offen offset:2048 sc1
  v_mfma_f32_16x16x32_bf16 v[144:147], a[224:227], a[204:207], v[144:147]
  ds_read_b64_tr_b16 a[244:245], v19 offset:42048
  v_mfma_f32_16x16x32_bf16 v[144:147], a[228:231], a[220:223], v[144:147]
  buffer_load_dwordx4 a[112:115], v15, s[64:67], s71 offen sc1
  v_mfma_f32_16x16x32_bf16 v[176:179], a[224:227], a[192:195], v[176:179]
  ds_read_b64_tr_b16 a[246:247], v19 offset:46176
  v_mfma_f32_16x16x32_bf16 v[176:179], a[228:231], a[208:211], v[176:179]
  s_waitcnt lgkmcnt(4)
  v_mfma_f32_16x16x32_bf16 v[84:87], a[232:235], a[196:199], v[84:87]
  ds_read_b64_tr_b16 a[248:249], v19 offset:33800
  buffer_load_dwordx4 v[220:223], v28, s[56:59], 0 offen offset:3072 sc1
  v_mfma_f32_16x16x32_bf16 v[84:87], a[236:239], a[212:215], v[84:87]
  v_mfma_f32_16x16x32_bf16 v[116:119], a[232:235], a[200:203], v[116:119]
  ds_read_b64_tr_b16 a[250:251], v19 offset:37928
  buffer_load_dwordx4 a[116:119], v16, s[64:67], s71 offen sc1
  v_mfma_f32_16x16x32_bf16 v[116:119], a[236:239], a[216:219], v[116:119]
  v_mfma_f32_16x16x32_bf16 v[148:151], a[232:235], a[204:207], v[148:151]
  ds_read_b64_tr_b16 a[252:253], v19 offset:42056
  v_mfma_f32_16x16x32_bf16 v[148:151], a[236:239], a[220:223], v[148:151]
  buffer_load_dwordx4 v[224:227], v29, s[56:59], 0 offen offset:0 sc1
  v_mfma_f32_16x16x32_bf16 v[180:183], a[232:235], a[192:195], v[180:183]
  ds_read_b64_tr_b16 a[254:255], v19 offset:46184
  v_mfma_f32_16x16x32_bf16 v[180:183], a[236:239], a[208:211], v[180:183]
  s_waitcnt lgkmcnt(4)
  s_waitcnt vmcnt(43)
  s_barrier
  buffer_load_dwordx4 v4, s[52:55], s68 offen offset:3096 lds
  v_mfma_f32_16x16x32_bf16 v[88:91], a[240:243], a[196:199], v[88:91]
  ds_read_b64_tr_b16 a[224:225], v19 offset:49536
  v_mfma_f32_16x16x32_bf16 v[88:91], a[244:247], a[212:215], v[88:91]
  v_mfma_f32_16x16x32_bf16 v[120:123], a[240:243], a[200:203], v[120:123]
  ds_read_b64_tr_b16 a[226:227], v19 offset:53664
  buffer_load_dwordx4 v[228:231], v29, s[56:59], 0 offen offset:1024 sc1
  v_mfma_f32_16x16x32_bf16 v[120:123], a[244:247], a[216:219], v[120:123]
  v_mfma_f32_16x16x32_bf16 v[152:155], a[240:243], a[204:207], v[152:155]
  ds_read_b64_tr_b16 a[228:229], v19 offset:57792
  buffer_load_dwordx4 a[120:123], v17, s[64:67], s71 offen sc1
  v_mfma_f32_16x16x32_bf16 v[152:155], a[244:247], a[220:223], v[152:155]
  v_mfma_f32_16x16x32_bf16 v[184:187], a[240:243], a[192:195], v[184:187]
  ds_read_b64_tr_b16 a[230:231], v19 offset:61920
  v_mfma_f32_16x16x32_bf16 v[184:187], a[244:247], a[208:211], v[184:187]
  s_waitcnt lgkmcnt(4)
  buffer_load_dwordx4 v[232:235], v29, s[56:59], 0 offen offset:2048 sc1
  v_mfma_f32_16x16x32_bf16 v[92:95], a[248:251], a[196:199], v[92:95]
  ds_read_b64_tr_b16 a[232:233], v19 offset:49544
  v_mfma_f32_16x16x32_bf16 v[92:95], a[252:255], a[212:215], v[92:95]
  buffer_load_dwordx4 a[124:127], v18, s[64:67], s71 offen sc1
  v_mfma_f32_16x16x32_bf16 v[124:127], a[248:251], a[200:203], v[124:127]
  ds_read_b64_tr_b16 a[234:235], v19 offset:53672
  v_mfma_f32_16x16x32_bf16 v[124:127], a[252:255], a[216:219], v[124:127]
  v_mfma_f32_16x16x32_bf16 v[156:159], a[248:251], a[204:207], v[156:159]
  ds_read_b64_tr_b16 a[236:237], v19 offset:57800
  buffer_load_dwordx4 v[236:239], v29, s[56:59], 0 offen offset:3072 sc1
  v_mfma_f32_16x16x32_bf16 v[156:159], a[252:255], a[220:223], v[156:159]
  v_mfma_f32_16x16x32_bf16 v[188:191], a[248:251], a[192:195], v[188:191]
  ds_read_b64_tr_b16 a[238:239], v19 offset:61928
  v_mfma_f32_16x16x32_bf16 v[188:191], a[252:255], a[208:211], v[188:191]
  s_waitcnt lgkmcnt(4)
  v_mfma_f32_16x16x32_bf16 v[64:67], a[224:227], a[4:7], v[64:67]
  s_add_u32 s73, s88, 56
  s_lshl_b32 s68, s73, 17
  s_lshl_b32 s70, s73, 13
  ds_read_b64_tr_b16 a[240:241], v19 offset:49792
  v_mfma_f32_16x16x32_bf16 v[64:67], a[228:231], a[20:23], v[64:67]
  s_add_u32 s71, s70, 4096
  s_add_u32 m0, s74, 0
  v_mfma_f32_16x16x32_bf16 v[96:99], a[224:227], a[8:11], v[96:99]
  ds_read_b64_tr_b16 a[242:243], v19 offset:53920
  buffer_load_dwordx4 v1, s[52:55], s68 offen offset:0 lds
  v_mfma_f32_16x16x32_bf16 v[96:99], a[228:231], a[24:27], v[96:99]
  v_mfma_f32_16x16x32_bf16 v[128:131], a[224:227], a[12:15], v[128:131]
  ds_read_b64_tr_b16 a[244:245], v19 offset:58048
  v_mfma_f32_16x16x32_bf16 v[128:131], a[228:231], a[28:31], v[128:131]
  v_mfma_f32_16x16x32_bf16 v[160:163], a[224:227], a[0:3], v[160:163]
  ds_read_b64_tr_b16 a[246:247], v19 offset:62176
  v_mfma_f32_16x16x32_bf16 v[160:163], a[228:231], a[16:19], v[160:163]
  s_waitcnt lgkmcnt(4)
  v_mfma_f32_16x16x32_bf16 v[68:71], a[232:235], a[4:7], v[68:71]
  ds_read_b64_tr_b16 a[248:249], v19 offset:49800
  v_mfma_f32_16x16x32_bf16 v[68:71], a[236:239], a[20:23], v[68:71]
  v_mfma_f32_16x16x32_bf16 v[100:103], a[232:235], a[8:11], v[100:103]
  ds_read_b64_tr_b16 a[250:251], v19 offset:53928
  v_mfma_f32_16x16x32_bf16 v[100:103], a[236:239], a[24:27], v[100:103]
  v_mfma_f32_16x16x32_bf16 v[132:135], a[232:235], a[12:15], v[132:135]
  ds_read_b64_tr_b16 a[252:253], v19 offset:58056
  buffer_load_dwordx4 a[128:131], v15, s[64:67], s70 offen sc1
  v_mfma_f32_16x16x32_bf16 v[132:135], a[236:239], a[28:31], v[132:135]
  v_mfma_f32_16x16x32_bf16 v[164:167], a[232:235], a[0:3], v[164:167]
  ds_read_b64_tr_b16 a[254:255], v19 offset:62184
  v_mfma_f32_16x16x32_bf16 v[164:167], a[236:239], a[16:19], v[164:167]
  s_waitcnt lgkmcnt(4)
  v_mfma_f32_16x16x32_bf16 v[72:75], a[240:243], a[4:7], v[72:75]
  ds_read_b64_tr_b16 a[224:225], v19 offset:50048
  v_mfma_f32_16x16x32_bf16 v[72:75], a[244:247], a[20:23], v[72:75]
  v_mfma_f32_16x16x32_bf16 v[104:107], a[240:243], a[8:11], v[104:107]
  ds_read_b64_tr_b16 a[226:227], v19 offset:54176
  v_mfma_f32_16x16x32_bf16 v[104:107], a[244:247], a[24:27], v[104:107]
  v_mfma_f32_16x16x32_bf16 v[136:139], a[240:243], a[12:15], v[136:139]
  ds_read_b64_tr_b16 a[228:229], v19 offset:58304
  v_mfma_f32_16x16x32_bf16 v[136:139], a[244:247], a[28:31], v[136:139]
  v_mfma_f32_16x16x32_bf16 v[168:171], a[240:243], a[0:3], v[168:171]
  ds_read_b64_tr_b16 a[230:231], v19 offset:62432
  buffer_load_dwordx4 v2, s[52:55], s68 offen offset:1032 lds
  v_mfma_f32_16x16x32_bf16 v[168:171], a[244:247], a[16:19], v[168:171]
  s_waitcnt lgkmcnt(4)
  v_mfma_f32_16x16x32_bf16 v[76:79], a[248:251], a[4:7], v[76:79]
  ds_read_b64_tr_b16 a[232:233], v19 offset:50056
  v_mfma_f32_16x16x32_bf16 v[76:79], a[252:255], a[20:23], v[76:79]
  v_mfma_f32_16x16x32_bf16 v[108:111], a[248:251], a[8:11], v[108:111]
  ds_read_b64_tr_b16 a[234:235], v19 offset:54184
  v_mfma_f32_16x16x32_bf16 v[108:111], a[252:255], a[24:27], v[108:111]
  v_mfma_f32_16x16x32_bf16 v[140:143], a[248:251], a[12:15], v[140:143]
  ds_read_b64_tr_b16 a[236:237], v19 offset:58312
  v_mfma_f32_16x16x32_bf16 v[140:143], a[252:255], a[28:31], v[140:143]
  v_mfma_f32_16x16x32_bf16 v[172:175], a[248:251], a[0:3], v[172:175]
  ds_read_b64_tr_b16 a[238:239], v19 offset:62440
  v_mfma_f32_16x16x32_bf16 v[172:175], a[252:255], a[16:19], v[172:175]
  s_waitcnt lgkmcnt(4)
  v_mfma_f32_16x16x32_bf16 v[80:83], a[224:227], a[4:7], v[80:83]
  ds_read_b64_tr_b16 a[240:241], v19 offset:50304
  buffer_load_dwordx4 v3, s[52:55], s68 offen offset:2064 lds
  v_mfma_f32_16x16x32_bf16 v[80:83], a[228:231], a[20:23], v[80:83]
  v_mfma_f32_16x16x32_bf16 v[112:115], a[224:227], a[8:11], v[112:115]
  ds_read_b64_tr_b16 a[242:243], v19 offset:54432
  v_mfma_f32_16x16x32_bf16 v[112:115], a[228:231], a[24:27], v[112:115]
  v_mfma_f32_16x16x32_bf16 v[144:147], a[224:227], a[12:15], v[144:147]
  ds_read_b64_tr_b16 a[244:245], v19 offset:58560
  v_mfma_f32_16x16x32_bf16 v[144:147], a[228:231], a[28:31], v[144:147]
  v_mfma_f32_16x16x32_bf16 v[176:179], a[224:227], a[0:3], v[176:179]
  ds_read_b64_tr_b16 a[246:247], v19 offset:62688
  v_mfma_f32_16x16x32_bf16 v[176:179], a[228:231], a[16:19], v[176:179]
  s_waitcnt lgkmcnt(4)
  v_mfma_f32_16x16x32_bf16 v[84:87], a[232:235], a[4:7], v[84:87]
  ds_read_b64_tr_b16 a[248:249], v19 offset:50312
  v_mfma_f32_16x16x32_bf16 v[84:87], a[236:239], a[20:23], v[84:87]
  v_mfma_f32_16x16x32_bf16 v[116:119], a[232:235], a[8:11], v[116:119]
  ds_read_b64_tr_b16 a[250:251], v19 offset:54440
  buffer_load_dwordx4 a[144:147], v15, s[64:67], s71 offen sc1
  v_mfma_f32_16x16x32_bf16 v[116:119], a[236:239], a[24:27], v[116:119]
  v_mfma_f32_16x16x32_bf16 v[148:151], a[232:235], a[12:15], v[148:151]
  ds_read_b64_tr_b16 a[252:253], v19 offset:58568
  v_mfma_f32_16x16x32_bf16 v[148:151], a[236:239], a[28:31], v[148:151]
  v_mfma_f32_16x16x32_bf16 v[180:183], a[232:235], a[0:3], v[180:183]
  ds_read_b64_tr_b16 a[254:255], v19 offset:62696
  v_mfma_f32_16x16x32_bf16 v[180:183], a[236:239], a[16:19], v[180:183]
  s_waitcnt lgkmcnt(4)
  s_waitcnt vmcnt(5)
  s_barrier
  v_mfma_f32_16x16x32_bf16 v[88:91], a[240:243], a[4:7], v[88:91]
  ds_read_b64_tr_b16 a[224:225], v20 offset:0
  v_mfma_f32_16x16x32_bf16 v[88:91], a[244:247], a[20:23], v[88:91]
  v_mfma_f32_16x16x32_bf16 v[120:123], a[240:243], a[8:11], v[120:123]
  ds_read_b64_tr_b16 a[226:227], v20 offset:4128
  v_mfma_f32_16x16x32_bf16 v[120:123], a[244:247], a[24:27], v[120:123]
  v_mfma_f32_16x16x32_bf16 v[152:155], a[240:243], a[12:15], v[152:155]
  ds_read_b64_tr_b16 a[228:229], v20 offset:8256
  buffer_load_dwordx4 v4, s[52:55], s68 offen offset:3096 lds
  v_mfma_f32_16x16x32_bf16 v[152:155], a[244:247], a[28:31], v[152:155]
  v_mfma_f32_16x16x32_bf16 v[184:187], a[240:243], a[0:3], v[184:187]
  ds_read_b64_tr_b16 a[230:231], v20 offset:12384
  v_mfma_f32_16x16x32_bf16 v[184:187], a[244:247], a[16:19], v[184:187]
  s_waitcnt lgkmcnt(4)
  v_mfma_f32_16x16x32_bf16 v[92:95], a[248:251], a[4:7], v[92:95]
  ds_read_b64_tr_b16 a[232:233], v20 offset:8
  v_mfma_f32_16x16x32_bf16 v[92:95], a[252:255], a[20:23], v[92:95]
  v_mfma_f32_16x16x32_bf16 v[124:127], a[248:251], a[8:11], v[124:127]
  ds_read_b64_tr_b16 a[234:235], v20 offset:4136
  v_mfma_f32_16x16x32_bf16 v[124:127], a[252:255], a[24:27], v[124:127]
  v_mfma_f32_16x16x32_bf16 v[156:159], a[248:251], a[12:15], v[156:159]
  ds_read_b64_tr_b16 a[236:237], v20 offset:8264
  v_mfma_f32_16x16x32_bf16 v[156:159], a[252:255], a[28:31], v[156:159]
  v_mfma_f32_16x16x32_bf16 v[188:191], a[248:251], a[0:3], v[188:191]
  ds_read_b64_tr_b16 a[238:239], v20 offset:12392
  v_mfma_f32_16x16x32_bf16 v[188:191], a[252:255], a[16:19], v[188:191]
  s_waitcnt lgkmcnt(4)
  v_mfma_f32_16x16x32_bf16 v[64:67], a[224:227], a[36:39], v[64:67]
  s_add_u32 s73, s89, 56
  s_lshl_b32 s68, s73, 17
  s_lshl_b32 s70, s73, 13
  ds_read_b64_tr_b16 a[240:241], v20 offset:256
  v_mfma_f32_16x16x32_bf16 v[64:67], a[228:231], a[52:55], v[64:67]
  s_add_u32 s71, s70, 4096
  s_add_u32 m0, s74, 16512
  v_mfma_f32_16x16x32_bf16 v[96:99], a[224:227], a[40:43], v[96:99]
  ds_read_b64_tr_b16 a[242:243], v20 offset:4384
  buffer_load_dwordx4 v1, s[52:55], s68 offen offset:0 lds
  v_mfma_f32_16x16x32_bf16 v[96:99], a[228:231], a[56:59], v[96:99]
  v_mfma_f32_16x16x32_bf16 v[128:131], a[224:227], a[44:47], v[128:131]
  ds_read_b64_tr_b16 a[244:245], v20 offset:8512
  v_mfma_f32_16x16x32_bf16 v[128:131], a[228:231], a[60:63], v[128:131]
  v_mfma_f32_16x16x32_bf16 v[32:35], v[242:245], v[192:195], v[32:35]
  v_mfma_f32_16x16x32_bf16 v[160:163], a[224:227], a[32:35], v[160:163]
  ds_read_b64_tr_b16 a[246:247], v20 offset:12640
  v_mfma_f32_16x16x32_bf16 v[160:163], a[228:231], a[48:51], v[160:163]
  s_waitcnt lgkmcnt(4)
  v_mfma_f32_16x16x32_bf16 v[36:39], v[240:243], v[192:195], v[36:39]
  v_mfma_f32_16x16x32_bf16 v[68:71], a[232:235], a[36:39], v[68:71]
  ds_read_b64_tr_b16 a[248:249], v20 offset:264
  v_mfma_f32_16x16x32_bf16 v[68:71], a[236:239], a[52:55], v[68:71]
  v_mfma_f32_16x16x32_bf16 v[40:43], v[242:245], v[196:199], v[40:43]
  v_mfma_f32_16x16x32_bf16 v[100:103], a[232:235], a[40:43], v[100:103]
  ds_read_b64_tr_b16 a[250:251], v20 offset:4392
  v_mfma_f32_16x16x32_bf16 v[100:103], a[236:239], a[56:59], v[100:103]
  v_mfma_f32_16x16x32_bf16 v[44:47], v[240:243], v[196:199], v[44:47]
  v_mfma_f32_16x16x32_bf16 v[132:135], a[232:235], a[44:47], v[132:135]
  ds_read_b64_tr_b16 a[252:253], v20 offset:8520
  buffer_load_dwordx4 a[160:163], v15, s[64:67], s70 offen sc1
  v_mfma_f32_16x16x32_bf16 v[132:135], a[236:239], a[60:63], v[132:135]
  v_mfma_f32_16x16x32_bf16 v[48:51], v[242:245], v[200:203], v[48:51]
  v_mfma_f32_16x16x32_bf16 v[164:167], a[232:235], a[32:35], v[164:167]
  ds_read_b64_tr_b16 a[254:255], v20 offset:12648
  v_mfma_f32_16x16x32_bf16 v[164:167], a[236:239], a[48:51], v[164:167]
  s_waitcnt lgkmcnt(4)
  v_mfma_f32_16x16x32_bf16 v[52:55], v[240:243], v[200:203], v[52:55]
  v_mfma_f32_16x16x32_bf16 v[72:75], a[240:243], a[36:39], v[72:75]
  ds_read_b64_tr_b16 a[224:225], v20 offset:512
  v_mfma_f32_16x16x32_bf16 v[72:75], a[244:247], a[52:55], v[72:75]
  v_mfma_f32_16x16x32_bf16 v[56:59], v[242:245], v[204:207], v[56:59]
  v_mfma_f32_16x16x32_bf16 v[104:107], a[240:243], a[40:43], v[104:107]
  ds_read_b64_tr_b16 a[226:227], v20 offset:4640
  v_mfma_f32_16x16x32_bf16 v[104:107], a[244:247], a[56:59], v[104:107]
  v_mfma_f32_16x16x32_bf16 v[60:63], v[240:243], v[204:207], v[60:63]
  v_mfma_f32_16x16x32_bf16 v[136:139], a[240:243], a[44:47], v[136:139]
  ds_read_b64_tr_b16 a[228:229], v20 offset:8768
  v_mfma_f32_16x16x32_bf16 v[136:139], a[244:247], a[60:63], v[136:139]
  v_mfma_f32_16x16x32_bf16 v[32:35], v[242:245], v[208:211], v[32:35]
  v_mfma_f32_16x16x32_bf16 v[168:171], a[240:243], a[32:35], v[168:171]
  ds_read_b64_tr_b16 a[230:231], v20 offset:12896
  buffer_load_dwordx4 v2, s[52:55], s68 offen offset:1032 lds
  v_mfma_f32_16x16x32_bf16 v[168:171], a[244:247], a[48:51], v[168:171]
  s_waitcnt lgkmcnt(4)
  v_mfma_f32_16x16x32_bf16 v[36:39], v[240:243], v[208:211], v[36:39]
  v_mfma_f32_16x16x32_bf16 v[76:79], a[248:251], a[36:39], v[76:79]
  ds_read_b64_tr_b16 a[232:233], v20 offset:520
  v_mfma_f32_16x16x32_bf16 v[76:79], a[252:255], a[52:55], v[76:79]
  v_mfma_f32_16x16x32_bf16 v[40:43], v[242:245], v[212:215], v[40:43]
  v_mfma_f32_16x16x32_bf16 v[108:111], a[248:251], a[40:43], v[108:111]
  ds_read_b64_tr_b16 a[234:235], v20 offset:4648
  v_mfma_f32_16x16x32_bf16 v[108:111], a[252:255], a[56:59], v[108:111]
  v_mfma_f32_16x16x32_bf16 v[44:47], v[240:243], v[212:215], v[44:47]
  v_mfma_f32_16x16x32_bf16 v[140:143], a[248:251], a[44:47], v[140:143]
  ds_read_b64_tr_b16 a[236:237], v20 offset:8776
  v_mfma_f32_16x16x32_bf16 v[140:143], a[252:255], a[60:63], v[140:143]
  v_mfma_f32_16x16x32_bf16 v[48:51], v[242:245], v[216:219], v[48:51]
  v_mfma_f32_16x16x32_bf16 v[172:175], a[248:251], a[32:35], v[172:175]
  ds_read_b64_tr_b16 a[238:239], v20 offset:12904
  v_mfma_f32_16x16x32_bf16 v[172:175], a[252:255], a[48:51], v[172:175]
  s_waitcnt lgkmcnt(4)
  v_mfma_f32_16x16x32_bf16 v[52:55], v[240:243], v[216:219], v[52:55]
  v_mfma_f32_16x16x32_bf16 v[80:83], a[224:227], a[36:39], v[80:83]
  ds_read_b64_tr_b16 a[240:241], v20 offset:768
  buffer_load_dwordx4 v3, s[52:55], s68 offen offset:2064 lds
  v_mfma_f32_16x16x32_bf16 v[80:83], a[228:231], a[52:55], v[80:83]
  v_mfma_f32_16x16x32_bf16 v[56:59], v[242:245], v[220:223], v[56:59]
  v_mfma_f32_16x16x32_bf16 v[112:115], a[224:227], a[40:43], v[112:115]
  ds_read_b64_tr_b16 a[242:243], v20 offset:4896
  v_mfma_f32_16x16x32_bf16 v[112:115], a[228:231], a[56:59], v[112:115]
  v_mfma_f32_16x16x32_bf16 v[60:63], v[240:243], v[220:223], v[60:63]
  v_mfma_f32_16x16x32_bf16 v[144:147], a[224:227], a[44:47], v[144:147]
  ds_read_b64_tr_b16 a[244:245], v20 offset:9024
  v_mfma_f32_16x16x32_bf16 v[144:147], a[228:231], a[60:63], v[144:147]
  v_mfma_f32_16x16x32_bf16 v[32:35], v[242:245], v[224:227], v[32:35]
  v_mfma_f32_16x16x32_bf16 v[176:179], a[224:227], a[32:35], v[176:179]
  ds_read_b64_tr_b16 a[246:247], v20 offset:13152
  v_mfma_f32_16x16x32_bf16 v[176:179], a[228:231], a[48:51], v[176:179]
  s_waitcnt lgkmcnt(4)
  v_mfma_f32_16x16x32_bf16 v[36:39], v[240:243], v[224:227], v[36:39]
  v_mfma_f32_16x16x32_bf16 v[84:87], a[232:235], a[36:39], v[84:87]
  ds_read_b64_tr_b16 a[248:249], v20 offset:776
  v_mfma_f32_16x16x32_bf16 v[84:87], a[236:239], a[52:55], v[84:87]
  v_mfma_f32_16x16x32_bf16 v[40:43], v[242:245], v[228:231], v[40:43]
  v_mfma_f32_16x16x32_bf16 v[116:119], a[232:235], a[40:43], v[116:119]
  ds_read_b64_tr_b16 a[250:251], v20 offset:4904
  buffer_load_dwordx4 a[176:179], v15, s[64:67], s71 offen sc1
  v_mfma_f32_16x16x32_bf16 v[116:119], a[236:239], a[56:59], v[116:119]
  v_mfma_f32_16x16x32_bf16 v[44:47], v[240:243], v[228:231], v[44:47]
  v_mfma_f32_16x16x32_bf16 v[148:151], a[232:235], a[44:47], v[148:151]
  ds_read_b64_tr_b16 a[252:253], v20 offset:9032
  v_mfma_f32_16x16x32_bf16 v[148:151], a[236:239], a[60:63], v[148:151]
  v_mfma_f32_16x16x32_bf16 v[48:51], v[242:245], v[232:235], v[48:51]
  v_mfma_f32_16x16x32_bf16 v[180:183], a[232:235], a[32:35], v[180:183]
  ds_read_b64_tr_b16 a[254:255], v20 offset:13160
  v_mfma_f32_16x16x32_bf16 v[180:183], a[236:239], a[48:51], v[180:183]
  s_waitcnt lgkmcnt(4)
  s_waitcnt vmcnt(35)
  s_barrier
  v_mfma_f32_16x16x32_bf16 v[52:55], v[240:243], v[232:235], v[52:55]
  v_mfma_f32_16x16x32_bf16 v[88:91], a[240:243], a[36:39], v[88:91]
  ds_read_b64_tr_b16 a[224:225], v20 offset:16512
  v_mfma_f32_16x16x32_bf16 v[88:91], a[244:247], a[52:55], v[88:91]
  v_mfma_f32_16x16x32_bf16 v[56:59], v[242:245], v[236:239], v[56:59]
  v_mfma_f32_16x16x32_bf16 v[120:123], a[240:243], a[40:43], v[120:123]
  ds_read_b64_tr_b16 a[226:227], v20 offset:20640
  v_mfma_f32_16x16x32_bf16 v[120:123], a[244:247], a[56:59], v[120:123]
  v_mfma_f32_16x16x32_bf16 v[60:63], v[240:243], v[236:239], v[60:63]
  v_mfma_f32_16x16x32_bf16 v[152:155], a[240:243], a[44:47], v[152:155]
  ds_read_b64_tr_b16 a[228:229], v20 offset:24768
  buffer_load_dwordx4 v4, s[52:55], s68 offen offset:3096 lds
  v_mfma_f32_16x16x32_bf16 v[152:155], a[244:247], a[60:63], v[152:155]
  v_mfma_f32_16x16x32_bf16 v[184:187], a[240:243], a[32:35], v[184:187]
  ds_read_b64_tr_b16 a[230:231], v20 offset:28896
  v_mfma_f32_16x16x32_bf16 v[184:187], a[244:247], a[48:51], v[184:187]
  s_waitcnt lgkmcnt(4)
  v_mfma_f32_16x16x32_bf16 v[92:95], a[248:251], a[36:39], v[92:95]
  ds_read_b64_tr_b16 a[232:233], v20 offset:16520
  v_mfma_f32_16x16x32_bf16 v[92:95], a[252:255], a[52:55], v[92:95]
  v_mfma_f32_16x16x32_bf16 v[124:127], a[248:251], a[40:43], v[124:127]
  ds_read_b64_tr_b16 a[234:235], v20 offset:20648
  v_mfma_f32_16x16x32_bf16 v[124:127], a[252:255], a[56:59], v[124:127]
  v_mfma_f32_16x16x32_bf16 v[156:159], a[248:251], a[44:47], v[156:159]
  ds_read_b64_tr_b16 a[236:237], v20 offset:24776
  v_mfma_f32_16x16x32_bf16 v[156:159], a[252:255], a[60:63], v[156:159]
  v_mfma_f32_16x16x32_bf16 v[188:191], a[248:251], a[32:35], v[188:191]
  ds_read_b64_tr_b16 a[238:239], v20 offset:28904
  v_mfma_f32_16x16x32_bf16 v[188:191], a[252:255], a[48:51], v[188:191]
  s_waitcnt lgkmcnt(4)
  v_mfma_f32_16x16x32_bf16 v[64:67], a[224:227], a[68:71], v[64:67]
  s_add_u32 s73, s90, 56
  s_lshl_b32 s68, s73, 17
  s_lshl_b32 s70, s73, 13
  ds_read_b64_tr_b16 a[240:241], v20 offset:16768
  v_mfma_f32_16x16x32_bf16 v[64:67], a[228:231], a[84:87], v[64:67]
  s_add_u32 s71, s70, 4096
  s_add_u32 m0, s74, 33024
  v_mul_f32 v246, 0xbfb8aa3b, v32
  v_mul_f32 v247, 0xbfb8aa3b, v33
  v_mfma_f32_16x16x32_bf16 v[96:99], a[224:227], a[72:75], v[96:99]
  ds_read_b64_tr_b16 a[242:243], v20 offset:20896
  buffer_load_dwordx4 v1, s[52:55], s68 offen offset:0 lds
  v_mul_f32 v248, 0xbfb8aa3b, v34
  v_mfma_f32_16x16x32_bf16 v[96:99], a[228:231], a[88:91], v[96:99]
  v_mul_f32 v249, 0xbfb8aa3b, v35
  v_mfma_f32_16x16x32_bf16 v[128:131], a[224:227], a[76:79], v[128:131]
  ds_read_b64_tr_b16 a[244:245], v20 offset:25024
  v_exp_f32 v246, v246
  v_exp_f32 v247, v247
  v_mfma_f32_16x16x32_bf16 v[128:131], a[228:231], a[92:95], v[128:131]
  v_exp_f32 v248, v248
  v_mfma_f32_16x16x32_bf16 v[160:163], a[224:227], a[64:67], v[160:163]
  ds_read_b64_tr_b16 a[246:247], v20 offset:29152
  v_exp_f32 v249, v249
  v_mfma_f32_16x16x32_bf16 v[160:163], a[228:231], a[80:83], v[160:163]
  s_waitcnt lgkmcnt(4)
  v_add_f32 v246, 1.0, v246
  v_add_f32 v247, 1.0, v247
  v_mfma_f32_16x16x32_bf16 v[68:71], a[232:235], a[68:71], v[68:71]
  ds_read_b64_tr_b16 a[248:249], v20 offset:16776
  v_add_f32 v248, 1.0, v248
  v_mfma_f32_16x16x32_bf16 v[68:71], a[236:239], a[84:87], v[68:71]
  v_add_f32 v249, 1.0, v249
  v_mfma_f32_16x16x32_bf16 v[100:103], a[232:235], a[72:75], v[100:103]
  ds_read_b64_tr_b16 a[250:251], v20 offset:20904
  v_rcp_f32 v246, v246
  v_mfma_f32_16x16x32_bf16 v[100:103], a[236:239], a[88:91], v[100:103]
  v_rcp_f32 v247, v247
  v_rcp_f32 v248, v248
  v_mfma_f32_16x16x32_bf16 v[132:135], a[232:235], a[76:79], v[132:135]
  ds_read_b64_tr_b16 a[252:253], v20 offset:25032
  buffer_load_dwordx4 a[192:195], v15, s[64:67], s70 offen sc1
  v_rcp_f32 v249, v249
  v_mfma_f32_16x16x32_bf16 v[132:135], a[236:239], a[92:95], v[132:135]
  v_mul_f32 v32, v32, v246
  v_mfma_f32_16x16x32_bf16 v[164:167], a[232:235], a[64:67], v[164:167]
  ds_read_b64_tr_b16 a[254:255], v20 offset:29160
  v_mul_f32 v33, v33, v247
  v_mul_f32 v34, v34, v248
  v_mfma_f32_16x16x32_bf16 v[164:167], a[236:239], a[80:83], v[164:167]
  s_waitcnt lgkmcnt(4)
  v_mul_f32 v35, v35, v249
  v_mfma_f32_16x16x32_bf16 v[72:75], a[240:243], a[68:71], v[72:75]
  ds_read_b64_tr_b16 a[224:225], v20 offset:17024
  v_mul_f32 v250, 0xbfb8aa3b, v36
  v_mfma_f32_16x16x32_bf16 v[72:75], a[244:247], a[84:87], v[72:75]
  v_mul_f32 v251, 0xbfb8aa3b, v37
  v_mfma_f32_16x16x32_bf16 v[104:107], a[240:243], a[72:75], v[104:107]
  ds_read_b64_tr_b16 a[226:227], v20 offset:21152
  v_mul_f32 v252, 0xbfb8aa3b, v38
  v_mul_f32 v253, 0xbfb8aa3b, v39
  v_mfma_f32_16x16x32_bf16 v[104:107], a[244:247], a[88:91], v[104:107]
  v_exp_f32 v250, v250
  v_mfma_f32_16x16x32_bf16 v[136:139], a[240:243], a[76:79], v[136:139]
  ds_read_b64_tr_b16 a[228:229], v20 offset:25280
  v_exp_f32 v251, v251
  v_mfma_f32_16x16x32_bf16 v[136:139], a[244:247], a[92:95], v[136:139]
  v_exp_f32 v252, v252
  v_exp_f32 v253, v253
  v_mfma_f32_16x16x32_bf16 v[168:171], a[240:243], a[64:67], v[168:171]
  ds_read_b64_tr_b16 a[230:231], v20 offset:29408
  buffer_load_dwordx4 v2, s[52:55], s68 offen offset:1032 lds
  v_add_f32 v250, 1.0, v250
  v_mfma_f32_16x16x32_bf16 v[168:171], a[244:247], a[80:83], v[168:171]
  s_waitcnt lgkmcnt(4)
  v_add_f32 v251, 1.0, v251
  v_mfma_f32_16x16x32_bf16 v[76:79], a[248:251], a[68:71], v[76:79]
  ds_read_b64_tr_b16 a[232:233], v20 offset:17032
  v_add_f32 v252, 1.0, v252
  v_mfma_f32_16x16x32_bf16 v[76:79], a[252:255], a[84:87], v[76:79]
  v_add_f32 v253, 1.0, v253
  v_rcp_f32 v250, v250
  v_mfma_f32_16x16x32_bf16 v[108:111], a[248:251], a[72:75], v[108:111]
  ds_read_b64_tr_b16 a[234:235], v20 offset:21160
  v_rcp_f32 v251, v251
  v_mfma_f32_16x16x32_bf16 v[108:111], a[252:255], a[88:91], v[108:111]
  v_rcp_f32 v252, v252
  v_mfma_f32_16x16x32_bf16 v[140:143], a[248:251], a[76:79], v[140:143]
  ds_read_b64_tr_b16 a[236:237], v20 offset:25288
  v_rcp_f32 v253, v253
  v_mul_f32 v36, v36, v250
  v_mfma_f32_16x16x32_bf16 v[140:143], a[252:255], a[92:95], v[140:143]
  v_mul_f32 v37, v37, v251
  v_mfma_f32_16x16x32_bf16 v[172:175], a[248:251], a[64:67], v[172:175]
  ds_read_b64_tr_b16 a[238:239], v20 offset:29416
  v_mul_f32 v38, v38, v252
  v_mfma_f32_16x16x32_bf16 v[172:175], a[252:255], a[80:83], v[172:175]
  s_waitcnt lgkmcnt(4)
  v_mul_f32 v39, v39, v253
  v_mfma_f32_16x16x32_bf16 v[80:83], a[224:227], a[68:71], v[80:83]
  ds_read_b64_tr_b16 a[240:241], v20 offset:17280
  buffer_load_dwordx4 v3, s[52:55], s68 offen offset:2064 lds
  v_mul_f32 v246, 0xbfb8aa3b, v40
  v_mul_f32 v247, 0xbfb8aa3b, v41
  v_mfma_f32_16x16x32_bf16 v[80:83], a[228:231], a[84:87], v[80:83]
  v_mul_f32 v248, 0xbfb8aa3b, v42
  v_mfma_f32_16x16x32_bf16 v[112:115], a[224:227], a[72:75], v[112:115]
  ds_read_b64_tr_b16 a[242:243], v20 offset:21408
  v_mul_f32 v249, 0xbfb8aa3b, v43
  v_mfma_f32_16x16x32_bf16 v[112:115], a[228:231], a[88:91], v[112:115]
  v_exp_f32 v246, v246
  v_exp_f32 v247, v247
  v_mfma_f32_16x16x32_bf16 v[144:147], a[224:227], a[76:79], v[144:147]
  ds_read_b64_tr_b16 a[244:245], v20 offset:25536
  v_exp_f32 v248, v248
  v_mfma_f32_16x16x32_bf16 v[144:147], a[228:231], a[92:95], v[144:147]
  v_exp_f32 v249, v249
  v_mfma_f32_16x16x32_bf16 v[176:179], a[224:227], a[64:67], v[176:179]
  ds_read_b64_tr_b16 a[246:247], v20 offset:29664
  v_add_f32 v246, 1.0, v246
  v_add_f32 v247, 1.0, v247
  v_mfma_f32_16x16x32_bf16 v[176:179], a[228:231], a[80:83], v[176:179]
  s_waitcnt lgkmcnt(4)
  v_add_f32 v248, 1.0, v248
  v_mfma_f32_16x16x32_bf16 v[84:87], a[232:235], a[68:71], v[84:87]
  ds_read_b64_tr_b16 a[248:249], v20 offset:17288
  v_add_f32 v249, 1.0, v249
  v_mfma_f32_16x16x32_bf16 v[84:87], a[236:239], a[84:87], v[84:87]
  v_rcp_f32 v246, v246
  v_mfma_f32_16x16x32_bf16 v[116:119], a[232:235], a[72:75], v[116:119]
  ds_read_b64_tr_b16 a[250:251], v20 offset:21416
  buffer_load_dwordx4 a[208:211], v15, s[64:67], s71 offen sc1
  v_rcp_f32 v247, v247
  v_rcp_f32 v248, v248
  v_mfma_f32_16x16x32_bf16 v[116:119], a[236:239], a[88:91], v[116:119]
  v_rcp_f32 v249, v249
  v_mfma_f32_16x16x32_bf16 v[148:151], a[232:235], a[76:79], v[148:151]
  ds_read_b64_tr_b16 a[252:253], v20 offset:25544
  v_mul_f32 v40, v40, v246
  v_mfma_f32_16x16x32_bf16 v[148:151], a[236:239], a[92:95], v[148:151]
  v_mul_f32 v41, v41, v247
  v_mul_f32 v42, v42, v248
  v_mfma_f32_16x16x32_bf16 v[180:183], a[232:235], a[64:67], v[180:183]
  ds_read_b64_tr_b16 a[254:255], v20 offset:29672
  v_mul_f32 v43, v43, v249
  v_mfma_f32_16x16x32_bf16 v[180:183], a[236:239], a[80:83], v[180:183]
  s_waitcnt lgkmcnt(4)
  s_waitcnt vmcnt(18)
  s_barrier
  v_mul_f32 v250, 0xbfb8aa3b, v44
  v_mfma_f32_16x16x32_bf16 v[88:91], a[240:243], a[68:71], v[88:91]
  ds_read_b64_tr_b16 a[224:225], v20 offset:33024
  v_mul_f32 v251, 0xbfb8aa3b, v45
  v_mfma_f32_16x16x32_bf16 v[88:91], a[244:247], a[84:87], v[88:91]
  v_mul_f32 v252, 0xbfb8aa3b, v46
  v_mul_f32 v253, 0xbfb8aa3b, v47
  v_mfma_f32_16x16x32_bf16 v[120:123], a[240:243], a[72:75], v[120:123]
  ds_read_b64_tr_b16 a[226:227], v20 offset:37152
  v_exp_f32 v250, v250
  v_mfma_f32_16x16x32_bf16 v[120:123], a[244:247], a[88:91], v[120:123]
  v_exp_f32 v251, v251
  v_mfma_f32_16x16x32_bf16 v[152:155], a[240:243], a[76:79], v[152:155]
  ds_read_b64_tr_b16 a[228:229], v20 offset:41280
  buffer_load_dwordx4 v4, s[52:55], s68 offen offset:3096 lds
  v_exp_f32 v252, v252
  v_exp_f32 v253, v253
  v_mfma_f32_16x16x32_bf16 v[152:155], a[244:247], a[92:95], v[152:155]
  v_add_f32 v250, 1.0, v250
  v_mfma_f32_16x16x32_bf16 v[184:187], a[240:243], a[64:67], v[184:187]
  ds_read_b64_tr_b16 a[230:231], v20 offset:45408
  v_add_f32 v251, 1.0, v251
  v_mfma_f32_16x16x32_bf16 v[184:187], a[244:247], a[80:83], v[184:187]
  s_waitcnt lgkmcnt(4)
  v_add_f32 v252, 1.0, v252
  v_mfma_f32_16x16x32_bf16 v[92:95], a[248:251], a[68:71], v[92:95]
  ds_read_b64_tr_b16 a[232:233], v20 offset:33032
  v_add_f32 v253, 1.0, v253
  v_rcp_f32 v250, v250
  v_mfma_f32_16x16x32_bf16 v[92:95], a[252:255], a[84:87], v[92:95]
  v_rcp_f32 v251, v251
  v_mfma_f32_16x16x32_bf16 v[124:127], a[248:251], a[72:75], v[124:127]
  ds_read_b64_tr_b16 a[234:235], v20 offset:37160
  v_rcp_f32 v252, v252
  v_mfma_f32_16x16x32_bf16 v[124:127], a[252:255], a[88:91], v[124:127]
  v_rcp_f32 v253, v253
  v_mul_f32 v44, v44, v250
  v_mfma_f32_16x16x32_bf16 v[156:159], a[248:251], a[76:79], v[156:159]
  ds_read_b64_tr_b16 a[236:237], v20 offset:41288
  v_mul_f32 v45, v45, v251
  v_mfma_f32_16x16x32_bf16 v[156:159], a[252:255], a[92:95], v[156:159]
  v_mul_f32 v46, v46, v252
  v_mfma_f32_16x16x32_bf16 v[188:191], a[248:251], a[64:67], v[188:191]
  ds_read_b64_tr_b16 a[238:239], v20 offset:45416
  v_mul_f32 v47, v47, v253
  v_mfma_f32_16x16x32_bf16 v[188:191], a[252:255], a[80:83], v[188:191]
  s_waitcnt lgkmcnt(4)
  v_mfma_f32_16x16x32_bf16 v[64:67], a[224:227], a[100:103], v[64:67]
  s_add_u32 s73, s91, 56
  s_lshl_b32 s68, s73, 17
  s_lshl_b32 s70, s73, 13
  ds_read_b64_tr_b16 a[240:241], v20 offset:33280
  v_mfma_f32_16x16x32_bf16 v[64:67], a[228:231], a[116:119], v[64:67]
  s_add_u32 s71, s70, 4096
  s_add_u32 m0, s74, 49536
  v_mul_f32 v246, 0xbfb8aa3b, v48
  v_mfma_f32_16x16x32_bf16 v[96:99], a[224:227], a[104:107], v[96:99]
  ds_read_b64_tr_b16 a[242:243], v20 offset:37408
  buffer_load_dwordx4 v1, s[52:55], s68 offen offset:0 lds
  v_mul_f32 v247, 0xbfb8aa3b, v49
  v_mfma_f32_16x16x32_bf16 v[96:99], a[228:231], a[120:123], v[96:99]
  v_mfma_f32_16x16x32_bf16 v[128:131], a[224:227], a[108:111], v[128:131]
  ds_read_b64_tr_b16 a[244:245], v20 offset:41536
  v_mul_f32 v248, 0xbfb8aa3b, v50
  v_mfma_f32_16x16x32_bf16 v[128:131], a[228:231], a[124:127], v[128:131]
  v_mul_f32 v249, 0xbfb8aa3b, v51
  v_mfma_f32_16x16x32_bf16 v[160:163], a[224:227], a[96:99], v[160:163]
  ds_read_b64_tr_b16 a[246:247], v20 offset:45664
  v_mfma_f32_16x16x32_bf16 v[160:163], a[228:231], a[112:115], v[160:163]
  s_waitcnt lgkmcnt(4)
  v_exp_f32 v246, v246
  v_mfma_f32_16x16x32_bf16 v[68:71], a[232:235], a[100:103], v[68:71]
  ds_read_b64_tr_b16 a[248:249], v20 offset:33288
  v_exp_f32 v247, v247
  v_cvt_pk_bf16_f32 v192, v64, v65
  v_mfma_f32_16x16x32_bf16 v[68:71], a[236:239], a[116:119], v[68:71]
  v_mfma_f32_16x16x32_bf16 v[100:103], a[232:235], a[104:107], v[100:103]
  ds_read_b64_tr_b16 a[250:251], v20 offset:37416
  v_exp_f32 v248, v248
  v_cvt_pk_bf16_f32 v208, v96, v97
  v_mfma_f32_16x16x32_bf16 v[100:103], a[236:239], a[120:123], v[100:103]
  v_exp_f32 v249, v249
  v_cvt_pk_bf16_f32 v193, v66, v67
  v_mfma_f32_16x16x32_bf16 v[132:135], a[232:235], a[108:111], v[132:135]
  ds_read_b64_tr_b16 a[252:253], v20 offset:41544
  buffer_load_dwordx4 a[0:3], v15, s[64:67], s70 offen sc1
  v_cvt_pk_bf16_f32 v224, v128, v129
  v_mfma_f32_16x16x32_bf16 v[132:135], a[236:239], a[124:127], v[132:135]
  v_add_f32 v246, 1.0, v246
  v_cvt_pk_bf16_f32 v209, v98, v99
  v_mfma_f32_16x16x32_bf16 v[164:167], a[232:235], a[96:99], v[164:167]
  ds_read_b64_tr_b16 a[254:255], v20 offset:45672
  v_add_f32 v247, 1.0, v247
  v_mfma_f32_16x16x32_bf16 v[164:167], a[236:239], a[112:115], v[164:167]
  s_waitcnt lgkmcnt(4)
  v_cvt_pk_bf16_f32 v225, v130, v131
  v_mfma_f32_16x16x32_bf16 v[72:75], a[240:243], a[100:103], v[72:75]
  ds_read_b64_tr_b16 a[224:225], v20 offset:33536
  v_add_f32 v248, 1.0, v248
  v_cvt_pk_bf16_f32 v194, v68, v69
  v_mfma_f32_16x16x32_bf16 v[72:75], a[244:247], a[116:119], v[72:75]
  v_mfma_f32_16x16x32_bf16 v[104:107], a[240:243], a[104:107], v[104:107]
  ds_read_b64_tr_b16 a[226:227], v20 offset:37664
  v_add_f32 v249, 1.0, v249
  v_cvt_pk_bf16_f32 v210, v100, v101
  v_mfma_f32_16x16x32_bf16 v[104:107], a[244:247], a[120:123], v[104:107]
  v_rcp_f32 v246, v246
  v_cvt_pk_bf16_f32 v195, v70, v71
  v_mfma_f32_16x16x32_bf16 v[136:139], a[240:243], a[108:111], v[136:139]
  ds_read_b64_tr_b16 a[228:229], v20 offset:41792
  v_cvt_pk_bf16_f32 v226, v132, v133
  v_mfma_f32_16x16x32_bf16 v[136:139], a[244:247], a[124:127], v[136:139]
  v_rcp_f32 v247, v247
  v_cvt_pk_bf16_f32 v211, v102, v103
  v_mfma_f32_16x16x32_bf16 v[168:171], a[240:243], a[96:99], v[168:171]
  ds_read_b64_tr_b16 a[230:231], v20 offset:45920
  buffer_load_dwordx4 v2, s[52:55], s68 offen offset:1032 lds
  v_rcp_f32 v248, v248
  v_mfma_f32_16x16x32_bf16 v[168:171], a[244:247], a[112:115], v[168:171]
  s_waitcnt lgkmcnt(4)
  v_cvt_pk_bf16_f32 v227, v134, v135
  v_mfma_f32_16x16x32_bf16 v[76:79], a[248:251], a[100:103], v[76:79]
  ds_read_b64_tr_b16 a[232:233], v20 offset:33544
  v_rcp_f32 v249, v249
  v_cvt_pk_bf16_f32 v196, v72, v73
  v_mfma_f32_16x16x32_bf16 v[76:79], a[252:255], a[116:119], v[76:79]
  v_mul_f32 v48, v48, v246
  v_mfma_f32_16x16x32_bf16 v[108:111], a[248:251], a[104:107], v[108:111]
  ds_read_b64_tr_b16 a[234:235], v20 offset:37672
  v_cvt_pk_bf16_f32 v212, v104, v105
  v_mfma_f32_16x16x32_bf16 v[108:111], a[252:255], a[120:123], v[108:111]
  v_mul_f32 v49, v49, v247
  v_cvt_pk_bf16_f32 v197, v74, v75
  v_mfma_f32_16x16x32_bf16 v[140:143], a[248:251], a[108:111], v[140:143]
  ds_read_b64_tr_b16 a[236:237], v20 offset:41800
  v_mul_f32 v50, v50, v248
  v_cvt_pk_bf16_f32 v228, v136, v137
  v_mfma_f32_16x16x32_bf16 v[140:143], a[252:255], a[124:127], v[140:143]
  v_cvt_pk_bf16_f32 v213, v106, v107
  v_mfma_f32_16x16x32_bf16 v[172:175], a[248:251], a[96:99], v[172:175]
  ds_read_b64_tr_b16 a[238:239], v20 offset:45928
  v_mul_f32 v51, v51, v249
  v_mfma_f32_16x16x32_bf16 v[172:175], a[252:255], a[112:115], v[172:175]
  s_waitcnt lgkmcnt(4)
  v_cvt_pk_bf16_f32 v229, v138, v139
  v_mfma_f32_16x16x32_bf16 v[80:83], a[224:227], a[100:103], v[80:83]
  ds_read_b64_tr_b16 a[240:241], v20 offset:33792
  buffer_load_dwordx4 v3, s[52:55], s68 offen offset:2064 lds
  v_mul_f32 v250, 0xbfb8aa3b, v52
  v_cvt_pk_bf16_f32 v198, v76, v77
  v_mfma_f32_16x16x32_bf16 v[80:83], a[228:231], a[116:119], v[80:83]
  v_mul_f32 v251, 0xbfb8aa3b, v53
  v_mfma_f32_16x16x32_bf16 v[112:115], a[224:227], a[104:107], v[112:115]
  ds_read_b64_tr_b16 a[242:243], v20 offset:37920
  v_cvt_pk_bf16_f32 v214, v108, v109
  v_mfma_f32_16x16x32_bf16 v[112:115], a[228:231], a[120:123], v[112:115]
  v_mul_f32 v252, 0xbfb8aa3b, v54
  v_cvt_pk_bf16_f32 v199, v78, v79
  v_mfma_f32_16x16x32_bf16 v[144:147], a[224:227], a[108:111], v[144:147]
  ds_read_b64_tr_b16 a[244:245], v20 offset:42048
  v_mul_f32 v253, 0xbfb8aa3b, v55
  v_cvt_pk_bf16_f32 v230, v140, v141
  v_mfma_f32_16x16x32_bf16 v[144:147], a[228:231], a[124:127], v[144:147]
  v_cvt_pk_bf16_f32 v215, v110, v111
  v_mfma_f32_16x16x32_bf16 v[176:179], a[224:227], a[96:99], v[176:179]
  ds_read_b64_tr_b16 a[246:247], v20 offset:46176
  v_exp_f32 v250, v250
  v_mfma_f32_16x16x32_bf16 v[176:179], a[228:231], a[112:115], v[176:179]
  s_waitcnt lgkmcnt(4)
  v_exp_f32 v251, v251
  v_cvt_pk_bf16_f32 v231, v142, v143
  v_mfma_f32_16x16x32_bf16 v[84:87], a[232:235], a[100:103], v[84:87]
  ds_read_b64_tr_b16 a[248:249], v20 offset:33800
  v_cvt_pk_bf16_f32 v200, v80, v81
  v_mfma_f32_16x16x32_bf16 v[84:87], a[236:239], a[116:119], v[84:87]
  v_exp_f32 v252, v252
  v_mfma_f32_16x16x32_bf16 v[116:119], a[232:235], a[104:107], v[116:119]
  ds_read_b64_tr_b16 a[250:251], v20 offset:37928
  buffer_load_dwordx4 a[16:19], v15, s[64:67], s71 offen sc1
  v_exp_f32 v253, v253
  v_cvt_pk_bf16_f32 v216, v112, v113
  v_mfma_f32_16x16x32_bf16 v[116:119], a[236:239], a[120:123], v[116:119]
  v_cvt_pk_bf16_f32 v201, v82, v83
  v_mfma_f32_16x16x32_bf16 v[148:151], a[232:235], a[108:111], v[148:151]
  ds_read_b64_tr_b16 a[252:253], v20 offset:42056
  v_add_f32 v250, 1.0, v250
  v_cvt_pk_bf16_f32 v232, v144, v145
  v_mfma_f32_16x16x32_bf16 v[148:151], a[236:239], a[124:127], v[148:151]
  v_add_f32 v251, 1.0, v251
  v_cvt_pk_bf16_f32 v217, v114, v115
  v_mfma_f32_16x16x32_bf16 v[180:183], a[232:235], a[96:99], v[180:183]
  ds_read_b64_tr_b16 a[254:255], v20 offset:46184
  v_mfma_f32_16x16x32_bf16 v[180:183], a[236:239], a[112:115], v[180:183]
  s_waitcnt lgkmcnt(4)
  s_waitcnt vmcnt(17)
  s_barrier
  v_add_f32 v252, 1.0, v252
  v_cvt_pk_bf16_f32 v233, v146, v147
  v_mfma_f32_16x16x32_bf16 v[88:91], a[240:243], a[100:103], v[88:91]
  ds_read_b64_tr_b16 a[224:225], v19 offset:0
  v_cvt_pk_bf16_f32 v202, v84, v85
  v_mfma_f32_16x16x32_bf16 v[88:91], a[244:247], a[116:119], v[88:91]
  v_add_f32 v253, 1.0, v253
  v_mfma_f32_16x16x32_bf16 v[120:123], a[240:243], a[104:107], v[120:123]
  ds_read_b64_tr_b16 a[226:227], v19 offset:4128
  v_rcp_f32 v250, v250
  v_cvt_pk_bf16_f32 v218, v116, v117
  v_mfma_f32_16x16x32_bf16 v[120:123], a[244:247], a[120:123], v[120:123]
  v_cvt_pk_bf16_f32 v203, v86, v87
  v_mfma_f32_16x16x32_bf16 v[152:155], a[240:243], a[108:111], v[152:155]
  ds_read_b64_tr_b16 a[228:229], v19 offset:8256
  buffer_load_dwordx4 v4, s[52:55], s68 offen offset:3096 lds
  v_rcp_f32 v251, v251
  v_cvt_pk_bf16_f32 v234, v148, v149
  v_mfma_f32_16x16x32_bf16 v[152:155], a[244:247], a[124:127], v[152:155]
  v_rcp_f32 v252, v252
  v_cvt_pk_bf16_f32 v219, v118, v119
  v_mfma_f32_16x16x32_bf16 v[184:187], a[240:243], a[96:99], v[184:187]
  ds_read_b64_tr_b16 a[230:231], v19 offset:12384
  v_mfma_f32_16x16x32_bf16 v[184:187], a[244:247], a[112:115], v[184:187]
  s_waitcnt lgkmcnt(4)
  v_rcp_f32 v253, v253
  v_cvt_pk_bf16_f32 v235, v150, v151
  v_mfma_f32_16x16x32_bf16 v[92:95], a[248:251], a[100:103], v[92:95]
  ds_read_b64_tr_b16 a[232:233], v19 offset:8
  v_mul_f32 v52, v52, v250
  v_cvt_pk_bf16_f32 v204, v88, v89
  v_mfma_f32_16x16x32_bf16 v[92:95], a[252:255], a[116:119], v[92:95]
  v_mfma_f32_16x16x32_bf16 v[124:127], a[248:251], a[104:107], v[124:127]
  ds_read_b64_tr_b16 a[234:235], v19 offset:4136
  v_mul_f32 v53, v53, v251
  v_cvt_pk_bf16_f32 v220, v120, v121
  v_mfma_f32_16x16x32_bf16 v[124:127], a[252:255], a[120:123], v[124:127]
  v_mul_f32 v54, v54, v252
  v_cvt_pk_bf16_f32 v205, v90, v91
  v_mfma_f32_16x16x32_bf16 v[156:159], a[248:251], a[108:111], v[156:159]
  ds_read_b64_tr_b16 a[236:237], v19 offset:8264
  v_cvt_pk_bf16_f32 v236, v152, v153
  v_mfma_f32_16x16x32_bf16 v[156:159], a[252:255], a[124:127], v[156:159]
  v_mul_f32 v55, v55, v253
  v_cvt_pk_bf16_f32 v221, v122, v123
  v_mfma_f32_16x16x32_bf16 v[188:191], a[248:251], a[96:99], v[188:191]
  ds_read_b64_tr_b16 a[238:239], v19 offset:12392
  v_mfma_f32_16x16x32_bf16 v[188:191], a[252:255], a[112:115], v[188:191]
  s_waitcnt lgkmcnt(4)
  v_cvt_pk_bf16_f32 v237, v154, v155
  v_mfma_f32_16x16x32_bf16 v[160:163], a[224:227], a[128:131], v[160:163]
  s_add_u32 s73, s88, 60
  s_lshl_b32 s68, s73, 17
  s_lshl_b32 s70, s73, 13
  ds_read_b64_tr_b16 a[240:241], v19 offset:256
  ds_read_b64_tr_b16 a[242:243], v19 offset:4384
  ds_read_b64_tr_b16 a[244:245], v19 offset:8512
  v_mfma_f32_16x16x32_bf16 v[160:163], a[228:231], a[144:147], v[160:163]
  s_waitcnt lgkmcnt(3)
  s_add_u32 s71, s70, 4096
  s_add_u32 m0, s74, 66048
  ds_read_b64_tr_b16 a[246:247], v19 offset:12640
  ds_read_b64_tr_b16 a[248:249], v19 offset:264
  v_mfma_f32_16x16x32_bf16 v[164:167], a[232:235], a[128:131], v[164:167]
  ds_read_b64_tr_b16 a[250:251], v19 offset:4392
  ds_read_b64_tr_b16 a[252:253], v19 offset:8520
  ds_read_b64_tr_b16 a[254:255], v19 offset:12648
  buffer_load_dwordx4 v1, s[52:55], s68 offen offset:0 lds
  v_cvt_pk_bf16_f32 v206, v92, v93
  v_mfma_f32_16x16x32_bf16 v[164:167], a[236:239], a[144:147], v[164:167]
  s_waitcnt lgkmcnt(4)
  ds_read_b64_tr_b16 a[224:225], v19 offset:512
  ds_read_b64_tr_b16 a[226:227], v19 offset:4640
  v_cvt_pk_bf16_f32 v207, v94, v95
  v_cvt_pk_bf16_f32 v222, v124, v125
  v_mfma_f32_16x16x32_bf16 v[168:171], a[240:243], a[128:131], v[168:171]
  ds_read_b64_tr_b16 a[228:229], v19 offset:8768
  ds_read_b64_tr_b16 a[230:231], v19 offset:12896
  buffer_load_dwordx4 a[32:35], v15, s[64:67], s70 offen sc1
  v_cvt_pk_bf16_f32 v223, v126, v127
  v_cvt_pk_bf16_f32 v238, v156, v157
  v_mfma_f32_16x16x32_bf16 v[168:171], a[244:247], a[144:147], v[168:171]
  s_waitcnt lgkmcnt(4)
  ds_read_b64_tr_b16 a[232:233], v19 offset:520
  ds_read_b64_tr_b16 a[234:235], v19 offset:4648
  ds_read_b64_tr_b16 a[236:237], v19 offset:8776
  v_cvt_pk_bf16_f32 v239, v158, v159
  v_mfma_f32_16x16x32_bf16 v[172:175], a[248:251], a[128:131], v[172:175]
  ds_read_b64_tr_b16 a[238:239], v19 offset:12904
  ds_read_b64_tr_b16 a[240:241], v19 offset:768
  buffer_load_dwordx4 v2, s[52:55], s68 offen offset:1032 lds
  v_mfma_f32_16x16x32_bf16 v[172:175], a[252:255], a[144:147], v[172:175]
  s_waitcnt lgkmcnt(5)
  ds_read_b64_tr_b16 a[242:243], v19 offset:4896
  ds_read_b64_tr_b16 a[244:245], v19 offset:9024
  ds_read_b64_tr_b16 a[246:247], v19 offset:13152
  v_mfma_f32_16x16x32_bf16 v[176:179], a[224:227], a[128:131], v[176:179]
  ds_read_b64_tr_b16 a[248:249], v19 offset:776
  ds_read_b64_tr_b16 a[250:251], v19 offset:4904
  buffer_load_dwordx4 v3, s[52:55], s68 offen offset:2064 lds
  v_mfma_f32_16x16x32_bf16 v[176:179], a[228:231], a[144:147], v[176:179]
  s_waitcnt lgkmcnt(6)
  s_waitcnt vmcnt(16)
  s_barrier
  ds_read_b64_tr_b16 a[252:253], v19 offset:9032
  ds_read_b64_tr_b16 a[254:255], v19 offset:13160
  v_mfma_f32_16x16x32_bf16 v[180:183], a[232:235], a[128:131], v[180:183]
  ds_read_b64_tr_b16 a[224:225], v19 offset:16512
  ds_read_b64_tr_b16 a[226:227], v19 offset:20640
  buffer_load_dwordx4 a[48:51], v15, s[64:67], s71 offen sc1
  v_mfma_f32_16x16x32_bf16 v[180:183], a[236:239], a[144:147], v[180:183]
  s_waitcnt lgkmcnt(6)
  ds_read_b64_tr_b16 a[228:229], v19 offset:24768
  ds_read_b64_tr_b16 a[230:231], v19 offset:28896
  v_mfma_f32_16x16x32_bf16 v[184:187], a[240:243], a[128:131], v[184:187]
  ds_read_b64_tr_b16 a[232:233], v19 offset:16520
  ds_read_b64_tr_b16 a[234:235], v19 offset:20648
  buffer_load_dwordx4 v4, s[52:55], s68 offen offset:3096 lds
  v_mfma_f32_16x16x32_bf16 v[184:187], a[244:247], a[144:147], v[184:187]
  s_waitcnt lgkmcnt(6)
  ds_read_b64_tr_b16 a[236:237], v19 offset:24776
  ds_read_b64_tr_b16 a[238:239], v19 offset:28904
  v_mfma_f32_16x16x32_bf16 v[188:191], a[248:251], a[128:131], v[188:191]
  ds_read_b64_tr_b16 a[240:241], v19 offset:16768
  ds_read_b64_tr_b16 a[242:243], v19 offset:20896
  v_mfma_f32_16x16x32_bf16 v[188:191], a[252:255], a[144:147], v[188:191]
  s_waitcnt lgkmcnt(6)
  ds_read_b64_tr_b16 a[244:245], v19 offset:25024
  ds_read_b64_tr_b16 a[246:247], v19 offset:29152
  v_mfma_f32_16x16x32_bf16 v[160:163], a[224:227], a[160:163], v[160:163]
  s_add_u32 s73, s89, 60
  s_lshl_b32 s68, s73, 17
  s_lshl_b32 s70, s73, 13
  ds_read_b64_tr_b16 a[248:249], v19 offset:16776
  ds_read_b64_tr_b16 a[250:251], v19 offset:20904
  v_mfma_f32_16x16x32_bf16 v[160:163], a[228:231], a[176:179], v[160:163]
  s_waitcnt lgkmcnt(6)
  s_add_u32 s71, s70, 4096
  s_add_u32 m0, s74, 82560
  ds_read_b64_tr_b16 a[252:253], v19 offset:25032
  ds_read_b64_tr_b16 a[254:255], v19 offset:29160
  v_mul_f32 v246, 0xbfb8aa3b, v56
  v_mfma_f32_16x16x32_bf16 v[164:167], a[232:235], a[160:163], v[164:167]
  ds_read_b64_tr_b16 a[224:225], v19 offset:17024
  ds_read_b64_tr_b16 a[226:227], v19 offset:21152
  buffer_load_dwordx4 v1, s[52:55], s68 offen offset:0 lds
  v_mul_f32 v247, 0xbfb8aa3b, v57
  v_mfma_f32_16x16x32_bf16 v[164:167], a[236:239], a[176:179], v[164:167]
  s_waitcnt lgkmcnt(6)
  ds_read_b64_tr_b16 a[228:229], v19 offset:25280
  ds_read_b64_tr_b16 a[230:231], v19 offset:29408
  buffer_store_dwordx4 v[192:195], v12, s[76:79], 0 offen offset:0 sc1
  v_mul_f32 v248, 0xbfb8aa3b, v58
  v_mfma_f32_16x16x32_bf16 v[168:171], a[240:243], a[160:163], v[168:171]
  ds_read_b64_tr_b16 a[232:233], v19 offset:17032
  ds_read_b64_tr_b16 a[234:235], v19 offset:21160
  buffer_load_dwordx4 a[64:67], v15, s[64:67], s70 offen sc1
  v_mfma_f32_16x16x32_bf16 v[168:171], a[244:247], a[176:179], v[168:171]
  s_waitcnt lgkmcnt(6)
  ds_read_b64_tr_b16 a[236:237], v19 offset:25288
  ds_read_b64_tr_b16 a[238:239], v19 offset:29416
  buffer_store_dwordx4 v[196:199], v12, s[76:79], 0 offen offset:1024 sc1
  v_mul_f32 v249, 0xbfb8aa3b, v59
  v_mfma_f32_16x16x32_bf16 v[172:175], a[248:251], a[160:163], v[172:175]
  ds_read_b64_tr_b16 a[240:241], v19 offset:17280
  ds_read_b64_tr_b16 a[242:243], v19 offset:21408
  v_exp_f32 v246, v246
  v_mfma_f32_16x16x32_bf16 v[172:175], a[252:255], a[176:179], v[172:175]
  s_waitcnt lgkmcnt(6)
  ds_read_b64_tr_b16 a[244:245], v19 offset:25536
  ds_read_b64_tr_b16 a[246:247], v19 offset:29664
  buffer_load_dwordx4 v2, s[52:55], s68 offen offset:1032 lds
  v_mfma_f32_16x16x32_bf16 v[176:179], a[224:227], a[160:163], v[176:179]
  ds_read_b64_tr_b16 a[248:249], v19 offset:17288
  ds_read_b64_tr_b16 a[250:251], v19 offset:21416
  buffer_store_dwordx4 v[200:203], v12, s[76:79], 0 offen offset:2048 sc1
  v_exp_f32 v247, v247
  v_mfma_f32_16x16x32_bf16 v[176:179], a[228:231], a[176:179], v[176:179]
  s_waitcnt lgkmcnt(6)
  s_waitcnt vmcnt(18)
  s_barrier
  ds_read_b64_tr_b16 a[252:253], v19 offset:25544
  ds_read_b64_tr_b16 a[254:255], v19 offset:29672
  buffer_load_dwordx4 v3, s[52:55], s68 offen offset:2064 lds
  v_exp_f32 v248, v248
  v_mfma_f32_16x16x32_bf16 v[180:183], a[232:235], a[160:163], v[180:183]
  ds_read_b64_tr_b16 a[224:225], v19 offset:33024
  ds_read_b64_tr_b16 a[226:227], v19 offset:37152
  v_exp_f32 v249, v249
  v_mfma_f32_16x16x32_bf16 v[180:183], a[236:239], a[176:179], v[180:183]
  s_waitcnt lgkmcnt(6)
  ds_read_b64_tr_b16 a[228:229], v19 offset:41280
  ds_read_b64_tr_b16 a[230:231], v19 offset:45408
  buffer_store_dwordx4 v[204:207], v12, s[76:79], 0 offen offset:3072 sc1
  v_mfma_f32_16x16x32_bf16 v[184:187], a[240:243], a[160:163], v[184:187]
  ds_read_b64_tr_b16 a[232:233], v19 offset:33032
  ds_read_b64_tr_b16 a[234:235], v19 offset:37160
  buffer_load_dwordx4 a[80:83], v15, s[64:67], s71 offen sc1
  v_add_f32 v246, 1.0, v246
  v_mfma_f32_16x16x32_bf16 v[184:187], a[244:247], a[176:179], v[184:187]
  s_waitcnt lgkmcnt(6)
  ds_read_b64_tr_b16 a[236:237], v19 offset:41288
  ds_read_b64_tr_b16 a[238:239], v19 offset:45416
  buffer_load_dwordx4 v4, s[52:55], s68 offen offset:3096 lds
  v_add_f32 v247, 1.0, v247
  v_mfma_f32_16x16x32_bf16 v[188:191], a[248:251], a[160:163], v[188:191]
  ds_read_b64_tr_b16 a[240:241], v19 offset:33280
  ds_read_b64_tr_b16 a[242:243], v19 offset:37408
  v_mfma_f32_16x16x32_bf16 v[188:191], a[252:255], a[176:179], v[188:191]
  s_waitcnt lgkmcnt(6)
  ds_read_b64_tr_b16 a[244:245], v19 offset:41536
  ds_read_b64_tr_b16 a[246:247], v19 offset:45664
  v_mfma_f32_16x16x32_bf16 v[160:163], a[224:227], a[192:195], v[160:163]
  s_add_u32 s73, s90, 60
  s_lshl_b32 s68, s73, 17
  s_lshl_b32 s70, s73, 13
  ds_read_b64_tr_b16 a[248:249], v19 offset:33288
  ds_read_b64_tr_b16 a[250:251], v19 offset:37416
  v_mfma_f32_16x16x32_bf16 v[160:163], a[228:231], a[208:211], v[160:163]
  s_waitcnt lgkmcnt(6)
  s_add_u32 s71, s70, 4096
  s_add_u32 m0, s74, 99072
  ds_read_b64_tr_b16 a[252:253], v19 offset:41544
  ds_read_b64_tr_b16 a[254:255], v19 offset:45672
  v_add_f32 v248, 1.0, v248
  v_mfma_f32_16x16x32_bf16 v[164:167], a[232:235], a[192:195], v[164:167]
  ds_read_b64_tr_b16 a[224:225], v19 offset:33536
  ds_read_b64_tr_b16 a[226:227], v19 offset:37664
  buffer_load_dwordx4 v1, s[52:55], s68 offen offset:0 lds
  v_add_f32 v249, 1.0, v249
  v_mfma_f32_16x16x32_bf16 v[164:167], a[236:239], a[208:211], v[164:167]
  s_waitcnt lgkmcnt(6)
  ds_read_b64_tr_b16 a[228:229], v19 offset:41792
  ds_read_b64_tr_b16 a[230:231], v19 offset:45920
  buffer_store_dwordx4 v[208:211], v12, s[76:79], s83 offen offset:0 sc1
  v_rcp_f32 v246, v246
  v_mfma_f32_16x16x32_bf16 v[168:171], a[240:243], a[192:195], v[168:171]
  ds_read_b64_tr_b16 a[232:233], v19 offset:33544
  ds_read_b64_tr_b16 a[234:235], v19 offset:37672
  buffer_load_dwordx4 a[96:99], v15, s[64:67], s70 offen sc1
  v_mfma_f32_16x16x32_bf16 v[168:171], a[244:247], a[208:211], v[168:171]
  s_waitcnt lgkmcnt(6)
  ds_read_b64_tr_b16 a[236:237], v19 offset:41800
  ds_read_b64_tr_b16 a[238:239], v19 offset:45928
  buffer_store_dwordx4 v[212:215], v12, s[76:79], s83 offen offset:1024 sc1
  v_rcp_f32 v247, v247
  v_mfma_f32_16x16x32_bf16 v[172:175], a[248:251], a[192:195], v[172:175]
  ds_read_b64_tr_b16 a[240:241], v19 offset:33792
  ds_read_b64_tr_b16 a[242:243], v19 offset:37920
  v_rcp_f32 v248, v248
  v_mfma_f32_16x16x32_bf16 v[172:175], a[252:255], a[208:211], v[172:175]
  s_waitcnt lgkmcnt(6)
  ds_read_b64_tr_b16 a[244:245], v19 offset:42048
  ds_read_b64_tr_b16 a[246:247], v19 offset:46176
  buffer_load_dwordx4 v2, s[52:55], s68 offen offset:1032 lds
  v_mfma_f32_16x16x32_bf16 v[176:179], a[224:227], a[192:195], v[176:179]
  ds_read_b64_tr_b16 a[248:249], v19 offset:33800
  ds_read_b64_tr_b16 a[250:251], v19 offset:37928
  buffer_store_dwordx4 v[216:219], v12, s[76:79], s83 offen offset:2048 sc1
  v_rcp_f32 v249, v249
  v_mfma_f32_16x16x32_bf16 v[176:179], a[228:231], a[208:211], v[176:179]
  s_waitcnt lgkmcnt(6)
  s_waitcnt vmcnt(22)
  s_barrier
  ds_read_b64_tr_b16 a[252:253], v19 offset:42056
  ds_read_b64_tr_b16 a[254:255], v19 offset:46184
  buffer_load_dwordx4 v3, s[52:55], s68 offen offset:2064 lds
  v_mul_f32 v56, v56, v246
  v_mfma_f32_16x16x32_bf16 v[180:183], a[232:235], a[192:195], v[180:183]
  ds_read_b64_tr_b16 a[224:225], v19 offset:49536
  ds_read_b64_tr_b16 a[226:227], v19 offset:53664
  v_mul_f32 v57, v57, v247
  v_mfma_f32_16x16x32_bf16 v[180:183], a[236:239], a[208:211], v[180:183]
  s_waitcnt lgkmcnt(6)
  ds_read_b64_tr_b16 a[228:229], v19 offset:57792
  ds_read_b64_tr_b16 a[230:231], v19 offset:61920
  buffer_store_dwordx4 v[220:223], v12, s[76:79], s83 offen offset:3072 sc1
  v_mfma_f32_16x16x32_bf16 v[184:187], a[240:243], a[192:195], v[184:187]
  ds_read_b64_tr_b16 a[232:233], v19 offset:49544
  ds_read_b64_tr_b16 a[234:235], v19 offset:53672
  buffer_load_dwordx4 a[112:115], v15, s[64:67], s71 offen sc1
  v_mul_f32 v58, v58, v248
  v_mfma_f32_16x16x32_bf16 v[184:187], a[244:247], a[208:211], v[184:187]
  s_waitcnt lgkmcnt(6)
  ds_read_b64_tr_b16 a[236:237], v19 offset:57800
  ds_read_b64_tr_b16 a[238:239], v19 offset:61928
  buffer_load_dwordx4 v4, s[52:55], s68 offen offset:3096 lds
  v_mul_f32 v59, v59, v249
  v_mfma_f32_16x16x32_bf16 v[188:191], a[248:251], a[192:195], v[188:191]
  ds_read_b64_tr_b16 a[240:241], v19 offset:49792
  ds_read_b64_tr_b16 a[242:243], v19 offset:53920
  v_mfma_f32_16x16x32_bf16 v[188:191], a[252:255], a[208:211], v[188:191]
  s_waitcnt lgkmcnt(6)
  ds_read_b64_tr_b16 a[244:245], v19 offset:58048
  ds_read_b64_tr_b16 a[246:247], v19 offset:62176
  v_mfma_f32_16x16x32_bf16 v[160:163], a[224:227], a[0:3], v[160:163]
  s_add_u32 s73, s91, 60
  s_lshl_b32 s68, s73, 17
  s_lshl_b32 s70, s73, 13
  ds_read_b64_tr_b16 a[248:249], v19 offset:49800
  ds_read_b64_tr_b16 a[250:251], v19 offset:53928
  v_mfma_f32_16x16x32_bf16 v[160:163], a[228:231], a[16:19], v[160:163]
  s_waitcnt lgkmcnt(6)
  s_add_u32 s71, s70, 4096
  s_add_u32 m0, s74, 0
  ds_read_b64_tr_b16 a[252:253], v19 offset:58056
  ds_read_b64_tr_b16 a[254:255], v19 offset:62184
  v_mul_f32 v250, 0xbfb8aa3b, v60
  v_mfma_f32_16x16x32_bf16 v[164:167], a[232:235], a[0:3], v[164:167]
  ds_read_b64_tr_b16 a[224:225], v19 offset:50048
  ds_read_b64_tr_b16 a[226:227], v19 offset:54176
  buffer_load_dwordx4 v1, s[52:55], s68 offen offset:0 lds
  v_mul_f32 v251, 0xbfb8aa3b, v61
  v_mfma_f32_16x16x32_bf16 v[164:167], a[236:239], a[16:19], v[164:167]
  s_waitcnt lgkmcnt(6)
  ds_read_b64_tr_b16 a[228:229], v19 offset:58304
  ds_read_b64_tr_b16 a[230:231], v19 offset:62432
  buffer_store_dwordx4 v[224:227], v12, s[76:79], s84 offen offset:0 sc1
  v_mul_f32 v252, 0xbfb8aa3b, v62
  v_mfma_f32_16x16x32_bf16 v[168:171], a[240:243], a[0:3], v[168:171]
  ds_read_b64_tr_b16 a[232:233], v19 offset:50056
  ds_read_b64_tr_b16 a[234:235], v19 offset:54184
  buffer_load_dwordx4 a[128:131], v15, s[64:67], s70 offen sc1
  v_mfma_f32_16x16x32_bf16 v[168:171], a[244:247], a[16:19], v[168:171]
  s_waitcnt lgkmcnt(6)
  ds_read_b64_tr_b16 a[236:237], v19 offset:58312
  ds_read_b64_tr_b16 a[238:239], v19 offset:62440
  buffer_store_dwordx4 v[228:231], v12, s[76:79], s84 offen offset:1024 sc1
  v_mul_f32 v253, 0xbfb8aa3b, v63
  v_mfma_f32_16x16x32_bf16 v[172:175], a[248:251], a[0:3], v[172:175]
  ds_read_b64_tr_b16 a[240:241], v19 offset:50304
  ds_read_b64_tr_b16 a[242:243], v19 offset:54432
  v_exp_f32 v250, v250
  v_mfma_f32_16x16x32_bf16 v[172:175], a[252:255], a[16:19], v[172:175]
  s_waitcnt lgkmcnt(6)
  ds_read_b64_tr_b16 a[244:245], v19 offset:58560
  ds_read_b64_tr_b16 a[246:247], v19 offset:62688
  buffer_load_dwordx4 v2, s[52:55], s68 offen offset:1032 lds
  v_mfma_f32_16x16x32_bf16 v[176:179], a[224:227], a[0:3], v[176:179]
  ds_read_b64_tr_b16 a[248:249], v19 offset:50312
  ds_read_b64_tr_b16 a[250:251], v19 offset:54440
  buffer_store_dwordx4 v[232:235], v12, s[76:79], s84 offen offset:2048 sc1
  v_exp_f32 v251, v251
  v_mfma_f32_16x16x32_bf16 v[176:179], a[228:231], a[16:19], v[176:179]
  s_waitcnt lgkmcnt(6)
  s_waitcnt vmcnt(26)
  s_barrier
  ds_read_b64_tr_b16 a[252:253], v19 offset:58568
  ds_read_b64_tr_b16 a[254:255], v19 offset:62696
  buffer_load_dwordx4 v3, s[52:55], s68 offen offset:2064 lds
  v_exp_f32 v252, v252
  v_mfma_f32_16x16x32_bf16 v[180:183], a[232:235], a[0:3], v[180:183]
  ds_read_b64_tr_b16 a[224:225], v20 offset:0
  ds_read_b64_tr_b16 a[226:227], v20 offset:4128
  v_exp_f32 v253, v253
  v_mfma_f32_16x16x32_bf16 v[180:183], a[236:239], a[16:19], v[180:183]
  s_waitcnt lgkmcnt(6)
  ds_read_b64_tr_b16 a[228:229], v20 offset:8256
  ds_read_b64_tr_b16 a[230:231], v20 offset:12384
  buffer_store_dwordx4 v[236:239], v12, s[76:79], s84 offen offset:3072 sc1
  v_mfma_f32_16x16x32_bf16 v[184:187], a[240:243], a[0:3], v[184:187]
  ds_read_b64_tr_b16 a[232:233], v20 offset:8
  ds_read_b64_tr_b16 a[234:235], v20 offset:4136
  buffer_load_dwordx4 a[144:147], v15, s[64:67], s71 offen sc1
  v_add_f32 v250, 1.0, v250
  v_mfma_f32_16x16x32_bf16 v[184:187], a[244:247], a[16:19], v[184:187]
  s_waitcnt lgkmcnt(6)
  ds_read_b64_tr_b16 a[236:237], v20 offset:8264
  ds_read_b64_tr_b16 a[238:239], v20 offset:12392
  buffer_load_dwordx4 v4, s[52:55], s68 offen offset:3096 lds
  v_add_f32 v251, 1.0, v251
  v_mfma_f32_16x16x32_bf16 v[188:191], a[248:251], a[0:3], v[188:191]
  ds_read_b64_tr_b16 a[240:241], v20 offset:256
  ds_read_b64_tr_b16 a[242:243], v20 offset:4384
  v_mfma_f32_16x16x32_bf16 v[188:191], a[252:255], a[16:19], v[188:191]
  s_waitcnt lgkmcnt(6)
  ds_read_b64_tr_b16 a[244:245], v20 offset:8512
  ds_read_b64_tr_b16 a[246:247], v20 offset:12640
  v_mfma_f32_16x16x32_bf16 v[160:163], a[224:227], a[32:35], v[160:163]
  ds_read_b64_tr_b16 a[248:249], v20 offset:264
  ds_read_b64_tr_b16 a[250:251], v20 offset:4392
  v_mfma_f32_16x16x32_bf16 v[160:163], a[228:231], a[48:51], v[160:163]
  s_waitcnt lgkmcnt(6)
  ds_read_b64_tr_b16 a[252:253], v20 offset:8520
  ds_read_b64_tr_b16 a[254:255], v20 offset:12648
  v_add_f32 v252, 1.0, v252
  v_mfma_f32_16x16x32_bf16 v[164:167], a[232:235], a[32:35], v[164:167]
  ds_read_b64_tr_b16 a[224:225], v20 offset:512
  ds_read_b64_tr_b16 a[226:227], v20 offset:4640
  buffer_load_dwordx4 v[64:67], v21, s[36:39], 0 offen offset:0
  v_add_f32 v253, 1.0, v253
  v_mfma_f32_16x16x32_bf16 v[164:167], a[236:239], a[48:51], v[164:167]
  s_waitcnt lgkmcnt(6)
  ds_read_b64_tr_b16 a[228:229], v20 offset:8768
  ds_read_b64_tr_b16 a[230:231], v20 offset:12896
  v_rcp_f32 v250, v250
  v_mfma_f32_16x16x32_bf16 v[168:171], a[240:243], a[32:35], v[168:171]
  ds_read_b64_tr_b16 a[232:233], v20 offset:520
  ds_read_b64_tr_b16 a[234:235], v20 offset:4648
  v_mfma_f32_16x16x32_bf16 v[168:171], a[244:247], a[48:51], v[168:171]
  s_waitcnt lgkmcnt(6)
  ds_read_b64_tr_b16 a[236:237], v20 offset:8776
  ds_read_b64_tr_b16 a[238:239], v20 offset:12904
  buffer_load_dwordx4 v[68:71], v21, s[36:39], 0 offen offset:64
  v_rcp_f32 v251, v251
  v_mfma_f32_16x16x32_bf16 v[172:175], a[248:251], a[32:35], v[172:175]
  ds_read_b64_tr_b16 a[240:241], v20 offset:768
  ds_read_b64_tr_b16 a[242:243], v20 offset:4896
  v_rcp_f32 v252, v252
  v_mfma_f32_16x16x32_bf16 v[172:175], a[252:255], a[48:51], v[172:175]
  s_waitcnt lgkmcnt(6)
  ds_read_b64_tr_b16 a[244:245], v20 offset:9024
  ds_read_b64_tr_b16 a[246:247], v20 offset:13152
  v_mfma_f32_16x16x32_bf16 v[176:179], a[224:227], a[32:35], v[176:179]
  ds_read_b64_tr_b16 a[248:249], v20 offset:776
  ds_read_b64_tr_b16 a[250:251], v20 offset:4904
  buffer_load_dwordx4 v[72:75], v21, s[36:39], 0 offen offset:128
  v_rcp_f32 v253, v253
  v_mfma_f32_16x16x32_bf16 v[176:179], a[228:231], a[48:51], v[176:179]
  s_waitcnt lgkmcnt(6)
  s_waitcnt vmcnt(23)
  s_barrier
  ds_read_b64_tr_b16 a[252:253], v20 offset:9032
  ds_read_b64_tr_b16 a[254:255], v20 offset:13160
  v_mul_f32 v60, v60, v250
  v_mfma_f32_16x16x32_bf16 v[180:183], a[232:235], a[32:35], v[180:183]
  ds_read_b64_tr_b16 a[224:225], v20 offset:16512
  ds_read_b64_tr_b16 a[226:227], v20 offset:20640
  v_mul_f32 v61, v61, v251
  v_mfma_f32_16x16x32_bf16 v[180:183], a[236:239], a[48:51], v[180:183]
  s_waitcnt lgkmcnt(6)
  ds_read_b64_tr_b16 a[228:229], v20 offset:24768
  ds_read_b64_tr_b16 a[230:231], v20 offset:28896
  buffer_load_dwordx4 v[76:79], v21, s[36:39], 0 offen offset:192
  v_mfma_f32_16x16x32_bf16 v[184:187], a[240:243], a[32:35], v[184:187]
  ds_read_b64_tr_b16 a[232:233], v20 offset:16520
  ds_read_b64_tr_b16 a[234:235], v20 offset:20648
  v_mul_f32 v62, v62, v252
  v_mfma_f32_16x16x32_bf16 v[184:187], a[244:247], a[48:51], v[184:187]
  s_waitcnt lgkmcnt(6)
  ds_read_b64_tr_b16 a[236:237], v20 offset:24776
  ds_read_b64_tr_b16 a[238:239], v20 offset:28904
  v_mul_f32 v63, v63, v253
  v_mfma_f32_16x16x32_bf16 v[188:191], a[248:251], a[32:35], v[188:191]
  ds_read_b64_tr_b16 a[240:241], v20 offset:16768
  ds_read_b64_tr_b16 a[242:243], v20 offset:20896
  v_mfma_f32_16x16x32_bf16 v[188:191], a[252:255], a[48:51], v[188:191]
  s_waitcnt lgkmcnt(6)
  ds_read_b64_tr_b16 a[244:245], v20 offset:25024
  ds_read_b64_tr_b16 a[246:247], v20 offset:29152
  v_mfma_f32_16x16x32_bf16 v[160:163], a[224:227], a[64:67], v[160:163]
  ds_read_b64_tr_b16 a[248:249], v20 offset:16776
  ds_read_b64_tr_b16 a[250:251], v20 offset:20904
  v_mfma_f32_16x16x32_bf16 v[160:163], a[228:231], a[80:83], v[160:163]
  s_waitcnt lgkmcnt(6)
  ds_read_b64_tr_b16 a[252:253], v20 offset:25032
  ds_read_b64_tr_b16 a[254:255], v20 offset:29160
  v_mfma_f32_16x16x32_bf16 v[164:167], a[232:235], a[64:67], v[164:167]
  ds_read_b64_tr_b16 a[224:225], v20 offset:17024
  ds_read_b64_tr_b16 a[226:227], v20 offset:21152
  v_mfma_f32_16x16x32_bf16 v[164:167], a[236:239], a[80:83], v[164:167]
  s_waitcnt lgkmcnt(6)
  ds_read_b64_tr_b16 a[228:229], v20 offset:25280
  ds_read_b64_tr_b16 a[230:231], v20 offset:29408
  v_mfma_f32_16x16x32_bf16 v[168:171], a[240:243], a[64:67], v[168:171]
  ds_read_b64_tr_b16 a[232:233], v20 offset:17032
  ds_read_b64_tr_b16 a[234:235], v20 offset:21160
  v_mfma_f32_16x16x32_bf16 v[168:171], a[244:247], a[80:83], v[168:171]
  s_waitcnt lgkmcnt(6)
  ds_read_b64_tr_b16 a[236:237], v20 offset:25288
  ds_read_b64_tr_b16 a[238:239], v20 offset:29416
  v_mfma_f32_16x16x32_bf16 v[172:175], a[248:251], a[64:67], v[172:175]
  ds_read_b64_tr_b16 a[240:241], v20 offset:17280
  ds_read_b64_tr_b16 a[242:243], v20 offset:21408
  v_mfma_f32_16x16x32_bf16 v[172:175], a[252:255], a[80:83], v[172:175]
  s_waitcnt lgkmcnt(6)
  ds_read_b64_tr_b16 a[244:245], v20 offset:25536
  ds_read_b64_tr_b16 a[246:247], v20 offset:29664
  v_mfma_f32_16x16x32_bf16 v[176:179], a[224:227], a[64:67], v[176:179]
  ds_read_b64_tr_b16 a[248:249], v20 offset:17288
  ds_read_b64_tr_b16 a[250:251], v20 offset:21416
  v_mfma_f32_16x16x32_bf16 v[176:179], a[228:231], a[80:83], v[176:179]
  s_waitcnt lgkmcnt(6)
  s_waitcnt vmcnt(6)
  s_barrier
  ds_read_b64_tr_b16 a[252:253], v20 offset:25544
  ds_read_b64_tr_b16 a[254:255], v20 offset:29672
  v_mfma_f32_16x16x32_bf16 v[180:183], a[232:235], a[64:67], v[180:183]
  ds_read_b64_tr_b16 a[224:225], v20 offset:33024
  ds_read_b64_tr_b16 a[226:227], v20 offset:37152
  s_mov_b64 exec, 1
  buffer_store_dword v11, v30, s[44:47], 0 offen offset:4 sc1
  s_mov_b64 exec, -1
  v_mfma_f32_16x16x32_bf16 v[180:183], a[236:239], a[80:83], v[180:183]
  s_waitcnt lgkmcnt(6)
  ds_read_b64_tr_b16 a[228:229], v20 offset:41280
  ds_read_b64_tr_b16 a[230:231], v20 offset:45408
  v_mfma_f32_16x16x32_bf16 v[184:187], a[240:243], a[64:67], v[184:187]
  ds_read_b64_tr_b16 a[232:233], v20 offset:33032
  ds_read_b64_tr_b16 a[234:235], v20 offset:37160
  v_mfma_f32_16x16x32_bf16 v[184:187], a[244:247], a[80:83], v[184:187]
  s_waitcnt lgkmcnt(6)
  ds_read_b64_tr_b16 a[236:237], v20 offset:41288
  ds_read_b64_tr_b16 a[238:239], v20 offset:45416
  v_mfma_f32_16x16x32_bf16 v[188:191], a[248:251], a[64:67], v[188:191]
  ds_read_b64_tr_b16 a[240:241], v20 offset:33280
  ds_read_b64_tr_b16 a[242:243], v20 offset:37408
  v_mfma_f32_16x16x32_bf16 v[188:191], a[252:255], a[80:83], v[188:191]
  s_waitcnt lgkmcnt(6)
  ds_read_b64_tr_b16 a[244:245], v20 offset:41536
  ds_read_b64_tr_b16 a[246:247], v20 offset:45664
  v_mfma_f32_16x16x32_bf16 v[160:163], a[224:227], a[96:99], v[160:163]
  ds_read_b64_tr_b16 a[248:249], v20 offset:33288
  ds_read_b64_tr_b16 a[250:251], v20 offset:37416
  v_mfma_f32_16x16x32_bf16 v[160:163], a[228:231], a[112:115], v[160:163]
  s_waitcnt lgkmcnt(6)
  ds_read_b64_tr_b16 a[252:253], v20 offset:41544
  ds_read_b64_tr_b16 a[254:255], v20 offset:45672
  v_mfma_f32_16x16x32_bf16 v[164:167], a[232:235], a[96:99], v[164:167]
  ds_read_b64_tr_b16 a[224:225], v20 offset:33536
  ds_read_b64_tr_b16 a[226:227], v20 offset:37664
  v_mfma_f32_16x16x32_bf16 v[164:167], a[236:239], a[112:115], v[164:167]
  s_waitcnt lgkmcnt(6)
  ds_read_b64_tr_b16 a[228:229], v20 offset:41792
  ds_read_b64_tr_b16 a[230:231], v20 offset:45920
  v_mfma_f32_16x16x32_bf16 v[168:171], a[240:243], a[96:99], v[168:171]
  ds_read_b64_tr_b16 a[232:233], v20 offset:33544
  ds_read_b64_tr_b16 a[234:235], v20 offset:37672
  v_mfma_f32_16x16x32_bf16 v[168:171], a[244:247], a[112:115], v[168:171]
  s_waitcnt lgkmcnt(6)
  ds_read_b64_tr_b16 a[236:237], v20 offset:41800
  ds_read_b64_tr_b16 a[238:239], v20 offset:45928
  v_mfma_f32_16x16x32_bf16 v[172:175], a[248:251], a[96:99], v[172:175]
  ds_read_b64_tr_b16 a[240:241], v20 offset:33792
  ds_read_b64_tr_b16 a[242:243], v20 offset:37920
  v_mfma_f32_16x16x32_bf16 v[172:175], a[252:255], a[112:115], v[172:175]
  s_waitcnt lgkmcnt(6)
  ds_read_b64_tr_b16 a[244:245], v20 offset:42048
  ds_read_b64_tr_b16 a[246:247], v20 offset:46176
  v_mfma_f32_16x16x32_bf16 v[176:179], a[224:227], a[96:99], v[176:179]
  ds_read_b64_tr_b16 a[248:249], v20 offset:33800
  ds_read_b64_tr_b16 a[250:251], v20 offset:37928
  v_mfma_f32_16x16x32_bf16 v[176:179], a[228:231], a[112:115], v[176:179]
  s_waitcnt lgkmcnt(6)
  s_waitcnt vmcnt(5)
  s_barrier
  ds_read_b64_tr_b16 a[252:253], v20 offset:42056
  ds_read_b64_tr_b16 a[254:255], v20 offset:46184
  v_mfma_f32_16x16x32_bf16 v[180:183], a[232:235], a[96:99], v[180:183]
  ds_read_b64_tr_b16 a[224:225], v19 offset:0
  ds_read_b64_tr_b16 a[226:227], v19 offset:4128
  v_mfma_f32_16x16x32_bf16 v[180:183], a[236:239], a[112:115], v[180:183]
  s_waitcnt lgkmcnt(6)
  ds_read_b64_tr_b16 a[228:229], v19 offset:8256
  ds_read_b64_tr_b16 a[230:231], v19 offset:12384
  v_mfma_f32_16x16x32_bf16 v[184:187], a[240:243], a[96:99], v[184:187]
  ds_read_b64_tr_b16 a[232:233], v19 offset:8
  ds_read_b64_tr_b16 a[234:235], v19 offset:4136
  v_mfma_f32_16x16x32_bf16 v[184:187], a[244:247], a[112:115], v[184:187]
  s_waitcnt lgkmcnt(6)
  ds_read_b64_tr_b16 a[236:237], v19 offset:8264
  ds_read_b64_tr_b16 a[238:239], v19 offset:12392
  v_mfma_f32_16x16x32_bf16 v[188:191], a[248:251], a[96:99], v[188:191]
  ds_read_b64_tr_b16 a[240:241], v19 offset:256
  ds_read_b64_tr_b16 a[242:243], v19 offset:4384
  v_mfma_f32_16x16x32_bf16 v[188:191], a[252:255], a[112:115], v[188:191]
  s_waitcnt lgkmcnt(6)
  ds_read_b64_tr_b16 a[244:245], v19 offset:8512
  ds_read_b64_tr_b16 a[246:247], v19 offset:12640
  v_mfma_f32_16x16x32_bf16 v[160:163], a[224:227], a[128:131], v[160:163]
  ds_read_b64_tr_b16 a[248:249], v19 offset:264
  ds_read_b64_tr_b16 a[250:251], v19 offset:4392
  v_mfma_f32_16x16x32_bf16 v[160:163], a[228:231], a[144:147], v[160:163]
  s_waitcnt lgkmcnt(6)
  ds_read_b64_tr_b16 a[252:253], v19 offset:8520
  ds_read_b64_tr_b16 a[254:255], v19 offset:12648
  v_mfma_f32_16x16x32_bf16 v[164:167], a[232:235], a[128:131], v[164:167]
  ds_read_b64_tr_b16 a[224:225], v19 offset:512
  ds_read_b64_tr_b16 a[226:227], v19 offset:4640
  buffer_load_dword v10, v13, s[44:47], 0 offen offset:4 sc1
  v_mfma_f32_16x16x32_bf16 v[164:167], a[236:239], a[144:147], v[164:167]
  s_waitcnt lgkmcnt(6)
  ds_read_b64_tr_b16 a[228:229], v19 offset:8768
  ds_read_b64_tr_b16 a[230:231], v19 offset:12896
  v_mfma_f32_16x16x32_bf16 v[168:171], a[240:243], a[128:131], v[168:171]
  ds_read_b64_tr_b16 a[232:233], v19 offset:520
  ds_read_b64_tr_b16 a[234:235], v19 offset:4648
  v_mfma_f32_16x16x32_bf16 v[168:171], a[244:247], a[144:147], v[168:171]
  s_waitcnt lgkmcnt(6)
  ds_read_b64_tr_b16 a[236:237], v19 offset:8776
  ds_read_b64_tr_b16 a[238:239], v19 offset:12904
  v_mfma_f32_16x16x32_bf16 v[172:175], a[248:251], a[128:131], v[172:175]
  ds_read_b64_tr_b16 a[240:241], v19 offset:768
  ds_read_b64_tr_b16 a[242:243], v19 offset:4896
  v_mfma_f32_16x16x32_bf16 v[172:175], a[252:255], a[144:147], v[172:175]
  s_waitcnt lgkmcnt(6)
  ds_read_b64_tr_b16 a[244:245], v19 offset:9024
  ds_read_b64_tr_b16 a[246:247], v19 offset:13152
  v_mfma_f32_16x16x32_bf16 v[176:179], a[224:227], a[128:131], v[176:179]
  ds_read_b64_tr_b16 a[248:249], v19 offset:776
  ds_read_b64_tr_b16 a[250:251], v19 offset:4904
  v_mfma_f32_16x16x32_bf16 v[176:179], a[228:231], a[144:147], v[176:179]
  s_waitcnt lgkmcnt(6)
  ds_read_b64_tr_b16 a[252:253], v19 offset:9032
  ds_read_b64_tr_b16 a[254:255], v19 offset:13160
  v_mfma_f32_16x16x32_bf16 v[180:183], a[232:235], a[128:131], v[180:183]
  v_mfma_f32_16x16x32_bf16 v[180:183], a[236:239], a[144:147], v[180:183]
  s_waitcnt lgkmcnt(4)
  v_mfma_f32_16x16x32_bf16 v[184:187], a[240:243], a[128:131], v[184:187]
  v_mfma_f32_16x16x32_bf16 v[184:187], a[244:247], a[144:147], v[184:187]
  s_waitcnt lgkmcnt(0)
  v_mfma_f32_16x16x32_bf16 v[188:191], a[248:251], a[128:131], v[188:191]
  v_mfma_f32_16x16x32_bf16 v[188:191], a[252:255], a[144:147], v[188:191]
  s_waitcnt vmcnt(0)
  v_cmp_ne_u32 vcc, 1, v10
  s_nop 1
  s_cbranch_vccnz LC_sloww
LC_backw:
  buffer_load_dwordx4 v[192:195], v27, s[76:79], 0 offen offset:0 sc1
  buffer_load_dwordx4 v[196:199], v27, s[76:79], 0 offen offset:1024 sc1
  buffer_load_dwordx4 v[200:203], v27, s[76:79], 0 offen offset:2048 sc1
  buffer_load_dwordx4 v[204:207], v27, s[76:79], 0 offen offset:3072 sc1
  buffer_load_dwordx4 v[208:211], v28, s[76:79], 0 offen offset:0 sc1
  buffer_load_dwordx4 v[212:215], v28, s[76:79], 0 offen offset:1024 sc1
  buffer_load_dwordx4 v[216:219], v28, s[76:79], 0 offen offset:2048 sc1
  buffer_load_dwordx4 v[220:223], v28, s[76:79], 0 offen offset:3072 sc1
  buffer_load_dwordx4 v[224:227], v29, s[76:79], 0 offen offset:0 sc1
  buffer_load_dwordx4 v[228:231], v29, s[76:79], 0 offen offset:1024 sc1
  buffer_load_dwordx4 v[232:235], v29, s[76:79], 0 offen offset:2048 sc1
  buffer_load_dwordx4 v[236:239], v29, s[76:79], 0 offen offset:3072 sc1
  s_waitcnt vmcnt(11)
  v_mfma_f32_16x16x32_bf16 v[160:163], v[242:245], v[192:195], v[160:163]
  v_mfma_f32_16x16x32_bf16 v[164:167], v[240:243], v[192:195], v[164:167]
  s_waitcnt vmcnt(10)
  v_mfma_f32_16x16x32_bf16 v[168:171], v[242:245], v[196:199], v[168:171]
  v_mfma_f32_16x16x32_bf16 v[172:175], v[240:243], v[196:199], v[172:175]
  s_waitcnt vmcnt(9)
  v_mfma_f32_16x16x32_bf16 v[176:179], v[242:245], v[200:203], v[176:179]
  v_mfma_f32_16x16x32_bf16 v[180:183], v[240:243], v[200:203], v[180:183]
  s_waitcnt vmcnt(8)
  v_mfma_f32_16x16x32_bf16 v[184:187], v[242:245], v[204:207], v[184:187]
  v_mfma_f32_16x16x32_bf16 v[188:191], v[240:243], v[204:207], v[188:191]
  s_waitcnt vmcnt(7)
  v_mfma_f32_16x16x32_bf16 v[160:163], v[242:245], v[208:211], v[160:163]
  v_mfma_f32_16x16x32_bf16 v[164:167], v[240:243], v[208:211], v[164:167]
  s_waitcnt vmcnt(6)
  v_mfma_f32_16x16x32_bf16 v[168:171], v[242:245], v[212:215], v[168:171]
  v_mfma_f32_16x16x32_bf16 v[172:175], v[240:243], v[212:215], v[172:175]
  s_waitcnt vmcnt(5)
  v_mfma_f32_16x16x32_bf16 v[176:179], v[242:245], v[216:219], v[176:179]
  v_mfma_f32_16x16x32_bf16 v[180:183], v[240:243], v[216:219], v[180:183]
  s_waitcnt vmcnt(4)
  v_mfma_f32_16x16x32_bf16 v[184:187], v[242:245], v[220:223], v[184:187]
  v_mfma_f32_16x16x32_bf16 v[188:191], v[240:243], v[220:223], v[188:191]
  s_waitcnt vmcnt(3)
  v_mfma_f32_16x16x32_bf16 v[160:163], v[242:245], v[224:227], v[160:163]
  v_mfma_f32_16x16x32_bf16 v[164:167], v[240:243], v[224:227], v[164:167]
  s_waitcnt vmcnt(2)
  v_mfma_f32_16x16x32_bf16 v[168:171], v[242:245], v[228:231], v[168:171]
  v_mfma_f32_16x16x32_bf16 v[172:175], v[240:243], v[228:231], v[172:175]
  s_waitcnt vmcnt(1)
  v_mfma_f32_16x16x32_bf16 v[176:179], v[242:245], v[232:235], v[176:179]
  v_mfma_f32_16x16x32_bf16 v[180:183], v[240:243], v[232:235], v[180:183]
  s_waitcnt vmcnt(0)
  v_mfma_f32_16x16x32_bf16 v[184:187], v[242:245], v[236:239], v[184:187]
  v_mfma_f32_16x16x32_bf16 v[188:191], v[240:243], v[236:239], v[188:191]
  s_nop 7
  v_lshlrev_b32 v246, 16, v64
  v_and_b32 v247, 0xffff0000, v64
  v_fmac_f32 v246, v160, v32
  v_fmac_f32 v247, v161, v33
  v_cvt_pk_bf16_f32 v64, v246, v247
  v_lshlrev_b32 v246, 16, v65
  v_and_b32 v247, 0xffff0000, v65
  v_fmac_f32 v246, v162, v34
  v_fmac_f32 v247, v163, v35
  v_cvt_pk_bf16_f32 v65, v246, v247
  v_lshlrev_b32 v246, 16, v66
  v_and_b32 v247, 0xffff0000, v66
  v_fmac_f32 v246, v164, v36
  v_fmac_f32 v247, v165, v37
  v_cvt_pk_bf16_f32 v66, v246, v247
  v_lshlrev_b32 v246, 16, v67
  v_and_b32 v247, 0xffff0000, v67
  v_fmac_f32 v246, v166, v38
  v_fmac_f32 v247, v167, v39
  v_cvt_pk_bf16_f32 v67, v246, v247
  buffer_store_dwordx4 v[64:67], v21, s[40:43], 0 offen offset:0
  v_lshlrev_b32 v246, 16, v68
  v_and_b32 v247, 0xffff0000, v68
  v_fmac_f32 v246, v168, v40
  v_fmac_f32 v247, v169, v41
  v_cvt_pk_bf16_f32 v68, v246, v247
  v_lshlrev_b32 v246, 16, v69
  v_and_b32 v247, 0xffff0000, v69
  v_fmac_f32 v246, v170, v42
  v_fmac_f32 v247, v171, v43
  v_cvt_pk_bf16_f32 v69, v246, v247
  v_lshlrev_b32 v246, 16, v70
  v_and_b32 v247, 0xffff0000, v70
  v_fmac_f32 v246, v172, v44
  v_fmac_f32 v247, v173, v45
  v_cvt_pk_bf16_f32 v70, v246, v247
  v_lshlrev_b32 v246, 16, v71
  v_and_b32 v247, 0xffff0000, v71
  v_fmac_f32 v246, v174, v46
  v_fmac_f32 v247, v175, v47
  v_cvt_pk_bf16_f32 v71, v246, v247
  buffer_store_dwordx4 v[68:71], v21, s[40:43], 0 offen offset:64
  v_lshlrev_b32 v246, 16, v72
  v_and_b32 v247, 0xffff0000, v72
  v_fmac_f32 v246, v176, v48
  v_fmac_f32 v247, v177, v49
  v_cvt_pk_bf16_f32 v72, v246, v247
  v_lshlrev_b32 v246, 16, v73
  v_and_b32 v247, 0xffff0000, v73
  v_fmac_f32 v246, v178, v50
  v_fmac_f32 v247, v179, v51
  v_cvt_pk_bf16_f32 v73, v246, v247
  v_lshlrev_b32 v246, 16, v74
  v_and_b32 v247, 0xffff0000, v74
  v_fmac_f32 v246, v180, v52
  v_fmac_f32 v247, v181, v53
  v_cvt_pk_bf16_f32 v74, v246, v247
  v_lshlrev_b32 v246, 16, v75
  v_and_b32 v247, 0xffff0000, v75
  v_fmac_f32 v246, v182, v54
  v_fmac_f32 v247, v183, v55
  v_cvt_pk_bf16_f32 v75, v246, v247
  buffer_store_dwordx4 v[72:75], v21, s[40:43], 0 offen offset:128
  v_lshlrev_b32 v246, 16, v76
  v_and_b32 v247, 0xffff0000, v76
  v_fmac_f32 v246, v184, v56
  v_fmac_f32 v247, v185, v57
  v_cvt_pk_bf16_f32 v76, v246, v247
  v_lshlrev_b32 v246, 16, v77
  v_and_b32 v247, 0xffff0000, v77
  v_fmac_f32 v246, v186, v58
  v_fmac_f32 v247, v187, v59
  v_cvt_pk_bf16_f32 v77, v246, v247
  v_lshlrev_b32 v246, 16, v78
  v_and_b32 v247, 0xffff0000, v78
  v_fmac_f32 v246, v188, v60
  v_fmac_f32 v247, v189, v61
  v_cvt_pk_bf16_f32 v78, v246, v247
  v_lshlrev_b32 v246, 16, v79
  v_and_b32 v247, 0xffff0000, v79
  v_fmac_f32 v246, v190, v62
  v_fmac_f32 v247, v191, v63
  v_cvt_pk_bf16_f32 v79, v246, v247
  buffer_store_dwordx4 v[76:79], v21, s[40:43], 0 offen offset:192
LK_end:
)ASM"
    : [v_tid] "+v"(tid)
    : [s_ka] "s"(ka), [s_bid] "s"(blockIdx.x)
    : "v1", "v2", "v3", "v4", "v5", "v6", "v7", "v8", "v9", "v10", "v11", "v12", "v13", "v14", "v15", "v16", "v17", "v18", "v19", "v20", "v21", "v22", "v23", "v24", "v25", "v26", "v27", "v28", "v29", "v30", "v31", "v32", "v33", "v34", "v35", "v36", "v37", "v38", "v39", "v40", "v41", "v42", "v43", "v44", "v45", "v46", "v47", "v48", "v49", "v50", "v51", "v52", "v53", "v54", "v55", "v56", "v57", "v58", "v59", "v60", "v61", "v62", "v63", "v64", "v65", "v66", "v67", "v68", "v69", "v70", "v71", "v72", "v73", "v74", "v75", "v76", "v77", "v78", "v79", "v80", "v81", "v82", "v83", "v84", "v85", "v86", "v87", "v88", "v89", "v90", "v91", "v92", "v93", "v94", "v95", "v96", "v97", "v98", "v99", "v100", "v101", "v102", "v103", "v104", "v105", "v106", "v107", "v108", "v109", "v110", "v111", "v112", "v113", "v114", "v115", "v116", "v117", "v118", "v119", "v120", "v121", "v122", "v123", "v124", "v125", "v126", "v127", "v128", "v129", "v130", "v131", "v132", "v133", "v134", "v135", "v136", "v137", "v138", "v139", "v140", "v141", "v142", "v143", "v144", "v145", "v146", "v147", "v148", "v149", "v150", "v151", "v152", "v153", "v154", "v155", "v156", "v157", "v158", "v159", "v160", "v161", "v162", "v163", "v164", "v165", "v166", "v167", "v168", "v169", "v170", "v171", "v172", "v173", "v174", "v175", "v176", "v177", "v178", "v179", "v180", "v181", "v182", "v183", "v184", "v185", "v186", "v187", "v188", "v189", "v190", "v191", "v192", "v193", "v194", "v195", "v196", "v197", "v198", "v199", "v200", "v201", "v202", "v203", "v204", "v205", "v206", "v207", "v208", "v209", "v210", "v211", "v212", "v213", "v214", "v215", "v216", "v217", "v218", "v219", "v220", "v221", "v222", "v223", "v224", "v225", "v226", "v227", "v228", "v229", "v230", "v231", "v232", "v233", "v234", "v235", "v236", "v237", "v238", "v239", "v240", "v241", "v242", "v243", "v244", "v245", "v246", "v247", "v248", "v249", "v250", "v251", "v252", "v253", "v254", "v255", "a0", "a1", "a2", "a3", "a4", "a5", "a6", "a7", "a8", "a9", "a10", "a11", "a12", "a13", "a14", "a15", "a16", "a17", "a18", "a19", "a20", "a21", "a22", "a23", "a24", "a25", "a26", "a27", "a28", "a29", "a30", "a31", "a32", "a33", "a34", "a35", "a36", "a37", "a38", "a39", "a40", "a41", "a42", "a43", "a44", "a45", "a46", "a47", "a48", "a49", "a50", "a51", "a52", "a53", "a54", "a55", "a56", "a57", "a58", "a59", "a60", "a61", "a62", "a63", "a64", "a65", "a66", "a67", "a68", "a69", "a70", "a71", "a72", "a73", "a74", "a75", "a76", "a77", "a78", "a79", "a80", "a81", "a82", "a83", "a84", "a85", "a86", "a87", "a88", "a89", "a90", "a91", "a92", "a93", "a94", "a95", "a96", "a97", "a98", "a99", "a100", "a101", "a102", "a103", "a104", "a105", "a106", "a107", "a108", "a109", "a110", "a111", "a112", "a113", "a114", "a115", "a116", "a117", "a118", "a119", "a120", "a121", "a122", "a123", "a124", "a125", "a126", "a127", "a128", "a129", "a130", "a131", "a132", "a133", "a134", "a135", "a136", "a137", "a138", "a139", "a140", "a141", "a142", "a143", "a144", "a145", "a146", "a147", "a148", "a149", "a150", "a151", "a152", "a153", "a154", "a155", "a156", "a157", "a158", "a159", "a160", "a161", "a162", "a163", "a164", "a165", "a166", "a167", "a168", "a169", "a170", "a171", "a172", "a173", "a174", "a175", "a176", "a177", "a178", "a179", "a180", "a181", "a182", "a183", "a184", "a185", "a186", "a187", "a188", "a189", "a190", "a191", "a192", "a193", "a194", "a195", "a196", "a197", "a198", "a199", "a200", "a201", "a202", "a203", "a204", "a205", "a206", "a207", "a208", "a209", "a210", "a211", "a212", "a213", "a214", "a215", "a216", "a217", "a218", "a219", "a220", "a221", "a222", "a223", "a224", "a225", "a226", "a227", "a228", "a229", "a230", "a231", "a232", "a233", "a234", "a235", "a236", "a237", "a238", "a239", "a240", "a241", "a242", "a243", "a244", "a245", "a246", "a247", "a248", "a249", "a250", "a251", "a252", "a253", "a254", "a255", "s3", "s4", "s5", "s6", "s7", "s8", "s9", "s10", "s11", "s12", "s13", "s14", "s15", "s16", "s17", "s18", "s19", "s20", "s21", "s22", "s23", "s24", "s25", "s26", "s27", "s28", "s29", "s30", "s31", "s36", "s37", "s38", "s39", "s40", "s41", "s42", "s43", "s44", "s45", "s46", "s47", "s48", "s49", "s50", "s51", "s52", "s53", "s54", "s55", "s56", "s57", "s58", "s59", "s60", "s61", "s62", "s63", "s64", "s65", "s66", "s67", "s68", "s69", "s70", "s71", "s72", "s73", "s74", "s75", "s76", "s77", "s78", "s79", "s80", "s81", "s82", "s83", "s84", "s85", "s86", "s87", "s88", "s89", "s90", "s91", "s92", "s93", "s94", "s95", "s96", "s97", "s98", "s99", "vcc", "scc", "memory");
}

extern "C" void kernel_launch(void* const* d_in, const int* in_sizes, int n_in, void* d_out, int out_size, void* d_ws, size_t ws_size, hipStream_t stream) {
  const u16* x = (const u16*)d_in[0]; const u16* kc = (const u16*)d_in[1]; const u16* vc = (const u16*)d_in[2];
  const u16* wq = (const u16*)d_in[3]; const u16* wo = (const u16*)d_in[4];
  const u16* wg = (const u16*)d_in[6]; const u16* wu = (const u16*)d_in[7]; const u16* wd = (const u16*)d_in[8];
  char* ws = (char*)d_ws;
  u16* xnf = (u16*)ws; u16* Hf = (u16*)(ws + (4u << 20)); u16* Of = (u16*)(ws + (20u << 20));
  char* pbuf = ws + (40u << 20); unsigned* flags = (unsigned*)(ws + (64u << 20));
  kernBC<<<256, 256, 0, stream>>>(xnf, wq, wg, wu, kc, vc, Hf, Of, wo, wd, flags, x, (u16*)d_out, pbuf);
}
```
